```python
import torch

def _softmax(x, axis=-1):
    return torch.softmax(x, dim=axis)

def _gelu(x):
    return torch.nn.functional.gelu(x, approximate='tanh')

def _silu(x):
    return torch.nn.functional.silu(x)

def _sigmoid(x):
    return torch.sigmoid(x)

def _layernorm(x, axis=-1, eps=1e-05):
    m = x.mean(dim=axis, keepdim=True)
    v = x.var(dim=axis, keepdim=True, unbiased=False)
    return (x - m) / torch.sqrt(v + eps)

def _scatter_add(tgt, idx, src, axis=0):
    return tgt.clone().index_add_(axis, idx, src)

def _segment_sum(src, seg, n):
    out = torch.zeros((n,) + src.shape[1:], dtype=src.dtype)
    return out.index_add_(0, seg, src)

def _pad(x, pw):
    flat = tuple((v for pair in reversed(pw) for v in pair))
    return torch.nn.functional.pad(x, flat)
kernel_name = 'adaLN_cond__softmax_full_mha__mixer_gmlp__prompt'

def setup_inputs():

    _w = lambda a: (lambda t: t.to(torch.bfloat16) if t.is_floating_point() else t)(torch.as_tensor(a))

    def _A_setup():
        B, S, D = (1, 2048, 640)
        return {'x': _w((lambda *s: torch.randn(*s, dtype=torch.bfloat16) * 0.05)(B, S, D)), 'wqkv': _w((lambda *s: torch.randn(*s, dtype=torch.bfloat16) * 0.05)(D, 3 * D)), 'wo': _w((lambda *s: torch.randn(*s, dtype=torch.bfloat16) * 0.05)(D, D))}
    _A_in = _A_setup()

    def _B_setup():
        B, S, D, F = (1, 2048, 640, 1728)
        Fh = F // 2
        return {'x': _w((lambda *s: torch.randn(*s, dtype=torch.bfloat16) * 0.05)(B, S, D)), 'w_in': _w((lambda *s: torch.randn(*s, dtype=torch.bfloat16) * 0.4)(D, 2 * Fh)), 'w_sp': _w(0.01 * (lambda *s: torch.randn(*s, dtype=torch.bfloat16) * 0.4)(S, S)), 'b_sp': _w(1.0 + 0.01 * (lambda *s: torch.randn(*s, dtype=torch.bfloat16) * 0.05)(S)), 'w_out': _w((lambda *s: torch.randn(*s, dtype=torch.bfloat16) * 0.05)(Fh, D))}
    _B_in = _B_setup()
    return {'A_x': _A_in['x'], 'A_wqkv': _A_in['wqkv'], 'A_wo': _A_in['wo'], 'B_x': _B_in['x'], 'B_w_in': _B_in['w_in'], 'B_w_sp': _B_in['w_sp'], 'B_b_sp': _B_in['b_sp'], 'B_w_out': _B_in['w_out'], 'c': _w((lambda *s: torch.randn(*s, dtype=torch.bfloat16) * 0.1)(1, 640) * 8.0), 'w_mod': _w((lambda *s: torch.randn(*s, dtype=torch.bfloat16) * 0.1)(640, 6 * 640) * 640 ** (-0.5) * 8.0)}

def reference(A_x, A_wqkv, A_wo, B_x, B_w_in, B_w_sp, B_b_sp, B_w_out, c, w_mod):

    def _A_ref(x, wqkv, wo):
        B, S, D = x.shape
        H = 32
        Dh = D // H
        qkv = (lambda x, *d: x.permute(*d))((x @ wqkv).reshape(B, S, 3, H, Dh), 2, 0, 3, 1, 4)
        q, k, v = (qkv[0], qkv[1], qkv[2])
        scale = Dh ** (-0.5)
        scores = torch.einsum('bhqd,bhkd->bhqk', q, k) * scale
        p = _softmax(scores, axis=-1)
        o = torch.einsum('bhqk,bhkd->bhqd', p, v)
        return (lambda x, *d: x.permute(*d))(o, 0, 2, 1, 3).reshape(B, S, D) @ wo

    def _B_ref(x, w_in, w_sp, b_sp, w_out):
        B, S, D = x.shape
        F = w_in.shape[-1]
        Fh = F // 2
        z = _gelu(torch.einsum('bsd,df->bsf', x, w_in))
        z1 = z[..., :Fh].clone()
        z2 = _layernorm(z[..., Fh:], axis=-1)
        g = torch.einsum('ts,bsf->btf', w_sp, z2) + b_sp[None, :, None]
        return torch.einsum('bsf,fd->bsd', z1 * g, w_out)
    x = A_x
    mod = (_silu(c) @ w_mod).reshape(c.shape[0], 6, 1, -1)
    sa, ca, ga, sb, cb, gb = (mod[:, 0].clone(), mod[:, 1].clone(), mod[:, 2].clone(), mod[:, 3].clone(), mod[:, 4].clone(), mod[:, 5].clone())
    h = _layernorm(x, axis=-1) * (1 + ca) + sa
    x = x + ga * _A_ref(h, A_wqkv, A_wo)
    h = _layernorm(x, axis=-1) * (1 + cb) + sb
    x = x + gb * _B_ref(h, B_w_in, B_w_sp, B_b_sp, B_w_out)
    return x
```

```cpp
#include <hip/hip_runtime.h>
#include <hip/hip_bf16.h>
#include <stdint.h>

typedef __attribute__((ext_vector_type(8))) short bf16x8;
typedef __attribute__((ext_vector_type(4))) short bf16x4;
typedef __attribute__((ext_vector_type(2))) float f32x2;
typedef __attribute__((ext_vector_type(4))) float f32x4;
typedef __attribute__((ext_vector_type(16))) float f32x16;
typedef __attribute__((ext_vector_type(2))) unsigned u32x2;
typedef __attribute__((ext_vector_type(4))) unsigned u32x4;
typedef __attribute__((ext_vector_type(2))) __bf16 bf2_t;
typedef unsigned short u16;
typedef unsigned long long u64;
#define LAS __attribute__((address_space(3)))
#define GAS __attribute__((address_space(1)))
#define DI __device__ __forceinline__

constexpr int S_ = 2048, D_ = 640, NQKV = 1920, F_ = 1728, FH = 864;

constexpr size_t OFF_MOD = 0;
constexpr size_t OFF_XCH = 16384;
constexpr size_t OFF_XHAT = OFF_XCH + 131072;
constexpr size_t OFF_WQKV = OFF_XHAT + 2621440;
constexpr size_t OFF_WO = OFF_WQKV + 2457600;
constexpr size_t OFF_WIN = OFF_WO + 819200;
constexpr size_t OFF_WOUT = OFF_WIN + 2211840;
constexpr size_t OFF_Q = OFF_WOUT + 1105920;
constexpr size_t OFF_KV = OFF_Q + 32 * 81920;
constexpr size_t OFF_O = OFF_KV + 32 * 163840;
constexpr size_t OFF_X1 = OFF_O + 2621440;
constexpr size_t OFF_Z1 = OFF_X1 + 2621440;
constexpr size_t OFF_Z2 = OFF_Z1 + 3538944;
constexpr size_t OFF_U = OFF_Z2 + 3538944;

DI unsigned pack2(float lo, float hi) { return __builtin_bit_cast(unsigned, __builtin_convertvector((f32x2){lo, hi}, bf2_t)); }
DI float bflo(unsigned u) { return __uint_as_float(u << 16); }
DI float bfhi(unsigned u) { return __uint_as_float(u & 0xffff0000u); }
DI float rbf(float x) { return bflo(pack2(x, x)); }
DI float bf2f(u16 b) { return __uint_as_float((unsigned)b << 16); }
#define MFMA32(a, b, c) __builtin_amdgcn_mfma_f32_32x32x16_bf16((a), (b), (c), 0, 0, 0)
#define MFMA32K8(a, b, c) __builtin_amdgcn_mfma_f32_32x32x8bf16_1k((a), (b), (c), 0, 0, 0)
#define MFMA16(a, b, c) __builtin_amdgcn_mfma_f32_16x16x32_bf16((a), (b), (c), 0, 0, 0)
#define BAR() asm volatile("s_waitcnt lgkmcnt(0)\n\ts_barrier" ::: "memory")
#define WAITV(n) asm volatile("s_waitcnt vmcnt(" #n ")" ::: "memory")
typedef short v4i16_t __attribute__((ext_vector_type(4)));
DI bf16x4 lds_tr(const char* p) { return __builtin_bit_cast(bf16x4, __builtin_amdgcn_ds_read_tr16_b64_v4i16((LAS v4i16_t*)p)); }
DI void glds16(const char* g, char* l) { __builtin_amdgcn_global_load_lds((const GAS void*)g, (LAS void*)l, 16, 0, 0); }
DI float xhalf(float v) {
  auto rr = __builtin_amdgcn_permlane32_swap(__float_as_uint(v), __float_as_uint(v), false, false);
  return __uint_as_float((threadIdx.x & 32) ? rr[0] : rr[1]);
}
DI float ln_den(float var) { return rbf(__builtin_sqrtf(rbf(rbf(var) + 1e-5f))); }
DI int unit0_of(int j) { return j < 6 ? 7 * j : 42 + 6 * (j - 6); }

constexpr int K0_MOD = 120, K0_LN = 64, K0_JOBS = 600 + 200 + 540 + 270, K0_RL = (K0_JOBS + 3) / 4, K0_INIT = 8;
constexpr int K0_GRID = K0_MOD + K0_LN + K0_RL + K0_INIT;

__global__ __launch_bounds__(256) void k0_prep(const u16* __restrict__ x, const u16* __restrict__ wqkv, const u16* __restrict__ wo,
                                               const u16* __restrict__ win, const u16* __restrict__ wout, const u16* __restrict__ cvec,
                                               const u16* __restrict__ wmod, char* __restrict__ ws) {
  __shared__ __attribute__((aligned(16))) char smem[4 * 4608];
  const int tid = threadIdx.x, lane = tid & 63, wid = __builtin_amdgcn_readfirstlane(tid >> 6);
  int b = blockIdx.x;
  if (b < K0_MOD) {
    const int c = tid & 3, rg = tid >> 2, col0 = b * 32 + c * 8;
    u32x4 w[10]; u16 cv[10];
#pragma unroll
    for (int i = 0; i < 10; ++i) { w[i] = *(const u32x4*)(wmod + (size_t)(rg + 64 * i) * 3840 + col0); cv[i] = cvec[rg + 64 * i]; }
    float acc[8] = {0, 0, 0, 0, 0, 0, 0, 0};
#pragma unroll
    for (int i = 0; i < 10; ++i) {
      const float cf = bf2f(cv[i]); const float s = rbf(cf / (1.f + __expf(-cf)));
#pragma unroll
      for (int e = 0; e < 4; ++e) { acc[2 * e] += s * bflo(w[i][e]); acc[2 * e + 1] += s * bfhi(w[i][e]); }
    }
#pragma unroll
    for (int e = 0; e < 8; ++e) {
#pragma unroll
      for (int m = 4; m < 64; m <<= 1) acc[e] += __shfl_xor(acc[e], m);
    }
    float* red = (float*)smem;
    if (lane < 4) {
#pragma unroll
      for (int e = 0; e < 8; ++e) red[wid * 32 + lane * 8 + e] = acc[e];
    }
    __syncthreads();
    if (tid < 32) {
      float v = rbf(red[tid] + red[32 + tid] + red[64 + tid] + red[96 + tid]);
      const int col = b * 32 + tid, sec = col / 640;
      if (sec == 1 || sec == 4) v = rbf(1.f + v);
      ((float*)(ws + OFF_MOD))[col] = v;
    }
    return;
  }
  b -= K0_MOD;
  if (b < K0_LN) {
    const int r = lane & 31, h = lane >> 5;
    const u16* xr = x + (size_t)(b * 32 + r) * D_ + wid * 160 + h * 8;
    u32x4 v[10];
#pragma unroll
    for (int i = 0; i < 10; ++i) v[i] = *(const u32x4*)(xr + i * 16);
    float s = 0.f, q = 0.f;
#pragma unroll
    for (int i = 0; i < 10; ++i)
#pragma unroll
      for (int e = 0; e < 4; ++e) { const float a = bflo(v[i][e]), c = bfhi(v[i][e]); s += a + c; q += a * a + c * c; }
    s += xhalf(s); q += xhalf(q);
    float* red = (float*)smem;
    if (h == 0) { red[(wid * 32 + r) * 2] = s; red[(wid * 32 + r) * 2 + 1] = q; }
    __syncthreads();
    s = red[r * 2] + red[(32 + r) * 2] + red[(64 + r) * 2] + red[(96 + r) * 2];
    q = red[r * 2 + 1] + red[(32 + r) * 2 + 1] + red[(64 + r) * 2 + 1] + red[(96 + r) * 2 + 1];
    const float mean = s * (1.f / 640.f); const float var = fmaxf(q * (1.f / 640.f) - mean * mean, 0.f);
    const float rinv = 1.f / ln_den(var), mb = rbf(mean) * rinv;
    char* dst = ws + OFF_XHAT + ((size_t)b * 40 + wid * 10) * 1024 + lane * 16;
#pragma unroll
    for (int i = 0; i < 10; ++i) {
      u32x4 o;
#pragma unroll
      for (int e = 0; e < 4; ++e) o[e] = pack2(bflo(v[i][e]) * rinv - mb, bfhi(v[i][e]) * rinv - mb);
      *(u32x4*)(dst + i * 1024) = o;
    }
    return;
  }
  b -= K0_LN;
  if (b < K0_RL) {
    int job = b * 4 + wid;
    if (job >= K0_JOBS) return;
    const u16* W; int N, nks, kt, ng; char* dst; bool f32frag = false;
    if (job < 600) { W = wqkv; N = NQKV; kt = job / 30; ng = job % 30; dst = ws + OFF_WQKV; f32frag = true; nks = 40; }
    else if (job < 800) { job -= 600; W = wo; N = D_; kt = job / 10; ng = job % 10; dst = ws + OFF_WO; nks = 20; }
    else if (job < 1340) { job -= 800; W = win; N = F_; kt = job / 27; ng = job % 27; dst = ws + OFF_WIN; nks = 20; }
    else { job -= 1340; W = wout; N = D_; kt = job / 10; ng = job % 10; dst = ws + OFF_WOUT; nks = 27; }
    char* t = smem + wid * 4608;
    u32x4 v[4];
#pragma unroll
    for (int i = 0; i < 4; ++i) v[i] = *(const u32x4*)(W + (size_t)(kt * 32 + i * 8 + (lane >> 3)) * N + ng * 64 + (lane & 7) * 8);
#pragma unroll
    for (int i = 0; i < 4; ++i) *(u32x4*)(t + (i * 8 + (lane >> 3)) * 144 + (lane & 7) * 16) = v[i];
    asm volatile("s_waitcnt lgkmcnt(0)" ::: "memory");
    const int g = lane >> 4, q = (lane & 15) >> 2, p = lane & 3;
#pragma unroll
    for (int f = 0; f < 4; ++f) {
      int row, col; size_t o;
      if (f32frag) { row = 16 * (f >> 1) + 8 * (g >> 1) + q; col = 32 * (f & 1) + 16 * (g & 1) + 4 * p; o = ((size_t)(ng * 2 + (f & 1)) * 40 + kt * 2 + (f >> 1)) * 1024; }
      else { row = 8 * g + q; col = 16 * f + 4 * p; o = ((size_t)(ng * 4 + f) * nks + kt) * 1024; }
      const bf16x4 lo = lds_tr(t + row * 144 + col * 2), hi = lds_tr(t + (row + 4) * 144 + col * 2);
      *(bf16x8*)(dst + o + lane * 16) = __builtin_shufflevector(lo, hi, 0, 1, 2, 3, 4, 5, 6, 7);
    }
    return;
  }
  b -= K0_RL;
  {
    u32x4* p = (u32x4*)(ws + OFF_XCH) + b * 1024 + tid;
#pragma unroll
    for (int i = 0; i < 4; ++i) p[i * 256] = (u32x4){~0u, ~0u, ~0u, ~0u};
  }
}

constexpr float QSCALE = 0.22360679774997896f * 1.4426950408889634f;

template <bool ISV>
DI void k1_mma(const char* __restrict__ bsrc, const char* lds, f32x16 (&acc)[4]) {
  bf16x8 bfr[40];
#pragma unroll
  for (int k = 0; k < 28; ++k) bfr[k] = *(const bf16x8*)(bsrc + k * 1024);
#pragma unroll
  for (int m = 0; m < 4; ++m) acc[m] = (f32x16){};
  BAR();
#pragma unroll
  for (int s = 0; s < 10; ++s) {
    BAR();
    const char* al = lds + (s & 1) * 16384;
#pragma unroll
    for (int k = 0; k < 4; ++k)
#pragma unroll
      for (int m = 0; m < 4; ++m) {
        const bf16x8 af = *(const bf16x8*)(al + (k * 4 + m) * 1024);
        acc[m] = ISV ? MFMA32(af, bfr[s * 4 + k], acc[m]) : MFMA32(bfr[s * 4 + k], af, acc[m]);
      }
    if (s < 3) {
#pragma unroll
      for (int k = 28 + 4 * s; k < 32 + 4 * s; ++k) bfr[k] = *(const bf16x8*)(bsrc + k * 1024);
    }
  }
}

__global__ __launch_bounds__(512, 2) void k1_qkv(char* __restrict__ ws) {
  __shared__ __attribute__((aligned(16))) char smem[2 * 16384 + 5120];
  const int tid = threadIdx.x, lane = tid & 63, wid = __builtin_amdgcn_readfirstlane(tid >> 6);
  const int xcd = blockIdx.x & 7, idx = blockIdx.x >> 3;
  const int mt = xcd * 2 + (idx / 15), nt = idx % 15;
  float* cst = (float*)(smem + 32768);
  if (wid >= 4) {
    const int pw = wid - 4;
    const char* src = ws + OFF_XHAT + ((size_t)mt * 4 * 40 + pw) * 1024 + lane * 16;
    u32x4 a[10][4];
    f32x4 c0;
    const int ct = tid - 256;
    c0 = *(const f32x4*)(ws + OFF_MOD + ct * 16);
    f32x4 c1; if (ct < 64) c1 = *(const f32x4*)(ws + OFF_MOD + 4096 + ct * 16);
#pragma unroll
    for (int s = 0; s < 10; ++s)
#pragma unroll
      for (int m = 0; m < 4; ++m) a[s][m] = *(const u32x4*)(src + (size_t)(m * 40 + s * 4) * 1024);
    *(f32x4*)(cst + ct * 4) = c0; if (ct < 64) *(f32x4*)(cst + 1024 + ct * 4) = c1;
    BAR();
#pragma unroll
    for (int s = 0; s < 10; ++s) {
      const int k0 = (s * 4 + pw) * 16 + (lane >> 5) * 8;
      const f32x4 sh0 = *(const f32x4*)(cst + k0), sh1 = *(const f32x4*)(cst + k0 + 4);
      const f32x4 sc0 = *(const f32x4*)(cst + 640 + k0), sc1 = *(const f32x4*)(cst + 640 + k0 + 4);
#pragma unroll
      for (int m = 0; m < 4; ++m) {
        u32x4 o; const u32x4 v = a[s][m];
        o[0] = pack2(bflo(v[0]) * sc0[0] + sh0[0], bfhi(v[0]) * sc0[1] + sh0[1]);
        o[1] = pack2(bflo(v[1]) * sc0[2] + sh0[2], bfhi(v[1]) * sc0[3] + sh0[3]);
        o[2] = pack2(bflo(v[2]) * sc1[0] + sh1[0], bfhi(v[2]) * sc1[1] + sh1[1]);
        o[3] = pack2(bflo(v[3]) * sc1[2] + sh1[2], bfhi(v[3]) * sc1[3] + sh1[3]);
        *(u32x4*)(smem + (s & 1) * 16384 + (pw * 4 + m) * 1024 + lane * 16) = o;
      }
      BAR();
    }
    return;
  }
  const char* bsrc = ws + OFF_WQKV + (size_t)(nt * 4 + wid) * 40 * 1024 + lane * 16;
  f32x16 acc[4];
  const bool isV = nt >= 10;
  if (isV) k1_mma<true>(bsrc, smem + lane * 16, acc); else k1_mma<false>(bsrc, smem + lane * 16, acc);
  const int r = lane & 31, h = lane >> 5, n0 = nt * 128 + wid * 32;
  if (isV) {
    const int c = n0 + r - 1280, head = c / 20, d = c % 20;
#pragma unroll
    for (int m = 0; m < 4; ++m) {
      const int tile = mt * 4 + m;
      char* dst = ws + OFF_KV + (size_t)head * 163840 + (tile >> 3) * 20480 + 10240 + (tile & 7) * 1280 + (h * 20 + d) * 16;
#pragma unroll
      for (int s = 0; s < 2; ++s) {
        u32x4 o;
#pragma unroll
        for (int e = 0; e < 4; ++e) o[e] = pack2(acc[m][8 * s + 2 * e], acc[m][8 * s + 2 * e + 1]);
        *(u32x4*)(dst + s * 640) = o;
      }
    }
  } else {
    const bool isQ = nt < 5; const float sc = isQ ? QSCALE : 1.f;
#pragma unroll
    for (int qd = 0; qd < 4; ++qd) {
      const int c = n0 + 8 * qd + 4 * h - (isQ ? 0 : 640), head = c / 20, dq = (c % 20) >> 2;
#pragma unroll
      for (int m = 0; m < 4; ++m) {
        const int tile = mt * 4 + m;
        char *p1, *p2;
        if (isQ) { p1 = ws + OFF_Q + (size_t)head * 81920 + tile * 1280; p2 = p1 + 1024; }
        else { char* g = ws + OFF_KV + (size_t)head * 163840 + (tile >> 3) * 20480; p1 = g + (tile & 7) * 1024; p2 = g + 8192 + (tile & 7) * 256; }
        const unsigned w0 = pack2(acc[m][4 * qd] * sc, acc[m][4 * qd + 1] * sc), w1 = pack2(acc[m][4 * qd + 2] * sc, acc[m][4 * qd + 3] * sc);
        if (dq < 4) *(u32x2*)(p1 + ((dq >> 1) * 32 + r) * 16 + (dq & 1) * 8) = (u32x2){w0, w1};
        else { *(unsigned*)(p2 + r * 4) = w0; *(unsigned*)(p2 + 128 + r * 4) = w1; }
      }
    }
  }
}

DI void split3(float m, unsigned& w_h0, unsigned& w_h1) {
  const float n = -m; const float a = rbf(n), b = rbf(n - a), c = rbf(n - a - b);
  w_h0 = pack2(a, b); w_h1 = pack2(c, 0.f);
}

__global__ __launch_bounds__(512, 2) void k2_attn(char* __restrict__ ws) {
  extern __shared__ __attribute__((aligned(16))) char smem[];
  const int tid = threadIdx.x, lane = tid & 63, wid = __builtin_amdgcn_readfirstlane(tid >> 6);
  const int xcd = blockIdx.x & 7, idx = blockIdx.x >> 3, head = xcd * 4 + (idx >> 3), qb = idx & 7;
  const int r = lane & 31, h = lane >> 5, qg = wid & 3, kh = wid >> 2;
  const char* kv = ws + OFF_KV + (size_t)head * 163840;
  const char* qp = ws + OFF_Q + (size_t)head * 81920 + (qb * 8 + qg * 2) * 1280;
  const unsigned kconst = h == 0 ? 0x3f803f80u : 0x00003f80u;
  for (int attempt = 0; attempt < 2; ++attempt) {
    const bf16x8 qA1 = *(const bf16x8*)(qp + lane * 16), qB1 = *(const bf16x8*)(qp + 1280 + lane * 16);
    const unsigned qA2w = *(const unsigned*)(qp + 1024 + lane * 4), qB2w = *(const unsigned*)(qp + 1280 + 1024 + lane * 4);
    bf16x8 k1 = *(const bf16x8*)(kv + lane * 16); unsigned k2 = *(const unsigned*)(kv + 8192 + lane * 4);
    u32x4 st[20];
#pragma unroll
    for (int i = 0; i < 20; ++i) { const int c = (i % 5) * 512 + tid, g = (c >= 1280 ? 4 : 0) + i / 5, off = (c >= 1280 ? c - 1280 : c) * 16; st[i] = *(const u32x4*)(kv + g * 20480 + off); }
    float mA = -INFINITY, mB = -INFINITY;
    {
      const bf16x4 qA2 = __builtin_bit_cast(bf16x4, (u32x2){qA2w, 0u}), qB2 = __builtin_bit_cast(bf16x4, (u32x2){qB2w, 0u});
      const int nt = attempt ? 64 : 1;
      bf16x8 t1 = k1; unsigned t2 = k2;
      for (int t = 0; t < nt; ++t) {
        if (t) { t1 = *(const bf16x8*)(kv + (t >> 3) * 20480 + (t & 7) * 1024 + lane * 16); t2 = *(const unsigned*)(kv + (t >> 3) * 20480 + 8192 + (t & 7) * 256 + lane * 4); }
        const bf16x4 t2v = __builtin_bit_cast(bf16x4, (u32x2){t2, kconst});
        f32x16 a = MFMA32(t1, qA1, (f32x16){}); a = MFMA32K8(t2v, qA2, a);
        f32x16 b = MFMA32(t1, qB1, (f32x16){}); b = MFMA32K8(t2v, qB2, b);
#pragma unroll
        for (int i = 0; i < 16; ++i) { mA = fmaxf(mA, a[i]); mB = fmaxf(mB, b[i]); }
      }
      mA = fmaxf(mA, xhalf(mA)); mB = fmaxf(mB, xhalf(mB));
    }
    unsigned a0, a1, b0, b1; split3(mA, a0, a1); split3(mB, b0, b1);
    const bf16x4 qA2 = __builtin_bit_cast(bf16x4, (u32x2){qA2w, h ? a1 : a0}), qB2 = __builtin_bit_cast(bf16x4, (u32x2){qB2w, h ? b1 : b0});
    if (tid < 64) st[0] = (u32x4){0x3f803f80u, 0x3f803f80u, 0x3f803f80u, 0x3f803f80u};
#pragma unroll
    for (int i = 0; i < 5; ++i) { const int c = i * 512 + tid, g = (c >= 1280 ? 4 : 0), off = (c >= 1280 ? c - 1280 : c) * 16; *(u32x4*)(smem + g * 20480 + off) = st[i]; }
    BAR();
    f32x16 oA = (f32x16){}, oB = (f32x16){};
    const char* kp1 = smem + kh * 81920 + lane * 16;
    const char* kp2 = smem + kh * 81920 + 8192 + lane * 4;
    const bool vreal = r < 20;
    const char* vp = vreal ? smem + kh * 81920 + 10240 + (h * 20 + r) * 16 : smem;
    const int vinc = vreal ? 1280 : 0, vjump = vreal ? 10240 : 0;
    if (kh) { k1 = *(const bf16x8*)kp1; k2 = *(const unsigned*)kp2; }
    for (int j = 0; j < 4; ++j) {
#pragma unroll
      for (int t = 0; t < 8; ++t) {
        if (t > 0 || j > 0) { k1 = *(const bf16x8*)(kp1 + t * 1024); k2 = *(const unsigned*)(kp2 + t * 256); }
        const bf16x8 v0 = *(const bf16x8*)vp, v1 = *(const bf16x8*)(vp + 640);
        vp += vinc;
        const bf16x4 k2v = __builtin_bit_cast(bf16x4, (u32x2){k2, kconst});
        f32x16 a = MFMA32(k1, qA1, (f32x16){}); a = MFMA32K8(k2v, qA2, a);
        f32x16 b = MFMA32(k1, qB1, (f32x16){}); b = MFMA32K8(k2v, qB2, b);
        u32x4 p0, p1;
#pragma unroll
        for (int e = 0; e < 4; ++e) {
          p0[e] = pack2(__builtin_amdgcn_exp2f(a[2 * e]), __builtin_amdgcn_exp2f(a[2 * e + 1]));
          p1[e] = pack2(__builtin_amdgcn_exp2f(a[8 + 2 * e]), __builtin_amdgcn_exp2f(a[8 + 2 * e + 1]));
        }
        oA = MFMA32(v0, __builtin_bit_cast(bf16x8, p0), oA); oA = MFMA32(v1, __builtin_bit_cast(bf16x8, p1), oA);
#pragma unroll
        for (int e = 0; e < 4; ++e) {
          p0[e] = pack2(__builtin_amdgcn_exp2f(b[2 * e]), __builtin_amdgcn_exp2f(b[2 * e + 1]));
          p1[e] = pack2(__builtin_amdgcn_exp2f(b[8 + 2 * e]), __builtin_amdgcn_exp2f(b[8 + 2 * e + 1]));
        }
        oB = MFMA32(v0, __builtin_bit_cast(bf16x8, p0), oB); oB = MFMA32(v1, __builtin_bit_cast(bf16x8, p1), oB);
      }
      kp1 += 20480; kp2 += 20480; vp += vjump;
      if (j == 0) {
#pragma unroll
        for (int i = 5; i < 20; ++i) { const int c = (i % 5) * 512 + tid, g = (c >= 1280 ? 4 : 0) + i / 5, off = (c >= 1280 ? c - 1280 : c) * 16; *(u32x4*)(smem + g * 20480 + off) = st[i]; }
        BAR();
      }
    }
    BAR();
    {
      f32x4* ex = (f32x4*)(smem + 1024) + (wid * 3) * 64 + lane;
      const f32x16 give = kh ? oA : oB;
#pragma unroll
      for (int g = 0; g < 3; ++g) ex[g * 64] = (f32x4){give[4 * g], give[4 * g + 1], give[4 * g + 2], give[4 * g + 3]};
    }
    int* flag = (int*)smem;
    if (tid == 0) *flag = 0;
    BAR();
    float o[12];
    {
      const f32x4* ex = (const f32x4*)(smem + 1024) + ((wid ^ 4) * 3) * 64 + lane;
      const f32x16 keep = kh ? oB : oA;
#pragma unroll
      for (int g = 0; g < 3; ++g) { const f32x4 t = ex[g * 64];
#pragma unroll
        for (int e = 0; e < 4; ++e) o[4 * g + e] = keep[4 * g + e] + t[e]; }
    }
    const float lo = xhalf(o[8]); const float l = h ? o[8] : lo;
    float amax = 0.f;
#pragma unroll
    for (int i = 0; i < 12; ++i) amax = fmaxf(amax, fabsf(o[i]));
    bool ok = (l > 1e-30f) && (l < 1e30f) && (amax < 3e38f);
#pragma unroll
    for (int i = 0; i < 12; ++i) ok = ok && (o[i] == o[i]);
    if (attempt == 0) {
      if (!ok) *flag = 1;
      BAR();
      const int redo = *flag;
      BAR();
      if (redo) continue;
    }
    const float rl = 1.f / l;
    const int tok = qb * 256 + qg * 64 + kh * 32 + r;
    char* ob = ws + OFF_O + (size_t)(tok >> 4) * 20480 + (tok & 15) * 16;
#pragma unroll
    for (int g = 0; g < 3; ++g) {
      if (g < 2 || h == 0) {
        const int k = head * 20 + 8 * g + 4 * h;
        *(u32x2*)(ob + (k >> 5) * 1024 + ((k & 31) >> 3) * 256 + (k & 4) * 2) = (u32x2){pack2(o[4 * g] * rl, o[4 * g + 1] * rl), pack2(o[4 * g + 2] * rl, o[4 * g + 3] * rl)};
      }
    }
    break;
  }
}

template <int NKS, int KPS, bool FINAL>
__global__ __launch_bounds__(448) void k_proj(char* __restrict__ ws, const u16* __restrict__ x, u16* __restrict__ out) {
  extern __shared__ __attribute__((aligned(16))) char smem[];
  constexpr int NST = NKS / KPS;
  const int tid = threadIdx.x, lane = tid & 63, wid = __builtin_amdgcn_readfirstlane(tid >> 6);
  const int xcd = blockIdx.x & 7, idx = blockIdx.x >> 3, panel = xcd * 4 + (idx >> 3), cb = idx & 7;
  if (wid >= 5) {
    const int pw = wid - 5;
    const char* src = ws + (FINAL ? OFF_U : OFF_O) + (size_t)(panel * 4 + pw * 2) * NKS * 1024 + lane * 16;
    char* dst = smem + pw * 2 * NKS * 1024;
#pragma unroll
    for (int k = 0; k < NKS; ++k) { glds16(src + k * 1024, dst + k * 1024); glds16(src + (NKS + k) * 1024, dst + (NKS + k) * 1024); }
    if (NST == 4) { WAITV(30); BAR(); WAITV(20); BAR(); WAITV(10); BAR(); WAITV(0); BAR(); }
    else { WAITV(36); BAR(); WAITV(18); BAR(); WAITV(0); BAR(); }
    return;
  }
  const int u = cb * 5 + wid;
  const char* bsrc = ws + (FINAL ? OFF_WOUT : OFF_WO) + (size_t)u * NKS * 1024 + lane * 16;
  bf16x8 bfr[NKS];
#pragma unroll
  for (int k = 0; k < NKS; ++k) bfr[k] = *(const bf16x8*)(bsrc + k * 1024);
  const int n = u * 16 + (lane >> 4) * 4;
  const size_t fo = (size_t)(n >> 5) * 1024 + (((n & 31) >> 3) * 16 + (lane & 15)) * 16 + (n & 4) * 2;
  u32x2 res[4];
#pragma unroll
  for (int m = 0; m < 4; ++m) {
    if (FINAL) res[m] = *(const u32x2*)(ws + OFF_X1 + (size_t)(panel * 4 + m) * 20480 + fo);
    else res[m] = *(const u32x2*)(x + (size_t)(panel * 64 + m * 16 + (lane & 15)) * D_ + n);
  }
  const f32x4 gate = *(const f32x4*)(ws + OFF_MOD + ((FINAL ? 5 : 2) * 640 + n) * 4);
  f32x4 acc[4];
#pragma unroll
  for (int m = 0; m < 4; ++m) acc[m] = (f32x4){};
#pragma unroll
  for (int s = 0; s < NST; ++s) {
    BAR();
#pragma unroll
    for (int k = s * KPS; k < (s + 1) * KPS; ++k)
#pragma unroll
      for (int m = 0; m < 4; ++m) acc[m] = MFMA16(bfr[k], *(const bf16x8*)(smem + (m * NKS + k) * 1024 + lane * 16), acc[m]);
  }
#pragma unroll
  for (int m = 0; m < 4; ++m) {
    const float r0 = bflo(res[m][0]) + rbf(gate[0] * rbf(acc[m][0])), r1 = bfhi(res[m][0]) + rbf(gate[1] * rbf(acc[m][1]));
    const float r2 = bflo(res[m][1]) + rbf(gate[2] * rbf(acc[m][2])), r3 = bfhi(res[m][1]) + rbf(gate[3] * rbf(acc[m][3]));
    const u32x2 o = (u32x2){pack2(r0, r1), pack2(r2, r3)};
    if (FINAL) *(u32x2*)(out + (size_t)(panel * 64 + m * 16 + (lane & 15)) * D_ + n) = o;
    else *(u32x2*)(ws + OFF_X1 + (size_t)(panel * 4 + m) * 20480 + fo) = o;
  }
}

DI float gelu_t(float v) {
  const float x = rbf(v);
  const float u = x * (0.7978845608028654f + 0.7978845608028654f * 0.044715f * x * x);
  const float e = __builtin_amdgcn_exp2f(u * -2.8853900817779268f);
  return x * __builtin_amdgcn_rcpf(1.f + e);
}
constexpr int K4_A = 0, K4_CST = 81920, K4_ST = K4_CST + 5120, K4_ST2 = K4_ST + 2048, K4_FIN = K4_ST2 + 3584, K4_TR = K4_FIN + 512, K4_LDS = K4_TR + 7 * 2048;

__global__ __launch_bounds__(512, 2) void k4_win(char* __restrict__ ws) {
  extern __shared__ __attribute__((aligned(16))) char smem[];
  const int tid = threadIdx.x, lane = tid & 63, wid = __builtin_amdgcn_readfirstlane(tid >> 6);
  const int xcd = blockIdx.x & 7, idx = blockIdx.x >> 3, panel = xcd * 4 + (idx >> 3), cb = idx & 7;
  const int nu = cb < 6 ? 7 : 6, u = unit0_of(cb) + wid;
  const bool cons = wid < nu;
  const int mp = wid & 1, kq = wid >> 1, g = lane >> 4, r = lane & 15;
  u32x4 a[2][5];
  const char* asrc = ws + OFF_X1 + ((size_t)(panel * 4 + mp * 2) * 20 + kq * 5) * 1024 + lane * 16;
#pragma unroll
  for (int k = 0; k < 5; ++k) { a[0][k] = *(const u32x4*)(asrc + k * 1024); a[1][k] = *(const u32x4*)(asrc + (20 + k) * 1024); }
  f32x4 cv; if (tid < 320) cv = *(const f32x4*)(ws + OFF_MOD + 3 * 2560 + tid * 16);
  bf16x8 b2[20], b1[20];
  if (cons) {
    const char* s2 = ws + OFF_WIN + (size_t)(54 + u) * 20480 + lane * 16;
    const char* s1 = ws + OFF_WIN + (size_t)u * 20480 + lane * 16;
#pragma unroll
    for (int k = 0; k < 20; ++k) b2[k] = *(const bf16x8*)(s2 + k * 1024);
#pragma unroll
    for (int k = 0; k < 6; ++k) b1[k] = *(const bf16x8*)(s1 + k * 1024);
  }
  float* cst = (float*)(smem + K4_CST); float* stp = (float*)(smem + K4_ST);
#pragma unroll
  for (int m = 0; m < 2; ++m) {
    float s = 0.f, q = 0.f;
#pragma unroll
    for (int k = 0; k < 5; ++k)
#pragma unroll
      for (int e = 0; e < 4; ++e) { const float lo = bflo(a[m][k][e]), hi = bfhi(a[m][k][e]); s += lo + hi; q += lo * lo + hi * hi; }
    s += __shfl_xor(s, 16); q += __shfl_xor(q, 16); s += xhalf(s); q += xhalf(q);
    if (g == 0) *(f32x2*)(stp + (kq * 64 + (mp * 2 + m) * 16 + r) * 2) = (f32x2){s, q};
  }
  if (tid < 320) *(f32x4*)(cst + tid * 4) = cv;
  BAR();
  float rinv[2], mb[2];
#pragma unroll
  for (int m = 0; m < 2; ++m) {
    float s = 0.f, q = 0.f;
#pragma unroll
    for (int i = 0; i < 4; ++i) { const f32x2 t = *(const f32x2*)(stp + (i * 64 + (mp * 2 + m) * 16 + r) * 2); s += t[0]; q += t[1]; }
    const float mean = s * (1.f / 640.f), var = fmaxf(q * (1.f / 640.f) - mean * mean, 0.f);
    rinv[m] = 1.f / ln_den(var); mb[m] = rbf(mean) * rinv[m];
  }
#pragma unroll
  for (int k = 0; k < 5; ++k) {
    const int k0 = (kq * 5 + k) * 32 + g * 8;
    const f32x4 sh0 = *(const f32x4*)(cst + k0), sh1 = *(const f32x4*)(cst + k0 + 4);
    const f32x4 sc0 = *(const f32x4*)(cst + 640 + k0), sc1 = *(const f32x4*)(cst + 640 + k0 + 4);
#pragma unroll
    for (int m = 0; m < 2; ++m) {
      const u32x4 v = a[m][k]; u32x4 o; const float ri = rinv[m], mm = mb[m];
      o[0] = pack2((bflo(v[0]) * ri - mm) * sc0[0] + sh0[0], (bfhi(v[0]) * ri - mm) * sc0[1] + sh0[1]);
      o[1] = pack2((bflo(v[1]) * ri - mm) * sc0[2] + sh0[2], (bfhi(v[1]) * ri - mm) * sc0[3] + sh0[3]);
      o[2] = pack2((bflo(v[2]) * ri - mm) * sc1[0] + sh1[0], (bfhi(v[2]) * ri - mm) * sc1[1] + sh1[1]);
      o[3] = pack2((bflo(v[3]) * ri - mm) * sc1[2] + sh1[2], (bfhi(v[3]) * ri - mm) * sc1[3] + sh1[3]);
      *(u32x4*)(smem + ((mp * 2 + m) * 20 + kq * 5 + k) * 1024 + lane * 16) = o;
    }
  }
  BAR();
  float* st2 = (float*)(smem + K4_ST2); float* fin = (float*)(smem + K4_FIN);
  if (cons) {
    {
      const char* s1 = ws + OFF_WIN + (size_t)u * 20480 + lane * 16;
#pragma unroll
      for (int k = 6; k < 20; ++k) b1[k] = *(const bf16x8*)(s1 + k * 1024);
    }
    f32x4 acc[4];
#pragma unroll
    for (int m = 0; m < 4; ++m) acc[m] = (f32x4){};
#pragma unroll
    for (int k = 0; k < 20; ++k)
#pragma unroll
      for (int m = 0; m < 4; ++m) acc[m] = MFMA16(b2[k], *(const bf16x8*)(smem + (m * 20 + k) * 1024 + lane * 16), acc[m]);
    float z2[4][4];
#pragma unroll
    for (int m = 0; m < 4; ++m) {
      float s = 0.f, q = 0.f;
#pragma unroll
      for (int e = 0; e < 4; ++e) { const float z = rbf(gelu_t(acc[m][e])); z2[m][e] = z; s += z; q += z * z; }
      s += __shfl_xor(s, 16); q += __shfl_xor(q, 16); s += xhalf(s); q += xhalf(q);
      if (g == 0) *(f32x2*)(st2 + (wid * 64 + m * 16 + r) * 2) = (f32x2){s, q};
    }
    BAR();
#pragma unroll
    for (int m = 0; m < 4; ++m) acc[m] = (f32x4){};
#pragma unroll
    for (int k = 0; k < 20; ++k)
#pragma unroll
      for (int m = 0; m < 4; ++m) acc[m] = MFMA16(b1[k], *(const bf16x8*)(smem + (m * 20 + k) * 1024 + lane * 16), acc[m]);
    char* zd = ws + OFF_Z1 + (size_t)(panel * 54 + u) * 2048 + lane * 16;
#pragma unroll
    for (int mm = 0; mm < 2; ++mm) {
      u32x4 o;
      o[0] = pack2(gelu_t(acc[2 * mm][0]), gelu_t(acc[2 * mm][1])); o[1] = pack2(gelu_t(acc[2 * mm][2]), gelu_t(acc[2 * mm][3]));
      o[2] = pack2(gelu_t(acc[2 * mm + 1][0]), gelu_t(acc[2 * mm + 1][1])); o[3] = pack2(gelu_t(acc[2 * mm + 1][2]), gelu_t(acc[2 * mm + 1][3]));
      *(u32x4*)(zd + mm * 1024) = o;
    }
    BAR();
    char* tr = smem + K4_TR + wid * 2048;
#pragma unroll
    for (int m = 0; m < 4; ++m) {
      const f32x2 f = *(const f32x2*)(fin + (m * 16 + r) * 2);
      *(u32x2*)(tr + (m * 16 + r) * 32 + g * 8) = (u32x2){pack2(z2[m][0] * f[0] - f[1], z2[m][1] * f[0] - f[1]), pack2(z2[m][2] * f[0] - f[1], z2[m][3] * f[0] - f[1])};
    }
    asm volatile("s_waitcnt lgkmcnt(0)" ::: "memory");
    char* z2d = ws + OFF_Z2 + ((size_t)u * 64 + panel * 2) * 1024 + lane * 16;
#pragma unroll
    for (int ks = 0; ks < 2; ++ks) {
      const char* p = tr + (ks * 32 + 8 * g + (r >> 2)) * 32 + (r & 3) * 8;
      const bf16x4 lo = lds_tr(p), hi = lds_tr(p + 128);
      *(bf16x8*)(z2d + ks * 1024) = __builtin_shufflevector(lo, hi, 0, 1, 2, 3, 4, 5, 6, 7);
    }
  } else if (wid == 7) {
    BAR();
    float s = 0.f, q = 0.f;
    for (int i = 0; i < nu; ++i) { const f32x2 t = *(const f32x2*)(st2 + (i * 64 + lane) * 2); s += t[0]; q += t[1]; }
    GAS u64* xch = (GAS u64*)(ws + OFF_XCH) + panel * 512;
    unsigned qb = __float_as_uint(q); if (qb == ~0u) qb = 0x7fc00000u;
    __hip_atomic_store(xch + cb * 64 + lane, ((u64)qb << 32) | __float_as_uint(s), __ATOMIC_RELAXED, __HIP_MEMORY_SCOPE_AGENT);
    u64 v[8];
    for (unsigned spin = 0; spin < (1u << 22); ++spin) {
      bool ok = true;
#pragma unroll
      for (int i = 0; i < 8; ++i) { v[i] = __hip_atomic_load(xch + i * 64 + lane, __ATOMIC_RELAXED, __HIP_MEMORY_SCOPE_AGENT); ok = ok && ((unsigned)(v[i] >> 32) != ~0u); }
      if (__all(ok)) break;
      __builtin_amdgcn_s_sleep(1);
    }
    s = 0.f; q = 0.f;
#pragma unroll
    for (int i = 0; i < 8; ++i) { s += __uint_as_float((unsigned)v[i]); q += __uint_as_float((unsigned)(v[i] >> 32)); }
    const float mean = s * (1.f / 864.f), var = fmaxf(q * (1.f / 864.f) - mean * mean, 0.f);
    const float ri = 1.f / ln_den(var);
    *(f32x2*)(fin + lane * 2) = (f32x2){ri, rbf(mean) * ri};
    BAR();
  }
}

__global__ __launch_bounds__(512, 2) void k5_sp(char* __restrict__ ws, const u16* __restrict__ wsp, const u16* __restrict__ bsp) {
  extern __shared__ __attribute__((aligned(16))) char smem[];
  const int tid = threadIdx.x, lane = tid & 63, wid = __builtin_amdgcn_readfirstlane(tid >> 6);
  const int xcd = blockIdx.x & 7, idx = blockIdx.x >> 3, panel = xcd * 4 + (idx >> 3), cb = idx & 7;
  const int nu = cb < 6 ? 7 : 6, u = unit0_of(cb) + wid;
  if (wid == 7) {
    const char* src = (const char*)wsp + (size_t)(panel * 64 + (lane & 15)) * 4096 + (lane >> 4) * 16;
#define K5_ISSUE(st) do { _Pragma("unroll") for (int m = 0; m < 4; ++m) _Pragma("unroll") for (int k = 0; k < 4; ++k) \
      glds16(src + m * 65536 + (st) * 256 + k * 64, smem + ((st) & 3) * 16384 + (k * 4 + m) * 1024); } while (0)
    K5_ISSUE(0); K5_ISSUE(1); K5_ISSUE(2);
    for (int s = 0; s < 13; ++s) { WAITV(32); BAR(); K5_ISSUE(s + 3); }
    WAITV(32); BAR(); WAITV(16); BAR(); WAITV(0); BAR();
    return;
  }
  if (wid >= nu) return;
  const char* bsrc = ws + OFF_Z2 + (size_t)u * 65536 + lane * 16;
  bf16x8 bq[4][4];
#pragma unroll
  for (int s = 0; s < 3; ++s)
#pragma unroll
    for (int k = 0; k < 4; ++k) bq[s][k] = *(const bf16x8*)(bsrc + (s * 4 + k) * 1024);
  f32x4 acc[4];
#pragma unroll
  for (int m = 0; m < 4; ++m) acc[m] = (f32x4){};
  for (int so = 0; so < 16; so += 4) {
#pragma unroll
    for (int si = 0; si < 4; ++si) {
      const int s = so + si;
      if (s + 3 < 16) {
#pragma unroll
        for (int k = 0; k < 4; ++k) bq[(si + 3) & 3][k] = *(const bf16x8*)(bsrc + ((s + 3) * 4 + k) * 1024);
      }
      BAR();
#pragma unroll
      for (int k = 0; k < 4; ++k)
#pragma unroll
        for (int m = 0; m < 4; ++m) acc[m] = MFMA16(bq[si][k], *(const bf16x8*)(smem + si * 16384 + (k * 4 + m) * 1024 + lane * 16), acc[m]);
    }
  }
  const char* z1 = ws + OFF_Z1 + (size_t)(panel * 54 + u) * 2048 + lane * 16;
  const int n = u * 16 + (lane >> 4) * 4;
  const size_t fo = (size_t)(n >> 5) * 1024 + (((n & 31) >> 3) * 16 + (lane & 15)) * 16 + (n & 4) * 2;
#pragma unroll
  for (int mm = 0; mm < 2; ++mm) {
    const u32x4 z = *(const u32x4*)(z1 + mm * 1024);
#pragma unroll
    for (int i = 0; i < 2; ++i) {
      const int m = 2 * mm + i;
      const float bb = bf2f(bsp[panel * 64 + m * 16 + (lane & 15)]);
      const float g0 = rbf(rbf(acc[m][0]) + bb), g1 = rbf(rbf(acc[m][1]) + bb), g2 = rbf(rbf(acc[m][2]) + bb), g3 = rbf(rbf(acc[m][3]) + bb);
      *(u32x2*)(ws + OFF_U + (size_t)(panel * 4 + m) * 27648 + fo) =
          (u32x2){pack2(bflo(z[2 * i]) * g0, bfhi(z[2 * i]) * g1), pack2(bflo(z[2 * i + 1]) * g2, bfhi(z[2 * i + 1]) * g3)};
    }
  }
}

extern "C" void kernel_launch(void* const* d_in, const int* in_sizes, int n_in, void* d_out, int out_size, void* d_ws, size_t ws_size,
                              hipStream_t stream) {
  const u16* x = (const u16*)d_in[0];
  const u16* wqkv = (const u16*)d_in[1];
  const u16* wo = (const u16*)d_in[2];
  const u16* win = (const u16*)d_in[4];
  const u16* wsp = (const u16*)d_in[5];
  const u16* bsp = (const u16*)d_in[6];
  const u16* wout = (const u16*)d_in[7];
  const u16* cvec = (const u16*)d_in[8];
  const u16* wmod = (const u16*)d_in[9];
  char* ws = (char*)d_ws;
  u16* out = (u16*)d_out;
  (void)hipFuncSetAttribute((const void*)k2_attn, hipFuncAttributeMaxDynamicSharedMemorySize, 163840);
  (void)hipFuncSetAttribute((const void*)k_proj<20, 5, false>, hipFuncAttributeMaxDynamicSharedMemorySize, 81920);
  (void)hipFuncSetAttribute((const void*)k_proj<27, 9, true>, hipFuncAttributeMaxDynamicSharedMemorySize, 110592);
  (void)hipFuncSetAttribute((const void*)k4_win, hipFuncAttributeMaxDynamicSharedMemorySize, K4_LDS);
  (void)hipFuncSetAttribute((const void*)k5_sp, hipFuncAttributeMaxDynamicSharedMemorySize, 65536);
  k0_prep<<<K0_GRID, 256, 0, stream>>>(x, wqkv, wo, win, wout, cvec, wmod, ws);
  k1_qkv<<<240, 512, 0, stream>>>(ws);
  k2_attn<<<256, 512, 163840, stream>>>(ws);
  k_proj<20, 5, false><<<256, 448, 81920, stream>>>(ws, x, out);
  k4_win<<<256, 512, K4_LDS, stream>>>(ws);
  k5_sp<<<256, 512, 65536, stream>>>(ws, wsp, bsp);
  k_proj<27, 9, true><<<256, 448, 110592, stream>>>(ws, x, out);
}
```

```cpp
#include <hip/hip_runtime.h>
#include <hip/hip_bf16.h>
#include <stdint.h>
#include <utility>

typedef __attribute__((ext_vector_type(8))) short bf16x8;
typedef __attribute__((ext_vector_type(4))) short bf16x4;
typedef __attribute__((ext_vector_type(2))) float f32x2;
typedef __attribute__((ext_vector_type(4))) float f32x4;
typedef __attribute__((ext_vector_type(16))) float f32x16;
typedef __attribute__((ext_vector_type(2))) unsigned u32x2;
typedef __attribute__((ext_vector_type(4))) unsigned u32x4;
typedef __attribute__((ext_vector_type(2))) __bf16 bf2_t;
typedef __attribute__((ext_vector_type(2))) _Float16 h2_t;
typedef unsigned short u16;
typedef unsigned long long u64;
#define LAS __attribute__((address_space(3)))
#define GAS __attribute__((address_space(1)))
#define DI __device__ __forceinline__

constexpr int D_ = 640, NQKV = 1920, F_ = 1728;

constexpr size_t OFF_MOD = 0;
constexpr size_t OFF_CS = 16384;
constexpr size_t OFF_BI = OFF_CS + 8192;
constexpr size_t OFF_XCH = OFF_BI + 8192;
constexpr size_t OFF_ST1 = OFF_XCH + 131072;
constexpr size_t OFF_XHAT = OFF_ST1 + 262144;
constexpr size_t OFF_WQKV = OFF_XHAT + 2621440;
constexpr size_t OFF_WO = OFF_WQKV + 2457600;
constexpr size_t OFF_WIN = OFF_WO + 819200;
constexpr size_t OFF_WOUT = OFF_WIN + 2211840;
constexpr size_t OFF_Q = OFF_WOUT + 1105920;
constexpr size_t OFF_KV = OFF_Q + 32 * 81920;
constexpr size_t OFF_O = OFF_KV + 32 * 163840;
constexpr size_t OFF_X1 = OFF_O + 2621440;
constexpr size_t OFF_X1S = OFF_X1 + 2621440;
constexpr size_t OFF_Z1 = OFF_X1S + 2621440;
constexpr size_t OFF_Z2 = OFF_Z1 + 3538944;
constexpr size_t OFF_U = OFF_Z2 + 3538944;

#define STAMP(kid, k)
#define TDECL
#define TS(k)
#define TDUMP(kid)
DI unsigned pack2(float lo, float hi) { return __builtin_bit_cast(unsigned, __builtin_convertvector((f32x2){lo, hi}, bf2_t)); }
DI unsigned pack2s(float lo, float hi) { unsigned r; asm("v_cvt_pk_bf16_f32 %0, %1, %2" : "=v"(r) : "v"(lo), "v"(hi)); return r; }
DI unsigned pack2h(float lo, float hi) { return __builtin_bit_cast(unsigned, __builtin_convertvector((f32x2){lo, hi}, h2_t)); }
DI float bflo(unsigned u) { return __uint_as_float(u << 16); }
DI float bfhi(unsigned u) { return __uint_as_float(u & 0xffff0000u); }
DI float rbf(float x) { return bflo(pack2(x, x)); }
DI float bf2f(u16 b) { return __uint_as_float((unsigned)b << 16); }
#define MFMA32(a, b, c) __builtin_amdgcn_mfma_f32_32x32x16_bf16((a), (b), (c), 0, 0, 0)
#define MFMA32K8(a, b, c) __builtin_amdgcn_mfma_f32_32x32x8bf16_1k((a), (b), (c), 0, 0, 0)
#define MFMA16(a, b, c) __builtin_amdgcn_mfma_f32_16x16x32_bf16((a), (b), (c), 0, 0, 0)
#define BAR() asm volatile("s_waitcnt lgkmcnt(0)\n\ts_barrier" ::: "memory")
#define SCHED() __builtin_amdgcn_sched_barrier(0)
#define PIN(x) asm volatile("" : "+v"(x))
#define OCC2 __attribute__((amdgpu_waves_per_eu(2, 2)))
#define BF8(x) __builtin_bit_cast(bf16x8, x)
#define BF4(x) __builtin_bit_cast(bf16x4, x)
template <int N> DI void waitv() { asm volatile("s_waitcnt vmcnt(%0)" ::"i"(N) : "memory"); }
template <class F, int... I> DI void sfor_impl(F&& f, std::integer_sequence<int, I...>) { (f(std::integral_constant<int, I>{}), ...); }
template <int N, class F> DI void sfor(F&& f) { sfor_impl(f, std::make_integer_sequence<int, N>{}); }
typedef short v4i16_t __attribute__((ext_vector_type(4)));
DI bf16x4 lds_tr(const char* p) { return __builtin_bit_cast(bf16x4, __builtin_amdgcn_ds_read_tr16_b64_v4i16((LAS v4i16_t*)p)); }
DI void glds16(const void* g, unsigned lds_base) {
  unsigned sv;
  asm volatile("s_mov_b32 %0, m0\n\ts_mov_b32 m0, %2\n\ts_nop 0\n\tglobal_load_lds_dwordx4 %1, off\n\ts_mov_b32 m0, %0" : "=&s"(sv) : "v"(g), "s"(lds_base) : "memory");
}
DI unsigned lds_addr(const void* p) { return (unsigned)(uintptr_t)(const LAS void*)p; }
DI float xhalf(float v) {
  auto rr = __builtin_amdgcn_permlane32_swap(__float_as_uint(v), __float_as_uint(v), false, false);
  return __uint_as_float((threadIdx.x & 32) ? rr[0] : rr[1]);
}
DI float sum_g(float v) {
  auto a = __builtin_amdgcn_permlane16_swap(__float_as_uint(v), __float_as_uint(v), false, false);
  v = __uint_as_float(a[0]) + __uint_as_float(a[1]);
  auto b = __builtin_amdgcn_permlane32_swap(__float_as_uint(v), __float_as_uint(v), false, false);
  return __uint_as_float(b[0]) + __uint_as_float(b[1]);
}
template <int CTRL> DI float dpp_add(float v) { return v + __uint_as_float(__builtin_amdgcn_update_dpp(0u, __float_as_uint(v), CTRL, 0xf, 0xf, true)); }
DI float sum16(float v) {
  v = dpp_add<0x128>(v); v = dpp_add<0x124>(v); v = dpp_add<0x122>(v); return dpp_add<0x121>(v);
}
DI float mix_lo(unsigned h, float s, float b) { float r; asm("v_fma_mix_f32 %0, %1, %2, %3 op_sel_hi:[1,0,0]" : "=v"(r) : "v"(h), "v"(s), "v"(b)); return r; }
DI float mix_hi(unsigned h, float s, float b) { float r; asm("v_fma_mix_f32 %0, %1, %2, %3 op_sel:[1,0,0] op_sel_hi:[1,0,0]" : "=v"(r) : "v"(h), "v"(s), "v"(b)); return r; }
DI float ln_den(float var) { return rbf(__builtin_sqrtf(rbf(rbf(var) + 1e-5f))); }
DI int unit0_of(int j) { return j < 6 ? 7 * j : 42 + 6 * (j - 6); }

template <int NK, int MS, int KS, int PD>
DI void mma16_run(const char* al, const bf16x8 (&b)[NK], f32x4 (&acc)[4]) {
  bf16x8 af[PD + 1][4];
#pragma unroll
  for (int k = 0; k < PD; ++k)
#pragma unroll
    for (int m = 0; m < 4; ++m) af[k][m] = *(const bf16x8*)(al + m * MS + k * KS);
  SCHED();
#pragma unroll
  for (int k = 0; k < NK; ++k) {
#pragma unroll
    for (int m = 0; m < 4; ++m) {
      if (k + PD < NK) af[(k + PD) % (PD + 1)][m] = *(const bf16x8*)(al + m * MS + (k + PD) * KS);
      acc[m] = MFMA16(b[k], af[k % (PD + 1)][m], acc[m]);
      SCHED();
    }
  }
}

constexpr int K0_MOD = 120, K0_LN = 128, K0_JOBS = 600 + 200 + 270, K0_RL = (K0_JOBS + 3) / 4, K0_INIT = 8;
constexpr int K0_GRID = K0_MOD + K0_LN + K0_RL + K0_INIT;

__global__ __launch_bounds__(256) void k0_prep(const u16* __restrict__ x, const u16* __restrict__ wqkv, const u16* __restrict__ wo,
                                               const u16* __restrict__ wout, const u16* __restrict__ cvec,
                                               const u16* __restrict__ wmod, char* __restrict__ ws) {
  __shared__ __attribute__((aligned(16))) char smem[4 * 4608];
  const int tid = threadIdx.x, lane = tid & 63, wid = __builtin_amdgcn_readfirstlane(tid >> 6);
  int b = blockIdx.x;
  STAMP(0, 0);
  if (b < K0_MOD) {
    b = (b & 7) * 15 + (b >> 3);
    const int c = tid & 3, rg = tid >> 2, col0 = b * 32 + c * 8;
    u32x4 w[10]; u16 cv[10];
#pragma unroll
    for (int i = 0; i < 10; ++i) { w[i] = *(const u32x4*)(wmod + (size_t)(rg + 64 * i) * 3840 + col0); cv[i] = cvec[rg + 64 * i]; }
    float acc[8] = {0, 0, 0, 0, 0, 0, 0, 0};
#pragma unroll
    for (int i = 0; i < 10; ++i) {
      const float cf = bf2f(cv[i]); const float s = rbf(cf / (1.f + __expf(-cf)));
#pragma unroll
      for (int e = 0; e < 4; ++e) { acc[2 * e] += s * bflo(w[i][e]); acc[2 * e + 1] += s * bfhi(w[i][e]); }
    }
#pragma unroll
    for (int e = 0; e < 8; ++e) { acc[e] = sum_g(dpp_add<0x124>(dpp_add<0x128>(acc[e]))); }
    float* red = (float*)smem;
    if (lane < 4) {
#pragma unroll
      for (int e = 0; e < 8; ++e) red[wid * 32 + lane * 8 + e] = acc[e];
    }
    __syncthreads();
    if (tid < 32) {
      float v = rbf(red[tid] + red[32 + tid] + red[64 + tid] + red[96 + tid]);
      const int col = b * 32 + tid, sec = col / 640;
      if (sec == 1 || sec == 4) v = rbf(1.f + v);
      ((float*)(ws + OFF_MOD))[col] = v;
    }
    STAMP(0, 1); STAMP(0, 13);
    return;
  }
  b -= K0_MOD;
  if (b < K0_LN) {
    b = (b & 7) * 16 + (b >> 3);
    const int row = b * 16 + wid * 4 + (lane >> 4), c = lane & 15;
    const u16* xr = x + (size_t)row * D_ + c * 8;
    u32x4 v[5];
#pragma unroll
    for (int i = 0; i < 5; ++i) v[i] = *(const u32x4*)(xr + i * 128);
    float s = 0.f, q = 0.f;
#pragma unroll
    for (int i = 0; i < 5; ++i)
#pragma unroll
      for (int e = 0; e < 4; ++e) { const float a = bflo(v[i][e]), d = bfhi(v[i][e]); s += a + d; q += a * a + d * d; }
    s = sum16(s); q = sum16(q);
    const float mean = s * (1.f / 640.f); const float var = fmaxf(q * (1.f / 640.f) - mean * mean, 0.f);
    const float rinv = 1.f / ln_den(var), mb = rbf(mean) * rinv;
    char* dst = ws + OFF_XHAT + (size_t)row * 1280 + c * 16;
#pragma unroll
    for (int i = 0; i < 5; ++i) {
      u32x4 o;
#pragma unroll
      for (int e = 0; e < 4; ++e) o[e] = pack2h(bflo(v[i][e]) * rinv - mb, bfhi(v[i][e]) * rinv - mb);
      *(u32x4*)(dst + i * 256) = o;
    }
    STAMP(0, 2); STAMP(0, 13);
    return;
  }
  b -= K0_LN;
  if (b < K0_RL) {
    int job = b * 4 + wid;
    if (job >= K0_JOBS) return;
    const u16* W; int N, nks, kt, ng; char* dst; bool f32frag = false;
    if (job < 600) { W = wqkv; N = NQKV; kt = job / 30; ng = job % 30; dst = ws + OFF_WQKV; f32frag = true; nks = 40; }
    else if (job < 800) { job -= 600; W = wo; N = D_; kt = job / 10; ng = job % 10; dst = ws + OFF_WO; nks = 20; }
    else { job -= 800; W = wout; N = D_; kt = job / 10; ng = job % 10; dst = ws + OFF_WOUT; nks = 27; }
    char* t = smem + wid * 4608;
    u32x4 v[4];
#pragma unroll
    for (int i = 0; i < 4; ++i) v[i] = *(const u32x4*)(W + (size_t)(kt * 32 + i * 8 + (lane >> 3)) * N + ng * 64 + (lane & 7) * 8);
#pragma unroll
    for (int i = 0; i < 4; ++i) *(u32x4*)(t + (i * 8 + (lane >> 3)) * 144 + (lane & 7) * 16) = v[i];
    asm volatile("s_waitcnt lgkmcnt(0)" ::: "memory");
    const int g = lane >> 4, q = (lane & 15) >> 2, p = lane & 3;
#pragma unroll
    for (int f = 0; f < 4; ++f) {
      int row, col; size_t o;
      if (f32frag) { row = 16 * (f >> 1) + 8 * (g >> 1) + q; col = 32 * (f & 1) + 16 * (g & 1) + 4 * p; o = ((size_t)(ng * 2 + (f & 1)) * 40 + kt * 2 + (f >> 1)) * 1024; }
      else { row = 8 * g + q; col = 16 * f + 4 * p; o = ((size_t)(ng * 4 + f) * nks + kt) * 1024; }
      const bf16x4 lo = lds_tr(t + row * 144 + col * 2), hi = lds_tr(t + (row + 4) * 144 + col * 2);
      *(bf16x8*)(dst + o + lane * 16) = __builtin_shufflevector(lo, hi, 0, 1, 2, 3, 4, 5, 6, 7);
    }
    STAMP(0, 3); STAMP(0, 13);
    return;
  }
  b -= K0_RL;
  {
    u32x4* p = (u32x4*)(ws + OFF_XCH) + b * 1024 + tid;
#pragma unroll
    for (int i = 0; i < 4; ++i) p[i * 256] = (u32x4){~0u, ~0u, ~0u, ~0u};
  }
}

constexpr float QSCALE = 0.22360679774997896f * 1.4426950408889634f;
constexpr int K1_W = 3;

template <bool ISV>
DI void k1_mma(const char* __restrict__ bsrc, const char* smem, int lane, f32x16 (&acc)[4]) {
  bf16x8 bfr[40], af[4][4];
  const char* lds[4];
#pragma unroll
  for (int k = 0; k < 4; ++k) lds[k] = smem + (lane ^ (2 * k + (lane >> 5))) * 16;
#pragma unroll
  for (int k = 0; k < 4 * K1_W; ++k) bfr[k] = *(const bf16x8*)(bsrc + k * 1024);
#pragma unroll
  for (int m = 0; m < 4; ++m) acc[m] = (f32x16){};
  BAR();
  BAR();
#define K1_RD1(s, k, m) (af[k][m] = *(const bf16x8*)(lds[k] + ((s) & 1) * 16384 + ((k) * 4 + (m)) * 1024))
#define K1_STEP(s, k, RD, rs, rk, LD) do { _Pragma("unroll") for (int m = 0; m < 4; ++m) { if (RD) K1_RD1(rs, rk, m); \
      if (LD) bfr[4 * ((s) + K1_W) + m] = *(const bf16x8*)(bsrc + (4 * ((s) + K1_W) + m) * 1024); \
      acc[m] = ISV ? MFMA32(af[k][m], bfr[(s) * 4 + (k)], acc[m]) : MFMA32(bfr[(s) * 4 + (k)], af[k][m], acc[m]); SCHED(); } } while (0)
#pragma unroll
  for (int m = 0; m < 4; ++m) K1_RD1(0, 0, m);
#pragma unroll
  for (int m = 0; m < 4; ++m) K1_RD1(0, 1, m);
  SCHED();
#pragma unroll
  for (int s = 0; s < 10; ++s) {
    K1_STEP(s, 0, true, s, 2, false);
    K1_STEP(s, 1, true, s, 3, false);
    K1_STEP(s, 2, false, 0, 0, s + K1_W < 10);
    if (s < 9) {
      BAR();
#pragma unroll
      for (int m = 0; m < 4; ++m) K1_RD1(s + 1, 0, m);
      SCHED();
    }
    K1_STEP(s, 3, s < 9, s + 1, 1, false);
  }
}

__global__ __launch_bounds__(512, 2) OCC2 void k1_qkv(char* __restrict__ ws, const u16* __restrict__ win) {
  extern __shared__ __attribute__((aligned(16))) char smem[];
  const int tid = threadIdx.x, lane = tid & 63, wid = __builtin_amdgcn_readfirstlane(tid >> 6);
  const int xcd = blockIdx.x & 7, idx = blockIdx.x >> 3;
  TDECL; TS(0);
  if (idx >= 30) {
    const int ub = (xcd * 2 + idx - 30) * 7, u = ub + wid;
    char* vec = smem + 143360;
    f32x4 cv; if (tid < 320) cv = *(const f32x4*)(ws + OFF_MOD + 3 * 2560 + tid * 16);
    if (tid < 320) *(u32x2*)(vec + tid * 8) = (u32x2){pack2(cv[0], cv[1]), pack2(cv[2], cv[3])};
    {
      const int ncol = ub + 7 <= 108 ? 14 : 6;
      const unsigned dst = lds_addr(smem) + wid * 1024;
#pragma unroll
      for (int i = 0; i < 18; ++i) {
        const unsigned ci = i * 512 + (i < 17 ? tid : tid & 255), row = ci / 14, ch = ci % 14;
        glds16(win + (size_t)row * F_ + ub * 16 + (ch < ncol ? ch : 0) * 8, i < 17 ? dst + i * 8192 : lds_addr(smem) + (wid & 3) * 1024 + i * 8192);
      }
    }
    const int g = lane >> 4, r = lane & 15;
    f32x4 acc = (f32x4){};
    char* dst = ws + OFF_WIN + (size_t)u * 20480 + lane * 16;
    const char* tp = smem + (8 * g + (r >> 2)) * 224 + wid * 32 + (r & 3) * 8;
    const char* vp = vec + (r & 1) * 1280 + g * 16;
    const bool act = wid < 7 && u < 108;
    sfor<2>([&](auto H) __attribute__((always_inline)) {
      constexpr int hf = H;
      waitv<hf ? 0 : 9>();
      BAR();
      if (act) {
        bf16x4 lo[10], hi[10]; bf16x8 vv[10];
#pragma unroll
        for (int k = 0; k < 10; ++k) { const int kk = hf * 10 + k; lo[k] = lds_tr(tp + kk * 7168); hi[k] = lds_tr(tp + kk * 7168 + 896); vv[k] = *(const bf16x8*)(vp + kk * 64); }
#pragma unroll
        for (int k = 0; k < 10; ++k) {
          const bf16x8 raw = __builtin_shufflevector(lo[k], hi[k], 0, 1, 2, 3, 4, 5, 6, 7);
          acc = MFMA16(raw, vv[k], acc);
          *(bf16x8*)(dst + (hf * 10 + k) * 1024) = raw;
        }
      }
    });
    if (act && r < 2) *(f32x4*)(ws + (r ? OFF_CS : OFF_BI) + (u * 16 + g * 4) * 4) = acc;
    TS(11); TDUMP(1);
    __builtin_amdgcn_endpgm();
  }
  const int mt = xcd * 2 + (idx / 15), nt = idx % 15;
  if (wid < 4) {
    float* cst = (float*)(smem + 32768);
    const int c = lane & 7, rr = lane >> 3;
    const char* src = ws + OFF_XHAT + (size_t)(mt * 128 + wid * 32 + rr) * 1280 + c * 16;
    char* wdst = smem + ((c >> 1) * 4 + wid) * 1024 + (c & 1) * 512 + (rr ^ c) * 16;
    u32x4 a[10][4];
    const f32x4 c0 = *(const f32x4*)(ws + OFF_MOD + tid * 16);
    f32x4 c1; if (tid < 64) c1 = *(const f32x4*)(ws + OFF_MOD + 4096 + tid * 16);
#pragma unroll
    for (int s = 0; s < K1_W; ++s)
#pragma unroll
      for (int m = 0; m < 4; ++m) a[s][m] = *(const u32x4*)(src + m * 10240 + s * 128);
    *(f32x4*)(cst + tid * 4) = c0; if (tid < 64) *(f32x4*)(cst + 1024 + tid * 4) = c1;
    BAR();
    const float* cp = cst + c * 8;
    f32x4 kc[10][4];
#pragma unroll
    for (int s = 0; s < 10; ++s) { kc[s][0] = *(const f32x4*)(cp + s * 64); kc[s][1] = *(const f32x4*)(cp + s * 64 + 4); kc[s][2] = *(const f32x4*)(cp + s * 64 + 640); kc[s][3] = *(const f32x4*)(cp + s * 64 + 644); }
    SCHED();
#pragma unroll
    for (int s = 0; s < 10; ++s) {
      if (s + K1_W < 10) {
#pragma unroll
        for (int m = 0; m < 4; ++m) a[s + K1_W][m] = *(const u32x4*)(src + m * 10240 + (s + K1_W) * 128);
      }
#pragma unroll
      for (int m = 0; m < 4; ++m) {
        u32x4 o; const u32x4 v = a[s][m]; const f32x4 sh0 = kc[s][0], sh1 = kc[s][1], sc0 = kc[s][2], sc1 = kc[s][3];
        o[0] = pack2(mix_lo(v[0], sc0[0], sh0[0]), mix_hi(v[0], sc0[1], sh0[1]));
        o[1] = pack2(mix_lo(v[1], sc0[2], sh0[2]), mix_hi(v[1], sc0[3], sh0[3]));
        o[2] = pack2(mix_lo(v[2], sc1[0], sh1[0]), mix_hi(v[2], sc1[1], sh1[1]));
        o[3] = pack2(mix_lo(v[3], sc1[2], sh1[2]), mix_hi(v[3], sc1[3], sh1[3]));
        *(u32x4*)(wdst + (s & 1) * 16384 + m * 128) = o;
      }
      BAR();
    }
    TDUMP(1);
    __builtin_amdgcn_endpgm();
  }
  const int cw = wid - 4;
  const char* bsrc = ws + OFF_WQKV + (size_t)(nt * 4 + cw) * 40 * 1024 + lane * 16;
  f32x16 acc[4];
  const bool isV = nt >= 10;
  if (isV) k1_mma<true>(bsrc, smem, lane, acc); else k1_mma<false>(bsrc, smem, lane, acc);
  TS(12);
  const int r = lane & 31, h = lane >> 5, n0 = nt * 128 + cw * 32;
  if (isV) {
    const unsigned c = n0 + r - 1280, head = c / 20, d = c % 20;
    char* base = ws + OFF_KV + (mt >> 1) * 20480 + 10240 + (mt & 1) * 5120;
    const unsigned off = head * 163840u + (h * 20 + d) * 16;
#pragma unroll
    for (int m = 0; m < 4; ++m)
#pragma unroll
      for (int s = 0; s < 2; ++s) {
        u32x4 o;
#pragma unroll
        for (int e = 0; e < 4; ++e) o[e] = pack2(acc[m][8 * s + 2 * e], acc[m][8 * s + 2 * e + 1]);
        *(u32x4*)(base + off + m * 1280 + s * 640) = o;
      }
  } else {
    const bool isQ = nt < 5; const float sc = isQ ? QSCALE : 1.f;
    char* base = isQ ? ws + OFF_Q + mt * 5120 : ws + OFF_KV + (mt >> 1) * 20480 + (mt & 1) * 5120;
    const unsigned hs = isQ ? 81920u : 163840u;
#pragma unroll
    for (int qd = 0; qd < 4; ++qd) {
      const unsigned c = n0 + 8 * qd + 4 * h - (isQ ? 0 : 640), head = c / 20, dq = (c % 20) >> 2;
      const unsigned off = head * hs + (dq < 4 ? ((dq >> 1) * 32 + r) * 16 + (dq & 1) * 8 : 1024 + r * 8);
#pragma unroll
      for (int m = 0; m < 4; ++m)
        *(u32x2*)(base + off + m * 1280) = (u32x2){pack2(acc[m][4 * qd] * sc, acc[m][4 * qd + 1] * sc), pack2(acc[m][4 * qd + 2] * sc, acc[m][4 * qd + 3] * sc)};
    }
  }
  TDUMP(1);
}

DI void split3(float m, unsigned& w_h0, unsigned& w_h1) {
  const float n = -m; const float a = rbf(n), b = rbf(n - a), c = rbf(n - a - b);
  w_h0 = pack2(a, b); w_h1 = pack2(c, 0.f);
}

__global__ __launch_bounds__(512, 2) OCC2 void k2_attn(char* __restrict__ ws) {
  extern __shared__ __attribute__((aligned(16))) char smem[];
  const int tid = threadIdx.x, lane = tid & 63, wid = __builtin_amdgcn_readfirstlane(tid >> 6);
  const int xcd = blockIdx.x & 7, idx = blockIdx.x >> 3, head = xcd * 4 + (idx >> 3), qb = idx & 7;
  const int r = lane & 31, h = lane >> 5, qg = wid & 3, kh = wid >> 2;
  unsigned kvo = OFF_KV + head * 163840, qo = OFF_Q + head * 81920 + (qb * 8 + qg * 2) * 1280;
  const unsigned kconst = h == 0 ? 0x3f803f80u : 0x00003f80u;
  const unsigned so16 = (tid & 255) * 16, l16 = lane * 16, l2 = 1024 + r * 8 + h * 4;
  TDECL; TS(0);
  for (int attempt = 0; attempt < 2; ++attempt) {
    asm volatile("" : "+s"(kvo), "+s"(qo));
    const char* kv = ws + kvo; const char* qp = ws + qo;
    const char* kvs = kv + kh * 81920 + so16;
    const unsigned ldst = lds_addr(smem) + kh * 81920 + qg * 1024;
    if (wid == 0) { const unsigned f = (tid & 1) ? 0u : 0x3f803f80u; *(u32x4*)(smem + l16) = (u32x4){f, f, f, f}; }
    else glds16(kvs, ldst);
#pragma unroll
    for (int i = 1; i < 5; ++i) glds16(kvs + i * 4096, ldst + i * 4096);
    const bf16x8 qA1 = *(const bf16x8*)(qp + l16), qB1 = *(const bf16x8*)(qp + 1280 + l16);
    const unsigned qA2w = *(const unsigned*)(qp + l2), qB2w = *(const unsigned*)(qp + 1280 + l2);
    bf16x8 k1 = *(const bf16x8*)(kv + l16); unsigned k2 = *(const unsigned*)(kv + l2);
    float mA = -INFINITY, mB = -INFINITY;
    {
      const bf16x4 qA2 = BF4(((u32x2){qA2w, 0u})), qB2 = BF4(((u32x2){qB2w, 0u}));
      const int nt = attempt ? 64 : 1;
      bf16x8 t1 = k1; unsigned t2 = k2;
      for (int t = 0; t < nt; ++t) {
        if (t) { const char* tp = kv + (t >> 3) * 20480 + (t & 7) * 1280; t1 = *(const bf16x8*)(tp + l16); t2 = *(const unsigned*)(tp + l2); }
        const bf16x4 t2v = BF4(((u32x2){t2, kconst}));
        f32x16 a = MFMA32(t1, qA1, (f32x16){}); a = MFMA32K8(t2v, qA2, a);
        f32x16 b = MFMA32(t1, qB1, (f32x16){}); b = MFMA32K8(t2v, qB2, b);
#pragma unroll
        for (int i = 0; i < 16; ++i) { mA = fmaxf(mA, a[i]); mB = fmaxf(mB, b[i]); }
      }
      mA = fmaxf(mA, xhalf(mA)); mB = fmaxf(mB, xhalf(mB));
    }
    unsigned a0, a1, b0, b1; split3(mA, a0, a1); split3(mB, b0, b1);
    const bf16x4 qA2 = BF4(((u32x2){qA2w, h ? a1 : a0})), qB2 = BF4(((u32x2){qB2w, h ? b1 : b0}));
    char* lh = smem + kh * 81920;
    waitv<0>();
    TS(1);
    BAR();
    TS(2);
    f32x16 oA = (f32x16){}, oB = (f32x16){};
    const char* kp1 = lh + l16;
    const char* kp2 = lh + l2;
    const bool vreal = r < 20;
    const char* vp = vreal ? lh + 10240 + (h * 20 + r) * 16 : smem + (r == 20 ? 0 : 16);
    const int vinc = vreal ? 1280 : 0, vjump = vreal ? 10240 : 0;
    if (kh) { k1 = *(const bf16x8*)kp1; k2 = *(const unsigned*)kp2; }
    bf16x8 kf1[2], vf[2][2]; u32x2 kf2[2];
    kf1[0] = k1; kf2[0] = (u32x2){k2, kconst}; kf2[1] = (u32x2){0u, kconst};
    vf[1][0] = (bf16x8){}; vf[1][1] = (bf16x8){};
    u32x4 pA0, pA1, pB0 = (u32x4){}, pB1 = (u32x4){};
    f32x4 e0 = (f32x4){}, e1;
    f32x16 sA = MFMA32(kf1[0], qA1, (f32x16){}), sB;
    sA = MFMA32K8(BF4(kf2[0]), qA2, sA);
#define EX2(x) __builtin_amdgcn_exp2f(x)
#define GRP(MF, S, i, EN, EP, P, w) do { MF; SCHED(); EN[0] = EX2(S[i]); P[w] = pack2(EP[0], EP[1]); EN[1] = EX2(S[i + 1]); P[w + 1] = pack2(EP[2], EP[3]); \
      EN[2] = EX2(S[i + 2]); EN[3] = EX2(S[i + 3]); PIN(P); PIN(EN); SCHED(); } while (0)
    auto group = [&](auto FIRST, int jump, int j) __attribute__((always_inline)) {
#pragma unroll
      for (int t = 0; t < 8; ++t) {
        const int c = t & 1, n = c ^ 1;
        if (FIRST) { if (t == 0) __builtin_amdgcn_s_setprio(3); if (t == 3) __builtin_amdgcn_s_setprio(2); if (t == 5) __builtin_amdgcn_s_setprio(1); if (t == 6) __builtin_amdgcn_s_setprio(0); if (t == 7) __builtin_amdgcn_s_setprio(3); }
        else { if (t == 4 && j == 2) __builtin_amdgcn_s_setprio(2); if (t == 3 && j == 3) __builtin_amdgcn_s_setprio(1); if (t == 6 && j == 3) __builtin_amdgcn_s_setprio(0); }
        if (FIRST && t < 5) {
#pragma unroll
          for (int i = 5 + 3 * t; i < 8 + 3 * t; ++i) glds16(kvs + i * 4096, ldst + i * 4096);
        }
        if (t == 7) {
          if (FIRST) { waitv<0>(); BAR(); }
          kp1 += jump; kp2 += jump;
        }
        SCHED();
        kf1[n] = *(const bf16x8*)(kp1 + ((t + 1) & 7) * 1280); kf2[n][0] = *(const unsigned*)(kp2 + ((t + 1) & 7) * 1280);
        GRP(sB = MFMA32(kf1[c], qB1, (f32x16){}), sA, 0, e1, e0, pB1, 2);
        vf[c][0] = *(const bf16x8*)vp; vf[c][1] = *(const bf16x8*)(vp + 640); vp += vinc;
        GRP(sB = MFMA32K8(BF4(kf2[c]), qB2, sB), sA, 4, e0, e1, pA0, 0);
        GRP(oB = MFMA32(vf[n][0], BF8(pB0), oB), sA, 8, e1, e0, pA0, 2);
        GRP(oB = MFMA32(vf[n][1], BF8(pB1), oB), sA, 12, e0, e1, pA1, 0);
        GRP(sA = MFMA32(kf1[n], qA1, (f32x16){}), sB, 0, e1, e0, pA1, 2);
        GRP(sA = MFMA32K8(BF4(kf2[n]), qA2, sA), sB, 4, e0, e1, pB0, 0);
        GRP(oA = MFMA32(vf[c][0], BF8(pA0), oA), sB, 8, e1, e0, pB0, 2);
        GRP(oA = MFMA32(vf[c][1], BF8(pA1), oA), sB, 12, e0, e1, pB1, 0);
      }
      vp += vjump;
    };
    group(std::true_type{}, 20480, 0);
    TS(3);
#pragma nounroll
    for (int j = 1; j < 4; ++j) {
      group(std::false_type{}, j < 3 ? 20480 : 0, j);
    }
    pB1[2] = pack2(e0[0], e0[1]); pB1[3] = pack2(e0[2], e0[3]);
    oB = MFMA32(vf[1][0], BF8(pB0), oB); oB = MFMA32(vf[1][1], BF8(pB1), oB);
    {
      f32x4* ex = (f32x4*)(lh + 1024) + (qg * 3) * 64 + lane;
      const f32x16 give = kh ? oA : oB;
#pragma unroll
      for (int g = 0; g < 3; ++g) ex[g * 64] = (f32x4){give[4 * g], give[4 * g + 1], give[4 * g + 2], give[4 * g + 3]};
    }
    int* flag = (int*)(smem + 528);
    BAR();
    TS(8);
    float o[12];
    {
      const f32x4* ex = (const f32x4*)(smem + (kh ^ 1) * 81920 + 1024) + (qg * 3) * 64 + lane;
      const f32x16 keep = kh ? oB : oA;
#pragma unroll
      for (int g = 0; g < 3; ++g) { const f32x4 t = ex[g * 64];
#pragma unroll
        for (int e = 0; e < 4; ++e) o[4 * g + e] = keep[4 * g + e] + t[e]; }
    }
    const float lo = xhalf(o[8]); const float l = h ? o[8] : lo;
    bool ok = (l > 1e-30f) && (l < 1e30f);
#pragma unroll
    for (int i = 0; i < 12; ++i) ok = ok && (fabsf(o[i]) < 3e38f);
    const float rl = 1.f / l;
    const int tok = qb * 256 + qg * 64 + kh * 32 + r;
    char* ob = ws + OFF_O + (size_t)(tok >> 4) * 20480 + (tok & 15) * 16;
#pragma unroll
    for (int g = 0; g < 3; ++g) {
      if (g < 2 || h == 0) {
        const int k = head * 20 + 8 * g + 4 * h;
        *(u32x2*)(ob + (k >> 5) * 1024 + ((k & 31) >> 3) * 256 + (k & 4) * 2) = (u32x2){pack2(o[4 * g] * rl, o[4 * g + 1] * rl), pack2(o[4 * g + 2] * rl, o[4 * g + 3] * rl)};
      }
    }
    if (attempt) break;
    if (!ok) *flag = 1;
    BAR();
    const int redo = *(volatile int*)flag;
    if (!redo) break;
    BAR();
  }
  TDUMP(2);
}

template <int NKS, bool FINAL, int KH>
DI void proj_body(char* __restrict__ ws, const u16* __restrict__ x, u16* __restrict__ out, char* smem, int panel, int cb, int m, int lane) {
  constexpr int K0 = KH ? (NKS + 1) / 2 : 0, NK = KH ? NKS / 2 : (NKS + 1) / 2, NKEEP = KH ? 2 : 3, UK = KH ? 3 : 0, UG = KH ? 0 : 3;
  TDECL; TS(0);
  const int mt = panel * 4 + m, g = lane >> 4, r = lane & 15;
  bf16x8 a[NK];
  const char* asrc = ws + (FINAL ? OFF_U : OFF_O) + ((size_t)mt * NKS + K0) * 1024 + lane * 16;
#pragma unroll
  for (int k = 0; k < NK; ++k) a[k] = *(const bf16x8*)(asrc + k * 1024);
  u32x2 res[NKEEP]; f32x4 gate[NKEEP], cbv[NKEEP]; unsigned fo[NKEEP];
#pragma unroll
  for (int i = 0; i < NKEEP; ++i) {
    const int n = (cb * 5 + UK + i) * 16 + g * 4;
    fo[i] = (n >> 5) * 1024 + (((n & 31) >> 3) * 16 + r) * 16 + (n & 4) * 2;
    if (FINAL) res[i] = *(const u32x2*)(ws + OFF_X1 + (size_t)mt * 20480 + fo[i]);
    else res[i] = *(const u32x2*)(x + (size_t)(mt * 16 + r) * D_ + n);
    gate[i] = *(const f32x4*)(ws + OFF_MOD + ((FINAL ? 5 : 2) * 640 + n) * 4);
    if (!FINAL) cbv[i] = *(const f32x4*)(ws + OFF_MOD + (4 * 640 + n) * 4);
  }
  TS(1);
  waitv<NK + (FINAL ? 2 : 3) * NKEEP>();
  BAR();
  TS(2);
  constexpr int PD = 3;
  const char* bl = smem + K0 * 1024 + lane * 16;
  bf16x8 bf[PD + 1][5]; f32x4 acc[5];
#pragma unroll
  for (int u = 0; u < 5; ++u) acc[u] = (f32x4){};
#pragma unroll
  for (int k = 0; k < PD; ++k)
#pragma unroll
    for (int u = 0; u < 5; ++u) bf[k][u] = *(const bf16x8*)(bl + (u * NKS + k) * 1024);
  SCHED();
#pragma unroll
  for (int k = 0; k < NK; ++k) {
#pragma unroll
    for (int u = 0; u < 5; ++u) {
      if (k + PD < NK) bf[(k + PD) % (PD + 1)][u] = *(const bf16x8*)(bl + (u * NKS + k + PD) * 1024);
      acc[u] = MFMA16(bf[k % (PD + 1)][u], a[k], acc[u]);
      SCHED();
    }
  }
  TS(3);
  f32x4* ex = (f32x4*)(smem + 5 * NKS * 1024) + m * 5 * 64 + lane;
#pragma unroll
  for (int i = 0; i < 5 - NKEEP; ++i) ex[(UG + i) * 64] = acc[UG + i];
  f32x4 part[NKEEP];
#pragma unroll
  for (int i = 0; i < NKEEP; ++i) {
    part[i] = (f32x4){bflo(res[i][0]), bfhi(res[i][0]), bflo(res[i][1]), bfhi(res[i][1])} + gate[i] * acc[UK + i];
    PIN(part[i]);
  }
  char* ob = FINAL ? (char*)(out + (size_t)(mt * 16 + r) * D_ + (cb * 5 + UK) * 16 + g * 4) : ws + OFF_X1 + (size_t)mt * 20480;
  PIN(ob);
  BAR();
  TS(4);
  f32x4 oth[NKEEP];
#pragma unroll
  for (int i = 0; i < NKEEP; ++i) oth[i] = ex[(UK + i) * 64];
  f32x4 sv = (f32x4){}, qv = (f32x4){};
#pragma unroll
  for (int i = 0; i < NKEEP; ++i) {
    const f32x4 v = part[i] + gate[i] * oth[i];
    const u32x2 o = (u32x2){pack2(v[0], v[1]), pack2(v[2], v[3])};
    if (FINAL) *(GAS u32x2*)(ob + i * 32) = o;
    else {
      *(GAS u32x2*)(ob + fo[i]) = o;
      const f32x4 w = v * cbv[i];
      *(GAS u32x2*)(ob + (OFF_X1S - OFF_X1) + fo[i]) = (u32x2){pack2(w[0], w[1]), pack2(w[2], w[3])};
      sv += v; qv += v * v;
    }
  }
  if (!FINAL) {
    const float s = sum_g((sv[0] + sv[1]) + (sv[2] + sv[3])), q = sum_g((qv[0] + qv[1]) + (qv[2] + qv[3]));
    if (g == 0) *(f32x2*)(ws + OFF_ST1 + ((size_t)(mt * 16 + r) * 16 + cb * 2 + KH) * 8) = (f32x2){s, q};
  }
  TDUMP(FINAL ? 6 : 3);
  __builtin_amdgcn_endpgm();
}

template <int NKS, bool FINAL>
__global__ __launch_bounds__(512, 2) OCC2 void k_proj(char* __restrict__ ws, const u16* __restrict__ x, u16* __restrict__ out) {
  extern __shared__ __attribute__((aligned(16))) char smem[];
  const int tid = threadIdx.x, lane = tid & 63, wid = __builtin_amdgcn_readfirstlane(tid >> 6);
  const int xcd = blockIdx.x & 7, idx = blockIdx.x >> 3, panel = xcd * 4 + (idx >> 3), cb = idx & 7;
  {
    const char* src = ws + (FINAL ? OFF_WOUT : OFF_WO) + (size_t)cb * 5 * NKS * 1024 + tid * 16;
    const unsigned dst = lds_addr(smem) + wid * 1024;
    constexpr int NF = 5 * NKS;
#pragma unroll
    for (int i = 0; i < (NF + 7) / 8; ++i)
      if (i * 8 + 7 < NF || i * 8 + wid < NF) glds16(src + i * 8192, dst + i * 8192);
  }
  if (wid < 4) proj_body<NKS, FINAL, 0>(ws, x, out, smem, panel, cb, wid, lane);
  else proj_body<NKS, FINAL, 1>(ws, x, out, smem, panel, cb, wid - 4, lane);
}

DI float gelu_t(float x) {
  constexpr float C1 = -2.8853900817779268f * 0.7978845608028654f, C2 = C1 * 0.044715f;
  const float e = __builtin_amdgcn_exp2f(x * __builtin_fmaf(x * x, C2, C1));
  return x * __builtin_amdgcn_rcpf(1.f + e);
}
constexpr int K4_LN = 81920, K4_ST2 = K4_LN + 512, K4_FIN = K4_ST2 + 3584, K4_CNT = K4_FIN + 512, K4_TR = K4_CNT + 16, K4_LDS = K4_TR + 7 * 2048;
constexpr int K4_PRE = 12;

__global__ __launch_bounds__(512, 2) OCC2 void k4_win(char* __restrict__ ws) {
  extern __shared__ __attribute__((aligned(16))) char smem[];
  const int tid = threadIdx.x, lane = tid & 63, wid = __builtin_amdgcn_readfirstlane(tid >> 6);
  const int xcd = blockIdx.x & 7, idx = blockIdx.x >> 3, panel = xcd * 4 + (idx >> 3), cb = idx & 7;
  const int nu = cb < 6 ? 7 : 6, u = unit0_of(cb) + wid;
  const int g = lane >> 4, r = lane & 15;
  TDECL; TS(0);
  {
    const char* src = ws + OFF_X1S + (size_t)panel * 81920 + tid * 16;
    const unsigned dst = lds_addr(smem) + wid * 1024;
#pragma unroll
    for (int i = 0; i < 10; ++i) glds16(src + i * 8192, dst + i * 8192);
  }
  float* lnp = (float*)(smem + K4_LN); float* st2 = (float*)(smem + K4_ST2); float* fin = (float*)(smem + K4_FIN);
  int* cnt = (int*)(smem + K4_CNT);
  if (wid < nu) {
    __builtin_amdgcn_s_setprio(1);
    bf16x8 b2[20], b1[20];
    const char* s2 = ws + OFF_WIN + (size_t)(54 + u) * 20480 + lane * 16;
    const char* s1 = ws + OFF_WIN + (size_t)u * 20480 + lane * 16;
#pragma unroll
    for (int k = 0; k < K4_PRE; ++k) b2[k] = *(const bf16x8*)(s2 + k * 1024);
    TS(1);
    waitv<K4_PRE>();
    BAR();
    TS(2);
#pragma unroll
    for (int k = K4_PRE; k < 20; ++k) b2[k] = *(const bf16x8*)(s2 + k * 1024);
    const f32x4 cs2 = *(const f32x4*)(ws + OFF_CS + ((54 + u) * 16 + g * 4) * 4), bi2 = *(const f32x4*)(ws + OFF_BI + ((54 + u) * 16 + g * 4) * 4);
    const f32x4 cs1 = *(const f32x4*)(ws + OFF_CS + (u * 16 + g * 4) * 4), bi1 = *(const f32x4*)(ws + OFF_BI + (u * 16 + g * 4) * 4);
#pragma unroll
    for (int k = 0; k < 8; ++k) b1[k] = *(const bf16x8*)(s1 + k * 1024);
    f32x4 acc[4];
#pragma unroll
    for (int m = 0; m < 4; ++m) acc[m] = (f32x4){};
    SCHED();
    mma16_run<20, 20480, 1024, 2>(smem + lane * 16, b2, acc);
#pragma unroll
    for (int k = 8; k < 20; ++k) b1[k] = *(const bf16x8*)(s1 + k * 1024);
    TS(4);
    float z2[4][4]; f32x2 ln[4];
#pragma unroll
    for (int m = 0; m < 4; ++m) ln[m] = *(const f32x2*)(lnp + (m * 16 + r) * 2);
#pragma unroll
    for (int m = 0; m < 4; ++m) {
      float s = 0.f, q = 0.f;
#pragma unroll
      for (int e = 0; e < 4; ++e) { const float z = gelu_t(__builtin_fmaf(acc[m][e], ln[m][0], __builtin_fmaf(ln[m][1], cs2[e], bi2[e]))); z2[m][e] = z; s += z; q = __builtin_fmaf(z, z, q); }
      s = sum_g(s); q = sum_g(q);
      if (g == 0) *(f32x2*)(st2 + (wid * 64 + m * 16 + r) * 2) = (f32x2){s, q};
    }
    asm volatile("" ::: "memory");
    if (lane == 0) __hip_atomic_fetch_add(cnt, 1, __ATOMIC_RELAXED, __HIP_MEMORY_SCOPE_WORKGROUP);
    asm volatile("" ::: "memory");
    __builtin_amdgcn_s_setprio(0);
#pragma unroll
    for (int m = 0; m < 4; ++m) acc[m] = (f32x4){};
    TS(5);
    mma16_run<20, 20480, 1024, 3>(smem + lane * 16, b1, acc);
    TS(6);
    char* zd = ws + OFF_Z1 + (size_t)(panel * 54 + u) * 2048 + lane * 16;
#pragma unroll
    for (int mm = 0; mm < 2; ++mm) {
      u32x4 o;
#pragma unroll
      for (int i = 0; i < 2; ++i) {
        const int m = 2 * mm + i; float z[4];
#pragma unroll
        for (int e = 0; e < 4; ++e) z[e] = gelu_t(__builtin_fmaf(acc[m][e], ln[m][0], __builtin_fmaf(ln[m][1], cs1[e], bi1[e])));
        o[2 * i] = pack2s(z[0], z[1]); o[2 * i + 1] = pack2s(z[2], z[3]);
      }
      *(u32x4*)(zd + mm * 1024) = o;
    }
    TS(7);
    BAR();
    TS(8);
    char* tr = smem + K4_TR + wid * 2048;
#pragma unroll
    for (int m = 0; m < 4; ++m) {
      const f32x2 f = *(const f32x2*)(fin + (m * 16 + r) * 2);
      *(u32x2*)(tr + (m * 16 + r) * 32 + g * 8) = (u32x2){pack2s(z2[m][0] * f[0] - f[1], z2[m][1] * f[0] - f[1]), pack2s(z2[m][2] * f[0] - f[1], z2[m][3] * f[0] - f[1])};
    }
    asm volatile("s_waitcnt lgkmcnt(0)" ::: "memory");
    char* z2d = ws + OFF_Z2 + ((size_t)panel * 2 * 54 + u) * 1024 + lane * 16;
#pragma unroll
    for (int ks = 0; ks < 2; ++ks) {
      const char* p = tr + (ks * 32 + 8 * g + (r >> 2)) * 32 + (r & 3) * 8;
      const bf16x4 lo = lds_tr(p), hi = lds_tr(p + 128);
      *(bf16x8*)(z2d + ks * 55296) = __builtin_shufflevector(lo, hi, 0, 1, 2, 3, 4, 5, 6, 7);
    }
  } else if (wid == 7) {
    __builtin_amdgcn_s_setprio(2);
    {
      const f32x4* sp = (const f32x4*)(ws + OFF_ST1 + (size_t)(panel * 64 + lane) * 128);
      float s = 0.f, q = 0.f; f32x4 t[8];
#pragma unroll
      for (int i = 0; i < 8; ++i) t[i] = sp[i];
#pragma unroll
      for (int i = 0; i < 8; ++i) { s += t[i][0] + t[i][2]; q += t[i][1] + t[i][3]; }
      const float mean = s * (1.f / 640.f), var = fmaxf(q * (1.f / 640.f) - mean * mean, 0.f);
      const float ri = 1.f / ln_den(var);
      *(f32x2*)(lnp + lane * 2) = (f32x2){ri, -rbf(mean) * ri};
      if (lane == 0) *cnt = 0;
    }
    waitv<0>();
    BAR();
    for (unsigned spin = 0; spin < (1u << 24); ++spin) {
      if (__hip_atomic_load(cnt, __ATOMIC_RELAXED, __HIP_MEMORY_SCOPE_WORKGROUP) >= nu) break;
      __builtin_amdgcn_s_sleep(2);
    }
    asm volatile("" ::: "memory");
    float s = 0.f, q = 0.f;
    for (int i = 0; i < nu; ++i) { const f32x2 t = *(const f32x2*)(st2 + (i * 64 + lane) * 2); s += t[0]; q += t[1]; }
    GAS u64* xch = (GAS u64*)(ws + OFF_XCH) + panel * 512;
    unsigned qb = __float_as_uint(q); if (qb == ~0u) qb = 0x7fc00000u;
    __hip_atomic_store(xch + cb * 64 + lane, ((u64)qb << 32) | __float_as_uint(s), __ATOMIC_RELAXED, __HIP_MEMORY_SCOPE_AGENT);
    u64 v[8];
    for (unsigned spin = 0; spin < (1u << 22); ++spin) {
      bool ok = true;
#pragma unroll
      for (int i = 0; i < 8; ++i) { v[i] = __hip_atomic_load(xch + i * 64 + lane, __ATOMIC_RELAXED, __HIP_MEMORY_SCOPE_AGENT); ok = ok && ((unsigned)(v[i] >> 32) != ~0u); }
      if (__all(ok)) break;
      __builtin_amdgcn_s_sleep(1);
    }
    s = 0.f; q = 0.f;
#pragma unroll
    for (int i = 0; i < 8; ++i) { s += __uint_as_float((unsigned)v[i]); q += __uint_as_float((unsigned)(v[i] >> 32)); }
    const float mean = s * (1.f / 864.f), var = fmaxf(q * (1.f / 864.f) - mean * mean, 0.f);
    const float ri = 1.f / ln_den(var);
    *(f32x2*)(fin + lane * 2) = (f32x2){ri, rbf(mean) * ri};
    TS(7);
    BAR();
  } else {
    waitv<0>();
    BAR();
  }
  TDUMP(4);
}

#define K5L 3
constexpr int K5_L = K5L, K5_R = K5_L + 1, K5_EX = K5_R * 16384, K5_LDS = K5_EX + 8 * 4096;

template <int NU>
DI void k5_body(char* __restrict__ ws, const u16* __restrict__ wsp, const u16* __restrict__ bsp, char* smem, int panel, int u0, int wid, int lane) {
  TDECL; TS(0);
  constexpr int NA = NU ? NU : 1, NLD = 2 + 2 * NU;
  const int kh = wid >> 2, g = lane >> 4, r = lane & 15;
  const char* asrc = (const char*)wsp + (size_t)(panel * 64 + (wid & 3) * 16 + (lane >> 3)) * 4096 + kh * 128 + ((lane & 7) ^ (lane >> 3)) * 16;
  const unsigned adst = lds_addr(smem) + wid * 2048;
  const char* bsrc = ws + OFF_Z2 + (size_t)(kh * 2 * 54 + u0) * 1024 + lane * 16;
  const char* al[2][2];
#pragma unroll
  for (int k = 0; k < 2; ++k)
#pragma unroll
    for (int j = 0; j < 2; ++j) al[k][j] = smem + kh * 8192 + (j ^ kh) * 4096 + r * 128 + ((4 * k + g) ^ (r & 7)) * 16;
  bf16x8 bq[16][2][NA], af[2][2][4]; f32x4 acc[NA][4];
  u32x4 z[NA]; u16 bb[2];
#pragma unroll
  for (int i = 0; i < NU; ++i) z[i] = *(const u32x4*)(ws + OFF_Z1 + (size_t)(panel * 54 + u0 + i) * 2048 + kh * 1024 + lane * 16);
#pragma unroll
  for (int j = 0; j < 2; ++j) bb[j] = bsp[panel * 64 + (2 * kh + j) * 16 + r];
  asm volatile("" ::: "memory");
  auto issue1 = [&](auto S, auto I) __attribute__((always_inline)) {
    constexpr int s = S, i = I;
    if constexpr (s < 16 && i < NLD) {
      if constexpr (i < 2) glds16(asrc + s * 256 + i * 32768, adst + (s % K5_R) * 16384 + i * 1024);
      else { bq[s][(i - 2) / NA][(i - 2) % NA] = *(const bf16x8*)(bsrc + ((i - 2) % NA) * 1024 + (s * 4 + (i - 2) / NA) * 55296); asm volatile("" ::: "memory"); }
    }
  };
  auto rd1 = [&](auto S, int k, int m) __attribute__((always_inline)) {
    constexpr int s = S;
    if constexpr (NU && s < 16) af[s & 1][k][m] = *(const bf16x8*)(al[k][m >> 1] + (s % K5_R) * 16384 + (m & 1) * 2048);
  };
  sfor<K5_L>([&](auto S) __attribute__((always_inline)) { sfor<NLD>([&](auto I) __attribute__((always_inline)) { issue1(S, I); }); });
#pragma unroll
  for (int i = 0; i < NU; ++i) {
    const float b0 = bf2f(bb[0]), b1 = bf2f(bb[1]);
    acc[i][0] = (f32x4){b0, b0, b0, b0}; acc[i][1] = (f32x4){b1, b1, b1, b1}; acc[i][2] = (f32x4){}; acc[i][3] = (f32x4){};
  }
  waitv<(K5_L - 1) * NLD>();
  BAR();
  TS(1);
#pragma unroll
  for (int k = 0; k < 2; ++k)
#pragma unroll
    for (int m = 0; m < 4; ++m) rd1(std::integral_constant<int, 0>{}, k, m);
  sfor<NLD / 2>([&](auto I) __attribute__((always_inline)) { issue1(std::integral_constant<int, K5_L>{}, I); });
  SCHED();
  sfor<16>([&](auto S) __attribute__((always_inline)) {
    constexpr int s = S;
    sfor<4>([&](auto M) __attribute__((always_inline)) {
      constexpr int m = M;
#pragma unroll
      for (int i = 0; i < NU; ++i) acc[i][m] = MFMA16(bq[s][0][i], af[s & 1][0][m], acc[i][m]);
      if constexpr (m < NLD / 2) issue1(std::integral_constant<int, s + K5_L>{}, std::integral_constant<int, NLD / 2 + m>{});
      SCHED();
    });
    if constexpr (s < 15) {
      constexpr int last = s + K5_L > 15 ? 15 : s + K5_L;
      waitv<(last - (s + 1)) * NLD>();
      BAR();
    }
    sfor<4>([&](auto M) __attribute__((always_inline)) {
      constexpr int m = M;
      rd1(std::integral_constant<int, s + 1>{}, 0, m); rd1(std::integral_constant<int, s + 1>{}, 1, m);
#pragma unroll
      for (int i = 0; i < NU; ++i) acc[i][m] = MFMA16(bq[s][1][i], af[s & 1][1][m], acc[i][m]);
      if constexpr (m < NLD / 2) issue1(std::integral_constant<int, s + 1 + K5_L>{}, M);
      SCHED();
    });
  });
  TS(6);
  if (NU == 0) { BAR(); __builtin_amdgcn_endpgm(); }
  f32x4* exw = (f32x4*)(smem + K5_EX + wid * 4096) + lane;
  const f32x4* exr = (const f32x4*)(smem + K5_EX + (wid ^ 4) * 4096) + lane;
#pragma unroll
  for (int i = 0; i < NU; ++i)
#pragma unroll
    for (int j = 0; j < 2; ++j) exw[(i * 2 + j) * 64] = acc[i][2 + j];
  f32x4 zf[NA][2]; unsigned fo[NA];
#pragma unroll
  for (int i = 0; i < NU; ++i) {
    const int n = (u0 + i) * 16 + g * 4;
    fo[i] = (n >> 5) * 1024 + (((n & 31) >> 3) * 16 + r) * 16 + (n & 4) * 2; PIN(fo[i]);
#pragma unroll
    for (int j = 0; j < 2; ++j) {
      zf[i][j] = (f32x4){bflo(z[i][2 * j]), bfhi(z[i][2 * j]), bflo(z[i][2 * j + 1]), bfhi(z[i][2 * j + 1])};
      acc[i][j] *= zf[i][j]; PIN(zf[i][j]); PIN(acc[i][j]);
    }
  }
  char* ub = ws + OFF_U + (size_t)(panel * 4 + 2 * kh) * 27648;
  BAR();
  f32x4 oth[NA][2];
#pragma unroll
  for (int i = 0; i < NU; ++i)
#pragma unroll
    for (int j = 0; j < 2; ++j) oth[i][j] = exr[(i * 2 + j) * 64];
#pragma unroll
  for (int i = 0; i < NU; ++i)
#pragma unroll
    for (int j = 0; j < 2; ++j) {
      const f32x4 v = acc[i][j] + zf[i][j] * oth[i][j];
      *(u32x2*)(ub + j * 27648 + fo[i]) = (u32x2){pack2(v[0], v[1]), pack2(v[2], v[3])};
    }
  TDUMP(5);
  __builtin_amdgcn_endpgm();
}

__global__ __launch_bounds__(512, 2) OCC2 void k5_sp(char* __restrict__ ws, const u16* __restrict__ wsp, const u16* __restrict__ bsp) {
  extern __shared__ __attribute__((aligned(16))) char smem[];
  const int tid = threadIdx.x, lane = tid & 63, wid = __builtin_amdgcn_readfirstlane(tid >> 6);
#define K5MAP 0
  const int xcd = blockIdx.x & 7, idx = blockIdx.x >> 3;
  const int panel = (xcd >> K5MAP) * (4 << K5MAP) + (idx >> (3 - K5MAP)), cb = (xcd & ((1 << K5MAP) - 1)) * (8 >> K5MAP) + (idx & ((8 >> K5MAP) - 1));
  const int up = wid & 3, u0 = unit0_of(cb) + 2 * up;
  if (up < 3) k5_body<2>(ws, wsp, bsp, smem, panel, u0, wid, lane);
  else if (cb < 6) k5_body<1>(ws, wsp, bsp, smem, panel, u0, wid, lane);
  else k5_body<0>(ws, wsp, bsp, smem, panel, u0, wid, lane);
}

extern "C" void kernel_launch(void* const* d_in, const int* in_sizes, int n_in, void* d_out, int out_size, void* d_ws, size_t ws_size,
                              hipStream_t stream) {
  const u16* x = (const u16*)d_in[0];
  const u16* wqkv = (const u16*)d_in[1];
  const u16* wo = (const u16*)d_in[2];
  const u16* win = (const u16*)d_in[4];
  const u16* wsp = (const u16*)d_in[5];
  const u16* bsp = (const u16*)d_in[6];
  const u16* wout = (const u16*)d_in[7];
  const u16* cvec = (const u16*)d_in[8];
  const u16* wmod = (const u16*)d_in[9];
  char* ws = (char*)d_ws;
  u16* out = (u16*)d_out;
  constexpr int L3 = 100 * 1024 + 20480, L6 = 135 * 1024 + 20480;
  (void)hipFuncSetAttribute((const void*)k1_qkv, hipFuncAttributeMaxDynamicSharedMemorySize, 148480);
  (void)hipFuncSetAttribute((const void*)k2_attn, hipFuncAttributeMaxDynamicSharedMemorySize, 163840);
  (void)hipFuncSetAttribute((const void*)k_proj<20, false>, hipFuncAttributeMaxDynamicSharedMemorySize, L3);
  (void)hipFuncSetAttribute((const void*)k_proj<27, true>, hipFuncAttributeMaxDynamicSharedMemorySize, L6);
  (void)hipFuncSetAttribute((const void*)k4_win, hipFuncAttributeMaxDynamicSharedMemorySize, K4_LDS);
  (void)hipFuncSetAttribute((const void*)k5_sp, hipFuncAttributeMaxDynamicSharedMemorySize, K5_LDS);
  k0_prep<<<K0_GRID, 256, 0, stream>>>(x, wqkv, wo, wout, cvec, wmod, ws);
  k1_qkv<<<256, 512, 148480, stream>>>(ws, win);
  k2_attn<<<256, 512, 163840, stream>>>(ws);
  k_proj<20, false><<<256, 512, L3, stream>>>(ws, x, out);
  k4_win<<<256, 512, K4_LDS, stream>>>(ws);
  k5_sp<<<256, 512, K5_LDS, stream>>>(ws, wsp, bsp);
  k_proj<27, true><<<256, 512, L6, stream>>>(ws, x, out);
}
```

```cpp
#include <hip/hip_runtime.h>
#include <hip/hip_bf16.h>
#include <stdint.h>
#include <utility>

typedef __attribute__((ext_vector_type(8))) short bf16x8;
typedef __attribute__((ext_vector_type(4))) short bf16x4;
typedef __attribute__((ext_vector_type(2))) float f32x2;
typedef __attribute__((ext_vector_type(4))) float f32x4;
typedef __attribute__((ext_vector_type(16))) float f32x16;
typedef __attribute__((ext_vector_type(2))) unsigned u32x2;
typedef __attribute__((ext_vector_type(4))) unsigned u32x4;
typedef __attribute__((ext_vector_type(2))) __bf16 bf2_t;
typedef __attribute__((ext_vector_type(2))) _Float16 h2_t;
typedef unsigned short u16;
typedef unsigned long long u64;
#define LAS __attribute__((address_space(3)))
#define GAS __attribute__((address_space(1)))
#define DI __device__ __forceinline__

constexpr int D_ = 640, NQKV = 1920, F_ = 1728;

constexpr size_t OFF_MOD = 0;
constexpr size_t OFF_CS = 16384;
constexpr size_t OFF_BI = OFF_CS + 8192;
constexpr size_t OFF_MODP = OFF_BI + 8192;
constexpr size_t OFF_CBP = OFF_MODP + 40960;
constexpr size_t OFF_XCH = OFF_CBP + 20 * 16384;
constexpr size_t OFF_ST1 = OFF_XCH + 131072;
constexpr size_t OFF_XHAT = OFF_ST1 + 262144;
constexpr size_t OFF_WQKV = OFF_XHAT + 2621440;
constexpr size_t OFF_WO = OFF_WQKV + 2457600;
constexpr size_t OFF_WIN = OFF_WO + 819200;
constexpr size_t OFF_WOUT = OFF_WIN + 2211840;
constexpr size_t OFF_Q = OFF_WOUT + 1105920;
constexpr size_t OFF_KV = OFF_Q + 32 * 81920;
constexpr size_t OFF_O = OFF_KV + 32 * 163840;
constexpr size_t OFF_X1 = OFF_O + 2621440;
constexpr size_t OFF_X1S = OFF_X1 + 2621440;
constexpr size_t OFF_Z1 = OFF_X1S + 2621440;
constexpr size_t OFF_Z2 = OFF_Z1 + 3538944;
constexpr size_t OFF_U = OFF_Z2 + 3538944;

#define STAMP(kid, k)
#define TDECL
#define TS(k)
#define TDUMP(kid)
DI unsigned pack2(float lo, float hi) { return __builtin_bit_cast(unsigned, __builtin_convertvector((f32x2){lo, hi}, bf2_t)); }
DI unsigned pack2s(float lo, float hi) { unsigned r; asm("v_cvt_pk_bf16_f32 %0, %1, %2" : "=v"(r) : "v"(lo), "v"(hi)); return r; }
DI unsigned pack2h(float lo, float hi) { return __builtin_bit_cast(unsigned, __builtin_convertvector((f32x2){lo, hi}, h2_t)); }
DI float bflo(unsigned u) { return __uint_as_float(u << 16); }
DI float bfhi(unsigned u) { return __uint_as_float(u & 0xffff0000u); }
DI float rbf(float x) { return bflo(pack2(x, x)); }
DI float bf2f(u16 b) { return __uint_as_float((unsigned)b << 16); }
#define MFMA32(a, b, c) __builtin_amdgcn_mfma_f32_32x32x16_bf16((a), (b), (c), 0, 0, 0)
#define MFMA32K8(a, b, c) __builtin_amdgcn_mfma_f32_32x32x8bf16_1k((a), (b), (c), 0, 0, 0)
#define MFMA16(a, b, c) __builtin_amdgcn_mfma_f32_16x16x32_bf16((a), (b), (c), 0, 0, 0)
#define BAR() asm volatile("s_waitcnt lgkmcnt(0)\n\ts_barrier" ::: "memory")
#define SCHED() __builtin_amdgcn_sched_barrier(0)
#define PIN(x) asm volatile("" : "+v"(x))
#define OCC2 __attribute__((amdgpu_waves_per_eu(2, 2)))
#define BF8(x) __builtin_bit_cast(bf16x8, x)
#define BF4(x) __builtin_bit_cast(bf16x4, x)
template <int N> DI void waitv() { asm volatile("s_waitcnt vmcnt(%0)" ::"i"(N) : "memory"); }
template <class F, int... I> DI void sfor_impl(F&& f, std::integer_sequence<int, I...>) { (f(std::integral_constant<int, I>{}), ...); }
template <int N, class F> DI void sfor(F&& f) { sfor_impl(f, std::make_integer_sequence<int, N>{}); }
typedef short v4i16_t __attribute__((ext_vector_type(4)));
DI bf16x4 lds_tr(const char* p) { return __builtin_bit_cast(bf16x4, __builtin_amdgcn_ds_read_tr16_b64_v4i16((LAS v4i16_t*)p)); }
DI void glds16(const void* g, unsigned lds_base) {
  unsigned sv;
  asm volatile("s_mov_b32 %0, m0\n\ts_mov_b32 m0, %2\n\ts_nop 0\n\tglobal_load_lds_dwordx4 %1, off\n\ts_mov_b32 m0, %0" : "=&s"(sv) : "v"(g), "s"(lds_base) : "memory");
}
DI void l2touch(const void* g, unsigned lds_scratch) {
  unsigned sv;
  asm volatile("s_mov_b32 %0, m0\n\ts_mov_b32 m0, %2\n\ts_nop 0\n\tglobal_load_lds_dword %1, off\n\ts_mov_b32 m0, %0" : "=&s"(sv) : "v"(g), "s"(lds_scratch) : "memory");
}
DI void store_wt(void* p, u32x4 v) { asm volatile("global_store_dwordx4 %0, %1, off sc1\n\ts_nop 1" ::"v"(p), "v"(v) : "memory"); }
#define LDNT(p) __builtin_nontemporal_load(p)
DI float silu_bf(u16 c) { const float f = __uint_as_float((unsigned)c << 16); const float v = f / (1.f + __expf(-f)); return __uint_as_float(__builtin_bit_cast(unsigned, __builtin_convertvector((f32x2){v, v}, bf2_t)) << 16); }
DI unsigned lds_addr(const void* p) { return (unsigned)(uintptr_t)(const LAS void*)p; }
DI float xhalf(float v) {
  auto rr = __builtin_amdgcn_permlane32_swap(__float_as_uint(v), __float_as_uint(v), false, false);
  return __uint_as_float((threadIdx.x & 32) ? rr[0] : rr[1]);
}
DI float sum_g(float v) {
  auto a = __builtin_amdgcn_permlane16_swap(__float_as_uint(v), __float_as_uint(v), false, false);
  v = __uint_as_float(a[0]) + __uint_as_float(a[1]);
  auto b = __builtin_amdgcn_permlane32_swap(__float_as_uint(v), __float_as_uint(v), false, false);
  return __uint_as_float(b[0]) + __uint_as_float(b[1]);
}
template <int CTRL> DI float dpp_add(float v) { return v + __uint_as_float(__builtin_amdgcn_update_dpp(0u, __float_as_uint(v), CTRL, 0xf, 0xf, true)); }
DI float sum16(float v) {
  v = dpp_add<0x128>(v); v = dpp_add<0x124>(v); v = dpp_add<0x122>(v); return dpp_add<0x121>(v);
}
DI float mix_lo(unsigned h, float s, float b) { float r; asm("v_fma_mix_f32 %0, %1, %2, %3 op_sel_hi:[1,0,0]" : "=v"(r) : "v"(h), "v"(s), "v"(b)); return r; }
DI float mix_hi(unsigned h, float s, float b) { float r; asm("v_fma_mix_f32 %0, %1, %2, %3 op_sel:[1,0,0] op_sel_hi:[1,0,0]" : "=v"(r) : "v"(h), "v"(s), "v"(b)); return r; }
DI float ln_den(float var) { return rbf(__builtin_sqrtf(rbf(rbf(var) + 1e-5f))); }
DI int unit0_of(int j) { return j < 6 ? 7 * j : 42 + 6 * (j - 6); }

template <int NK, int MS, int KS, int PD>
DI void mma16_run(const char* al, const bf16x8 (&b)[NK], f32x4 (&acc)[4]) {
  bf16x8 af[PD + 1][4];
#pragma unroll
  for (int k = 0; k < PD; ++k)
#pragma unroll
    for (int m = 0; m < 4; ++m) af[k][m] = *(const bf16x8*)(al + m * MS + k * KS);
  SCHED();
#pragma unroll
  for (int k = 0; k < NK; ++k) {
#pragma unroll
    for (int m = 0; m < 4; ++m) {
      if (k + PD < NK) af[(k + PD) % (PD + 1)][m] = *(const bf16x8*)(al + m * MS + (k + PD) * KS);
      acc[m] = MFMA16(b[k], af[k % (PD + 1)][m], acc[m]);
      SCHED();
    }
  }
}

constexpr int K0_MOD = 160, K0_LN = 128, K0_RL = 150, K0_INIT = 8;
constexpr int K0_GRID = K0_MOD + K0_LN + K0_RL + K0_INIT;

__global__ __launch_bounds__(256) void k0_prep(const u16* __restrict__ x, const u16* __restrict__ wqkv, const u16* __restrict__ cvec,
                                               const u16* __restrict__ wmod, char* __restrict__ ws) {
  __shared__ __attribute__((aligned(16))) char smem[4 * 4608];
  const int tid = threadIdx.x, lane = tid & 63, wid = __builtin_amdgcn_readfirstlane(tid >> 6);
  int b = blockIdx.x;
  STAMP(0, 0);
  if (b < K0_MOD) {
    const int cg = b >> 2, ks = b & 3, c = tid & 7, row0 = ks * 160 + (tid >> 3), col0 = cg * 64 + (cg >= 20 ? 640 : 0);
    u32x4 w[5]; u16 cv[5];
#pragma unroll
    for (int i = 0; i < 5; ++i) { w[i] = LDNT((const u32x4*)(wmod + (size_t)(row0 + 32 * i) * 3840 + col0 + c * 8)); cv[i] = cvec[row0 + 32 * i]; }
    float acc[8] = {0, 0, 0, 0, 0, 0, 0, 0};
#pragma unroll
    for (int i = 0; i < 5; ++i) {
      const float s = silu_bf(cv[i]);
#pragma unroll
      for (int e = 0; e < 4; ++e) { acc[2 * e] += s * bflo(w[i][e]); acc[2 * e + 1] += s * bfhi(w[i][e]); }
    }
#pragma unroll
    for (int e = 0; e < 8; ++e) acc[e] = sum_g(dpp_add<0x128>(acc[e]));
    float* red = (float*)smem;
    if (lane < 8) { *(f32x4*)(red + wid * 64 + lane * 8) = (f32x4){acc[0], acc[1], acc[2], acc[3]}; *(f32x4*)(red + wid * 64 + lane * 8 + 4) = (f32x4){acc[4], acc[5], acc[6], acc[7]}; }
    __syncthreads();
    if (tid < 64) ((float*)(ws + OFF_MODP))[ks * 2560 + cg * 64 + tid] = (red[tid] + red[64 + tid]) + (red[128 + tid] + red[192 + tid]);
    STAMP(0, 1); STAMP(0, 13);
    return;
  }
  b -= K0_MOD;
  if (b < K0_LN) {
    b = (b & 7) * 16 + (b >> 3);
    const int row = b * 16 + wid * 4 + (lane >> 4), c = lane & 15;
    const u16* xr = x + (size_t)row * D_ + c * 8;
    u32x4 v[5];
#pragma unroll
    for (int i = 0; i < 5; ++i) v[i] = *(const u32x4*)(xr + i * 128);
    float s = 0.f, q = 0.f;
#pragma unroll
    for (int i = 0; i < 5; ++i)
#pragma unroll
      for (int e = 0; e < 4; ++e) { const float a = bflo(v[i][e]), d = bfhi(v[i][e]); s += a + d; q += a * a + d * d; }
    s = sum16(s); q = sum16(q);
    const float mean = s * (1.f / 640.f); const float var = fmaxf(q * (1.f / 640.f) - mean * mean, 0.f);
    const float rinv = 1.f / ln_den(var), mb = rbf(mean) * rinv;
    char* dst = ws + OFF_XHAT + (size_t)row * 1280 + c * 16;
#pragma unroll
    for (int i = 0; i < 5; ++i) {
      u32x4 o;
#pragma unroll
      for (int e = 0; e < 4; ++e) o[e] = pack2h(bflo(v[i][e]) * rinv - mb, bfhi(v[i][e]) * rinv - mb);
      *(u32x4*)(dst + i * 256) = o;
    }
    STAMP(0, 2); STAMP(0, 13);
    return;
  }
  b -= K0_LN;
  if (b < K0_RL) {
    const int job = b * 4 + wid, kt = job / 30, ng = job % 30; char* dst = ws + OFF_WQKV;
    char* t = smem + wid * 4608;
    u32x4 v[4];
#pragma unroll
    for (int i = 0; i < 4; ++i) v[i] = LDNT((const u32x4*)(wqkv + (size_t)(kt * 32 + i * 8 + (lane >> 3)) * NQKV + ng * 64 + (lane & 7) * 8));
#pragma unroll
    for (int i = 0; i < 4; ++i) *(u32x4*)(t + (i * 8 + (lane >> 3)) * 144 + (lane & 7) * 16) = v[i];
    asm volatile("s_waitcnt lgkmcnt(0)" ::: "memory");
    const int g = lane >> 4, q = (lane & 15) >> 2, p = lane & 3;
#pragma unroll
    for (int f = 0; f < 4; ++f) {
      const int row = 16 * (f >> 1) + 8 * (g >> 1) + q, col = 32 * (f & 1) + 16 * (g & 1) + 4 * p; const size_t o = ((size_t)(ng * 2 + (f & 1)) * 40 + kt * 2 + (f >> 1)) * 1024;
      const bf16x4 lo = lds_tr(t + row * 144 + col * 2), hi = lds_tr(t + (row + 4) * 144 + col * 2);
      *(bf16x8*)(dst + o + lane * 16) = __builtin_shufflevector(lo, hi, 0, 1, 2, 3, 4, 5, 6, 7);
    }
    STAMP(0, 3); STAMP(0, 13);
    return;
  }
  b -= K0_RL;
  {
    u32x4* p = (u32x4*)(ws + OFF_XCH) + b * 1024 + tid;
#pragma unroll
    for (int i = 0; i < 4; ++i) p[i * 256] = (u32x4){~0u, ~0u, ~0u, ~0u};
  }
}

constexpr float QSCALE = 0.22360679774997896f * 1.4426950408889634f;
constexpr int K1_W = 3;

template <bool ISV>
DI void k1_mma(const char* __restrict__ bsrc, const char* smem, int lane, f32x16 (&acc)[4]) {
  bf16x8 bfr[40], af[4][4];
  const char* lds[4];
#pragma unroll
  for (int k = 0; k < 4; ++k) lds[k] = smem + (lane ^ (2 * k + (lane >> 5))) * 16;
#pragma unroll
  for (int k = 0; k < 4 * K1_W; ++k) bfr[k] = *(const bf16x8*)(bsrc + k * 1024);
#pragma unroll
  for (int m = 0; m < 4; ++m) acc[m] = (f32x16){};
  BAR();
  BAR();
#define K1_RD1(s, k, m) (af[k][m] = *(const bf16x8*)(lds[k] + ((s) & 1) * 16384 + ((k) * 4 + (m)) * 1024))
#define K1_STEP(s, k, RD, rs, rk, LD) do { _Pragma("unroll") for (int m = 0; m < 4; ++m) { if (RD) K1_RD1(rs, rk, m); \
      if (LD) bfr[4 * ((s) + K1_W) + m] = *(const bf16x8*)(bsrc + (4 * ((s) + K1_W) + m) * 1024); \
      acc[m] = ISV ? MFMA32(af[k][m], bfr[(s) * 4 + (k)], acc[m]) : MFMA32(bfr[(s) * 4 + (k)], af[k][m], acc[m]); SCHED(); } } while (0)
#pragma unroll
  for (int m = 0; m < 4; ++m) K1_RD1(0, 0, m);
#pragma unroll
  for (int m = 0; m < 4; ++m) K1_RD1(0, 1, m);
  SCHED();
#pragma unroll
  for (int s = 0; s < 10; ++s) {
    K1_STEP(s, 0, true, s, 2, false);
    K1_STEP(s, 1, true, s, 3, false);
    K1_STEP(s, 2, false, 0, 0, s + K1_W < 10);
    if (s < 9) {
      BAR();
#pragma unroll
      for (int m = 0; m < 4; ++m) K1_RD1(s + 1, 0, m);
      SCHED();
    }
    K1_STEP(s, 3, s < 9, s + 1, 1, false);
  }
}

__global__ __launch_bounds__(512, 2) OCC2 void k1_qkv(char* __restrict__ ws, const u16* __restrict__ cvec, const u16* __restrict__ wmod) {
  extern __shared__ __attribute__((aligned(16))) char smem[];
  const int tid = threadIdx.x, lane = tid & 63, wid = __builtin_amdgcn_readfirstlane(tid >> 6);
  const int xcd = blockIdx.x & 7, idx = blockIdx.x >> 3;
  TDECL; TS(0);
  if (idx >= 30) {
    const int sb = xcd * 2 + idx - 30, col0 = (sb < 8 ? 1280 : 3200 - 640) + sb * 80, c = tid % 10, rg = tid / 10;
    float* sil = (float*)smem; float* red = (float*)(smem + 4096);
    for (int i = tid; i < 660; i += 512) sil[i] = i < 640 ? silu_bf(cvec[i]) : 0.f;
    if (tid < 80) {
      const float* pp = (const float*)(ws + OFF_MODP) + 1280 + sb * 80 + tid;
      float v = rbf((pp[0] + pp[2560]) + (pp[5120] + pp[7680]));
      if (sb >= 8) v = rbf(1.f + v);
      ((float*)(ws + OFF_MOD))[1920 + sb * 80 + tid] = v;
    }
    u32x4 w[13];
#pragma unroll
    for (int i = 0; i < 13; ++i) { const int row = min(rg + 50 * i, 639); w[i] = LDNT((const u32x4*)(wmod + (size_t)row * 3840 + col0 + c * 8)); }
    BAR();
    float acc[8] = {0, 0, 0, 0, 0, 0, 0, 0};
#pragma unroll
    for (int i = 0; i < 13; ++i) {
      const float s = sil[rg + 50 * i];
#pragma unroll
      for (int e = 0; e < 4; ++e) { acc[2 * e] += s * bflo(w[i][e]); acc[2 * e + 1] += s * bfhi(w[i][e]); }
    }
    if (rg < 50) { *(f32x4*)(red + rg * 80 + c * 8) = (f32x4){acc[0], acc[1], acc[2], acc[3]}; *(f32x4*)(red + rg * 80 + c * 8 + 4) = (f32x4){acc[4], acc[5], acc[6], acc[7]}; }
    BAR();
    if (tid < 80) {
      float v = 0.f;
#pragma unroll
      for (int i = 0; i < 50; ++i) v += red[i * 80 + tid];
      ((float*)(ws + OFF_MOD))[col0 + tid] = rbf(v);
    }
    TS(11); TDUMP(1);
    __builtin_amdgcn_endpgm();
  }
  const int mt = xcd * 2 + (idx / 15), nt = idx % 15;
  if (wid < 4) {
    float* cst = (float*)(smem + 32768);
    const int c = lane & 7, rr = lane >> 3;
    const char* src = ws + OFF_XHAT + (size_t)(mt * 128 + wid * 32 + rr) * 1280 + c * 16;
    char* wdst = smem + ((c >> 1) * 4 + wid) * 1024 + (c & 1) * 512 + (rr ^ c) * 16;
    u32x4 a[10][4];
    f32x4 c0[4], c1[4];
#pragma unroll
    for (int i = 0; i < 4; ++i) { c0[i] = *(const f32x4*)(ws + OFF_MODP + i * 10240 + tid * 16); if (wid == 0) c1[i] = *(const f32x4*)(ws + OFF_MODP + i * 10240 + 4096 + tid * 16); }
#pragma unroll
    for (int s = 0; s < K1_W; ++s)
#pragma unroll
      for (int m = 0; m < 4; ++m) a[s][m] = *(const u32x4*)(src + m * 10240 + s * 128);
    {
      f32x4 v = (c0[0] + c0[1]) + (c0[2] + c0[3]); const float one = tid >= 160 ? 1.f : 0.f;
      *(f32x4*)(cst + tid * 4) = (f32x4){rbf(one + rbf(v[0])), rbf(one + rbf(v[1])), rbf(one + rbf(v[2])), rbf(one + rbf(v[3]))};
      if (wid == 0) { v = (c1[0] + c1[1]) + (c1[2] + c1[3]); *(f32x4*)(cst + 1024 + tid * 4) = (f32x4){rbf(1.f + rbf(v[0])), rbf(1.f + rbf(v[1])), rbf(1.f + rbf(v[2])), rbf(1.f + rbf(v[3]))}; }
    }
    BAR();
    const float* cp = cst + c * 8;
    f32x4 kc[10][4];
#pragma unroll
    for (int s = 0; s < 8; ++s) { kc[s][0] = *(const f32x4*)(cp + s * 64); kc[s][1] = *(const f32x4*)(cp + s * 64 + 4); kc[s][2] = *(const f32x4*)(cp + s * 64 + 640); kc[s][3] = *(const f32x4*)(cp + s * 64 + 644); }
    SCHED();
#pragma unroll
    for (int s = 0; s < 10; ++s) {
      if (s + K1_W < 10) {
#pragma unroll
        for (int m = 0; m < 4; ++m) a[s + K1_W][m] = *(const u32x4*)(src + m * 10240 + (s + K1_W) * 128);
      }
#pragma unroll
      for (int m = 0; m < 4; ++m) {
        u32x4 o; const u32x4 v = a[s][m]; const f32x4 sh0 = kc[s][0], sh1 = kc[s][1], sc0 = kc[s][2], sc1 = kc[s][3];
        o[0] = pack2(mix_lo(v[0], sc0[0], sh0[0]), mix_hi(v[0], sc0[1], sh0[1]));
        o[1] = pack2(mix_lo(v[1], sc0[2], sh0[2]), mix_hi(v[1], sc0[3], sh0[3]));
        o[2] = pack2(mix_lo(v[2], sc1[0], sh1[0]), mix_hi(v[2], sc1[1], sh1[1]));
        o[3] = pack2(mix_lo(v[3], sc1[2], sh1[2]), mix_hi(v[3], sc1[3], sh1[3]));
        *(u32x4*)(wdst + (s & 1) * 16384 + m * 128) = o;
      }
      if (s < 2) { kc[s + 8][0] = *(const f32x4*)(cp + (s + 8) * 64); kc[s + 8][1] = *(const f32x4*)(cp + (s + 8) * 64 + 4); kc[s + 8][2] = *(const f32x4*)(cp + (s + 8) * 64 + 640); kc[s + 8][3] = *(const f32x4*)(cp + (s + 8) * 64 + 644); }
      BAR();
    }
    TDUMP(1);
    __builtin_amdgcn_endpgm();
  }
  const int cw = wid - 4;
  const char* bsrc = ws + OFF_WQKV + (size_t)(nt * 4 + cw) * 40 * 1024 + lane * 16;
  f32x16 acc[4];
  const bool isV = nt >= 10;
  if (isV) k1_mma<true>(bsrc, smem, lane, acc); else k1_mma<false>(bsrc, smem, lane, acc);
  TS(12);
  const int r = lane & 31, h = lane >> 5, n0 = nt * 128 + cw * 32;
  if (isV) {
    const unsigned c = n0 + r - 1280, head = c / 20, d = c % 20;
    char* base = ws + OFF_KV + (mt >> 1) * 20480 + 10240 + (mt & 1) * 5120;
    const unsigned off = head * 163840u + (h * 20 + d) * 16;
#pragma unroll
    for (int m = 0; m < 4; ++m)
#pragma unroll
      for (int s = 0; s < 2; ++s) {
        u32x4 o;
#pragma unroll
        for (int e = 0; e < 4; ++e) o[e] = pack2(acc[m][8 * s + 2 * e], acc[m][8 * s + 2 * e + 1]);
        *(u32x4*)(base + off + m * 1280 + s * 640) = o;
      }
  } else {
    const bool isQ = nt < 5; const float sc = isQ ? QSCALE : 1.f;
    char* base = isQ ? ws + OFF_Q + mt * 5120 : ws + OFF_KV + (mt >> 1) * 20480 + (mt & 1) * 5120;
    const unsigned hs = isQ ? 81920u : 163840u;
#pragma unroll
    for (int qd = 0; qd < 4; ++qd) {
      const unsigned c = n0 + 8 * qd + 4 * h - (isQ ? 0 : 640), head = c / 20, dq = (c % 20) >> 2;
      const unsigned off = head * hs + (dq < 4 ? ((dq >> 1) * 32 + r) * 16 + (dq & 1) * 8 : 1024 + r * 8);
#pragma unroll
      for (int m = 0; m < 4; ++m)
        *(u32x2*)(base + off + m * 1280) = (u32x2){pack2(acc[m][4 * qd] * sc, acc[m][4 * qd + 1] * sc), pack2(acc[m][4 * qd + 2] * sc, acc[m][4 * qd + 3] * sc)};
    }
  }
  TDUMP(1);
}

DI void split3(float m, unsigned& w_h0, unsigned& w_h1) {
  const float n = -m; const float a = rbf(n), b = rbf(n - a), c = rbf(n - a - b);
  w_h0 = pack2(a, b); w_h1 = pack2(c, 0.f);
}

__global__ __launch_bounds__(512, 2) OCC2 void k2_attn(char* __restrict__ ws, const char* __restrict__ wsp, const u16* __restrict__ wo, const u16* __restrict__ win, const u16* __restrict__ wout) {
  extern __shared__ __attribute__((aligned(16))) char smem[];
  const int tid = threadIdx.x, lane = tid & 63, wid = __builtin_amdgcn_readfirstlane(tid >> 6);
  const int xcd = blockIdx.x & 7, idx = blockIdx.x >> 3, head = xcd * 4 + (idx >> 3), qb = idx & 7;
  const int r = lane & 31, h = lane >> 5, qg = wid & 3, kh = wid >> 2;
  unsigned kvo = OFF_KV + head * 163840, qo = OFF_Q + head * 81920 + (qb * 8 + qg * 2) * 1280;
  const unsigned kconst = h == 0 ? 0x3f803f80u : 0x00003f80u;
  const unsigned so16 = (tid & 255) * 16, l16 = lane * 16, l2 = 1024 + r * 8 + h * 4;
  TDECL; TS(0);
  for (int attempt = 0; attempt < 2; ++attempt) {
    asm volatile("" : "+s"(kvo), "+s"(qo));
    const char* kv = ws + kvo; const char* qp = ws + qo;
    const char* kvs = kv + kh * 81920 + so16;
    const unsigned ldst = lds_addr(smem) + kh * 81920 + qg * 1024;
    if (wid == 0) { const unsigned f = (tid & 1) ? 0u : 0x3f803f80u; *(u32x4*)(smem + l16) = (u32x4){f, f, f, f}; }
    else glds16(kvs, ldst);
#pragma unroll
    for (int i = 1; i < 5; ++i) glds16(kvs + i * 4096, ldst + i * 4096);
    const bf16x8 qA1 = *(const bf16x8*)(qp + l16), qB1 = *(const bf16x8*)(qp + 1280 + l16);
    const unsigned qA2w = *(const unsigned*)(qp + l2), qB2w = *(const unsigned*)(qp + 1280 + l2);
    bf16x8 k1 = *(const bf16x8*)(kv + l16); unsigned k2 = *(const unsigned*)(kv + l2);
    float mA = -INFINITY, mB = -INFINITY;
    {
      const bf16x4 qA2 = BF4(((u32x2){qA2w, 0u})), qB2 = BF4(((u32x2){qB2w, 0u}));
      const int nt = attempt ? 64 : 1;
      bf16x8 t1 = k1; unsigned t2 = k2;
      for (int t = 0; t < nt; ++t) {
        if (t) { const char* tp = kv + (t >> 3) * 20480 + (t & 7) * 1280; t1 = *(const bf16x8*)(tp + l16); t2 = *(const unsigned*)(tp + l2); }
        const bf16x4 t2v = BF4(((u32x2){t2, kconst}));
        f32x16 a = MFMA32(t1, qA1, (f32x16){}); a = MFMA32K8(t2v, qA2, a);
        f32x16 b = MFMA32(t1, qB1, (f32x16){}); b = MFMA32K8(t2v, qB2, b);
#pragma unroll
        for (int i = 0; i < 16; ++i) { mA = fmaxf(mA, a[i]); mB = fmaxf(mB, b[i]); }
      }
      mA = fmaxf(mA, xhalf(mA)); mB = fmaxf(mB, xhalf(mB));
    }
    unsigned a0, a1, b0, b1; split3(mA, a0, a1); split3(mB, b0, b1);
    const bf16x4 qA2 = BF4(((u32x2){qA2w, h ? a1 : a0})), qB2 = BF4(((u32x2){qB2w, h ? b1 : b0}));
    char* lh = smem + kh * 81920;
    waitv<0>();
    TS(1);
    BAR();
    TS(2);
    f32x16 oA = (f32x16){}, oB = (f32x16){};
    const char* kp1 = lh + l16;
    const char* kp2 = lh + l2;
    const bool vreal = r < 20;
    const char* vp = vreal ? lh + 10240 + (h * 20 + r) * 16 : smem + (r == 20 ? 0 : 16);
    const int vinc = vreal ? 1280 : 0, vjump = vreal ? 10240 : 0;
    if (kh) { k1 = *(const bf16x8*)kp1; k2 = *(const unsigned*)kp2; }
    bf16x8 kf1[2], vf[2][2]; u32x2 kf2[2];
    kf1[0] = k1; kf2[0] = (u32x2){k2, kconst}; kf2[1] = (u32x2){0u, kconst};
    vf[1][0] = (bf16x8){}; vf[1][1] = (bf16x8){};
    u32x4 pA0, pA1, pB0 = (u32x4){}, pB1 = (u32x4){};
    f32x4 e0 = (f32x4){}, e1;
    f32x16 sA = MFMA32(kf1[0], qA1, (f32x16){}), sB;
    sA = MFMA32K8(BF4(kf2[0]), qA2, sA);
#define EX2(x) __builtin_amdgcn_exp2f(x)
#define GRP(MF, S, i, EN, EP, P, w) do { MF; SCHED(); EN[0] = EX2(S[i]); P[w] = pack2(EP[0], EP[1]); EN[1] = EX2(S[i + 1]); P[w + 1] = pack2(EP[2], EP[3]); \
      EN[2] = EX2(S[i + 2]); EN[3] = EX2(S[i + 3]); PIN(P); PIN(EN); SCHED(); } while (0)
    auto group = [&](auto FIRST, int jump, int j) __attribute__((always_inline)) {
#pragma unroll
      for (int t = 0; t < 8; ++t) {
        const int c = t & 1, n = c ^ 1;
        if (FIRST) { if (t == 0) __builtin_amdgcn_s_setprio(3); if (t == 3) __builtin_amdgcn_s_setprio(2); if (t == 5) __builtin_amdgcn_s_setprio(1); if (t == 6) __builtin_amdgcn_s_setprio(0); if (t == 7) __builtin_amdgcn_s_setprio(3); }
        else { if (t == 4 && j == 2) __builtin_amdgcn_s_setprio(2); if (t == 3 && j == 3) __builtin_amdgcn_s_setprio(1); if (t == 6 && j == 3) __builtin_amdgcn_s_setprio(0); }
        if (FIRST && t < 5) {
#pragma unroll
          for (int i = 5 + 3 * t; i < 8 + 3 * t; ++i) glds16(kvs + i * 4096, ldst + i * 4096);
        }
        if (t == 7) {
          if (FIRST) { waitv<0>(); BAR(); }
          kp1 += jump; kp2 += jump;
        }
        SCHED();
        kf1[n] = *(const bf16x8*)(kp1 + ((t + 1) & 7) * 1280); kf2[n][0] = *(const unsigned*)(kp2 + ((t + 1) & 7) * 1280);
        GRP(sB = MFMA32(kf1[c], qB1, (f32x16){}), sA, 0, e1, e0, pB1, 2);
        vf[c][0] = *(const bf16x8*)vp; vf[c][1] = *(const bf16x8*)(vp + 640); vp += vinc;
        GRP(sB = MFMA32K8(BF4(kf2[c]), qB2, sB), sA, 4, e0, e1, pA0, 0);
        GRP(oB = MFMA32(vf[n][0], BF8(pB0), oB), sA, 8, e1, e0, pA0, 2);
        GRP(oB = MFMA32(vf[n][1], BF8(pB1), oB), sA, 12, e0, e1, pA1, 0);
        GRP(sA = MFMA32(kf1[n], qA1, (f32x16){}), sB, 0, e1, e0, pA1, 2);
        GRP(sA = MFMA32K8(BF4(kf2[n]), qA2, sA), sB, 4, e0, e1, pB0, 0);
        GRP(oA = MFMA32(vf[c][0], BF8(pA0), oA), sB, 8, e1, e0, pB0, 2);
        GRP(oA = MFMA32(vf[c][1], BF8(pA1), oA), sB, 12, e0, e1, pB1, 0);
      }
      vp += vjump;
    };
    group(std::true_type{}, 20480, 0);
    TS(3);
    int job = blockIdx.x * 4 + qg;
    const bool side = kh == 0 && job < 1010 && attempt == 0;
    const char* stile = smem + 1024 + qg * 4096;
    const char* svec = smem + 81920 + qg * 256;
    int kt = 0, ng = 0;
    if (side) {
      const u16* W = win; int N = F_;
      if (job < 540) { kt = job / 27; ng = job % 27; }
      else if (job < 740) { W = wo; N = D_; kt = (job - 540) / 10; ng = (job - 540) % 10; }
      else { W = wout; N = D_; kt = (job - 740) / 10; ng = (job - 740) % 10; }
      const u16* src = W + (size_t)(kt * 32 + (lane >> 3)) * N + ng * 64 + ((lane & 7) ^ (lane >> 3)) * 8;
#pragma unroll
      for (int i = 0; i < 4; ++i) glds16(src + (size_t)i * 8 * N, lds_addr(stile) + i * 1024);
      l2touch((const float*)(ws + OFF_MOD) + (4 - h) * 640 + (kt < 20 ? kt : 0) * 32 + r, lds_addr(svec));
    }
#pragma nounroll
    for (int j = 1; j < 4; ++j) {
      if (j == 2 && kh) l2touch(wsp + ((size_t)job * 64 + lane) * 128, lds_addr(smem) + 768);
      if (j == 3 && side) {
        waitv<0>();
        const int g4 = lane >> 4, r4 = lane & 15, row = 8 * g4 + (r4 >> 2);
        const f32x4 sv0 = *(const f32x4*)(svec + (r4 & 1) * 128 + g4 * 32), sv1 = *(const f32x4*)(svec + (r4 & 1) * 128 + g4 * 32 + 16);
        const bf16x8 vec = BF8(((u32x4){pack2(sv0[0], sv0[1]), pack2(sv0[2], sv0[3]), pack2(sv1[0], sv1[1]), pack2(sv1[2], sv1[3])}));
        char* dst = job < 540 ? ws + OFF_WIN : job < 740 ? ws + OFF_WO : ws + OFF_WOUT;
        const int nks = job < 740 ? 20 : 27;
#pragma unroll
        for (int f = 0; f < 4; ++f) {
          const int ch = 2 * f + ((r4 & 3) >> 1);
          const bf16x4 lo = lds_tr(stile + row * 128 + (ch ^ (row & 7)) * 16 + (r4 & 1) * 8), hi = lds_tr(stile + (row + 4) * 128 + (ch ^ ((row + 4) & 7)) * 16 + (r4 & 1) * 8);
          const bf16x8 fr = __builtin_shufflevector(lo, hi, 0, 1, 2, 3, 4, 5, 6, 7);
          store_wt(dst + ((size_t)(ng * 4 + f) * nks + kt) * 1024 + l16, __builtin_bit_cast(u32x4, fr));
          const f32x4 pr = MFMA16(fr, vec, (f32x4){});
          if (job < 540 && r4 < 2) *(f32x4*)(ws + OFF_CBP + kt * 16384 + r4 * 8192 + ((ng * 4 + f) * 16 + g4 * 4) * 4) = pr;
        }
      }
      group(std::false_type{}, j < 3 ? 20480 : 0, j);
    }
    pB1[2] = pack2(e0[0], e0[1]); pB1[3] = pack2(e0[2], e0[3]);
    oB = MFMA32(vf[1][0], BF8(pB0), oB); oB = MFMA32(vf[1][1], BF8(pB1), oB);
    {
      f32x4* ex = (f32x4*)(lh + 1024 + qg * 4096) + lane;
      const f32x16 give = kh ? oA : oB;
#pragma unroll
      for (int g = 0; g < 3; ++g) ex[g * 64] = (f32x4){give[4 * g], give[4 * g + 1], give[4 * g + 2], give[4 * g + 3]};
    }
    int* flag = (int*)(smem + 528);
    BAR();
    TS(8);
    float o[12];
    {
      const f32x4* ex = (const f32x4*)(smem + (kh ^ 1) * 81920 + 1024 + qg * 4096) + lane;
      const f32x16 keep = kh ? oB : oA;
#pragma unroll
      for (int g = 0; g < 3; ++g) { const f32x4 t = ex[g * 64];
#pragma unroll
        for (int e = 0; e < 4; ++e) o[4 * g + e] = keep[4 * g + e] + t[e]; }
    }
    const float lo = xhalf(o[8]); const float l = h ? o[8] : lo;
    bool ok = (l > 1e-30f) && (l < 1e30f);
#pragma unroll
    for (int i = 0; i < 12; ++i) ok = ok && (fabsf(o[i]) < 3e38f);
    const float rl = 1.f / l;
    const int tok = qb * 256 + qg * 64 + kh * 32 + r;
    char* ob = ws + OFF_O + (size_t)(tok >> 4) * 20480 + (tok & 15) * 16;
#pragma unroll
    for (int g = 0; g < 3; ++g) {
      if (g < 2 || h == 0) {
        const int k = head * 20 + 8 * g + 4 * h;
        *(u32x2*)(ob + (k >> 5) * 1024 + ((k & 31) >> 3) * 256 + (k & 4) * 2) = (u32x2){pack2(o[4 * g] * rl, o[4 * g + 1] * rl), pack2(o[4 * g + 2] * rl, o[4 * g + 3] * rl)};
      }
    }
    if (attempt) break;
    if (!ok) *flag = 1;
    BAR();
    const int redo = *(volatile LAS int*)flag;
    if (!redo) break;
    BAR();
  }
  TDUMP(2);
}

template <int NKS, bool FINAL, int KH>
DI void proj_body(char* __restrict__ ws, const u16* __restrict__ x, u16* __restrict__ out, char* smem, int panel, int cb, int m, int lane, bool side, const float (&t)[20]) {
  constexpr int K0 = KH ? (NKS + 1) / 2 : 0, NK = KH ? NKS / 2 : (NKS + 1) / 2, NKEEP = KH ? 2 : 3, UK = KH ? 3 : 0, UG = KH ? 0 : 3;
  TDECL; TS(0);
  const int mt = panel * 4 + m, g = lane >> 4, r = lane & 15;
  bf16x8 a[NK];
  const char* asrc = ws + (FINAL ? OFF_U : OFF_O) + ((size_t)mt * NKS + K0) * 1024 + lane * 16;
#pragma unroll
  for (int k = 0; k < NK; ++k) a[k] = *(const bf16x8*)(asrc + k * 1024);
  u32x2 res[NKEEP]; f32x4 gate[NKEEP], cbv[NKEEP]; unsigned fo[NKEEP];
#pragma unroll
  for (int i = 0; i < NKEEP; ++i) {
    const int n = (cb * 5 + UK + i) * 16 + g * 4;
    fo[i] = (n >> 5) * 1024 + (((n & 31) >> 3) * 16 + r) * 16 + (n & 4) * 2;
    if (FINAL) res[i] = *(const u32x2*)(ws + OFF_X1 + (size_t)mt * 20480 + fo[i]);
    else res[i] = *(const u32x2*)(x + (size_t)(mt * 16 + r) * D_ + n);
    gate[i] = *(const f32x4*)(ws + OFF_MOD + ((FINAL ? 5 : 2) * 640 + n) * 4);
    if (!FINAL) cbv[i] = *(const f32x4*)(ws + OFF_MOD + (4 * 640 + n) * 4);
  }
  TS(1);
  waitv<NK + (FINAL ? 2 : 3) * NKEEP>();
  BAR();
  TS(2);
  constexpr int PD = 3;
  const char* bl = smem + K0 * 1024 + lane * 16;
  bf16x8 bf[PD + 1][5]; f32x4 acc[5];
#pragma unroll
  for (int u = 0; u < 5; ++u) acc[u] = (f32x4){};
#pragma unroll
  for (int k = 0; k < PD; ++k)
#pragma unroll
    for (int u = 0; u < 5; ++u) bf[k][u] = *(const bf16x8*)(bl + (u * NKS + k) * 1024);
  SCHED();
#pragma unroll
  for (int k = 0; k < NK; ++k) {
#pragma unroll
    for (int u = 0; u < 5; ++u) {
      if (k + PD < NK) bf[(k + PD) % (PD + 1)][u] = *(const bf16x8*)(bl + (u * NKS + k + PD) * 1024);
      acc[u] = MFMA16(bf[k % (PD + 1)][u], a[k], acc[u]);
      SCHED();
    }
  }
  TS(3);
  f32x4* ex = (f32x4*)(smem + 5 * NKS * 1024) + m * 5 * 64 + lane;
#pragma unroll
  for (int i = 0; i < 5 - NKEEP; ++i) ex[(UG + i) * 64] = acc[UG + i];
  f32x4 part[NKEEP];
#pragma unroll
  for (int i = 0; i < NKEEP; ++i) {
    part[i] = (f32x4){bflo(res[i][0]), bfhi(res[i][0]), bflo(res[i][1]), bfhi(res[i][1])} + gate[i] * acc[UK + i];
    PIN(part[i]);
  }
  char* ob = FINAL ? (char*)(out + (size_t)(mt * 16 + r) * D_ + (cb * 5 + UK) * 16 + g * 4) : ws + OFF_X1 + (size_t)mt * 20480;
  PIN(ob);
  BAR();
  TS(4);
  f32x4 oth[NKEEP];
#pragma unroll
  for (int i = 0; i < NKEEP; ++i) oth[i] = ex[(UK + i) * 64];
  f32x4 sv = (f32x4){}, qv = (f32x4){};
#pragma unroll
  for (int i = 0; i < NKEEP; ++i) {
    const f32x4 v = part[i] + gate[i] * oth[i];
    const u32x2 o = (u32x2){pack2(v[0], v[1]), pack2(v[2], v[3])};
    if (FINAL) *(GAS u32x2*)(ob + i * 32) = o;
    else {
      *(GAS u32x2*)(ob + fo[i]) = o;
      const f32x4 w = v * cbv[i];
      *(GAS u32x2*)(ob + (OFF_X1S - OFF_X1) + fo[i]) = (u32x2){pack2(w[0], w[1]), pack2(w[2], w[3])};
      sv += v; qv += v * v;
    }
  }
  if (!FINAL) {
    const float s = sum_g((sv[0] + sv[1]) + (sv[2] + sv[3])), q = sum_g((qv[0] + qv[1]) + (qv[2] + qv[3]));
    if (g == 0) *(f32x2*)(ws + OFF_ST1 + ((size_t)(mt * 16 + r) * 16 + cb * 2 + KH) * 8) = (f32x2){s, q};
  }
  if (!FINAL && KH && side) {
    float v = 0.f;
#pragma unroll
    for (int i = 0; i < 20; ++i) v += t[i];
    *(float*)(ws + OFF_CS + blockIdx.x * 256 + lane * 4) = v;
  }
  TDUMP(FINAL ? 6 : 3);
  __builtin_amdgcn_endpgm();
}

template <int NKS, bool FINAL>
__global__ __launch_bounds__(512, 2) OCC2 void k_proj(char* __restrict__ ws, const u16* __restrict__ x, u16* __restrict__ out) {
  extern __shared__ __attribute__((aligned(16))) char smem[];
  const int tid = threadIdx.x, lane = tid & 63, wid = __builtin_amdgcn_readfirstlane(tid >> 6);
  const int xcd = blockIdx.x & 7, idx = blockIdx.x >> 3, panel = xcd * 4 + (idx >> 3), cb = idx & 7;
  const bool side = !FINAL && wid == 7 && blockIdx.x < 64 && (blockIdx.x & 31) < 27;
  float t[20];
  if (side) {
#pragma unroll
    for (int i = 0; i < 20; ++i) t[i] = *(const float*)(ws + OFF_CBP + i * 16384 + blockIdx.x * 256 + lane * 4);
  }
  {
    const char* src = ws + (FINAL ? OFF_WOUT : OFF_WO) + (size_t)cb * 5 * NKS * 1024 + tid * 16;
    const unsigned dst = lds_addr(smem) + wid * 1024;
    constexpr int NF = 5 * NKS;
#pragma unroll
    for (int i = 0; i < (NF + 7) / 8; ++i)
      if (i * 8 + 7 < NF || i * 8 + wid < NF) glds16(src + i * 8192, dst + i * 8192);
  }
  if (wid < 4) proj_body<NKS, FINAL, 0>(ws, x, out, smem, panel, cb, wid, lane, false, t);
  else proj_body<NKS, FINAL, 1>(ws, x, out, smem, panel, cb, wid - 4, lane, side, t);
}

DI float gelu_t(float x) {
  constexpr float C1 = -2.8853900817779268f * 0.7978845608028654f, C2 = C1 * 0.044715f;
  const float e = __builtin_amdgcn_exp2f(x * __builtin_fmaf(x * x, C2, C1));
  return x * __builtin_amdgcn_rcpf(1.f + e);
}
constexpr int K4_LN = 81920, K4_ST2 = K4_LN + 512, K4_FIN = K4_ST2 + 3584, K4_CNT = K4_FIN + 512, K4_TR = K4_CNT + 16, K4_LDS = K4_TR + 7 * 2048;
constexpr int K4_PRE = 12;

__global__ __launch_bounds__(512, 2) OCC2 void k4_win(char* __restrict__ ws) {
  extern __shared__ __attribute__((aligned(16))) char smem[];
  const int tid = threadIdx.x, lane = tid & 63, wid = __builtin_amdgcn_readfirstlane(tid >> 6);
  const int xcd = blockIdx.x & 7, idx = blockIdx.x >> 3, panel = xcd * 4 + (idx >> 3), cb = idx & 7;
  const int nu = cb < 6 ? 7 : 6, u = unit0_of(cb) + wid;
  const int g = lane >> 4, r = lane & 15;
  TDECL; TS(0);
  {
    const char* src = ws + OFF_X1S + (size_t)panel * 81920 + tid * 16;
    const unsigned dst = lds_addr(smem) + wid * 1024;
#pragma unroll
    for (int i = 0; i < 10; ++i) glds16(src + i * 8192, dst + i * 8192);
  }
  float* lnp = (float*)(smem + K4_LN); float* st2 = (float*)(smem + K4_ST2); float* fin = (float*)(smem + K4_FIN);
  int* cnt = (int*)(smem + K4_CNT);
  if (wid < nu) {
    __builtin_amdgcn_s_setprio(1);
    bf16x8 b2[20], b1[20];
    const char* s2 = ws + OFF_WIN + (size_t)(54 + u) * 20480 + lane * 16;
    const char* s1 = ws + OFF_WIN + (size_t)u * 20480 + lane * 16;
#pragma unroll
    for (int k = 0; k < K4_PRE; ++k) b2[k] = *(const bf16x8*)(s2 + k * 1024);
    TS(1);
    waitv<K4_PRE>();
    BAR();
    TS(2);
#pragma unroll
    for (int k = K4_PRE; k < 20; ++k) b2[k] = *(const bf16x8*)(s2 + k * 1024);
    const f32x4 cs2 = *(const f32x4*)(ws + OFF_CS + ((54 + u) * 16 + g * 4) * 4), bi2 = *(const f32x4*)(ws + OFF_BI + ((54 + u) * 16 + g * 4) * 4);
    const f32x4 cs1 = *(const f32x4*)(ws + OFF_CS + (u * 16 + g * 4) * 4), bi1 = *(const f32x4*)(ws + OFF_BI + (u * 16 + g * 4) * 4);
#pragma unroll
    for (int k = 0; k < 8; ++k) b1[k] = *(const bf16x8*)(s1 + k * 1024);
    f32x4 acc[4];
#pragma unroll
    for (int m = 0; m < 4; ++m) acc[m] = (f32x4){};
    SCHED();
    mma16_run<20, 20480, 1024, 2>(smem + lane * 16, b2, acc);
#pragma unroll
    for (int k = 8; k < 20; ++k) b1[k] = *(const bf16x8*)(s1 + k * 1024);
    TS(4);
    float z2[4][4]; f32x2 ln[4];
#pragma unroll
    for (int m = 0; m < 4; ++m) ln[m] = *(const f32x2*)(lnp + (m * 16 + r) * 2);
#pragma unroll
    for (int m = 0; m < 4; ++m) {
      float s = 0.f, q = 0.f;
#pragma unroll
      for (int e = 0; e < 4; ++e) { const float z = gelu_t(__builtin_fmaf(acc[m][e], ln[m][0], __builtin_fmaf(ln[m][1], cs2[e], bi2[e]))); z2[m][e] = z; s += z; q = __builtin_fmaf(z, z, q); }
      s = sum_g(s); q = sum_g(q);
      if (g == 0) *(f32x2*)(st2 + (wid * 64 + m * 16 + r) * 2) = (f32x2){s, q};
    }
    asm volatile("" ::: "memory");
    if (lane == 0) __hip_atomic_fetch_add(cnt, 1, __ATOMIC_RELAXED, __HIP_MEMORY_SCOPE_WORKGROUP);
    asm volatile("" ::: "memory");
    __builtin_amdgcn_s_setprio(0);
#pragma unroll
    for (int m = 0; m < 4; ++m) acc[m] = (f32x4){};
    TS(5);
    mma16_run<20, 20480, 1024, 3>(smem + lane * 16, b1, acc);
    TS(6);
    char* zd = ws + OFF_Z1 + (size_t)(panel * 54 + u) * 2048 + lane * 16;
#pragma unroll
    for (int mm = 0; mm < 2; ++mm) {
      u32x4 o;
#pragma unroll
      for (int i = 0; i < 2; ++i) {
        const int m = 2 * mm + i; float z[4];
#pragma unroll
        for (int e = 0; e < 4; ++e) z[e] = gelu_t(__builtin_fmaf(acc[m][e], ln[m][0], __builtin_fmaf(ln[m][1], cs1[e], bi1[e])));
        o[2 * i] = pack2s(z[0], z[1]); o[2 * i + 1] = pack2s(z[2], z[3]);
      }
      *(u32x4*)(zd + mm * 1024) = o;
    }
    TS(7);
    BAR();
    TS(8);
    char* tr = smem + K4_TR + wid * 2048;
#pragma unroll
    for (int m = 0; m < 4; ++m) {
      const f32x2 f = *(const f32x2*)(fin + (m * 16 + r) * 2);
      *(u32x2*)(tr + (m * 16 + r) * 32 + g * 8) = (u32x2){pack2s(z2[m][0] * f[0] - f[1], z2[m][1] * f[0] - f[1]), pack2s(z2[m][2] * f[0] - f[1], z2[m][3] * f[0] - f[1])};
    }
    asm volatile("s_waitcnt lgkmcnt(0)" ::: "memory");
    char* z2d = ws + OFF_Z2 + ((size_t)panel * 2 * 54 + u) * 1024 + lane * 16;
#pragma unroll
    for (int ks = 0; ks < 2; ++ks) {
      const char* p = tr + (ks * 32 + 8 * g + (r >> 2)) * 32 + (r & 3) * 8;
      const bf16x4 lo = lds_tr(p), hi = lds_tr(p + 128);
      *(bf16x8*)(z2d + ks * 55296) = __builtin_shufflevector(lo, hi, 0, 1, 2, 3, 4, 5, 6, 7);
    }
  } else if (wid == 7) {
    __builtin_amdgcn_s_setprio(2);
    {
      const f32x4* sp = (const f32x4*)(ws + OFF_ST1 + (size_t)(panel * 64 + lane) * 128);
      float s = 0.f, q = 0.f; f32x4 t[8];
#pragma unroll
      for (int i = 0; i < 8; ++i) t[i] = sp[i];
#pragma unroll
      for (int i = 0; i < 8; ++i) { s += t[i][0] + t[i][2]; q += t[i][1] + t[i][3]; }
      const float mean = s * (1.f / 640.f), var = fmaxf(q * (1.f / 640.f) - mean * mean, 0.f);
      const float ri = 1.f / ln_den(var);
      *(f32x2*)(lnp + lane * 2) = (f32x2){ri, -rbf(mean) * ri};
      if (lane == 0) *cnt = 0;
    }
    waitv<0>();
    BAR();
    for (unsigned spin = 0; spin < (1u << 24); ++spin) {
      if (__hip_atomic_load(cnt, __ATOMIC_RELAXED, __HIP_MEMORY_SCOPE_WORKGROUP) >= nu) break;
      __builtin_amdgcn_s_sleep(2);
    }
    asm volatile("" ::: "memory");
    float s = 0.f, q = 0.f;
    for (int i = 0; i < nu; ++i) { const f32x2 t = *(const f32x2*)(st2 + (i * 64 + lane) * 2); s += t[0]; q += t[1]; }
    GAS u64* xch = (GAS u64*)(ws + OFF_XCH) + panel * 512;
    unsigned qb = __float_as_uint(q); if (qb == ~0u) qb = 0x7fc00000u;
    __hip_atomic_store(xch + cb * 64 + lane, ((u64)qb << 32) | __float_as_uint(s), __ATOMIC_RELAXED, __HIP_MEMORY_SCOPE_AGENT);
    u64 v[8];
    for (unsigned spin = 0; spin < (1u << 22); ++spin) {
      bool ok = true;
#pragma unroll
      for (int i = 0; i < 8; ++i) { v[i] = __hip_atomic_load(xch + i * 64 + lane, __ATOMIC_RELAXED, __HIP_MEMORY_SCOPE_AGENT); ok = ok && ((unsigned)(v[i] >> 32) != ~0u); }
      if (__all(ok)) break;
      __builtin_amdgcn_s_sleep(1);
    }
    s = 0.f; q = 0.f;
#pragma unroll
    for (int i = 0; i < 8; ++i) { s += __uint_as_float((unsigned)v[i]); q += __uint_as_float((unsigned)(v[i] >> 32)); }
    const float mean = s * (1.f / 864.f), var = fmaxf(q * (1.f / 864.f) - mean * mean, 0.f);
    const float ri = 1.f / ln_den(var);
    *(f32x2*)(fin + lane * 2) = (f32x2){ri, rbf(mean) * ri};
    TS(7);
    BAR();
  } else {
    waitv<0>();
    BAR();
  }
  TDUMP(4);
}

#define K5L 3
constexpr int K5_L = K5L, K5_R = K5_L + 1, K5_EX = K5_R * 16384, K5_LDS = K5_EX + 8 * 4096;

template <int NU>
DI void k5_body(char* __restrict__ ws, const u16* __restrict__ wsp, const u16* __restrict__ bsp, char* smem, int panel, int u0, int wid, int lane) {
  TDECL; TS(0);
  constexpr int NA = NU ? NU : 1, NLD = 2 + 2 * NU;
  const int kh = wid >> 2, g = lane >> 4, r = lane & 15;
  const char* asrc = (const char*)wsp + (size_t)(panel * 64 + (wid & 3) * 16 + (lane >> 3)) * 4096 + kh * 128 + ((lane & 7) ^ (lane >> 3)) * 16;
  const unsigned adst = lds_addr(smem) + wid * 2048;
  const char* bsrc = ws + OFF_Z2 + (size_t)(kh * 2 * 54 + u0) * 1024 + lane * 16;
  const char* al[2][2];
#pragma unroll
  for (int k = 0; k < 2; ++k)
#pragma unroll
    for (int j = 0; j < 2; ++j) al[k][j] = smem + kh * 8192 + (j ^ kh) * 4096 + r * 128 + ((4 * k + g) ^ (r & 7)) * 16;
  bf16x8 bq[16][2][NA], af[2][2][4]; f32x4 acc[NA][4];
  u32x4 z[NA]; u16 bb[2];
#pragma unroll
  for (int i = 0; i < NU; ++i) z[i] = *(const u32x4*)(ws + OFF_Z1 + (size_t)(panel * 54 + u0 + i) * 2048 + kh * 1024 + lane * 16);
#pragma unroll
  for (int j = 0; j < 2; ++j) bb[j] = bsp[panel * 64 + (2 * kh + j) * 16 + r];
  asm volatile("" ::: "memory");
  auto issue1 = [&](auto S, auto I) __attribute__((always_inline)) {
    constexpr int s = S, i = I;
    if constexpr (s < 16 && i < NLD) {
      if constexpr (i < 2) glds16(asrc + s * 256 + i * 32768, adst + (s % K5_R) * 16384 + i * 1024);
      else { bq[s][(i - 2) / NA][(i - 2) % NA] = *(const bf16x8*)(bsrc + ((i - 2) % NA) * 1024 + (s * 4 + (i - 2) / NA) * 55296); asm volatile("" ::: "memory"); }
    }
  };
  auto rd1 = [&](auto S, int k, int m) __attribute__((always_inline)) {
    constexpr int s = S;
    if constexpr (NU && s < 16) af[s & 1][k][m] = *(const bf16x8*)(al[k][m >> 1] + (s % K5_R) * 16384 + (m & 1) * 2048);
  };
  sfor<K5_L>([&](auto S) __attribute__((always_inline)) { sfor<NLD>([&](auto I) __attribute__((always_inline)) { issue1(S, I); }); });
#pragma unroll
  for (int i = 0; i < NU; ++i) {
    const float b0 = bf2f(bb[0]), b1 = bf2f(bb[1]);
    acc[i][0] = (f32x4){b0, b0, b0, b0}; acc[i][1] = (f32x4){b1, b1, b1, b1}; acc[i][2] = (f32x4){}; acc[i][3] = (f32x4){};
  }
  waitv<(K5_L - 1) * NLD>();
  BAR();
  TS(1);
#pragma unroll
  for (int k = 0; k < 2; ++k)
#pragma unroll
    for (int m = 0; m < 4; ++m) rd1(std::integral_constant<int, 0>{}, k, m);
  sfor<NLD / 2>([&](auto I) __attribute__((always_inline)) { issue1(std::integral_constant<int, K5_L>{}, I); });
  SCHED();
  sfor<16>([&](auto S) __attribute__((always_inline)) {
    constexpr int s = S;
    sfor<4>([&](auto M) __attribute__((always_inline)) {
      constexpr int m = M;
#pragma unroll
      for (int i = 0; i < NU; ++i) acc[i][m] = MFMA16(bq[s][0][i], af[s & 1][0][m], acc[i][m]);
      if constexpr (m < NLD / 2) issue1(std::integral_constant<int, s + K5_L>{}, std::integral_constant<int, NLD / 2 + m>{});
      SCHED();
    });
    if constexpr (s < 15) {
      constexpr int last = s + K5_L > 15 ? 15 : s + K5_L;
      waitv<(last - (s + 1)) * NLD>();
      BAR();
    }
    sfor<4>([&](auto M) __attribute__((always_inline)) {
      constexpr int m = M;
      rd1(std::integral_constant<int, s + 1>{}, 0, m); rd1(std::integral_constant<int, s + 1>{}, 1, m);
#pragma unroll
      for (int i = 0; i < NU; ++i) acc[i][m] = MFMA16(bq[s][1][i], af[s & 1][1][m], acc[i][m]);
      if constexpr (m < NLD / 2) issue1(std::integral_constant<int, s + 1 + K5_L>{}, M);
      SCHED();
    });
  });
  TS(6);
  if (NU == 0) { BAR(); __builtin_amdgcn_endpgm(); }
  f32x4* exw = (f32x4*)(smem + K5_EX + wid * 4096) + lane;
  const f32x4* exr = (const f32x4*)(smem + K5_EX + (wid ^ 4) * 4096) + lane;
#pragma unroll
  for (int i = 0; i < NU; ++i)
#pragma unroll
    for (int j = 0; j < 2; ++j) exw[(i * 2 + j) * 64] = acc[i][2 + j];
  f32x4 zf[NA][2]; unsigned fo[NA];
#pragma unroll
  for (int i = 0; i < NU; ++i) {
    const int n = (u0 + i) * 16 + g * 4;
    fo[i] = (n >> 5) * 1024 + (((n & 31) >> 3) * 16 + r) * 16 + (n & 4) * 2; PIN(fo[i]);
#pragma unroll
    for (int j = 0; j < 2; ++j) {
      zf[i][j] = (f32x4){bflo(z[i][2 * j]), bfhi(z[i][2 * j]), bflo(z[i][2 * j + 1]), bfhi(z[i][2 * j + 1])};
      acc[i][j] *= zf[i][j]; PIN(zf[i][j]); PIN(acc[i][j]);
    }
  }
  char* ub = ws + OFF_U + (size_t)(panel * 4 + 2 * kh) * 27648;
  BAR();
  f32x4 oth[NA][2];
#pragma unroll
  for (int i = 0; i < NU; ++i)
#pragma unroll
    for (int j = 0; j < 2; ++j) oth[i][j] = exr[(i * 2 + j) * 64];
#pragma unroll
  for (int i = 0; i < NU; ++i)
#pragma unroll
    for (int j = 0; j < 2; ++j) {
      const f32x4 v = acc[i][j] + zf[i][j] * oth[i][j];
      *(u32x2*)(ub + j * 27648 + fo[i]) = (u32x2){pack2(v[0], v[1]), pack2(v[2], v[3])};
    }
  TDUMP(5);
  __builtin_amdgcn_endpgm();
}

__global__ __launch_bounds__(512, 2) OCC2 void k5_sp(char* __restrict__ ws, const u16* __restrict__ wsp, const u16* __restrict__ bsp) {
  extern __shared__ __attribute__((aligned(16))) char smem[];
  const int tid = threadIdx.x, lane = tid & 63, wid = __builtin_amdgcn_readfirstlane(tid >> 6);
#define K5MAP 0
  const int xcd = blockIdx.x & 7, idx = blockIdx.x >> 3;
  const int panel = (xcd >> K5MAP) * (4 << K5MAP) + (idx >> (3 - K5MAP)), cb = (xcd & ((1 << K5MAP) - 1)) * (8 >> K5MAP) + (idx & ((8 >> K5MAP) - 1));
  const int up = wid & 3, u0 = unit0_of(cb) + 2 * up;
  if (up < 3) k5_body<2>(ws, wsp, bsp, smem, panel, u0, wid, lane);
  else if (cb < 6) k5_body<1>(ws, wsp, bsp, smem, panel, u0, wid, lane);
  else k5_body<0>(ws, wsp, bsp, smem, panel, u0, wid, lane);
}

extern "C" void kernel_launch(void* const* d_in, const int* in_sizes, int n_in, void* d_out, int out_size, void* d_ws, size_t ws_size,
                              hipStream_t stream) {
  const u16* x = (const u16*)d_in[0];
  const u16* wqkv = (const u16*)d_in[1];
  const u16* wo = (const u16*)d_in[2];
  const u16* win = (const u16*)d_in[4];
  const u16* wsp = (const u16*)d_in[5];
  const u16* bsp = (const u16*)d_in[6];
  const u16* wout = (const u16*)d_in[7];
  const u16* cvec = (const u16*)d_in[8];
  const u16* wmod = (const u16*)d_in[9];
  char* ws = (char*)d_ws;
  u16* out = (u16*)d_out;
  constexpr int L3 = 100 * 1024 + 20480, L6 = 135 * 1024 + 20480;
  (void)hipFuncSetAttribute((const void*)k1_qkv, hipFuncAttributeMaxDynamicSharedMemorySize, 98304);
  (void)hipFuncSetAttribute((const void*)k2_attn, hipFuncAttributeMaxDynamicSharedMemorySize, 163840);
  (void)hipFuncSetAttribute((const void*)k_proj<20, false>, hipFuncAttributeMaxDynamicSharedMemorySize, L3);
  (void)hipFuncSetAttribute((const void*)k_proj<27, true>, hipFuncAttributeMaxDynamicSharedMemorySize, L6);
  (void)hipFuncSetAttribute((const void*)k4_win, hipFuncAttributeMaxDynamicSharedMemorySize, K4_LDS);
  (void)hipFuncSetAttribute((const void*)k5_sp, hipFuncAttributeMaxDynamicSharedMemorySize, K5_LDS);
  k0_prep<<<K0_GRID, 256, 0, stream>>>(x, wqkv, cvec, wmod, ws);
  k1_qkv<<<256, 512, 98304, stream>>>(ws, cvec, wmod);
  k2_attn<<<256, 512, 163840, stream>>>(ws, (const char*)wsp, wo, win, wout);
  k_proj<20, false><<<256, 512, L3, stream>>>(ws, x, out);
  k4_win<<<256, 512, K4_LDS, stream>>>(ws);
  k5_sp<<<256, 512, K5_LDS, stream>>>(ws, wsp, bsp);
  k_proj<27, true><<<256, 512, L6, stream>>>(ws, x, out);
}
```

```cpp
#include <hip/hip_runtime.h>
#include <hip/hip_bf16.h>
#include <stdint.h>
#include <utility>

typedef __attribute__((ext_vector_type(8))) short bf16x8;
typedef __attribute__((ext_vector_type(4))) short bf16x4;
typedef __attribute__((ext_vector_type(2))) float f32x2;
typedef __attribute__((ext_vector_type(4))) float f32x4;
typedef __attribute__((ext_vector_type(16))) float f32x16;
typedef __attribute__((ext_vector_type(2))) unsigned u32x2;
typedef __attribute__((ext_vector_type(4))) unsigned u32x4;
typedef __attribute__((ext_vector_type(2))) __bf16 bf2_t;
typedef __attribute__((ext_vector_type(2))) _Float16 h2_t;
typedef unsigned short u16;
typedef unsigned long long u64;
#define LAS __attribute__((address_space(3)))
#define GAS __attribute__((address_space(1)))
#define DI __device__ __forceinline__

constexpr int D_ = 640, NQKV = 1920, F_ = 1728;

constexpr unsigned OFF_CNT = 0;
constexpr unsigned OFF_MOD = 32768;
constexpr unsigned OFF_MODP = OFF_MOD + 16384;
constexpr unsigned OFF_CBP = OFF_MODP + 40960;
constexpr unsigned OFF_XCH = OFF_CBP + 5 * 16384;
constexpr unsigned OFF_ST1 = OFF_XCH + 131072;
constexpr unsigned OFF_XHAT = OFF_ST1 + 262144;
constexpr unsigned OFF_WQKV = OFF_XHAT + 2621440;
constexpr unsigned OFF_WO = OFF_WQKV + 2457600;
constexpr unsigned OFF_WIN = OFF_WO + 819200;
constexpr unsigned OFF_WOUT = OFF_WIN + 2211840;
constexpr unsigned OFF_Q = OFF_WOUT + 1105920;
constexpr unsigned OFF_KV = OFF_Q + 32 * 81920;
constexpr unsigned OFF_O = OFF_KV + 32 * 163840;
constexpr unsigned OFF_X1S = OFF_O + 2621440;
constexpr unsigned OFF_Z2 = OFF_X1S + 2621440;
constexpr unsigned OFF_U = OFF_Z2 + 3538944;
constexpr unsigned WS_USED = OFF_U + 3538944;
constexpr int C_QKV = 0, C_MOD = 15, C_ATT = 16, C_REL = 24, C_X1 = 32, C_Z2 = 64, C_U = 72;

#define STAMP(kid, k)
#define TDECL
#define TS(k)
#define TDUMP()
DI unsigned pack2(float lo, float hi) { return __builtin_bit_cast(unsigned, __builtin_convertvector((f32x2){lo, hi}, bf2_t)); }
DI unsigned pack2s(float lo, float hi) { unsigned r; asm("v_cvt_pk_bf16_f32 %0, %1, %2" : "=v"(r) : "v"(lo), "v"(hi)); return r; }
DI unsigned pack2h(float lo, float hi) { return __builtin_bit_cast(unsigned, __builtin_convertvector((f32x2){lo, hi}, h2_t)); }
DI float bflo(unsigned u) { return __uint_as_float(u << 16); }
DI float bfhi(unsigned u) { return __uint_as_float(u & 0xffff0000u); }
DI float rbf(float x) { return bflo(pack2(x, x)); }
DI float bf2f(u16 b) { return __uint_as_float((unsigned)b << 16); }
#define MFMA32(a, b, c) __builtin_amdgcn_mfma_f32_32x32x16_bf16((a), (b), (c), 0, 0, 0)
#define MFMA32K8(a, b, c) __builtin_amdgcn_mfma_f32_32x32x8bf16_1k((a), (b), (c), 0, 0, 0)
#define MFMA16(a, b, c) __builtin_amdgcn_mfma_f32_16x16x32_bf16((a), (b), (c), 0, 0, 0)
#define BAR() asm volatile("s_waitcnt lgkmcnt(0)\n\ts_barrier" ::: "memory")
#define SCHED() __builtin_amdgcn_sched_barrier(0)
#define PIN(x) asm volatile("" : "+v"(x))
#define OCC2 __attribute__((amdgpu_waves_per_eu(2, 2)))
#define BF8(x) __builtin_bit_cast(bf16x8, x)
#define BF4(x) __builtin_bit_cast(bf16x4, x)
template <int N> DI void waitv() { asm volatile("s_waitcnt vmcnt(%0)" ::"i"(N) : "memory"); }
template <class F, int... I> DI void sfor_impl(F&& f, std::integer_sequence<int, I...>) { (f(std::integral_constant<int, I>{}), ...); }
template <int N, class F> DI void sfor(F&& f) { sfor_impl(f, std::make_integer_sequence<int, N>{}); }
typedef short v4i16_t __attribute__((ext_vector_type(4)));
DI bf16x4 lds_tr(const char* p) { return __builtin_bit_cast(bf16x4, __builtin_amdgcn_ds_read_tr16_b64_v4i16((LAS v4i16_t*)p)); }
DI void glds16(const void* g, unsigned lds_base) {
  unsigned sv;
  asm volatile("s_mov_b32 %0, m0\n\ts_mov_b32 m0, %2\n\ts_nop 0\n\tglobal_load_lds_dwordx4 %1, off\n\ts_mov_b32 m0, %0" : "=&s"(sv) : "v"(g), "s"(lds_base) : "memory");
}
DI void glds16c(const void* g, unsigned lds_base) {
  unsigned sv;
  asm volatile("s_mov_b32 %0, m0\n\ts_mov_b32 m0, %2\n\ts_nop 0\n\tglobal_load_lds_dwordx4 %1, off sc1\n\ts_mov_b32 m0, %0" : "=&s"(sv) : "v"(g), "s"(lds_base) : "memory");
}
DI void glds4c(const void* g, unsigned lds_base) {
  unsigned sv;
  asm volatile("s_mov_b32 %0, m0\n\ts_mov_b32 m0, %2\n\ts_nop 0\n\tglobal_load_lds_dword %1, off sc1\n\ts_mov_b32 m0, %0" : "=&s"(sv) : "v"(g), "s"(lds_base) : "memory");
}
typedef __amdgpu_buffer_rsrc_t rsrc_t;
DI u32x4 ldc16(rsrc_t rs, unsigned voff, unsigned soff) { return __builtin_amdgcn_raw_buffer_load_b128(rs, voff, soff, 16); }
DI unsigned ldc4(rsrc_t rs, unsigned voff, unsigned soff) { return __builtin_amdgcn_raw_buffer_load_b32(rs, voff, soff, 16); }
DI void stc16(rsrc_t rs, unsigned voff, unsigned soff, u32x4 v) { __builtin_amdgcn_raw_buffer_store_b128(v, rs, voff, soff, 16); asm volatile("s_nop 1" ::"v"(v) : "memory"); }
DI void stc8(rsrc_t rs, unsigned voff, unsigned soff, u32x2 v) { __builtin_amdgcn_raw_buffer_store_b64(v, rs, voff, soff, 16); }
DI void stc4(rsrc_t rs, unsigned voff, unsigned soff, unsigned v) { __builtin_amdgcn_raw_buffer_store_b32(v, rs, voff, soff, 16); }
DI f32x4 ldc16f(rsrc_t rs, unsigned voff, unsigned soff) { return __builtin_bit_cast(f32x4, ldc16(rs, voff, soff)); }
DI void arrive(char* ws, int c, int lane) { if (lane == 0) __hip_atomic_fetch_add((GAS unsigned*)(ws + OFF_CNT + c * 256), 1u, __ATOMIC_RELAXED, __HIP_MEMORY_SCOPE_AGENT); }
DI void poll_ge(char* ws, int c, unsigned thr) {
  const GAS unsigned* p = (const GAS unsigned*)(ws + OFF_CNT + c * 256);
#pragma nounroll
  for (unsigned spin = 0; spin < (1u << 21); ++spin) {
    if (__all(__hip_atomic_load(p, __ATOMIC_RELAXED, __HIP_MEMORY_SCOPE_AGENT) >= thr)) break;
    __builtin_amdgcn_s_sleep(1);
  }
  asm volatile("" ::: "memory");
}
DI int fresh_tid() { int t = threadIdx.x; asm volatile("" : "+v"(t)); return t; }
DI void l2touch(const void* g, unsigned lds_scratch) {
  unsigned sv;
  asm volatile("s_mov_b32 %0, m0\n\ts_mov_b32 m0, %2\n\ts_nop 0\n\tglobal_load_lds_dword %1, off\n\ts_mov_b32 m0, %0" : "=&s"(sv) : "v"(g), "s"(lds_scratch) : "memory");
}
#define LDNT(p) __builtin_nontemporal_load(p)
DI float silu_bf(u16 c) { const float f = __uint_as_float((unsigned)c << 16); const float v = f / (1.f + __expf(-f)); return __uint_as_float(__builtin_bit_cast(unsigned, __builtin_convertvector((f32x2){v, v}, bf2_t)) << 16); }
DI unsigned lds_addr(const void* p) { return (unsigned)(uintptr_t)(const LAS void*)p; }
DI float xhalf(float v) {
  auto rr = __builtin_amdgcn_permlane32_swap(__float_as_uint(v), __float_as_uint(v), false, false);
  return __uint_as_float((threadIdx.x & 32) ? rr[0] : rr[1]);
}
DI float sum_g(float v) {
  auto a = __builtin_amdgcn_permlane16_swap(__float_as_uint(v), __float_as_uint(v), false, false);
  v = __uint_as_float(a[0]) + __uint_as_float(a[1]);
  auto b = __builtin_amdgcn_permlane32_swap(__float_as_uint(v), __float_as_uint(v), false, false);
  return __uint_as_float(b[0]) + __uint_as_float(b[1]);
}
template <int CTRL> DI float dpp_add(float v) { return v + __uint_as_float(__builtin_amdgcn_update_dpp(0u, __float_as_uint(v), CTRL, 0xf, 0xf, true)); }
DI float sum16(float v) {
  v = dpp_add<0x128>(v); v = dpp_add<0x124>(v); v = dpp_add<0x122>(v); return dpp_add<0x121>(v);
}
DI float mix_lo(unsigned h, float s, float b) { float r; asm("v_fma_mix_f32 %0, %1, %2, %3 op_sel_hi:[1,0,0]" : "=v"(r) : "v"(h), "v"(s), "v"(b)); return r; }
DI float mix_hi(unsigned h, float s, float b) { float r; asm("v_fma_mix_f32 %0, %1, %2, %3 op_sel:[1,0,0] op_sel_hi:[1,0,0]" : "=v"(r) : "v"(h), "v"(s), "v"(b)); return r; }
DI float ln_den(float var) { return rbf(__builtin_sqrtf(rbf(rbf(var) + 1e-5f))); }
DI unsigned a16_off(int n, int r) { return (n >> 5) * 1024 + (((n & 31) >> 3) * 16 + r) * 16 + (n & 4) * 2; }
DI int unit0_of(int j) { return j < 6 ? 7 * j : 42 + 6 * (j - 6); }

template <int NK, int MS, int KS, int PD>
DI void mma16_run(const char* al, const bf16x8 (&b)[NK], f32x4 (&acc)[4]) {
  bf16x8 af[PD + 1][4];
#pragma unroll
  for (int k = 0; k < PD; ++k)
#pragma unroll
    for (int m = 0; m < 4; ++m) af[k][m] = *(const bf16x8*)(al + m * MS + k * KS);
  SCHED();
#pragma unroll
  for (int k = 0; k < NK; ++k) {
#pragma unroll
    for (int m = 0; m < 4; ++m) {
      if (k + PD < NK) af[(k + PD) % (PD + 1)][m] = *(const bf16x8*)(al + m * MS + (k + PD) * KS);
      acc[m] = MFMA16(b[k], af[k % (PD + 1)][m], acc[m]);
      SCHED();
    }
  }
}

constexpr int K0_MOD = 160, K0_LN = 128, K0_RL = 240, K0_INIT = 10;
constexpr int K0_GRID = K0_MOD + K0_LN + K0_RL + K0_INIT;

__global__ __launch_bounds__(256) void k0_prep(const u16* __restrict__ x, const u16* __restrict__ wqkv, const u16* __restrict__ cvec,
                                               const u16* __restrict__ wmod, char* __restrict__ ws) {
  __shared__ __attribute__((aligned(16))) char smem[32 * 336];
  const int tid = threadIdx.x, lane = tid & 63, wid = __builtin_amdgcn_readfirstlane(tid >> 6);
  int b = blockIdx.x;
  STAMP(0, 0);
  if (b < K0_MOD) {
    const int cg = b >> 2, ks = b & 3, c = tid & 7, row0 = ks * 160 + (tid >> 3), col0 = cg * 64 + (cg >= 20 ? 640 : 0);
    u32x4 w[5]; u16 cv[5];
#pragma unroll
    for (int i = 0; i < 5; ++i) { w[i] = LDNT((const u32x4*)(wmod + (size_t)(row0 + 32 * i) * 3840 + col0 + c * 8)); cv[i] = cvec[row0 + 32 * i]; }
    float acc[8] = {0, 0, 0, 0, 0, 0, 0, 0};
#pragma unroll
    for (int i = 0; i < 5; ++i) {
      const float s = silu_bf(cv[i]);
#pragma unroll
      for (int e = 0; e < 4; ++e) { acc[2 * e] += s * bflo(w[i][e]); acc[2 * e + 1] += s * bfhi(w[i][e]); }
    }
#pragma unroll
    for (int e = 0; e < 8; ++e) acc[e] = sum_g(dpp_add<0x128>(acc[e]));
    float* red = (float*)smem;
    if (lane < 8) { *(f32x4*)(red + wid * 64 + lane * 8) = (f32x4){acc[0], acc[1], acc[2], acc[3]}; *(f32x4*)(red + wid * 64 + lane * 8 + 4) = (f32x4){acc[4], acc[5], acc[6], acc[7]}; }
    __syncthreads();
    if (tid < 64) ((float*)(ws + OFF_MODP))[ks * 2560 + cg * 64 + tid] = (red[tid] + red[64 + tid]) + (red[128 + tid] + red[192 + tid]);
    STAMP(0, 1); STAMP(0, 13);
    return;
  }
  b -= K0_MOD;
  if (b < K0_LN) {
    b = (b & 7) * 16 + (b >> 3);
    const int row = b * 16 + wid * 4 + (lane >> 4), c = lane & 15;
    const u16* xr = x + (size_t)row * D_ + c * 8;
    u32x4 v[5];
#pragma unroll
    for (int i = 0; i < 5; ++i) v[i] = *(const u32x4*)(xr + i * 128);
    float s = 0.f, q = 0.f;
#pragma unroll
    for (int i = 0; i < 5; ++i)
#pragma unroll
      for (int e = 0; e < 4; ++e) { const float a = bflo(v[i][e]), d = bfhi(v[i][e]); s += a + d; q += a * a + d * d; }
    s = sum16(s); q = sum16(q);
    const float mean = s * (1.f / 640.f); const float var = fmaxf(q * (1.f / 640.f) - mean * mean, 0.f);
    const float rinv = 1.f / ln_den(var), mb = rbf(mean) * rinv;
    char* dst = ws + OFF_XHAT + (size_t)row * 1280 + c * 16;
#pragma unroll
    for (int i = 0; i < 5; ++i) {
      u32x4 o;
#pragma unroll
      for (int e = 0; e < 4; ++e) o[e] = pack2h(bflo(v[i][e]) * rinv - mb, bfhi(v[i][e]) * rinv - mb);
      *(u32x4*)(dst + i * 256) = o;
    }
    STAMP(0, 2); STAMP(0, 13);
    return;
  }
  b -= K0_LN;
  if (b < K0_RL) {
    const int kt = b / 12, G = b % 12, sec = G >> 2, gq = G & 3;
    u32x4 v[3];
#pragma unroll
    for (int i = 0; i < 3; ++i) { const int ci = min(i * 256 + tid, 639); v[i] = LDNT((const u32x4*)(wqkv + (size_t)(kt * 32 + ci / 20) * NQKV + G * 160 + (ci % 20) * 8)); }
#pragma unroll
    for (int i = 0; i < 3; ++i) { const int ci = min(i * 256 + tid, 639); *(u32x4*)(smem + (ci / 20) * 336 + (ci % 20) * 16) = v[i]; }
    __syncthreads();
    const int g = lane >> 4, q = (lane & 15) >> 2, p = lane & 3, rho = 16 * (g & 1) + 4 * p, qd = rho >> 3, h = (rho >> 2) & 1;
    for (int f = wid; f < 10; f += 4) {
      const int t = f >> 1, ks = f & 1;
      const int col = sec == 2 ? 32 * t + rho : t < 4 ? 20 * (2 * t + h) + 4 * qd : 20 * (4 * h + qd) + 16;
      const int T = sec == 2 ? 40 + gq * 5 + t : sec * 20 + (t < 4 ? 4 * gq + t : 16 + gq);
      const char* pp = smem + (16 * ks + 8 * (g >> 1) + q) * 336 + col * 2;
      const bf16x4 lo = lds_tr(pp), hi = lds_tr(pp + 4 * 336);
      *(bf16x8*)(ws + OFF_WQKV + ((size_t)T * 40 + kt * 2 + ks) * 1024 + lane * 16) = __builtin_shufflevector(lo, hi, 0, 1, 2, 3, 4, 5, 6, 7);
    }
    STAMP(0, 3); STAMP(0, 13);
    return;
  }
  b -= K0_RL;
  {
    u32x4* p = (u32x4*)(ws + (b < 8 ? OFF_XCH + b * 16384 : OFF_CNT + (b - 8) * 16384)) + tid;
    const unsigned f = b < 8 ? ~0u : 0u;
#pragma unroll
    for (int i = 0; i < 4; ++i) p[i * 256] = (u32x4){f, f, f, f};
  }
}

constexpr float QSCALE = 0.22360679774997896f * 1.4426950408889634f;
constexpr int K1_W = 3;

template <bool ISV>
DI void k1_mma(const char* __restrict__ bsrc, const char* smem, int lane, f32x16 (&acc)[4]) {
  bf16x8 bfr[40], af[4][4];
  const char* lds[4];
#pragma unroll
  for (int k = 0; k < 4; ++k) lds[k] = smem + (lane ^ (2 * k + (lane >> 5))) * 16;
#pragma unroll
  for (int k = 0; k < 4 * K1_W; ++k) bfr[k] = *(const bf16x8*)(bsrc + k * 1024);
#pragma unroll
  for (int m = 0; m < 4; ++m) acc[m] = (f32x16){};
  BAR();
  BAR();
#define K1_RD1(s, k, m) (af[k][m] = *(const bf16x8*)(lds[k] + ((s) & 1) * 16384 + ((k) * 4 + (m)) * 1024))
#define K1_STEP(s, k, RD, rs, rk, LD) do { _Pragma("unroll") for (int m = 0; m < 4; ++m) { if (RD) K1_RD1(rs, rk, m); \
      if (LD) bfr[4 * ((s) + K1_W) + m] = *(const bf16x8*)(bsrc + (4 * ((s) + K1_W) + m) * 1024); \
      acc[m] = ISV ? MFMA32(af[k][m], bfr[(s) * 4 + (k)], acc[m]) : MFMA32(bfr[(s) * 4 + (k)], af[k][m], acc[m]); SCHED(); } } while (0)
#pragma unroll
  for (int m = 0; m < 4; ++m) K1_RD1(0, 0, m);
#pragma unroll
  for (int m = 0; m < 4; ++m) K1_RD1(0, 1, m);
  SCHED();
#pragma unroll
  for (int s = 0; s < 10; ++s) {
    K1_STEP(s, 0, true, s, 2, false);
    K1_STEP(s, 1, true, s, 3, false);
    K1_STEP(s, 2, false, 0, 0, s + K1_W < 10);
    if (s < 9) {
      BAR();
#pragma unroll
      for (int m = 0; m < 4; ++m) K1_RD1(s + 1, 0, m);
      SCHED();
    }
    K1_STEP(s, 3, s < 9, s + 1, 1, false);
  }
}

#define TARG
#define TPASS

DI void phase_qkv(char* __restrict__ ws, rsrc_t rs, const u16* __restrict__ cvec, const u16* __restrict__ wmod, char* smem, int wid TARG) {
  const int tid = fresh_tid(), lane = tid & 63;
  const int xcd = blockIdx.x & 7, idx = blockIdx.x >> 3;
  int ctr;
  if (idx >= 30) {
    ctr = C_MOD;
    const int sb = xcd * 2 + idx - 30, col0 = (sb < 8 ? 1280 : 3200 - 640) + sb * 80, c = tid % 10, rg = tid / 10;
    float* sil = (float*)smem; float* red = (float*)(smem + 4096);
    u32x4 w[13];
#pragma unroll
    for (int i = 0; i < 13; ++i) { const int row = min(rg + 50 * i, 639); w[i] = LDNT((const u32x4*)(wmod + (size_t)row * 3840 + col0 + c * 8)); }
    for (int i = tid; i < 660; i += 512) sil[i] = i < 640 ? silu_bf(cvec[i]) : 0.f;
    if (tid < 80) {
      const float* pp = (const float*)(ws + OFF_MODP) + 1280 + sb * 80 + tid;
      float v = rbf((pp[0] + pp[2560]) + (pp[5120] + pp[7680]));
      if (sb >= 8) v = rbf(1.f + v);
      stc4(rs, (1920 + sb * 80 + tid) * 4, OFF_MOD, __float_as_uint(v));
    }
    BAR();
    float acc[8] = {0, 0, 0, 0, 0, 0, 0, 0};
#pragma unroll
    for (int i = 0; i < 13; ++i) {
      const float s = sil[rg + 50 * i];
#pragma unroll
      for (int e = 0; e < 4; ++e) { acc[2 * e] += s * bflo(w[i][e]); acc[2 * e + 1] += s * bfhi(w[i][e]); }
    }
    if (rg < 50) { *(f32x4*)(red + rg * 80 + c * 8) = (f32x4){acc[0], acc[1], acc[2], acc[3]}; *(f32x4*)(red + rg * 80 + c * 8 + 4) = (f32x4){acc[4], acc[5], acc[6], acc[7]}; }
    BAR();
    if (tid < 80) {
      float v = 0.f;
#pragma unroll
      for (int i = 0; i < 50; ++i) v += red[i * 80 + tid];
      stc4(rs, (col0 + tid) * 4, OFF_MOD, __float_as_uint(rbf(v)));
    }
  } else {
    const int mt = xcd * 2 + (idx / 15), nt = idx % 15;
    ctr = C_QKV + nt;
    if (wid < 4) {
      float* cst = (float*)(smem + 32768);
      const int c = lane & 7, rr = lane >> 3;
      const char* src = ws + OFF_XHAT + (size_t)(mt * 128 + wid * 32 + rr) * 1280 + c * 16;
      char* wdst = smem + ((c >> 1) * 4 + wid) * 1024 + (c & 1) * 512 + (rr ^ c) * 16;
      u32x4 a[10][4];
      f32x4 c0[4], c1[4];
#pragma unroll
      for (int i = 0; i < 4; ++i) { c0[i] = *(const f32x4*)(ws + OFF_MODP + i * 10240 + tid * 16); if (wid == 0) c1[i] = *(const f32x4*)(ws + OFF_MODP + i * 10240 + 4096 + tid * 16); }
#pragma unroll
      for (int s = 0; s < K1_W; ++s)
#pragma unroll
        for (int m = 0; m < 4; ++m) a[s][m] = *(const u32x4*)(src + m * 10240 + s * 128);
      {
        f32x4 v = (c0[0] + c0[1]) + (c0[2] + c0[3]); const float one = tid >= 160 ? 1.f : 0.f;
        *(f32x4*)(cst + tid * 4) = (f32x4){rbf(one + rbf(v[0])), rbf(one + rbf(v[1])), rbf(one + rbf(v[2])), rbf(one + rbf(v[3]))};
        if (wid == 0) { v = (c1[0] + c1[1]) + (c1[2] + c1[3]); *(f32x4*)(cst + 1024 + tid * 4) = (f32x4){rbf(1.f + rbf(v[0])), rbf(1.f + rbf(v[1])), rbf(1.f + rbf(v[2])), rbf(1.f + rbf(v[3]))}; }
      }
      BAR();
      const float* cp = cst + c * 8;
      f32x4 kc[10][4];
#define K1_KC(s) do { kc[s][0] = *(const f32x4*)(cp + (s) * 64); kc[s][1] = *(const f32x4*)(cp + (s) * 64 + 4); kc[s][2] = *(const f32x4*)(cp + (s) * 64 + 640); kc[s][3] = *(const f32x4*)(cp + (s) * 64 + 644); } while (0)
#pragma unroll
      for (int s = 0; s < 7; ++s) K1_KC(s);
      SCHED();
#pragma unroll
      for (int s = 0; s < 10; ++s) {
        if (s + K1_W < 10) {
#pragma unroll
          for (int m = 0; m < 4; ++m) a[s + K1_W][m] = *(const u32x4*)(src + m * 10240 + (s + K1_W) * 128);
        }
#pragma unroll
        for (int m = 0; m < 4; ++m) {
          u32x4 o; const u32x4 v = a[s][m]; const f32x4 sh0 = kc[s][0], sh1 = kc[s][1], sc0 = kc[s][2], sc1 = kc[s][3];
          o[0] = pack2(mix_lo(v[0], sc0[0], sh0[0]), mix_hi(v[0], sc0[1], sh0[1]));
          o[1] = pack2(mix_lo(v[1], sc0[2], sh0[2]), mix_hi(v[1], sc0[3], sh0[3]));
          o[2] = pack2(mix_lo(v[2], sc1[0], sh1[0]), mix_hi(v[2], sc1[1], sh1[1]));
          o[3] = pack2(mix_lo(v[3], sc1[2], sh1[2]), mix_hi(v[3], sc1[3], sh1[3]));
          *(u32x4*)(wdst + (s & 1) * 16384 + m * 128) = o;
        }
        if (s < 3) K1_KC(s + 7);
        BAR();
        if (s == 0) TS(1);
      }
    } else {
      const int cw = wid - 4;
      const char* bsrc = ws + OFF_WQKV + (size_t)(nt * 4 + cw) * 40 * 1024 + lane * 16;
      f32x16 acc[4];
      const int r = lane & 31, h = lane >> 5;
      if (nt >= 10) {
        k1_mma<true>(bsrc, smem, lane, acc);
        TS(2);
        const unsigned c = (nt - 10) * 128 + cw * 32 + r, head = c / 20, d = c % 20;
        const unsigned so = OFF_KV + (mt >> 1) * 20480 + 10240 + (mt & 1) * 5120, vo = head * 163840u + (h * 20 + d) * 16;
#pragma unroll
        for (int m = 0; m < 4; ++m)
#pragma unroll
          for (int s = 0; s < 2; ++s)
            stc16(rs, vo + s * 640, so + m * 1280, (u32x4){pack2(acc[m][8 * s], acc[m][8 * s + 1]), pack2(acc[m][8 * s + 2], acc[m][8 * s + 3]), pack2(acc[m][8 * s + 4], acc[m][8 * s + 5]), pack2(acc[m][8 * s + 6], acc[m][8 * s + 7])});
      } else {
        k1_mma<false>(bsrc, smem, lane, acc);
        TS(2);
        const bool isQ = nt < 5; const float sc = isQ ? QSCALE : 1.f;
        const unsigned so = isQ ? OFF_Q + mt * 5120 : OFF_KV + (mt >> 1) * 20480 + (mt & 1) * 5120, hs = isQ ? 81920u : 163840u;
        const int n5 = isQ ? nt : nt - 5;
        if (n5 < 4) {
          const unsigned vo = (2 * (n5 * 4 + cw) + h) * hs + r * 16;
#pragma unroll
          for (int m = 0; m < 4; ++m)
#pragma unroll
            for (int s = 0; s < 2; ++s)
              stc16(rs, vo + s * 512, so + m * 1280, (u32x4){pack2(acc[m][8 * s] * sc, acc[m][8 * s + 1] * sc), pack2(acc[m][8 * s + 2] * sc, acc[m][8 * s + 3] * sc),
                                                           pack2(acc[m][8 * s + 4] * sc, acc[m][8 * s + 5] * sc), pack2(acc[m][8 * s + 6] * sc, acc[m][8 * s + 7] * sc)});
        } else {
          const unsigned vo = (8 * cw + 4 * h) * hs + 1024 + r * 8;
#pragma unroll
          for (int m = 0; m < 4; ++m)
#pragma unroll
            for (int qd = 0; qd < 4; ++qd)
              stc8(rs, vo, so + qd * hs + m * 1280, (u32x2){pack2(acc[m][4 * qd] * sc, acc[m][4 * qd + 1] * sc), pack2(acc[m][4 * qd + 2] * sc, acc[m][4 * qd + 3] * sc)});
        }
      }
    }
  }
  TS(3);
  waitv<0>();
  BAR();
  TS(4);
  if (wid == 7) arrive(ws, ctr, lane);
}

DI void split3(float m, unsigned& w_h0, unsigned& w_h1) {
  const float n = -m; const float a = rbf(n), b = rbf(n - a), c = rbf(n - a - b);
  w_h0 = pack2(a, b); w_h1 = pack2(c, 0.f);
}

DI void phase_attn(char* __restrict__ ws, rsrc_t rs, const char* __restrict__ wsp, const u16* __restrict__ wo, const u16* __restrict__ win,
                   const u16* __restrict__ wout, char* smem, int wid TARG) {
  const int tid = fresh_tid(), lane = tid & 63;
  const int xcd = blockIdx.x & 7, idx = blockIdx.x >> 3, head = xcd * 4 + (idx >> 3), qb = idx & 7;
  const int r = lane & 31, h = lane >> 5, qg = wid & 3, kh = wid >> 2;
  if (wid == 7) {
    const int l7 = lane & 7;
    const int c = l7 == 0 ? head >> 3 : l7 == 1 ? 4 : l7 == 2 ? 5 + (head >> 3) : l7 == 3 ? 9 : l7 == 4 ? 10 + (20 * head) / 128 : l7 == 5 ? 10 + (20 * head + 19) / 128 : C_MOD;
    poll_ge(ws, C_QKV + c, 16);
  }
  BAR();
  TS(5);
  unsigned kvo = OFF_KV + head * 163840, qo = OFF_Q + head * 81920 + (qb * 8 + qg * 2) * 1280;
  const unsigned kconst = h == 0 ? 0x3f803f80u : 0x00003f80u;
  const unsigned so16 = (tid & 255) * 16, l16 = lane * 16, l2 = 1024 + r * 8 + h * 4;
  const int blk = blockIdx.x;
  for (int attempt = 0; attempt < 2; ++attempt) {
    asm volatile("" : "+s"(kvo), "+s"(qo));
    const char* kvs = ws + kvo + kh * 81920 + so16;
    const unsigned ldst = lds_addr(smem) + kh * 81920 + qg * 1024;
    if (wid == 0) { const unsigned f = (tid & 1) ? 0u : 0x3f803f80u; *(u32x4*)(smem + l16) = (u32x4){f, f, f, f}; }
    else glds16c(kvs, ldst);
#pragma unroll
    for (int i = 1; i < 5; ++i) glds16c(kvs + i * 4096, ldst + i * 4096);
    const bf16x8 qA1 = BF8(ldc16(rs, l16, qo)), qB1 = BF8(ldc16(rs, l16 + 1280, qo));
    const unsigned qA2w = ldc4(rs, l2, qo), qB2w = ldc4(rs, l2 + 1280, qo);
    bf16x8 k1 = BF8(ldc16(rs, l16, kvo)); unsigned k2 = ldc4(rs, l2, kvo);
    float mA = -INFINITY, mB = -INFINITY;
    {
      const bf16x4 qA2 = BF4(((u32x2){qA2w, 0u})), qB2 = BF4(((u32x2){qB2w, 0u}));
      const int nt = attempt ? 64 : 1;
      bf16x8 t1 = k1; unsigned t2 = k2;
      for (int t = 0; t < nt; ++t) {
        if (t) { const unsigned to = kvo + (t >> 3) * 20480 + (t & 7) * 1280; t1 = BF8(ldc16(rs, l16, to)); t2 = ldc4(rs, l2, to); }
        const bf16x4 t2v = BF4(((u32x2){t2, kconst}));
        f32x16 a = MFMA32(t1, qA1, (f32x16){}); a = MFMA32K8(t2v, qA2, a);
        f32x16 b = MFMA32(t1, qB1, (f32x16){}); b = MFMA32K8(t2v, qB2, b);
#pragma unroll
        for (int i = 0; i < 16; ++i) { mA = fmaxf(mA, a[i]); mB = fmaxf(mB, b[i]); }
      }
      mA = fmaxf(mA, xhalf(mA)); mB = fmaxf(mB, xhalf(mB));
    }
    unsigned a0, a1, b0, b1; split3(mA, a0, a1); split3(mB, b0, b1);
    const bf16x4 qA2 = BF4(((u32x2){qA2w, h ? a1 : a0})), qB2 = BF4(((u32x2){qB2w, h ? b1 : b0}));
    char* lh = smem + kh * 81920;
    waitv<0>();
    BAR();
    TS(6);
    f32x16 oA = (f32x16){}, oB = (f32x16){};
    const char* kp1 = lh + l16;
    const char* kp2 = lh + l2;
    const bool vreal = r < 20;
    const char* vp = vreal ? lh + 10240 + (h * 20 + r) * 16 : smem + (r == 20 ? 0 : 16);
    const int vinc = vreal ? 1280 : 0, vjump = vreal ? 10240 : 0;
    if (kh) { k1 = *(const bf16x8*)kp1; k2 = *(const unsigned*)kp2; }
    bf16x8 kf1[2], vf[2][2]; u32x2 kf2[2];
    kf1[0] = k1; kf2[0] = (u32x2){k2, kconst}; kf2[1] = (u32x2){0u, kconst};
    vf[1][0] = (bf16x8){}; vf[1][1] = (bf16x8){};
    u32x4 pA0, pA1, pB0 = (u32x4){}, pB1 = (u32x4){};
    f32x4 e0 = (f32x4){}, e1;
    f32x16 sA = MFMA32(kf1[0], qA1, (f32x16){}), sB;
    sA = MFMA32K8(BF4(kf2[0]), qA2, sA);
#define EX2(x) __builtin_amdgcn_exp2f(x)
#define GRP(MF, S, i, EN, EP, P, w) do { MF; SCHED(); EN[0] = EX2(S[i]); P[w] = pack2(EP[0], EP[1]); EN[1] = EX2(S[i + 1]); P[w + 1] = pack2(EP[2], EP[3]); \
      EN[2] = EX2(S[i + 2]); EN[3] = EX2(S[i + 3]); PIN(P); PIN(EN); SCHED(); } while (0)
    const unsigned relsc = lds_addr(smem) + 768;
    auto group = [&](auto FIRST, int jump, int j) __attribute__((always_inline)) {
#pragma unroll
      for (int t = 0; t < 8; ++t) {
        const int c = t & 1, n = c ^ 1;
        if (FIRST) { if (t == 0) __builtin_amdgcn_s_setprio(3); if (t == 3) __builtin_amdgcn_s_setprio(2); if (t == 5) __builtin_amdgcn_s_setprio(1); if (t == 6) __builtin_amdgcn_s_setprio(0); if (t == 7) __builtin_amdgcn_s_setprio(3); }
        else {
          if (t == 4 && j == 2) __builtin_amdgcn_s_setprio(2);
          if (t == 3 && j == 3) __builtin_amdgcn_s_setprio(1);
          if (t == 6 && j == 3) { __builtin_amdgcn_s_setprio(0); if (wid == 7) glds4c(ws + OFF_CNT + (C_REL + (lane & 7)) * 256, relsc); }
        }
        if (FIRST && t < 5) {
#pragma unroll
          for (int i = 5 + 3 * t; i < 8 + 3 * t; ++i) glds16c(kvs + i * 4096, ldst + i * 4096);
        }
        if (t == 7) {
          if (FIRST) { waitv<0>(); BAR(); }
          kp1 += jump; kp2 += jump;
        }
        SCHED();
        kf1[n] = *(const bf16x8*)(kp1 + ((t + 1) & 7) * 1280); kf2[n][0] = *(const unsigned*)(kp2 + ((t + 1) & 7) * 1280);
        GRP(sB = MFMA32(kf1[c], qB1, (f32x16){}), sA, 0, e1, e0, pB1, 2);
        vf[c][0] = *(const bf16x8*)vp; vf[c][1] = *(const bf16x8*)(vp + 640); vp += vinc;
        GRP(sB = MFMA32K8(BF4(kf2[c]), qB2, sB), sA, 4, e0, e1, pA0, 0);
        GRP(oB = MFMA32(vf[n][0], BF8(pB0), oB), sA, 8, e1, e0, pA0, 2);
        GRP(oB = MFMA32(vf[n][1], BF8(pB1), oB), sA, 12, e0, e1, pA1, 0);
        GRP(sA = MFMA32(kf1[n], qA1, (f32x16){}), sB, 0, e1, e0, pA1, 2);
        GRP(sA = MFMA32K8(BF4(kf2[n]), qA2, sA), sB, 4, e0, e1, pB0, 0);
        GRP(oA = MFMA32(vf[c][0], BF8(pA0), oA), sB, 8, e1, e0, pB0, 2);
        GRP(oA = MFMA32(vf[c][1], BF8(pA1), oA), sB, 12, e0, e1, pB1, 0);
      }
      vp += vjump;
    };
    group(std::true_type{}, 20480, 0);
    TS(7);
    const int j2 = (blk - (blk < 185 ? 135 : 185)) * 4 + qg;
    const bool side = kh == 0 && attempt == 0 && (blk < 185 || j2 < 270);
    char* stile = smem + 1024 + qg * 4096;
    const char* svec = smem + 81920 + qg * 256;
    const int kt = blk < 135 ? 4 * (blk % 5) + qg : j2 / 10, ng = blk < 135 ? blk / 5 : j2 % 10;
    if (side) {
      const u16* W = blk < 135 ? win : blk < 185 ? wo : wout; const int N = blk < 135 ? F_ : D_;
      const int j8 = lane >> 3;
#pragma unroll
      for (int i = 0; i < 4; ++i) {
        int row = kt * 32 + i * 8 + j8;
        if (blk >= 135 && blk < 185) { const int c = 4 * kt + i; row = c < 64 ? 20 * (c >> 1) + 4 * (c & 1) + (j8 & 3) + 8 * (j8 >> 2) : 20 * (2 * (c - 64) + (j8 >> 2)) + 16 + (j8 & 3); }
        glds16(W + (size_t)row * N + ng * 64 + ((lane & 7) ^ j8) * 8, lds_addr(stile) + i * 1024);
      }
      glds4c((const float*)(ws + OFF_MOD) + (4 - h) * 640 + (kt < 20 ? kt : 0) * 32 + r, lds_addr(svec));
    }
#pragma nounroll
    for (int j = 1; j < 4; ++j) {
      if (j == 2) {
        if (kh) l2touch(wsp + ((size_t)(blk * 4 + qg) * 64 + lane) * 128, lds_addr(smem) + 256);
        else if (side) {
          waitv<0>();
          const int g4 = lane >> 4, r4 = lane & 15, row = 8 * g4 + (r4 >> 2);
          const f32x4 sv0 = *(const f32x4*)(svec + (r4 & 1) * 128 + g4 * 32), sv1 = *(const f32x4*)(svec + (r4 & 1) * 128 + g4 * 32 + 16);
          const bf16x8 vec = BF8(((u32x4){pack2(sv0[0], sv0[1]), pack2(sv0[2], sv0[3]), pack2(sv1[0], sv1[1]), pack2(sv1[2], sv1[3])}));
          const unsigned nks = blk < 185 ? 20 : 27, dso = (blk < 135 ? OFF_WIN : blk < 185 ? OFF_WO : OFF_WOUT) + (ng * 4 * nks + kt) * 1024;
          bf16x8 fr[4]; f32x4 pr[4];
#pragma unroll
          for (int f = 0; f < 4; ++f) {
            const int ch = 2 * f + ((r4 & 3) >> 1);
            const bf16x4 lo = lds_tr(stile + row * 128 + (ch ^ (row & 7)) * 16 + (r4 & 1) * 8), hi = lds_tr(stile + (row + 4) * 128 + (ch ^ ((row + 4) & 7)) * 16 + (r4 & 1) * 8);
            fr[f] = __builtin_shufflevector(lo, hi, 0, 1, 2, 3, 4, 5, 6, 7);
          }
#pragma unroll
          for (int f = 0; f < 4; ++f) { stc16(rs, l16, dso + f * nks * 1024, __builtin_bit_cast(u32x4, fr[f])); pr[f] = MFMA16(fr[f], vec, (f32x4){}); }
          if (r4 < 2) {
#pragma unroll
            for (int f = 0; f < 4; ++f) *(f32x4*)(stile + 3072 + r4 * 256 + f * 64 + g4 * 16) = pr[f];
          }
        }
      }
      if (j == 3 && kh == 0 && attempt == 0) { waitv<0>(); arrive(ws, C_REL + xcd, lane); }
      group(std::false_type{}, j < 3 ? 20480 : 0, j);
    }
    TS(8);
    pB1[2] = pack2(e0[0], e0[1]); pB1[3] = pack2(e0[2], e0[3]);
    oB = MFMA32(vf[1][0], BF8(pB0), oB); oB = MFMA32(vf[1][1], BF8(pB1), oB);
    {
      f32x4* ex = (f32x4*)(lh + 1024 + qg * 4096) + lane;
      const f32x16 give = kh ? oA : oB;
#pragma unroll
      for (int g = 0; g < 3; ++g) ex[g * 64] = (f32x4){give[4 * g], give[4 * g + 1], give[4 * g + 2], give[4 * g + 3]};
    }
    int* flag = (int*)(smem + 528);
    BAR();
    TS(9);
    float o[12];
    {
      const f32x4* ex = (const f32x4*)(smem + (kh ^ 1) * 81920 + 1024 + qg * 4096) + lane;
      const f32x16 keep = kh ? oB : oA;
#pragma unroll
      for (int g = 0; g < 3; ++g) { const f32x4 t = ex[g * 64];
#pragma unroll
        for (int e = 0; e < 4; ++e) o[4 * g + e] = keep[4 * g + e] + t[e]; }
    }
    const float lo = xhalf(o[8]); const float l = h ? o[8] : lo;
    const float rl = __builtin_amdgcn_rcpf(l);
    const int tok = qb * 256 + qg * 64 + kh * 32 + r;
    const unsigned vo = (tok >> 4) * 20480 + (tok & 15) * 16;
    stc16(rs, vo + h * 256, OFF_O + (head >> 1) * 1024 + (head & 1) * 512,
          (u32x4){pack2(o[0] * rl, o[1] * rl), pack2(o[2] * rl, o[3] * rl), pack2(o[4] * rl, o[5] * rl), pack2(o[6] * rl, o[7] * rl)});
    if (h == 0) stc8(rs, vo, OFF_O + 16384 + (head >> 3) * 1024 + ((head >> 1) & 3) * 256 + (head & 1) * 8, (u32x2){pack2(o[8] * rl, o[9] * rl), pack2(o[10] * rl, o[11] * rl)});
    if (wid == 4 && blk < 135 && attempt == 0 && lane < 32) {
      const char* pp = smem + 1024 + 3072 + lane * 16;
      const f32x4 v = (*(const f32x4*)pp + *(const f32x4*)(pp + 4096)) + (*(const f32x4*)(pp + 8192) + *(const f32x4*)(pp + 12288));
      stc16(rs, (lane >> 4) * 8192 + (lane & 15) * 16, OFF_CBP + (blk % 5) * 16384 + (blk / 5) * 256, __builtin_bit_cast(u32x4, v));
    }
    bool ok = (l > 1e-30f) && (l < 1e30f);
#pragma unroll
    for (int i = 0; i < 12; ++i) ok = ok && (fabsf(o[i]) < 3e38f);
    if (!ok && attempt == 0) *flag = 1;
    waitv<0>();
    BAR();
    const int redo = __builtin_amdgcn_readfirstlane(*(volatile LAS int*)flag);
    if (!redo) break;
    BAR();
  }
  TS(10);
  if (wid == 7) arrive(ws, C_ATT + qb, lane);
  if (!__all(*(volatile LAS unsigned*)(smem + 768 + (lane & 7) * 4) >= 128u)) poll_ge(ws, C_REL + (lane & 7), 128);
}

template <bool FINAL, int KH>
DI void proj_body(char* __restrict__ ws, rsrc_t rs, const u16* __restrict__ x, u16* __restrict__ out, char* smem, int panel, int cb, int wid, int lane, u32x2 (&res)[3] TARG) {
  constexpr int NKS = FINAL ? 27 : 20;
  constexpr int K0 = KH ? (NKS + 1) / 2 : 0, NK = KH ? NKS / 2 : (NKS + 1) / 2, NKEEP = KH ? 2 : 3, UK = KH ? 3 : 0, UG = KH ? 0 : 3;
  const int m = wid & 3, mt = panel * 4 + m, g = lane >> 4, r = lane & 15;
  f32x4 gate[NKEEP], cbv[NKEEP];
#pragma unroll
  for (int i = 0; i < NKEEP; ++i) {
    const int n = (cb * 5 + UK + i) * 16;
    if (!FINAL) { res[i] = *(const u32x2*)(x + (size_t)(mt * 16 + r) * D_ + n + g * 4); cbv[i] = ldc16f(rs, g * 16, OFF_MOD + (4 * 640 + n) * 4); }
    gate[i] = ldc16f(rs, g * 16, OFF_MOD + ((FINAL ? 5 : 2) * 640 + n) * 4);
  }
  if (wid == 7) poll_ge(ws, FINAL ? C_U + panel : C_ATT + (panel >> 2), FINAL ? 8 : 32);
  waitv<0>();
  BAR();
  TS(FINAL ? 27 : 11);
  bf16x8 a[NK];
#pragma unroll
  for (int k = 0; k < NK; ++k) a[k] = BF8(ldc16(rs, lane * 16, (FINAL ? OFF_U : OFF_O) + (mt * NKS + K0 + k) * 1024));
  SCHED();
  constexpr int PD = FINAL ? 5 : 6;
  const char* bl = smem + (FINAL ? K0 * 1024 : 17408 + KH * 81920) + lane * 16;
  constexpr int US = FINAL ? 27 : 10;
  bf16x8 bf[PD + 1][5]; f32x4 acc[5];
#pragma unroll
  for (int u = 0; u < 5; ++u) acc[u] = (f32x4){};
#pragma unroll
  for (int k = 0; k < PD; ++k)
#pragma unroll
    for (int u = 0; u < 5; ++u) bf[k][u] = *(const bf16x8*)(bl + (u * US + k) * 1024);
  SCHED();
#pragma unroll
  for (int k = 0; k < NK; ++k) {
#pragma unroll
    for (int u = 0; u < 5; ++u) {
      if (k + PD < NK) bf[(k + PD) % (PD + 1)][u] = *(const bf16x8*)(bl + (u * US + k + PD) * 1024);
      acc[u] = MFMA16(bf[k % (PD + 1)][u], a[k], acc[u]);
      SCHED();
    }
  }
  TS(FINAL ? 28 : 12);
  f32x4* ex = (f32x4*)(smem + (FINAL ? 138240 : 68608)) + m * 5 * 64 + lane;
#pragma unroll
  for (int i = 0; i < 5 - NKEEP; ++i) ex[(UG + i) * 64] = acc[UG + i];
  f32x4 part[NKEEP];
#pragma unroll
  for (int i = 0; i < NKEEP; ++i) {
    part[i] = (f32x4){bflo(res[i][0]), bfhi(res[i][0]), bflo(res[i][1]), bfhi(res[i][1])} + gate[i] * acc[UK + i];
    PIN(part[i]);
  }
  BAR();
  f32x4 oth[NKEEP];
#pragma unroll
  for (int i = 0; i < NKEEP; ++i) oth[i] = ex[(UK + i) * 64];
  f32x4 sv = (f32x4){}, qv = (f32x4){};
#pragma unroll
  for (int i = 0; i < NKEEP; ++i) {
    const f32x4 v = part[i] + gate[i] * oth[i];
    const u32x2 o = (u32x2){pack2(v[0], v[1]), pack2(v[2], v[3])};
    if (FINAL) *(u32x2*)(out + (size_t)(mt * 16 + r) * D_ + (cb * 5 + UK + i) * 16 + g * 4) = o;
    else {
      res[i] = o;
      const f32x4 w = v * cbv[i]; const int un = cb * 5 + UK + i;
      stc8(rs, ((g >> 1) * 16 + r) * 16 + (g & 1) * 8, OFF_X1S + mt * 20480 + un * 512, (u32x2){pack2(w[0], w[1]), pack2(w[2], w[3])});
      sv += v; qv += v * v;
    }
  }
  if (!FINAL) {
    const float s = sum_g((sv[0] + sv[1]) + (sv[2] + sv[3])), q = sum_g((qv[0] + qv[1]) + (qv[2] + qv[3]));
    if (g == 0) stc8(rs, r * 8, OFF_ST1 + ((panel * 16 + cb * 2 + KH) * 64 + m * 16) * 8, (u32x2){__float_as_uint(s), __float_as_uint(q)});
  }
}

template <bool FINAL>
DI void phase_proj(char* __restrict__ ws, rsrc_t rs, const u16* __restrict__ x, u16* __restrict__ out, char* smem, int wid, u32x2 (&res)[3] TARG) {
  const int tid = fresh_tid(), lane = tid & 63;
  const int xcd = blockIdx.x & 7, idx = blockIdx.x >> 3, panel = xcd * 4 + (idx >> 3), cb = idx & 7;
  if (wid < 7) {
    constexpr int NF = FINAL ? 135 : 100;
    const char* src = ws + (FINAL ? OFF_WOUT : OFF_WO) + (size_t)cb * NF * 1024 + lane * 16;
#pragma unroll
    for (int i = 0; i < (NF + 6) / 7; ++i) {
      const int f = i * 7 + wid;
      if (i * 7 + 6 < NF || f < NF) {
        const int u = f / 20, k = f % 20;
        glds16c(src + f * 1024, lds_addr(smem) + (FINAL ? f * 1024 : 17408 + (k >= 10 ? 81920 : 0) + (u * 10 + k % 10) * 1024));
      }
    }
  }
  if (wid < 4) proj_body<FINAL, 0>(ws, rs, x, out, smem, panel, cb, wid, lane, res TPASS);
  else proj_body<FINAL, 1>(ws, rs, x, out, smem, panel, cb, wid, lane, res TPASS);
  if (!FINAL) {
    TS(13);
    waitv<0>();
    BAR();
    TS(14);
    if (wid == 7) arrive(ws, C_X1 + panel, lane);
  }
}

DI float gelu_t(float x) {
  constexpr float C1 = -2.8853900817779268f * 0.7978845608028654f, C2 = C1 * 0.044715f;
  const float e = __builtin_amdgcn_exp2f(x * __builtin_fmaf(x * x, C2, C1));
  return x * __builtin_amdgcn_rcpf(1.f + e);
}
constexpr int L4_Z1 = 114688, L4_TR = L4_Z1 + 14336, L4_LN = L4_TR + 14336, L4_ST2 = L4_LN + 512, L4_FIN = L4_ST2 + 3584, L4_CB = L4_FIN + 512, L4_CNT = L4_CB + 2048;

DI void phase_win(char* __restrict__ ws, rsrc_t rs, char* smem, int wid TARG) {
  const int tid = fresh_tid(), lane = tid & 63;
  const int xcd = blockIdx.x & 7, idx = blockIdx.x >> 3, panel = xcd * 4 + (idx >> 3), cb = idx & 7;
  const int nu = cb < 6 ? 7 : 6, u0 = unit0_of(cb), u = u0 + wid;
  const int g = lane >> 4, r = lane & 15;
  float* lnp = (float*)(smem + L4_LN); float* st2 = (float*)(smem + L4_ST2); float* fin = (float*)(smem + L4_FIN); float* cbl = (float*)(smem + L4_CB);
  int* cnt = (int*)(smem + L4_CNT);
  auto stage_a = [&]() __attribute__((always_inline)) {
    const char* src = ws + OFF_X1S + (size_t)panel * 81920 + tid * 16;
#pragma unroll
    for (int i = 0; i < 10; ++i) glds16c(src + i * 8192, lds_addr(smem) + wid * 1024 + i * 8192);
  };
  if (wid < nu) {
    __builtin_amdgcn_s_setprio(1);
    bf16x8 b2[20], b1[20];
    const unsigned s2 = OFF_WIN + (54 + u) * 20480, s1 = OFF_WIN + u * 20480;
#pragma unroll
    for (int k = 0; k < 20; ++k) b2[k] = BF8(ldc16(rs, lane * 16, s2 + k * 1024));
    BAR();
    TS(15);
    stage_a();
#pragma unroll
    for (int k = 0; k < 8; ++k) b1[k] = BF8(ldc16(rs, lane * 16, s1 + k * 1024));
    waitv<8>();
    BAR();
    TS(16);
#pragma unroll
    for (int k = 8; k < 20; ++k) b1[k] = BF8(ldc16(rs, lane * 16, s1 + k * 1024));
    f32x4 acc[4];
#pragma unroll
    for (int m = 0; m < 4; ++m) acc[m] = (f32x4){};
    SCHED();
    mma16_run<20, 20480, 1024, 2>(smem + lane * 16, b2, acc);
    TS(17);
    float z2[4][4]; f32x2 ln[4];
#pragma unroll
    for (int m = 0; m < 4; ++m) ln[m] = *(const f32x2*)(lnp + (m * 16 + r) * 2);
    {
      const f32x4 cs2 = *(const f32x4*)(cbl + 112 + wid * 16 + g * 4), bi2 = *(const f32x4*)(cbl + 256 + 112 + wid * 16 + g * 4);
#pragma unroll
      for (int m = 0; m < 4; ++m) {
        float s = 0.f, q = 0.f;
#pragma unroll
        for (int e = 0; e < 4; ++e) { const float z = gelu_t(__builtin_fmaf(acc[m][e], ln[m][0], __builtin_fmaf(ln[m][1], cs2[e], bi2[e]))); z2[m][e] = z; s += z; q = __builtin_fmaf(z, z, q); }
        s = sum_g(s); q = sum_g(q);
        if (g == 0) *(f32x2*)(st2 + (wid * 64 + m * 16 + r) * 2) = (f32x2){s, q};
      }
    }
    asm volatile("" ::: "memory");
    if (lane == 0) __hip_atomic_fetch_add(cnt, 1, __ATOMIC_RELAXED, __HIP_MEMORY_SCOPE_WORKGROUP);
    asm volatile("" ::: "memory");
    __builtin_amdgcn_s_setprio(0);
#pragma unroll
    for (int m = 0; m < 4; ++m) acc[m] = (f32x4){};
    TS(18);
    mma16_run<20, 20480, 1024, 3>(smem + lane * 16, b1, acc);
    TS(19);
    {
      const f32x4 cs1 = *(const f32x4*)(cbl + wid * 16 + g * 4), bi1 = *(const f32x4*)(cbl + 256 + wid * 16 + g * 4);
#pragma unroll
      for (int mm = 0; mm < 2; ++mm) {
        u32x4 o;
#pragma unroll
        for (int i = 0; i < 2; ++i) {
          const int m = 2 * mm + i; float z[4];
#pragma unroll
          for (int e = 0; e < 4; ++e) z[e] = gelu_t(__builtin_fmaf(acc[m][e], ln[m][0], __builtin_fmaf(ln[m][1], cs1[e], bi1[e])));
          o[2 * i] = pack2s(z[0], z[1]); o[2 * i + 1] = pack2s(z[2], z[3]);
        }
        *(u32x4*)(smem + L4_Z1 + wid * 2048 + mm * 1024 + lane * 16) = o;
      }
    }
    TS(20);
    BAR();
    TS(21);
    char* tr = smem + L4_TR + wid * 2048;
#pragma unroll
    for (int m = 0; m < 4; ++m) {
      const f32x2 f = *(const f32x2*)(fin + (m * 16 + r) * 2);
      *(u32x2*)(tr + (m * 16 + r) * 32 + g * 8) = (u32x2){pack2s(z2[m][0] * f[0] - f[1], z2[m][1] * f[0] - f[1]), pack2s(z2[m][2] * f[0] - f[1], z2[m][3] * f[0] - f[1])};
    }
#pragma unroll
    for (int ks = 0; ks < 2; ++ks) {
      const char* p = tr + (ks * 32 + 8 * g + (r >> 2)) * 32 + (r & 3) * 8;
      const bf16x4 lo = lds_tr(p), hi = lds_tr(p + 128);
      stc16(rs, lane * 16, OFF_Z2 + ((panel * 2 + ks) * 54 + u) * 1024, __builtin_bit_cast(u32x4, (bf16x8)__builtin_shufflevector(lo, hi, 0, 1, 2, 3, 4, 5, 6, 7)));
    }
  } else if (wid == 7) {
    __builtin_amdgcn_s_setprio(2);
    if (lane == 0) *cnt = 0;
    poll_ge(ws, lane < 8 ? C_ATT + lane : C_X1 + panel, lane < 8 ? 32 : 8);
    BAR();
    {
      stage_a();
      u32x2 t[16]; f32x4 cp[5][2];
#pragma unroll
      for (int i = 0; i < 16; ++i) t[i] = __builtin_amdgcn_raw_buffer_load_b64(rs, lane * 8, OFF_ST1 + (panel * 16 + i) * 512, 16);
      const unsigned co = lane < 28 ? (u0 * 16 + lane * 4) * 4 : ((54 + u0) * 16 + min(lane - 28, 27) * 4) * 4;
#pragma unroll
      for (int i = 0; i < 5; ++i) { cp[i][0] = ldc16f(rs, co, OFF_CBP + i * 16384); cp[i][1] = ldc16f(rs, co, OFF_CBP + i * 16384 + 8192); }
      float s = 0.f, q = 0.f;
#pragma unroll
      for (int i = 0; i < 16; ++i) { s += __uint_as_float(t[i][0]); q += __uint_as_float(t[i][1]); }
      const float mean = s * (1.f / 640.f), var = fmaxf(q * (1.f / 640.f) - mean * mean, 0.f);
      const float ri = 1.f / ln_den(var);
      *(f32x2*)(lnp + lane * 2) = (f32x2){ri, -rbf(mean) * ri};
#pragma unroll
      for (int v = 0; v < 2; ++v) *(f32x4*)(cbl + v * 256 + lane * 4) = ((cp[0][v] + cp[1][v]) + (cp[2][v] + cp[3][v])) + cp[4][v];
    }
    waitv<0>();
    BAR();
#pragma nounroll
    for (unsigned spin = 0; spin < (1u << 24); ++spin) {
      if (__hip_atomic_load(cnt, __ATOMIC_RELAXED, __HIP_MEMORY_SCOPE_WORKGROUP) >= nu) break;
      __builtin_amdgcn_s_sleep(2);
    }
    asm volatile("" ::: "memory");
    float s = 0.f, q = 0.f;
    for (int i = 0; i < nu; ++i) { const f32x2 t = *(const f32x2*)(st2 + (i * 64 + lane) * 2); s += t[0]; q += t[1]; }
    GAS u64* xch = (GAS u64*)(ws + OFF_XCH) + panel * 512;
    unsigned qb = __float_as_uint(q); if (qb == ~0u) qb = 0x7fc00000u;
    __hip_atomic_store(xch + cb * 64 + lane, ((u64)qb << 32) | __float_as_uint(s), __ATOMIC_RELAXED, __HIP_MEMORY_SCOPE_AGENT);
    u64 v[8];
#pragma nounroll
    for (unsigned spin = 0; spin < (1u << 21); ++spin) {
      bool ok = true;
#pragma unroll
      for (int i = 0; i < 8; ++i) { v[i] = __hip_atomic_load(xch + i * 64 + lane, __ATOMIC_RELAXED, __HIP_MEMORY_SCOPE_AGENT); ok = ok && ((unsigned)(v[i] >> 32) != ~0u); }
      if (__all(ok)) break;
      __builtin_amdgcn_s_sleep(1);
    }
    s = 0.f; q = 0.f;
#pragma unroll
    for (int i = 0; i < 8; ++i) { s += __uint_as_float((unsigned)v[i]); q += __uint_as_float((unsigned)(v[i] >> 32)); }
    const float mean = s * (1.f / 864.f), var = fmaxf(q * (1.f / 864.f) - mean * mean, 0.f);
    const float ri = 1.f / ln_den(var);
    *(f32x2*)(fin + lane * 2) = (f32x2){ri, rbf(mean) * ri};
    __builtin_amdgcn_s_setprio(0);
    TS(20);
    BAR();
  } else {
    BAR();
    stage_a();
    waitv<0>();
    BAR();
    BAR();
  }
  waitv<0>();
  BAR();
  TS(22);
  if (wid == 7) arrive(ws, C_Z2 + cb, lane);
}

constexpr int K5_R = 7, K5_LB = 3;

template <int NU>
DI void sp_body(char* __restrict__ ws, rsrc_t rs, const u16* __restrict__ wsp, const u16* __restrict__ bsp, char* smem, int panel, int cb, int wid, int lane TARG) {
  constexpr int NA = NU ? NU : 1, NB = 2 * NU;
  const int kh = wid >> 2, up = wid & 3, u0 = unit0_of(cb) + 2 * up, g = lane >> 4, r = lane & 15;
  const char* asrc = (const char*)wsp + (size_t)(panel * 64 + up * 16 + (lane >> 3)) * 4096 + kh * 128 + ((lane & 7) ^ (lane >> 3)) * 16;
  const unsigned adst = lds_addr(smem) + wid * 2048;
  u16 bb[2];
#pragma unroll
  for (int j = 0; j < 2; ++j) bb[j] = bsp[panel * 64 + (2 * kh + j) * 16 + r];
  auto dma = [&](auto S, auto I) __attribute__((always_inline)) {
    constexpr int s = S, i = I;
    if constexpr (s < 16) glds16(asrc + s * 256 + i * 32768, adst + (s % K5_R) * 16384 + i * 1024);
  };
  sfor<K5_R>([&](auto S) __attribute__((always_inline)) { dma(S, std::integral_constant<int, 0>{}); dma(S, std::integral_constant<int, 1>{}); });
  u32x4 z[NA];
#pragma unroll
  for (int i = 0; i < NU; ++i) z[i] = *(const u32x4*)(smem + L4_Z1 + (2 * up + i) * 2048 + kh * 1024 + lane * 16);
  const char* al[2][2];
#pragma unroll
  for (int k = 0; k < 2; ++k)
#pragma unroll
    for (int j = 0; j < 2; ++j) al[k][j] = smem + kh * 8192 + (j ^ kh) * 4096 + r * 128 + ((4 * k + g) ^ (r & 7)) * 16;
  bf16x8 bq[16][2][NA], af[2][2][4]; f32x4 acc[NA][4];
  if (wid == 7) poll_ge(ws, C_Z2 + cb, 32);
  waitv<0>();
#pragma unroll
  for (int i = 0; i < NU; ++i) {
    const float b0 = bf2f(bb[0]), b1 = bf2f(bb[1]);
    acc[i][0] = (f32x4){b0, b0, b0, b0}; acc[i][1] = (f32x4){b1, b1, b1, b1}; acc[i][2] = (f32x4){}; acc[i][3] = (f32x4){};
  }
  BAR();
  TS(23);
  const unsigned bso = OFF_Z2 + (kh * 2 * 54 + u0) * 1024;
  auto ldb = [&](auto S, auto I) __attribute__((always_inline)) {
    constexpr int s = S, i = I;
    if constexpr (s < 16 && i < NB) { bq[s][i / NA][i % NA] = BF8(ldc16(rs, lane * 16 + (i % NA) * 1024, bso + (s * 4 + i / NA) * 55296)); asm volatile("" ::: "memory"); }
  };
  auto rd1 = [&](auto S, int k, int m) __attribute__((always_inline)) {
    constexpr int s = S;
    if constexpr (NU && s < 16) af[s & 1][k][m] = *(const bf16x8*)(al[k][m >> 1] + (s % K5_R) * 16384 + (m & 1) * 2048);
  };
  sfor<K5_LB>([&](auto S) __attribute__((always_inline)) { sfor<4>([&](auto I) __attribute__((always_inline)) { ldb(S, I); }); });
#pragma unroll
  for (int k = 0; k < 2; ++k)
#pragma unroll
    for (int m = 0; m < 4; ++m) rd1(std::integral_constant<int, 0>{}, k, m);
  SCHED();
  sfor<16>([&](auto S) __attribute__((always_inline)) {
    constexpr int s = S;
    sfor<4>([&](auto M) __attribute__((always_inline)) {
      constexpr int m = M;
#pragma unroll
      for (int i = 0; i < NU; ++i) acc[i][m] = MFMA16(bq[s][0][i], af[s & 1][0][m], acc[i][m]);
      ldb(std::integral_constant<int, s + K5_LB>{}, M);
      SCHED();
    });
    if constexpr (s < 15) {
      if constexpr (s + 1 >= K5_R) waitv<(s + K5_LB < 16 ? NB : 0) + (s - 1 + K5_LB < 16 ? NB : 0) + (s - 1 + K5_R < 16 ? 2 : 0) + (s - 2 + K5_R < 16 ? 2 : 0)>();
      BAR();
    }
    sfor<4>([&](auto M) __attribute__((always_inline)) {
      constexpr int m = M;
      rd1(std::integral_constant<int, s + 1>{}, 0, m); rd1(std::integral_constant<int, s + 1>{}, 1, m);
#pragma unroll
      for (int i = 0; i < NU; ++i) acc[i][m] = MFMA16(bq[s][1][i], af[s & 1][1][m], acc[i][m]);
      if constexpr (m < 2) dma(std::integral_constant<int, s + K5_R>{}, M);
      SCHED();
    });
  });
  TS(24);
  f32x4* exw = (f32x4*)(smem + 32768 + wid * 4096) + lane;
  const f32x4* exr = (const f32x4*)(smem + 32768 + (wid ^ 4) * 4096) + lane;
#pragma unroll
  for (int i = 0; i < NU; ++i)
#pragma unroll
    for (int j = 0; j < 2; ++j) exw[(i * 2 + j) * 64] = acc[i][2 + j];
  f32x4 zf[NA][2];
#pragma unroll
  for (int i = 0; i < NU; ++i)
#pragma unroll
    for (int j = 0; j < 2; ++j) {
      zf[i][j] = (f32x4){bflo(z[i][2 * j]), bfhi(z[i][2 * j]), bflo(z[i][2 * j + 1]), bfhi(z[i][2 * j + 1])};
      acc[i][j] *= zf[i][j]; PIN(zf[i][j]); PIN(acc[i][j]);
    }
  BAR();
  f32x4 oth[NA][2];
#pragma unroll
  for (int i = 0; i < NU; ++i)
#pragma unroll
    for (int j = 0; j < 2; ++j) oth[i][j] = exr[(i * 2 + j) * 64];
#pragma unroll
  for (int i = 0; i < NU; ++i)
#pragma unroll
    for (int j = 0; j < 2; ++j) {
      const f32x4 v = acc[i][j] + zf[i][j] * oth[i][j];
      stc8(rs, ((g >> 1) * 16 + r) * 16 + (g & 1) * 8, OFF_U + (panel * 4 + 2 * kh + j) * 27648 + (u0 + i) * 512, (u32x2){pack2(v[0], v[1]), pack2(v[2], v[3])});
    }
}

DI void phase_sp(char* __restrict__ ws, rsrc_t rs, const u16* __restrict__ wsp, const u16* __restrict__ bsp, char* smem, int wid TARG) {
  const int tid = fresh_tid(), lane = tid & 63;
  const int xcd = blockIdx.x & 7, idx = blockIdx.x >> 3, panel = xcd * 4 + (idx >> 3), cb = idx & 7;
  if ((wid & 3) < 3) sp_body<2>(ws, rs, wsp, bsp, smem, panel, cb, wid, lane TPASS);
  else if (cb < 6) sp_body<1>(ws, rs, wsp, bsp, smem, panel, cb, wid, lane TPASS);
  else sp_body<0>(ws, rs, wsp, bsp, smem, panel, cb, wid, lane TPASS);
  TS(25);
  waitv<0>();
  BAR();
  TS(26);
  if (wid == 7) arrive(ws, C_U + panel, lane);
}

__global__ __launch_bounds__(512, 2) OCC2 void k_mega(char* __restrict__ ws, const u16* __restrict__ x, const u16* __restrict__ wo, const u16* __restrict__ win,
                                                     const u16* __restrict__ wsp, const u16* __restrict__ bsp, const u16* __restrict__ wout,
                                                     const u16* __restrict__ cvec, const u16* __restrict__ wmod, u16* __restrict__ out) {
  extern __shared__ __attribute__((aligned(16))) char smem[];
  const int wid = __builtin_amdgcn_readfirstlane(threadIdx.x >> 6);
  const rsrc_t rs = __builtin_amdgcn_make_buffer_rsrc(ws, 0, WS_USED, 0x00020000);
  TDECL; TS(0);
  u32x2 res[3];
  phase_qkv(ws, rs, cvec, wmod, smem, wid TPASS);
  phase_attn(ws, rs, (const char*)wsp, wo, win, wout, smem, wid TPASS);
  phase_proj<false>(ws, rs, x, out, smem, wid, res TPASS);
  phase_win(ws, rs, smem, wid TPASS);
  phase_sp(ws, rs, wsp, bsp, smem, wid TPASS);
  phase_proj<true>(ws, rs, x, out, smem, wid, res TPASS);
  TS(29);
  TDUMP();
}

extern "C" void kernel_launch(void* const* d_in, const int* in_sizes, int n_in, void* d_out, int out_size, void* d_ws, size_t ws_size,
                              hipStream_t stream) {
  const u16* x = (const u16*)d_in[0];
  const u16* wqkv = (const u16*)d_in[1];
  const u16* wo = (const u16*)d_in[2];
  const u16* win = (const u16*)d_in[4];
  const u16* wsp = (const u16*)d_in[5];
  const u16* bsp = (const u16*)d_in[6];
  const u16* wout = (const u16*)d_in[7];
  const u16* cvec = (const u16*)d_in[8];
  const u16* wmod = (const u16*)d_in[9];
  char* ws = (char*)d_ws;
  (void)hipFuncSetAttribute((const void*)k_mega, hipFuncAttributeMaxDynamicSharedMemorySize, 163840);
  k0_prep<<<K0_GRID, 256, 0, stream>>>(x, wqkv, cvec, wmod, ws);
  k_mega<<<256, 512, 163840, stream>>>(ws, x, wo, win, wsp, bsp, wout, cvec, wmod, (u16*)d_out);
}
```

```cpp
#include <hip/hip_runtime.h>
#include <hip/hip_bf16.h>
#include <stdint.h>
#include <utility>

typedef __attribute__((ext_vector_type(8))) short bf16x8;
typedef __attribute__((ext_vector_type(4))) short bf16x4;
typedef __attribute__((ext_vector_type(2))) float f32x2;
typedef __attribute__((ext_vector_type(4))) float f32x4;
typedef __attribute__((ext_vector_type(16))) float f32x16;
typedef __attribute__((ext_vector_type(2))) unsigned u32x2;
typedef __attribute__((ext_vector_type(4))) unsigned u32x4;
typedef __attribute__((ext_vector_type(2))) __bf16 bf2_t;
typedef __attribute__((ext_vector_type(2))) _Float16 h2_t;
typedef unsigned short u16;
typedef unsigned long long u64;
#define LAS __attribute__((address_space(3)))
#define GAS __attribute__((address_space(1)))
#define DI __device__ __forceinline__

constexpr int D_ = 640, NQKV = 1920, F_ = 1728;

constexpr unsigned OFF_CNT = 0;
constexpr unsigned OFF_MOD = 65536;
constexpr unsigned OFF_MODP = OFF_MOD + 16384;
constexpr unsigned OFF_CBP = OFF_MODP + 40960;
constexpr unsigned OFF_XCH = OFF_CBP + 5 * 16384;
constexpr unsigned OFF_ST1 = OFF_XCH + 131072;
constexpr unsigned OFF_XHAT = OFF_ST1 + 262144;
constexpr unsigned OFF_WQKV = OFF_XHAT + 2621440;
constexpr unsigned OFF_WO = OFF_WQKV + 2457600;
constexpr unsigned OFF_WIN = OFF_WO + 819200;
constexpr unsigned OFF_WOUT = OFF_WIN + 2211840;
constexpr unsigned OFF_Q = OFF_WOUT + 1105920;
constexpr unsigned OFF_KV = OFF_Q + 32 * 81920;
constexpr unsigned OFF_O = OFF_KV + 32 * 163840;
constexpr unsigned OFF_X1S = OFF_O + 2621440;
constexpr unsigned OFF_Z2 = OFF_X1S + 2621440;
constexpr unsigned OFF_U = OFF_Z2 + 3538944;
constexpr unsigned WS_USED = OFF_U + 3538944;
constexpr int C_QKV = 0, C_MOD = 15  , C_ATT = 128, C_REL = 136, C_X1 = 144, C_U = 176, C_Z2 = 208  ;

#define STAMP(kid, k)
#define TDECL
#define TS(k)
#define TDUMP()
DI unsigned pack2(float lo, float hi) { return __builtin_bit_cast(unsigned, __builtin_convertvector((f32x2){lo, hi}, bf2_t)); }
DI unsigned pack2s(float lo, float hi) { unsigned r; asm("v_cvt_pk_bf16_f32 %0, %1, %2" : "=v"(r) : "v"(lo), "v"(hi)); return r; }
DI float opq(float x) { asm("" : "+v"(x)); return x; }
DI unsigned pack2h(float lo, float hi) { return __builtin_bit_cast(unsigned, __builtin_convertvector((f32x2){lo, hi}, h2_t)); }
DI float bflo(unsigned u) { return __uint_as_float(u << 16); }
DI float bfhi(unsigned u) { return __uint_as_float(u & 0xffff0000u); }
DI float rbf(float x) { return bflo(pack2(x, x)); }
DI float bf2f(u16 b) { return __uint_as_float((unsigned)b << 16); }
#define MFMA32(a, b, c) __builtin_amdgcn_mfma_f32_32x32x16_bf16((a), (b), (c), 0, 0, 0)
#define MFMA32K8(a, b, c) __builtin_amdgcn_mfma_f32_32x32x8bf16_1k((a), (b), (c), 0, 0, 0)
#define MFMA16(a, b, c) __builtin_amdgcn_mfma_f32_16x16x32_bf16((a), (b), (c), 0, 0, 0)
#define BAR() asm volatile("s_waitcnt lgkmcnt(0)\n\ts_barrier" ::: "memory")
#define SCHED() __builtin_amdgcn_sched_barrier(0)
#define PIN(x) asm volatile("" : "+v"(x))
#define OCC2 __attribute__((amdgpu_waves_per_eu(2, 2)))
#define BF8(x) __builtin_bit_cast(bf16x8, x)
#define BF4(x) __builtin_bit_cast(bf16x4, x)
template <int N> DI void waitv() { asm volatile("s_waitcnt vmcnt(%0)" ::"i"(N) : "memory"); }
template <class F, int... I> DI void sfor_impl(F&& f, std::integer_sequence<int, I...>) { (f(std::integral_constant<int, I>{}), ...); }
template <int N, class F> DI void sfor(F&& f) { sfor_impl(f, std::make_integer_sequence<int, N>{}); }
typedef short v4i16_t __attribute__((ext_vector_type(4)));
DI bf16x4 lds_tr(const char* p) { return __builtin_bit_cast(bf16x4, __builtin_amdgcn_ds_read_tr16_b64_v4i16((LAS v4i16_t*)p)); }
DI void glds16(const void* g, unsigned lds_base) {
  unsigned sv;
  asm volatile("s_mov_b32 %0, m0\n\ts_mov_b32 m0, %2\n\ts_nop 0\n\tglobal_load_lds_dwordx4 %1, off\n\ts_mov_b32 m0, %0" : "=&s"(sv) : "v"(g), "s"(lds_base) : "memory");
}
DI void glds16c(const void* g, unsigned lds_base) {
  unsigned sv;
  asm volatile("s_mov_b32 %0, m0\n\ts_mov_b32 m0, %2\n\ts_nop 0\n\tglobal_load_lds_dwordx4 %1, off sc1\n\ts_mov_b32 m0, %0" : "=&s"(sv) : "v"(g), "s"(lds_base) : "memory");
}
DI void glds4c(const void* g, unsigned lds_base) {
  unsigned sv;
  asm volatile("s_mov_b32 %0, m0\n\ts_mov_b32 m0, %2\n\ts_nop 0\n\tglobal_load_lds_dword %1, off sc1\n\ts_mov_b32 m0, %0" : "=&s"(sv) : "v"(g), "s"(lds_base) : "memory");
}
typedef __amdgpu_buffer_rsrc_t rsrc_t;
DI u32x4 ldc16(rsrc_t rs, unsigned voff, unsigned soff) { return __builtin_amdgcn_raw_buffer_load_b128(rs, voff, soff, 16); }
DI unsigned ldc4(rsrc_t rs, unsigned voff, unsigned soff) { return __builtin_amdgcn_raw_buffer_load_b32(rs, voff, soff, 16); }
DI void stc16(rsrc_t rs, unsigned voff, unsigned soff, u32x4 v) { __builtin_amdgcn_raw_buffer_store_b128(v, rs, voff, soff, 16); asm volatile("s_nop 1" ::"v"(v) : "memory"); }
DI void stc8(rsrc_t rs, unsigned voff, unsigned soff, u32x2 v) { __builtin_amdgcn_raw_buffer_store_b64(v, rs, voff, soff, 16); }
DI void stc4(rsrc_t rs, unsigned voff, unsigned soff, unsigned v) { __builtin_amdgcn_raw_buffer_store_b32(v, rs, voff, soff, 16); }
DI f32x4 ldc16f(rsrc_t rs, unsigned voff, unsigned soff) { return __builtin_bit_cast(f32x4, ldc16(rs, voff, soff)); }
DI void arrive(char* ws, int c, int lane) { if (lane == 0) __hip_atomic_fetch_add((GAS unsigned*)(ws + OFF_CNT + c * 256), 1u, __ATOMIC_RELAXED, __HIP_MEMORY_SCOPE_AGENT); }
DI void poll_ge(char* ws, int c, unsigned thr) {
  const GAS unsigned* p = (const GAS unsigned*)(ws + OFF_CNT + c * 256);
#pragma nounroll
  for (unsigned spin = 0; spin < (1u << 21); ++spin) {
    if (__all(__hip_atomic_load(p, __ATOMIC_RELAXED, __HIP_MEMORY_SCOPE_AGENT) >= thr)) break;
    __builtin_amdgcn_s_sleep(1);
  }
  asm volatile("" ::: "memory");
}
DI int fresh_tid() { int t = threadIdx.x; asm volatile("" : "+v"(t)); return t; }
DI void l2touch(const void* g, unsigned lds_scratch) {
  unsigned sv;
  asm volatile("s_mov_b32 %0, m0\n\ts_mov_b32 m0, %2\n\ts_nop 0\n\tglobal_load_lds_dword %1, off\n\ts_mov_b32 m0, %0" : "=&s"(sv) : "v"(g), "s"(lds_scratch) : "memory");
}
#define LDNT(p) __builtin_nontemporal_load(p)
DI float silu_bf(u16 c) { const float f = __uint_as_float((unsigned)c << 16); const float v = f / (1.f + __expf(-f)); return __uint_as_float(__builtin_bit_cast(unsigned, __builtin_convertvector((f32x2){v, v}, bf2_t)) << 16); }
DI unsigned lds_addr(const void* p) { return (unsigned)(uintptr_t)(const LAS void*)p; }
DI float xhalf(float v) {
  auto rr = __builtin_amdgcn_permlane32_swap(__float_as_uint(v), __float_as_uint(v), false, false);
  return __uint_as_float((threadIdx.x & 32) ? rr[0] : rr[1]);
}
DI float sum_g(float v) {
  auto a = __builtin_amdgcn_permlane16_swap(__float_as_uint(v), __float_as_uint(v), false, false);
  v = __uint_as_float(a[0]) + __uint_as_float(a[1]);
  auto b = __builtin_amdgcn_permlane32_swap(__float_as_uint(v), __float_as_uint(v), false, false);
  return __uint_as_float(b[0]) + __uint_as_float(b[1]);
}
template <int CTRL> DI float dpp_add(float v) { return v + __uint_as_float(__builtin_amdgcn_update_dpp(0u, __float_as_uint(v), CTRL, 0xf, 0xf, true)); }
DI float sum16(float v) {
  v = dpp_add<0x128>(v); v = dpp_add<0x124>(v); v = dpp_add<0x122>(v); return dpp_add<0x121>(v);
}
DI u32x4 modulate8(u32x4 v, f32x4 sc0, f32x4 sc1, f32x4 sh0, f32x4 sh1) {
  unsigned o0, o1, o2, o3; float t0, t1, t2, t3;
  asm volatile(
      "v_fma_mix_f32 %0, %8, %12, %20 op_sel_hi:[1,0,0]\n\tv_fma_mix_f32 %4, %8, %13, %21 op_sel:[1,0,0] op_sel_hi:[1,0,0]\n\t"
      "v_fma_mix_f32 %1, %9, %14, %22 op_sel_hi:[1,0,0]\n\tv_fma_mix_f32 %5, %9, %15, %23 op_sel:[1,0,0] op_sel_hi:[1,0,0]\n\t"
      "v_fma_mix_f32 %2, %10, %16, %24 op_sel_hi:[1,0,0]\n\tv_fma_mix_f32 %6, %10, %17, %25 op_sel:[1,0,0] op_sel_hi:[1,0,0]\n\t"
      "v_fma_mix_f32 %3, %11, %18, %26 op_sel_hi:[1,0,0]\n\tv_fma_mix_f32 %7, %11, %19, %27 op_sel:[1,0,0] op_sel_hi:[1,0,0]\n\t"
      "v_cvt_pk_bf16_f32 %0, %0, %4\n\tv_cvt_pk_bf16_f32 %1, %1, %5\n\tv_cvt_pk_bf16_f32 %2, %2, %6\n\tv_cvt_pk_bf16_f32 %3, %3, %7"
      : "=&v"(o0), "=&v"(o1), "=&v"(o2), "=&v"(o3), "=&v"(t0), "=&v"(t1), "=&v"(t2), "=&v"(t3)
      : "v"(v[0]), "v"(v[1]), "v"(v[2]), "v"(v[3]), "v"(sc0[0]), "v"(sc0[1]), "v"(sc0[2]), "v"(sc0[3]), "v"(sc1[0]), "v"(sc1[1]), "v"(sc1[2]), "v"(sc1[3]),
        "v"(sh0[0]), "v"(sh0[1]), "v"(sh0[2]), "v"(sh0[3]), "v"(sh1[0]), "v"(sh1[1]), "v"(sh1[2]), "v"(sh1[3]));
  return (u32x4){o0, o1, o2, o3};
}
DI float ln_den(float var) { return rbf(__builtin_sqrtf(rbf(rbf(var) + 1e-5f))); }
DI unsigned a16_off(int n, int r) { return (n >> 5) * 1024 + (((n & 31) >> 3) * 16 + r) * 16 + (n & 4) * 2; }
DI int unit0_of(int j) { return j < 6 ? 7 * j : 42 + 6 * (j - 6); }

DI void K0ST(char* p, u32x4 v) { *(u32x4*)p = v; }
constexpr int K0_MOD = 160, K0_LN = 128, K0_RL = 240, K0_WO = 50, K0_INIT = 12;
constexpr int K0_GRID = K0_MOD + K0_LN + K0_RL + K0_WO + K0_INIT;

__global__ __launch_bounds__(256) void k0_prep(const u16* __restrict__ x, const u16* __restrict__ wqkv, const u16* __restrict__ wo, const u16* __restrict__ cvec,
                                               const u16* __restrict__ wmod, char* __restrict__ ws) {
  __shared__ __attribute__((aligned(16))) char smem[4 * 4608];
  const int tid = threadIdx.x, lane = tid & 63, wid = __builtin_amdgcn_readfirstlane(tid >> 6);
  int b = blockIdx.x;
  STAMP(0, 0);
  if (b < K0_MOD) {
    const int cg = b >> 2, ks = b & 3, c = tid & 7, row0 = ks * 160 + (tid >> 3), col0 = cg * 64 + (cg >= 20 ? 640 : 0);
    u32x4 w[5]; u16 cv[5];
#pragma unroll
    for (int i = 0; i < 5; ++i) { w[i] = LDNT((const u32x4*)(wmod + (size_t)(row0 + 32 * i) * 3840 + col0 + c * 8)); cv[i] = cvec[row0 + 32 * i]; }
    float acc[8] = {0, 0, 0, 0, 0, 0, 0, 0};
#pragma unroll
    for (int i = 0; i < 5; ++i) {
      const float s = silu_bf(cv[i]);
#pragma unroll
      for (int e = 0; e < 4; ++e) { acc[2 * e] += s * bflo(w[i][e]); acc[2 * e + 1] += s * bfhi(w[i][e]); }
    }
#pragma unroll
    for (int e = 0; e < 8; ++e) acc[e] = sum_g(dpp_add<0x128>(acc[e]));
    float* red = (float*)smem;
    if (lane < 8) { *(f32x4*)(red + wid * 64 + lane * 8) = (f32x4){acc[0], acc[1], acc[2], acc[3]}; *(f32x4*)(red + wid * 64 + lane * 8 + 4) = (f32x4){acc[4], acc[5], acc[6], acc[7]}; }
    __syncthreads();
    if (tid < 64) ((float*)(ws + OFF_MODP))[ks * 2560 + cg * 64 + tid] = (red[tid] + red[64 + tid]) + (red[128 + tid] + red[192 + tid]);
    STAMP(0, 1); STAMP(0, 13);
    return;
  }
  b -= K0_MOD;
  if (b < K0_LN) {
    b = (b & 7) * 16 + (b >> 3);
    const int row = b * 16 + wid * 4 + (lane >> 4), c = lane & 15;
    const u16* xr = x + (size_t)row * D_ + c * 8;
    u32x4 v[5];
#pragma unroll
    for (int i = 0; i < 5; ++i) v[i] = *(const u32x4*)(xr + i * 128);
    float s = 0.f, q = 0.f;
#pragma unroll
    for (int i = 0; i < 5; ++i)
#pragma unroll
      for (int e = 0; e < 4; ++e) { const float a = bflo(v[i][e]), d = bfhi(v[i][e]); s += a + d; q += a * a + d * d; }
    s = sum16(s); q = sum16(q);
    const float mean = s * (1.f / 640.f); const float var = fmaxf(q * (1.f / 640.f) - mean * mean, 0.f);
    const float rinv = 1.f / ln_den(var), mb = rbf(mean) * rinv;
    char* dst = ws + OFF_XHAT + (size_t)row * 1280 + c * 16;
#pragma unroll
    for (int i = 0; i < 5; ++i) {
      u32x4 o;
#pragma unroll
      for (int e = 0; e < 4; ++e) o[e] = pack2h(bflo(v[i][e]) * rinv - mb, bfhi(v[i][e]) * rinv - mb);
      K0ST(dst + i * 256, o);
    }
    STAMP(0, 2); STAMP(0, 13);
    return;
  }
  b -= K0_LN;
  if (b < K0_RL) {
    const int kt = b / 12, G = b % 12, sec = G >> 2, gq = G & 3;
    u32x4 v[3];
#pragma unroll
    for (int i = 0; i < 3; ++i) { const int ci = min(i * 256 + tid, 639); v[i] = LDNT((const u32x4*)(wqkv + (size_t)(kt * 32 + ci / 20) * NQKV + G * 160 + (ci % 20) * 8)); }
#pragma unroll
    for (int i = 0; i < 3; ++i) { const int ci = min(i * 256 + tid, 639); *(u32x4*)(smem + (ci / 20) * 336 + (ci % 20) * 16) = v[i]; }
    __syncthreads();
    const int g = lane >> 4, q = (lane & 15) >> 2, p = lane & 3, rho = 16 * (g & 1) + 4 * p, qd = rho >> 3, h = (rho >> 2) & 1;
    for (int f = wid; f < 10; f += 4) {
      const int t = f >> 1, ks = f & 1;
      const int col = sec == 2 ? 32 * t + rho : t < 4 ? 20 * (2 * t + h) + 4 * qd : 20 * (4 * h + qd) + 16;
      const int T = sec == 2 ? 40 + gq * 5 + t : sec * 20 + (t < 4 ? 4 * gq + t : 16 + gq);
      const char* pp = smem + (16 * ks + 8 * (g >> 1) + q) * 336 + col * 2;
      const bf16x4 lo = lds_tr(pp), hi = lds_tr(pp + 4 * 336);
      K0ST(ws + OFF_WQKV + ((size_t)T * 40 + kt * 2 + ks) * 1024 + lane * 16, __builtin_bit_cast(u32x4, (bf16x8)__builtin_shufflevector(lo, hi, 0, 1, 2, 3, 4, 5, 6, 7)));
    }
    STAMP(0, 3); STAMP(0, 13);
    return;
  }
  b -= K0_RL;
  if (b < K0_WO) {
    const int job = b * 4 + wid, kt = job / 10, ng = job % 10, j8 = lane >> 3;
    char* t = smem + wid * 4608;
    u32x4 v[4];
#pragma unroll
    for (int i = 0; i < 4; ++i) {
      const int c = 4 * kt + i, row = c < 64 ? 20 * (c >> 1) + 4 * (c & 1) + (j8 & 3) + 8 * (j8 >> 2) : 20 * (2 * (c - 64) + (j8 >> 2)) + 16 + (j8 & 3);
      v[i] = LDNT((const u32x4*)(wo + (size_t)row * D_ + ng * 64 + (lane & 7) * 8));
    }
#pragma unroll
    for (int i = 0; i < 4; ++i) *(u32x4*)(t + (i * 8 + j8) * 144 + (lane & 7) * 16) = v[i];
    const char* pp = t + (8 * (lane >> 4) + ((lane & 15) >> 2)) * 144 + (lane & 3) * 8;
#pragma unroll
    for (int f = 0; f < 4; ++f) {
      const bf16x4 lo = lds_tr(pp + f * 32), hi = lds_tr(pp + 4 * 144 + f * 32);
      K0ST(ws + OFF_WO + ((size_t)(ng * 4 + f) * 20 + kt) * 1024 + lane * 16, __builtin_bit_cast(u32x4, (bf16x8)__builtin_shufflevector(lo, hi, 0, 1, 2, 3, 4, 5, 6, 7)));
    }
    return;
  }
  b -= K0_WO;
  {
    u32x4* p = (u32x4*)(ws + (b < 8 ? OFF_XCH + b * 16384 : OFF_CNT + (b - 8) * 16384)) + tid;
    const unsigned f = b < 8 ? ~0u : 0u;
#pragma unroll
    for (int i = 0; i < 4; ++i) p[i * 256] = (u32x4){f, f, f, f};
  }
}

constexpr float QSCALE = 0.22360679774997896f * 1.4426950408889634f;
#define K1W 3
constexpr int K1_W = K1W;

template <bool ISV>
DI void k1_mma(const char* __restrict__ bsrc, const char* smem, int lane, f32x16 (&acc)[4]) {
  bf16x8 bfr[40], af[4][4];
  const char* lds[4];
#pragma unroll
  for (int k = 0; k < 4; ++k) lds[k] = smem + (lane ^ (2 * k + (lane >> 5))) * 16;
#pragma unroll
  for (int k = 0; k < 4 * K1_W; ++k) bfr[k] = *(const bf16x8*)(bsrc + k * 1024);
#pragma unroll
  for (int m = 0; m < 4; ++m) acc[m] = (f32x16){};
  BAR();
  BAR();
  __builtin_amdgcn_s_setprio(1);
#define K1_RD1(s, k, m) (af[k][m] = *(const bf16x8*)(lds[k] + ((s) % 3) * 16384 + ((k) * 4 + (m)) * 1024))
#define K1_STEP(s, k, RD, rs, rk, LD) do { _Pragma("unroll") for (int m = 0; m < 4; ++m) { if (RD) K1_RD1(rs, rk, m); \
      if (LD) bfr[4 * ((s) + K1_W) + m] = *(const bf16x8*)(bsrc + (4 * ((s) + K1_W) + m) * 1024); \
      acc[m] = ISV ? MFMA32(af[k][m], bfr[(s) * 4 + (k)], acc[m]) : MFMA32(bfr[(s) * 4 + (k)], af[k][m], acc[m]); SCHED(); } } while (0)
#pragma unroll
  for (int m = 0; m < 4; ++m) K1_RD1(0, 0, m);
#pragma unroll
  for (int m = 0; m < 4; ++m) K1_RD1(0, 1, m);
  SCHED();
#pragma unroll
  for (int s = 0; s < 10; ++s) {
    K1_STEP(s, 0, true, s, 2, s + K1_W < 10);
    K1_STEP(s, 1, true, s, 3, false);
    if (s < 9) asm volatile("s_barrier" ::: "memory");
    K1_STEP(s, 2, s < 9, s + 1, 0, false);
    K1_STEP(s, 3, s < 9, s + 1, 1, false);
  }
  __builtin_amdgcn_s_setprio(0);
}

#define TARG
#define TPASS

DI void phase_qkv(char* __restrict__ ws, rsrc_t rs, const u16* __restrict__ cvec, const u16* __restrict__ wmod, char* smem, int wid TARG) {
  const int tid = fresh_tid(), lane = tid & 63;
  const int xcd = blockIdx.x & 7, idx = blockIdx.x >> 3;
  int ctr;
  if (idx >= 30) {
    ctr = C_MOD;
    const int sb = xcd * 2 + idx - 30, col0 = (sb < 8 ? 1280 : 3200 - 640) + sb * 80, c = tid % 10, rg = tid / 10;
    float* sil = (float*)smem; float* red = (float*)(smem + 4096);
    u32x4 w[13];
#pragma unroll
    for (int i = 0; i < 13; ++i) { const int row = min(rg + 50 * i, 639); w[i] = LDNT((const u32x4*)(wmod + (size_t)row * 3840 + col0 + c * 8)); }
    for (int i = tid; i < 660; i += 512) sil[i] = i < 640 ? silu_bf(cvec[i]) : 0.f;
    if (tid < 80) {
      const float* pp = (const float*)(ws + OFF_MODP) + 1280 + sb * 80 + tid;
      float v = rbf((pp[0] + pp[2560]) + (pp[5120] + pp[7680]));
      if (sb >= 8) v = rbf(1.f + v);
      stc4(rs, (1920 + sb * 80 + tid) * 4, OFF_MOD, __float_as_uint(v));
    }
    BAR();
    float acc[8] = {0, 0, 0, 0, 0, 0, 0, 0};
#pragma unroll
    for (int i = 0; i < 13; ++i) {
      const float s = sil[rg + 50 * i];
#pragma unroll
      for (int e = 0; e < 4; ++e) { acc[2 * e] += s * bflo(w[i][e]); acc[2 * e + 1] += s * bfhi(w[i][e]); }
    }
    if (rg < 50) { *(f32x4*)(red + rg * 80 + c * 8) = (f32x4){acc[0], acc[1], acc[2], acc[3]}; *(f32x4*)(red + rg * 80 + c * 8 + 4) = (f32x4){acc[4], acc[5], acc[6], acc[7]}; }
    BAR();
    if (tid < 80) {
      float v = 0.f;
#pragma unroll
      for (int i = 0; i < 50; ++i) v += red[i * 80 + tid];
      stc4(rs, (col0 + tid) * 4, OFF_MOD, __float_as_uint(rbf(v)));
    }
  } else {
    const int mt = xcd * 2 + (idx / 15), nt = idx % 15;
    ctr = C_QKV + nt;
    if (wid < 4) {
      float* cst = (float*)(smem + 49152);
      const int c = lane & 7, rr = lane >> 3;
      const char* src = ws + OFF_XHAT + (size_t)(mt * 128 + wid * 32 + rr) * 1280 + c * 16;
      char* wdst = smem + ((c >> 1) * 4 + wid) * 1024 + (c & 1) * 512 + (rr ^ c) * 16;
      u32x4 a[10][4];
      f32x4 c0[4], c1[4];
#pragma unroll
      for (int i = 0; i < 4; ++i) { c0[i] = *(const f32x4*)(ws + OFF_MODP + i * 10240 + tid * 16); if (wid == 0) c1[i] = *(const f32x4*)(ws + OFF_MODP + i * 10240 + 4096 + tid * 16); }
#pragma unroll
      for (int s = 0; s < K1_W; ++s)
#pragma unroll
        for (int m = 0; m < 4; ++m) a[s][m] = *(const u32x4*)(src + m * 10240 + s * 128);
      {
        f32x4 v = (c0[0] + c0[1]) + (c0[2] + c0[3]); const float one = tid >= 160 ? 1.f : 0.f;
        *(f32x4*)(cst + tid * 4) = (f32x4){rbf(one + rbf(v[0])), rbf(one + rbf(v[1])), rbf(one + rbf(v[2])), rbf(one + rbf(v[3]))};
        if (wid == 0) { v = (c1[0] + c1[1]) + (c1[2] + c1[3]); *(f32x4*)(cst + 1024 + tid * 4) = (f32x4){rbf(1.f + rbf(v[0])), rbf(1.f + rbf(v[1])), rbf(1.f + rbf(v[2])), rbf(1.f + rbf(v[3]))}; }
      }
      BAR();
      const float* cp = cst + c * 8;
      f32x4 kc[10][4];
#define K1_KC(s) do { kc[s][0] = *(const f32x4*)(cp + (s) * 64); kc[s][1] = *(const f32x4*)(cp + (s) * 64 + 4); kc[s][2] = *(const f32x4*)(cp + (s) * 64 + 640); kc[s][3] = *(const f32x4*)(cp + (s) * 64 + 644); } while (0)
#pragma unroll
      for (int s = 0; s < 6; ++s) K1_KC(s);
      SCHED();
      u32x4 o[2][4];
#define K1_XF(s, m) (o[(s) & 1][m] = modulate8(a[s][m], kc[s][2], kc[s][3], kc[s][0], kc[s][1]))
#define K1_WR(s, m) (*(u32x4*)(wdst + ((s) % 3) * 16384 + (m) * 128) = o[(s) & 1][m])
#pragma unroll
      for (int m = 0; m < 4; ++m) { K1_XF(0, m); K1_WR(0, m); }
      BAR();
      TS(1);
#pragma unroll
      for (int m = 0; m < 4; ++m) a[K1_W][m] = *(const u32x4*)(src + m * 10240 + K1_W * 128);
#pragma unroll
      for (int m = 0; m < 4; ++m) K1_XF(1, m);
      SCHED();
#pragma unroll
      for (int s = 1; s < 10; ++s) {
#pragma unroll
        for (int m = 0; m < 4; ++m) {
          if (m == 0) K1_WR(s, 0);
          if (m < 3) K1_WR(s, m + 1);
          if (s + K1_W < 10) a[s + K1_W][m] = *(const u32x4*)(src + m * 10240 + (s + K1_W) * 128);
          if (s < 5) kc[s + 5][m] = *(const f32x4*)(cp + (s + 5) * 64 + (m & 1) * 4 + (m >> 1) * 640);
          SCHED();
          if (s < 9) K1_XF(s + 1, m);
          SCHED();
        }
        asm volatile("" :: "v"(o[s & 1][0]), "v"(o[s & 1][1]), "v"(o[s & 1][2]), "v"(o[s & 1][3]));
        BAR();
      }
    } else {
      const int cw = wid - 4;
      const char* bsrc = ws + OFF_WQKV + (size_t)(nt * 4 + cw) * 40 * 1024 + lane * 16;
      f32x16 acc[4];
      const int r = lane & 31, h = lane >> 5;
      if (nt >= 10) {
        k1_mma<true>(bsrc, smem, lane, acc);
        TS(2);
        const unsigned c = (nt - 10) * 128 + cw * 32 + r, head = c / 20, d = c % 20;
        const unsigned so = OFF_KV + (mt >> 1) * 20480 + 10240 + (mt & 1) * 5120, vo = head * 163840u + (h * 20 + d) * 16;
#pragma unroll
        for (int m = 0; m < 4; ++m)
#pragma unroll
          for (int s = 0; s < 2; ++s)
            stc16(rs, vo + s * 640, so + m * 1280, (u32x4){pack2(acc[m][8 * s], acc[m][8 * s + 1]), pack2(acc[m][8 * s + 2], acc[m][8 * s + 3]), pack2(acc[m][8 * s + 4], acc[m][8 * s + 5]), pack2(acc[m][8 * s + 6], acc[m][8 * s + 7])});
      } else {
        k1_mma<false>(bsrc, smem, lane, acc);
        TS(2);
        const bool isQ = nt < 5; const float sc = isQ ? QSCALE : 1.f;
        const unsigned so = isQ ? OFF_Q + mt * 5120 : OFF_KV + (mt >> 1) * 20480 + (mt & 1) * 5120, hs = isQ ? 81920u : 163840u;
        const int n5 = isQ ? nt : nt - 5;
        if (n5 < 4) {
          const unsigned vo = (2 * (n5 * 4 + cw) + h) * hs + r * 16;
#pragma unroll
          for (int m = 0; m < 4; ++m)
#pragma unroll
            for (int s = 0; s < 2; ++s)
              stc16(rs, vo + s * 512, so + m * 1280, (u32x4){pack2(acc[m][8 * s] * sc, acc[m][8 * s + 1] * sc), pack2(acc[m][8 * s + 2] * sc, acc[m][8 * s + 3] * sc),
                                                           pack2(acc[m][8 * s + 4] * sc, acc[m][8 * s + 5] * sc), pack2(acc[m][8 * s + 6] * sc, acc[m][8 * s + 7] * sc)});
        } else {
          const unsigned vo = (8 * cw + 4 * h) * hs + 1024 + r * 8;
#pragma unroll
          for (int m = 0; m < 4; ++m)
#pragma unroll
            for (int qd = 0; qd < 4; ++qd)
              stc8(rs, vo, so + qd * hs + m * 1280, (u32x2){pack2(acc[m][4 * qd] * sc, acc[m][4 * qd + 1] * sc), pack2(acc[m][4 * qd + 2] * sc, acc[m][4 * qd + 3] * sc)});
        }
      }
    }
  }
  TS(3);
  waitv<0>();
  BAR();
  TS(4);
  if (wid == 7 && lane < 8) __hip_atomic_fetch_add((GAS unsigned*)(ws + OFF_CNT + (lane * 16 + ctr) * 256), 1u, __ATOMIC_RELAXED, __HIP_MEMORY_SCOPE_AGENT);
}

template <bool FINAL> DI constexpr int proj_pos(int f) { return FINAL ? (f < 96 ? 65536 + f * 1024 : (f - 96) * 1024) : 17408 + (f % 20 >= 10 ? 81920 : 0) + (f / 20 * 10 + f % 10) * 1024; }
template <bool FINAL> DI u32x4 proj_ld(rsrc_t rs, int i, int wid, int lane) {
  constexpr int NF = FINAL ? 135 : 100;
  return ldc16(rs, lane * 16, (FINAL ? OFF_WOUT : OFF_WO) + (((blockIdx.x >> 3) & 7) * NF + min(i * 8 + wid, NF - 1)) * 1024);
}
template <bool FINAL> DI void proj_park(char* smem, int i, int wid, int lane, u32x4 v) {
  constexpr int NF = FINAL ? 135 : 100;
  *(u32x4*)(smem + proj_pos<FINAL>(min(i * 8 + wid, NF - 1)) + lane * 16) = v;
}

DI void split3(float m, unsigned& w_h0, unsigned& w_h1) {
  const float n = -m; const float a = rbf(n), b = rbf(n - a), c = rbf(n - a - b);
  w_h0 = pack2(a, b); w_h1 = pack2(c, 0.f);
}

DI void phase_attn(char* __restrict__ ws, rsrc_t rs, const char* __restrict__ wsp, const u16* __restrict__ win, const u16* __restrict__ wout, char* smem, int wid TARG) {
  const int tid = fresh_tid(), lane = tid & 63;
  const int xcd = blockIdx.x & 7, idx = blockIdx.x >> 3, head = xcd * 4 + (idx >> 3), qb = idx & 7;
  const int r = lane & 31, h = lane >> 5, qg = wid & 3, kh = wid >> 2;
  if (wid == 7) {
    const int l7 = lane & 7;
    const int c = l7 == 0 ? head >> 3 : l7 == 1 ? 4 : l7 == 2 ? 5 + (head >> 3) : l7 == 3 ? 9 : l7 == 4 ? 10 + (20 * head) / 128 : l7 == 5 ? 10 + (20 * head + 19) / 128 : C_MOD;
    poll_ge(ws, xcd * 16 + c, 16);
  }
  BAR();
  TS(5);
  unsigned kvo = OFF_KV + head * 163840, qo = OFF_Q + head * 81920 + (qb * 8 + qg * 2) * 1280;
  const unsigned kconst = h == 0 ? 0x3f803f80u : 0x00003f80u;
  const unsigned so16 = (tid & 255) * 16, l16 = lane * 16, l2 = 1024 + r * 8 + h * 4;
  const int blk = blockIdx.x;
  u32x4 wq[13];
  for (int attempt = 0; attempt < 2; ++attempt) {
    asm volatile("" : "+s"(kvo), "+s"(qo));
    const char* kvs = ws + kvo + kh * 81920 + so16;
    const unsigned ldst = lds_addr(smem) + kh * 81920 + qg * 1024;
    u32x4 qA1w, qB1w, k1w; unsigned qA2w, qB2w, k2, m0sv;
    asm volatile(
        "buffer_load_dwordx4 %0, %7, %9, %10 offen sc1\n\tbuffer_load_dwordx4 %1, %7, %9, %10 offen offset:1280 sc1\n\tbuffer_load_dwordx4 %2, %7, %9, %11 offen sc1\n\t"
        "buffer_load_dword %3, %8, %9, %10 offen sc1\n\tbuffer_load_dword %4, %8, %9, %10 offen offset:1280 sc1\n\tbuffer_load_dword %5, %8, %9, %11 offen sc1\n\t"
        "s_mov_b32 %6, m0\n\ts_mov_b32 m0, %17\n\ts_nop 0\n\tglobal_load_lds_dwordx4 %12, off sc1\n\t"
        "s_add_u32 m0, %18, 0x1000\n\ts_nop 0\n\tglobal_load_lds_dwordx4 %13, off sc1\n\t"
        "s_add_u32 m0, %18, 0x2000\n\ts_nop 0\n\tglobal_load_lds_dwordx4 %14, off sc1\n\t"
        "s_add_u32 m0, %18, 0x3000\n\ts_nop 0\n\tglobal_load_lds_dwordx4 %15, off sc1\n\t"
        "s_add_u32 m0, %18, 0x4000\n\ts_nop 0\n\tglobal_load_lds_dwordx4 %16, off sc1\n\t"
        "s_mov_b32 m0, %6\n\ts_waitcnt vmcnt(5)"
        : "=&v"(qA1w), "=&v"(qB1w), "=&v"(k1w), "=&v"(qA2w), "=&v"(qB2w), "=&v"(k2), "=&s"(m0sv)
        : "v"(l16), "v"(l2), "s"(rs), "s"(qo), "s"(kvo), "v"(kvs + (wid == 0 ? 4096 : 0)), "v"(kvs + 4096), "v"(kvs + 8192), "v"(kvs + 12288), "v"(kvs + 16384),
          "s"(ldst + (wid == 0 ? 4096 : 0)), "s"(ldst)
        : "memory", "scc");
    if (wid == 0) { const unsigned f = (tid & 1) ? 0u : 0x3f803f80u; *(u32x4*)(smem + l16) = (u32x4){f, f, f, f}; }
    const bf16x8 qA1 = BF8(qA1w), qB1 = BF8(qB1w);
    bf16x8 k1 = BF8(k1w);
    TS(30);
    float mA = -INFINITY, mB = -INFINITY;
    {
      const bf16x4 qA2 = BF4(((u32x2){qA2w, 0u})), qB2 = BF4(((u32x2){qB2w, 0u}));
      const int nt = attempt ? 64 : 1;
      bf16x8 t1 = k1; unsigned t2 = k2;
      for (int t = 0; t < nt; ++t) {
        if (t) { const unsigned to = kvo + (t >> 3) * 20480 + (t & 7) * 1280; t1 = BF8(ldc16(rs, l16, to)); t2 = ldc4(rs, l2, to); }
        const bf16x4 t2v = BF4(((u32x2){t2, kconst}));
        f32x16 a = MFMA32(t1, qA1, (f32x16){}); a = MFMA32K8(t2v, qA2, a);
        f32x16 b = MFMA32(t1, qB1, (f32x16){}); b = MFMA32K8(t2v, qB2, b);
#pragma unroll
        for (int i = 0; i < 16; ++i) { mA = fmaxf(mA, a[i]); mB = fmaxf(mB, b[i]); }
      }
      mA = fmaxf(mA, xhalf(mA)); mB = fmaxf(mB, xhalf(mB));
    }
    unsigned a0, a1, b0, b1; split3(mA, a0, a1); split3(mB, b0, b1);
    const bf16x4 qA2 = BF4(((u32x2){qA2w, h ? a1 : a0})), qB2 = BF4(((u32x2){qB2w, h ? b1 : b0}));
    char* lh = smem + kh * 81920;
    TS(31);
    waitv<0>();
    BAR();
    TS(6);
    f32x16 oA = (f32x16){}, oB = (f32x16){};
    const char* kp1 = lh + l16;
    const char* kp2 = lh + l2;
    const bool vreal = r < 20;
    const char* vp = vreal ? lh + 10240 + (h * 20 + r) * 16 : smem + (r == 20 ? 0 : 16);
    const int vinc = vreal ? 1280 : 0, vjump = vreal ? 10240 : 0;
    if (kh) { k1 = *(const bf16x8*)kp1; k2 = *(const unsigned*)kp2; }
    bf16x8 kf1[2], vf[2][2]; u32x2 kf2[2];
    kf1[0] = k1; kf2[0] = (u32x2){k2, kconst}; kf2[1] = (u32x2){0u, kconst};
    vf[1][0] = (bf16x8){}; vf[1][1] = (bf16x8){};
    u32x4 pA0, pA1, pB0 = (u32x4){}, pB1 = (u32x4){};
    f32x4 e0 = (f32x4){}, e1;
    f32x16 sA = MFMA32(kf1[0], qA1, (f32x16){}), sB;
    sA = MFMA32K8(BF4(kf2[0]), qA2, sA);
#define EX2(x) __builtin_amdgcn_exp2f(x)
#define GRP(MF, S, i, EN, EP, P, w) do { MF; SCHED(); EN[0] = EX2(S[i]); P[w] = pack2(EP[0], EP[1]); EN[1] = EX2(S[i + 1]); P[w + 1] = pack2(EP[2], EP[3]); \
      EN[2] = EX2(S[i + 2]); EN[3] = EX2(S[i + 3]); PIN(P); PIN(EN); SCHED(); } while (0)
    const unsigned relsc = lds_addr(smem) + 768;
    auto group = [&](auto FIRST, int jump, int j) __attribute__((always_inline)) {
#pragma unroll
      for (int t = 0; t < 8; ++t) {
        const int c = t & 1, n = c ^ 1;
        if (FIRST) { if (t == 0) __builtin_amdgcn_s_setprio(3); if (t == 3) __builtin_amdgcn_s_setprio(2); if (t == 5) __builtin_amdgcn_s_setprio(1); if (t == 6) __builtin_amdgcn_s_setprio(0); if (t == 7) __builtin_amdgcn_s_setprio(3); }
        else {
          if (t == 4 && j == 2) __builtin_amdgcn_s_setprio(2);
          if (t == 3 && j == 3) {
            __builtin_amdgcn_s_setprio(1);
#pragma unroll
            for (int i = 0; i < 13; ++i) wq[i] = proj_ld<false>(rs, i, wid, lane);
          }
          if (t == 6 && j == 3) { __builtin_amdgcn_s_setprio(0); if (wid == 7) glds4c(ws + OFF_CNT + (C_REL + (lane & 7)) * 256, relsc); }
        }
        if (FIRST && t < 5) {
#pragma unroll
          for (int i = 5 + 3 * t; i < 8 + 3 * t; ++i) glds16c(kvs + i * 4096, ldst + i * 4096);
        }
        if (t == 7) {
          if (FIRST) { waitv<0>(); BAR(); }
          kp1 += jump; kp2 += jump;
        }
        SCHED();
        kf1[n] = *(const bf16x8*)(kp1 + ((t + 1) & 7) * 1280); kf2[n][0] = *(const unsigned*)(kp2 + ((t + 1) & 7) * 1280);
        GRP(sB = MFMA32(kf1[c], qB1, (f32x16){}), sA, 0, e1, e0, pB1, 2);
        vf[c][0] = *(const bf16x8*)vp; vf[c][1] = *(const bf16x8*)(vp + 640); vp += vinc;
        GRP(sB = MFMA32K8(BF4(kf2[c]), qB2, sB), sA, 4, e0, e1, pA0, 0);
        GRP(oB = MFMA32(vf[n][0], BF8(pB0), oB), sA, 8, e1, e0, pA0, 2);
        GRP(oB = MFMA32(vf[n][1], BF8(pB1), oB), sA, 12, e0, e1, pA1, 0);
        GRP(sA = MFMA32(kf1[n], qA1, (f32x16){}), sB, 0, e1, e0, pA1, 2);
        GRP(sA = MFMA32K8(BF4(kf2[n]), qA2, sA), sB, 4, e0, e1, pB0, 0);
        GRP(oA = MFMA32(vf[c][0], BF8(pA0), oA), sB, 8, e1, e0, pB0, 2);
        GRP(oA = MFMA32(vf[c][1], BF8(pA1), oA), sB, 12, e0, e1, pB1, 0);
      }
      vp += vjump;
    };
    group(std::true_type{}, 20480, 0);
    TS(7);
    const int j2 = (blk - 135) * 4 + qg;
    const bool side = kh == 0 && attempt == 0 && j2 < 270;
    char* stile = smem + 1024 + qg * 4096;
    const char* svec = smem + 81920 + qg * 256;
    const int kt = blk < 135 ? 4 * (blk % 5) + qg : j2 / 10, ng = blk < 135 ? blk / 5 : j2 % 10;
    if (side) {
      const u16* W = blk < 135 ? win : wout; const int N = blk < 135 ? F_ : D_;
      const int j8 = lane >> 3;
#pragma unroll
      for (int i = 0; i < 4; ++i) {
        const int row = kt * 32 + i * 8 + j8;
        glds16(W + (size_t)row * N + ng * 64 + ((lane & 7) ^ j8) * 8, lds_addr(stile) + i * 1024);
      }
      glds4c((const float*)(ws + OFF_MOD) + (4 - h) * 640 + (kt < 20 ? kt : 0) * 32 + r, lds_addr(svec));
    }
#pragma nounroll
    for (int j = 1; j < 4; ++j) {
      if (j == 2) {
        if (kh) l2touch(wsp + ((size_t)(blk * 4 + qg) * 64 + lane) * 128, lds_addr(smem) + 256);
        else if (side) {
          waitv<0>();
          const int g4 = lane >> 4, r4 = lane & 15, row = 8 * g4 + (r4 >> 2);
          const f32x4 sv0 = *(const f32x4*)(svec + (r4 & 1) * 128 + g4 * 32), sv1 = *(const f32x4*)(svec + (r4 & 1) * 128 + g4 * 32 + 16);
          const bf16x8 vec = BF8(((u32x4){pack2(sv0[0], sv0[1]), pack2(sv0[2], sv0[3]), pack2(sv1[0], sv1[1]), pack2(sv1[2], sv1[3])}));
          const unsigned nks = blk < 135 ? 20 : 27, dso = (blk < 135 ? OFF_WIN : OFF_WOUT) + (ng * 4 * nks + kt) * 1024;
          bf16x8 fr[4]; f32x4 pr[4];
#pragma unroll
          for (int f = 0; f < 4; ++f) {
            const int ch = 2 * f + ((r4 & 3) >> 1);
            const bf16x4 lo = lds_tr(stile + row * 128 + (ch ^ (row & 7)) * 16 + (r4 & 1) * 8), hi = lds_tr(stile + (row + 4) * 128 + (ch ^ ((row + 4) & 7)) * 16 + (r4 & 1) * 8);
            fr[f] = __builtin_shufflevector(lo, hi, 0, 1, 2, 3, 4, 5, 6, 7);
          }
#pragma unroll
          for (int f = 0; f < 4; ++f) { stc16(rs, l16, dso + f * nks * 1024, __builtin_bit_cast(u32x4, fr[f])); pr[f] = MFMA16(fr[f], vec, (f32x4){}); }
          if (r4 < 2) {
#pragma unroll
            for (int f = 0; f < 4; ++f) *(f32x4*)(stile + 3072 + r4 * 256 + f * 64 + g4 * 16) = pr[f];
          }
        }
      }
      if (j == 3 && kh == 0 && attempt == 0) { waitv<0>(); arrive(ws, C_REL + xcd, lane); }
      group(std::false_type{}, j < 3 ? 20480 : 0, j);
    }
    TS(8);
    pB1[2] = pack2(e0[0], e0[1]); pB1[3] = pack2(e0[2], e0[3]);
    oB = MFMA32(vf[1][0], BF8(pB0), oB); oB = MFMA32(vf[1][1], BF8(pB1), oB);
    {
      f32x4* ex = (f32x4*)(lh + 1024 + qg * 4096) + lane;
      const f32x16 give = kh ? oA : oB;
#pragma unroll
      for (int g = 0; g < 3; ++g) ex[g * 64] = (f32x4){give[4 * g], give[4 * g + 1], give[4 * g + 2], give[4 * g + 3]};
    }
    int* flag = (int*)(smem + 528);
    BAR();
    TS(9);
    float o[12];
    {
      const f32x4* ex = (const f32x4*)(smem + (kh ^ 1) * 81920 + 1024 + qg * 4096) + lane;
      const f32x16 keep = kh ? oB : oA;
#pragma unroll
      for (int g = 0; g < 3; ++g) { const f32x4 t = ex[g * 64];
#pragma unroll
        for (int e = 0; e < 4; ++e) o[4 * g + e] = keep[4 * g + e] + t[e]; }
    }
    const float lo = xhalf(o[8]); const float l = h ? o[8] : lo;
    const float rl = __builtin_amdgcn_rcpf(l);
    const int tok = qb * 256 + qg * 64 + kh * 32 + r;
    const unsigned vo = (tok >> 4) * 20480 + (tok & 15) * 16;
    stc16(rs, vo + h * 256, OFF_O + (head >> 1) * 1024 + (head & 1) * 512,
          (u32x4){pack2(o[0] * rl, o[1] * rl), pack2(o[2] * rl, o[3] * rl), pack2(o[4] * rl, o[5] * rl), pack2(o[6] * rl, o[7] * rl)});
    if (h == 0) stc8(rs, vo, OFF_O + 16384 + (head >> 3) * 1024 + ((head >> 1) & 3) * 256 + (head & 1) * 8, (u32x2){pack2(o[8] * rl, o[9] * rl), pack2(o[10] * rl, o[11] * rl)});
    if (wid == 4 && blk < 135 && attempt == 0 && lane < 32) {
      const char* pp = smem + 1024 + 3072 + lane * 16;
      const f32x4 v = (*(const f32x4*)pp + *(const f32x4*)(pp + 4096)) + (*(const f32x4*)(pp + 8192) + *(const f32x4*)(pp + 12288));
      stc16(rs, (lane >> 4) * 8192 + (lane & 15) * 16, OFF_CBP + (blk % 5) * 16384 + (blk / 5) * 256, __builtin_bit_cast(u32x4, v));
    }
    bool ok = (l > 1e-30f) && (l < 1e30f);
#pragma unroll
    for (int i = 0; i < 12; ++i) ok = ok && (fabsf(o[i]) < 3e38f);
    if (!ok && attempt == 0) *flag = 1;
    waitv<0>();
    BAR();
    const int redo = __builtin_amdgcn_readfirstlane(*(volatile LAS int*)flag);
    if (!redo) break;
    BAR();
  }
  TS(10);
  if (wid == 7) arrive(ws, C_ATT + qb, lane);
#pragma unroll
  for (int i = 0; i < 13; ++i) proj_park<false>(smem, i, wid, lane, wq[i]);
  if (!__all(*(volatile LAS unsigned*)(smem + 768 + (lane & 7) * 4) >= 128u)) poll_ge(ws, C_REL + (lane & 7), 128);
}

template <bool FINAL, int KH, class HOOK>
DI void proj_body(char* __restrict__ ws, rsrc_t rs, const u16* __restrict__ x, u16* __restrict__ out, char* smem, int wid, u32x2 (&res)[3], HOOK&& after_mma TARG) {
  const int lane = fresh_tid() & 63;
  const int xcd = blockIdx.x & 7, idx = blockIdx.x >> 3, panel = xcd * 4 + (idx >> 3), cb = idx & 7;
  constexpr int NKS = FINAL ? 27 : 20;
  constexpr int K0 = KH ? (NKS + 1) / 2 : 0, NK = KH ? NKS / 2 : (NKS + 1) / 2, NKEEP = KH ? 2 : 3, UK = KH ? 3 : 0, UG = KH ? 0 : 3;
  const int m = wid & 3, mt = panel * 4 + m, g = lane >> 4, r = lane & 15;
  f32x4 gate[NKEEP], cbv[NKEEP];
#pragma unroll
  for (int i = 0; i < NKEEP; ++i) {
    const int n = (cb * 5 + UK + i) * 16;
    if (!FINAL) { res[i] = *(const u32x2*)(x + (size_t)(mt * 16 + r) * D_ + n + g * 4); cbv[i] = ldc16f(rs, g * 16, OFF_MOD + (4 * 640 + n) * 4); }
    gate[i] = ldc16f(rs, g * 16, OFF_MOD + ((FINAL ? 5 : 2) * 640 + n) * 4);
  }
  if (wid == 7) poll_ge(ws, FINAL ? C_U + panel : C_ATT + (panel >> 2), FINAL ? 8 : 32);
  waitv<0>();
  BAR();
  TS(FINAL ? 27 : 11);
  bf16x8 a[NK];
#pragma unroll
  for (int k = 0; k < NK; ++k) a[k] = BF8(ldc16(rs, lane * 16, (FINAL ? OFF_U : OFF_O) + (mt * NKS + K0 + k) * 1024));
  SCHED();
  constexpr int PD = FINAL ? 5 : 6;
  const char* bl = smem + lane * 16;
  bf16x8 bf[PD + 1][5]; f32x4 acc[5];
#pragma unroll
  for (int u = 0; u < 5; ++u) acc[u] = (f32x4){};
#pragma unroll
  for (int k = 0; k < PD; ++k)
#pragma unroll
    for (int u = 0; u < 5; ++u) bf[k][u] = *(const bf16x8*)(bl + proj_pos<FINAL>(u * NKS + K0 + k));
  SCHED();
#pragma unroll
  for (int k = 0; k < NK; ++k) {
#pragma unroll
    for (int u = 0; u < 5; ++u) {
      if (k + PD < NK) bf[(k + PD) % (PD + 1)][u] = *(const bf16x8*)(bl + proj_pos<FINAL>(u * NKS + K0 + k + PD));
      acc[u] = MFMA16(bf[k % (PD + 1)][u], a[k], acc[u]);
      SCHED();
    }
  }
  TS(FINAL ? 28 : 12);
  f32x4* ex = (f32x4*)(smem + (FINAL ? 40960 : 68608)) + m * 5 * 64 + lane;
#pragma unroll
  for (int i = 0; i < 5 - NKEEP; ++i) ex[(UG + i) * 64] = acc[UG + i];
  f32x4 part[NKEEP];
#pragma unroll
  for (int i = 0; i < NKEEP; ++i) {
    part[i] = (f32x4){bflo(res[i][0]), bfhi(res[i][0]), bflo(res[i][1]), bfhi(res[i][1])} + gate[i] * acc[UK + i];
    PIN(part[i]);
  }
  SCHED();
  after_mma();
  BAR();
  f32x4 oth[NKEEP];
#pragma unroll
  for (int i = 0; i < NKEEP; ++i) oth[i] = ex[(UK + i) * 64];
  f32x4 sv = (f32x4){}, qv = (f32x4){};
#pragma unroll
  for (int i = 0; i < NKEEP; ++i) {
    const f32x4 v = part[i] + gate[i] * oth[i];
    const u32x2 o = (u32x2){pack2(v[0], v[1]), pack2(v[2], v[3])};
    if (FINAL) *(u32x2*)(out + (size_t)(mt * 16 + r) * D_ + (cb * 5 + UK + i) * 16 + g * 4) = o;
    else {
      res[i] = o; PIN(res[i]);
      const f32x4 w = v * cbv[i]; const int un = cb * 5 + UK + i;
      stc8(rs, ((g >> 1) * 16 + r) * 16 + (g & 1) * 8, OFF_X1S + mt * 20480 + un * 512, (u32x2){pack2(w[0], w[1]), pack2(w[2], w[3])});
      sv += v; qv += v * v;
    }
  }
  if (!FINAL) {
    const float s = sum_g((sv[0] + sv[1]) + (sv[2] + sv[3])), q = sum_g((qv[0] + qv[1]) + (qv[2] + qv[3]));
    if (g == 0) stc8(rs, r * 8, OFF_ST1 + ((panel * 16 + cb * 2 + KH) * 64 + m * 16) * 8, (u32x2){__float_as_uint(s), __float_as_uint(q)});
    TS(13);
    waitv<0>();
    BAR();
    TS(14);
    if (wid == 7) arrive(ws, C_X1 + panel, lane);
  }
}

constexpr float GC1 = -2.8853900817779268f * 0.7978845608028654f, GC2 = GC1 * 0.044715f;
DI float gelu_s(float a, float k1, float k3, float den) {
  const float e = __builtin_amdgcn_exp2f(a * __builtin_fmaf(a * a, k3, k1));
  return a * __builtin_amdgcn_rcpf(__builtin_fmaf(e, den, den));
}
template <int I0> DI void gelu8(f32x16& a, const f32x4& ln, float& s, float& q) {
  float t[8];
#pragma unroll
  for (int i = 0; i < 8; ++i) t[i] = opq(a[I0 + i] * a[I0 + i]);
  SCHED();
#pragma unroll
  for (int i = 0; i < 8; ++i) t[i] = opq(__builtin_fmaf(t[i], ln[3], ln[2]));
  SCHED();
#pragma unroll
  for (int i = 0; i < 8; ++i) t[i] = opq(a[I0 + i] * t[i]);
  SCHED();
#pragma unroll
  for (int i = 0; i < 8; ++i) t[i] = opq(__builtin_amdgcn_exp2f(t[i]));
  SCHED();
#pragma unroll
  for (int i = 0; i < 8; ++i) t[i] = opq(__builtin_fmaf(t[i], ln[0], ln[0]));
  SCHED();
#pragma unroll
  for (int i = 0; i < 8; ++i) t[i] = opq(__builtin_amdgcn_rcpf(t[i]));
  SCHED();
#pragma unroll
  for (int i = 0; i < 8; ++i) t[i] = opq(a[I0 + i] * t[i]);
  SCHED();
  float s2 = opq(t[0] + t[1]), q2 = opq(t[0] * t[0]);
  s = opq(s + t[2]); q = opq(__builtin_fmaf(t[1], t[1], q));
#pragma unroll
  for (int i = 2; i < 8; i += 2) {
    q2 = opq(__builtin_fmaf(t[i], t[i], q2)); s2 = opq(s2 + t[i + 1]); q = opq(__builtin_fmaf(t[i + 1], t[i + 1], q));
    if (i < 6) s = opq(s + t[i + 2]);
  }
  s = opq(s + s2); q = opq(q + q2);
#pragma unroll
  for (int i = 0; i < 8; ++i) a[I0 + i] = t[i];
  SCHED();
}
constexpr int L4_Z1 = 114688, L4_TR = L4_Z1 + 16384, L4_LN = L4_TR + 16384, L4_ST2 = L4_LN + 1024, L4_FIN = L4_ST2 + 2048, L4_CB = L4_FIN + 512, L4_CNT = L4_CB + 2048;
constexpr int K5_R = 7, WIN_PRE = 28;
constexpr unsigned DEAD = 0x80000000u;
DI int sp_stage(int s) { const int idx = blockIdx.x >> 3; return (s + (idx & 7) * 2) & 15; }
DI unsigned sp_voff(int wid, int lane, int i) { return ((wid & 3) * 16 + i * 8 + (lane >> 3)) * 4096 + (wid >> 2) * 128 + ((lane & 7) ^ (4 * i + (lane >> 4))) * 16; }
DI const char* sp_base(const u16* wsp, int panel, int s) { return (const char*)wsp + (size_t)panel * 262144 + sp_stage(s) * 256; }
DI void sp_prefetch(u32x4 (&wr)[16 - K5_R][2], const u16* __restrict__ wsp, int panel, int wid, int lane) {
  const unsigned v0 = sp_voff(wid, lane, 0), v1 = sp_voff(wid, lane, 1);
#pragma unroll
  for (int s = K5_R; s < 16; ++s) { wr[s - K5_R][0] = *(const u32x4*)(sp_base(wsp, panel, s) + v0); wr[s - K5_R][1] = *(const u32x4*)(sp_base(wsp, panel, s) + v1); }
  asm volatile("" ::: "memory");
}
DI void sp_ring(const u16* __restrict__ wsp, char* smem, int panel, int wid, int lane) {
  const unsigned v0 = sp_voff(wid, lane, 0), v1 = sp_voff(wid, lane, 1);
#pragma unroll
  for (int s = 0; s < K5_R; ++s) { glds16(sp_base(wsp, panel, s) + v0, lds_addr(smem) + wid * 2048 + s * 16384); glds16(sp_base(wsp, panel, s) + v1, lds_addr(smem) + wid * 2048 + s * 16384 + 1024); }
}
DI unsigned win_boff(int wid, int lane) {
  const int cb = (blockIdx.x >> 3) & 7, u0 = unit0_of(cb), r = lane & 31, hi = r >> 4;
  const int u = wid < 3 ? 54 + u0 + 2 * wid + hi : wid == 3 ? (hi ? 0 : 54) + u0 + 6 : u0 + 2 * wid - 8 + hi;
  return u * 20480 + (lane >> 5) * 256 + (r & 15) * 16 + (wid == 3 && cb >= 6 ? DEAD : 0u);
}
#define WIN_LD(k) BF8(ldc16(rs, bo + ((k) & 7) * 512, OFF_WIN + ((k) >> 3) * 4096))
DI void stage_x1s(char* __restrict__ ws, char* smem, int panel, int wid, int tid) {
  const char* src = ws + OFF_X1S + (size_t)panel * 81920 + tid * 16;
#pragma unroll
  for (int i = 0; i < 10; ++i) glds16c(src + i * 8192, lds_addr(smem) + wid * 1024 + i * 8192);
}
DI void win_tail(char* __restrict__ ws, const u16* __restrict__ wsp, char* smem, int panel, int cb, int wid, int lane TARG) {
  BAR();
  TS(22);
  if (wid == 7 && lane < 4) __hip_atomic_fetch_add((GAS unsigned*)(ws + OFF_CNT + (C_Z2 + lane * 8 + cb) * 256), 1u, __ATOMIC_RELAXED, __HIP_MEMORY_SCOPE_AGENT);
}

template <bool ZA>
DI void win_body(char* __restrict__ ws, rsrc_t rs, const u16* __restrict__ wsp, char* smem, int wid, bf16x8 (&b)[40], u32x4 (&wr)[16 - K5_R][2] TARG) {
  const int tid = fresh_tid(), lane = tid & 63, r = lane & 31, h = lane >> 5;
  const int xcd = blockIdx.x & 7, idx = blockIdx.x >> 3, panel = xcd * 4 + (idx >> 3), cb = idx & 7, u0 = unit0_of(cb);
  const bool zb = wid < 3;
  const int la = 2 * (wid & 3), lb = wid == 3 ? 6 : la + 1;
  constexpr int PRE = ZA ? WIN_PRE : 0;
  float* lnp = (float*)(smem + L4_LN); float* st2 = (float*)(smem + L4_ST2); float* fin = (float*)(smem + L4_FIN); float* cbl = (float*)(smem + L4_CB);
  if (ZA) __builtin_amdgcn_s_setprio(1);
  const unsigned bo = win_boff(wid, lane);
  BAR();
  TS(15);
  stage_x1s(ws, smem, panel, wid, tid);
#pragma unroll
  for (int k = PRE; k < PRE + 4; ++k) b[k] = WIN_LD(k);
  waitv<4>();
  BAR();
  TS(16);
#pragma unroll
  for (int k = PRE + 4; k < 32; ++k) b[k] = WIN_LD(k);
  constexpr int PD = 3;
  const char* al = smem + (r >> 4) * 20480 + h * 256 + (r & 15) * 16;
  bf16x8 af[PD + 1][2]; f32x16 acc[2];
#pragma unroll
  for (int k = 0; k < PD; ++k) { af[k][0] = *(const bf16x8*)(al + k * 512); af[k][1] = *(const bf16x8*)(al + 40960 + k * 512); }
  f32x4 ln[2];
#pragma unroll
  for (int m = 0; m < 2; ++m) ln[m] = *(const f32x4*)(lnp + (m * 32 + r) * 4);
#pragma unroll
  for (int qd = 0; qd < 4; ++qd) {
    const int o = (qd < 2 ? (ZA ? 112 : 0) + la * 16 : (zb ? 112 : 0) + lb * 16) + 8 * (qd & 1) + 4 * h;
    const f32x4 cs = *(const f32x4*)(cbl + o), bi = *(const f32x4*)(cbl + 256 + o);
#pragma unroll
    for (int m = 0; m < 2; ++m)
#pragma unroll
      for (int e = 0; e < 4; ++e) { const float c0 = __builtin_fmaf(ln[m][1], cs[e], ln[m][0] * bi[e]); acc[m][4 * qd + e] = ZA ? c0 : opq(c0); }
  }
  SCHED();
  const unsigned wvo[2] = {sp_voff(wid, lane, 0), sp_voff(wid, lane, 1)};
#pragma unroll
  for (int k = 0; k < 40; ++k)
#pragma unroll
    for (int m = 0; m < 2; ++m) {
      if (k + PD < 40) af[(k + PD) % (PD + 1)][m] = *(const bf16x8*)(al + m * 40960 + (k + PD) * 512);
      acc[m] = MFMA32(b[k], af[k % (PD + 1)][m], acc[m]);
      if (k < 4) b[32 + 2 * k + m] = WIN_LD(32 + 2 * k + m);
      if (ZA && k >= 22 && m == 0) wr[(k - 22) >> 1][k & 1] = *(const u32x4*)(sp_base(wsp, panel, K5_R + ((k - 22) >> 1)) + wvo[k & 1]);
      SCHED();
    }
  TS(17);
#define WIN_Z(m, i) gelu_s(acc[m][i], ln[m][2], ln[m][3], ln[m][0])
#define WIN_Z1(m, qd) (*(u32x2*)(smem + L4_Z1 + ((qd) < 2 ? la : lb) * 2048 + (m) * 1024 + ((2 * ((qd) & 1) + h) * 16 + (r & 15)) * 16 + (r >> 4) * 8) = \
      (u32x2){pack2(WIN_Z(m, 4 * (qd)), WIN_Z(m, 4 * (qd) + 1)), pack2(WIN_Z(m, 4 * (qd) + 2), WIN_Z(m, 4 * (qd) + 3))})
  if (ZA) {
    float s[2] = {0.f, 0.f}, q[2] = {0.f, 0.f};
    gelu8<0>(acc[0], ln[0], s[0], q[0]); gelu8<0>(acc[1], ln[1], s[1], q[1]);
    if (zb) { gelu8<8>(acc[0], ln[0], s[0], q[0]); gelu8<8>(acc[1], ln[1], s[1], q[1]); }
#pragma unroll
    for (int m = 0; m < 2; ++m) {
      s[m] = opq(s[m] + xhalf(s[m])); q[m] = opq(q[m] + xhalf(q[m]));
      if (h == 0) *(f32x2*)(st2 + (wid * 64 + m * 32 + r) * 2) = (f32x2){s[m], q[m]};
    }
    asm volatile("" ::: "memory");
    if (lane == 0) __hip_atomic_fetch_add((int*)(smem + L4_CNT), 1, __ATOMIC_RELAXED, __HIP_MEMORY_SCOPE_WORKGROUP);
    asm volatile("" ::: "memory");
    __builtin_amdgcn_s_setprio(0);
    TS(18);
    if (!zb) { WIN_Z1(0, 2); WIN_Z1(0, 3); WIN_Z1(1, 2); WIN_Z1(1, 3); }
  } else {
    WIN_Z1(0, 0); WIN_Z1(0, 1); WIN_Z1(0, 2); WIN_Z1(0, 3); WIN_Z1(1, 0); WIN_Z1(1, 1); WIN_Z1(1, 2); WIN_Z1(1, 3);
  }
  TS(20);
  BAR();
  TS(21);
  if (ZA) {
    char* tr = smem + L4_TR + wid * 4096;
#pragma unroll
    for (int m = 0; m < 2; ++m) {
      const f32x2 f = *(const f32x2*)(fin + (m * 32 + r) * 2);
#pragma unroll
      for (int qd = 0; qd < 4; ++qd)
        *(u32x2*)(tr + (qd >> 1) * 2048 + (m * 32 + r) * 32 + (2 * (qd & 1) + h) * 8) =
            (u32x2){pack2(acc[m][4 * qd] * f[0] - f[1], acc[m][4 * qd + 1] * f[0] - f[1]), pack2(acc[m][4 * qd + 2] * f[0] - f[1], acc[m][4 * qd + 3] * f[0] - f[1])};
    }
    const char* p = tr + (8 * (lane >> 4) + ((lane & 15) >> 2)) * 32 + (lane & 3) * 8;
    bf16x4 lo[2][2], hi[2][2];
#pragma unroll
    for (int x = 0; x < 2; ++x)
#pragma unroll
      for (int ks = 0; ks < 2; ++ks) { lo[x][ks] = lds_tr(p + x * 2048 + ks * 1024); hi[x][ks] = lds_tr(p + x * 2048 + ks * 1024 + 128); }
#pragma unroll
    for (int x = 0; x < 2; ++x)
#pragma unroll
      for (int ks = 0; ks < 2; ++ks)
        stc16(rs, lane * 16 + x * 1024 + (zb || (x == 0 && cb < 6) ? 0u : DEAD), OFF_Z2 + ((panel * 2 + ks) * 54 + u0 + la) * 1024,
              __builtin_bit_cast(u32x4, (bf16x8)__builtin_shufflevector(lo[x][ks], hi[x][ks], 0, 1, 2, 3, 4, 5, 6, 7)));
    waitv<0>();
    win_tail(ws, wsp, smem, panel, cb, wid, lane TPASS);
    sp_ring(wsp, smem, panel, wid, lane);
  } else {
    __builtin_amdgcn_s_sleep(10);
    sp_ring(wsp, smem, panel, wid, lane);
    win_tail(ws, wsp, smem, panel, cb, wid, lane TPASS);
    sp_prefetch(wr, wsp, panel, wid, lane);
  }
}

DI void win_agent(char* __restrict__ ws, rsrc_t rs, const u16* __restrict__ wsp, char* smem, u32x4 (&wr)[16 - K5_R][2] TARG) {
  const int tid = fresh_tid(), lane = tid & 63, wid = 7;
  const int xcd = blockIdx.x & 7, idx = blockIdx.x >> 3, panel = xcd * 4 + (idx >> 3), cb = idx & 7, u0 = unit0_of(cb);
  float* lnp = (float*)(smem + L4_LN); float* st2 = (float*)(smem + L4_ST2); float* fin = (float*)(smem + L4_FIN); float* cbl = (float*)(smem + L4_CB);
  int* cnt = (int*)(smem + L4_CNT);
  __builtin_amdgcn_s_setprio(2);
  if (lane == 0) *cnt = 0;
  poll_ge(ws, lane < 8 ? C_ATT + lane : C_X1 + panel, lane < 8 ? 32 : 8);
  BAR();
  stage_x1s(ws, smem, panel, wid, tid);
  {
    u32x2 t[16]; f32x4 cp[5][2];
#pragma unroll
    for (int i = 0; i < 16; ++i) t[i] = __builtin_amdgcn_raw_buffer_load_b64(rs, lane * 8, OFF_ST1 + (panel * 16 + i) * 512, 16);
    const unsigned co = lane < 28 ? (u0 * 16 + lane * 4) * 4 : ((54 + u0) * 16 + min(lane - 28, 27) * 4) * 4;
#pragma unroll
    for (int i = 0; i < 5; ++i) { cp[i][0] = ldc16f(rs, co, OFF_CBP + i * 16384); cp[i][1] = ldc16f(rs, co, OFF_CBP + i * 16384 + 8192); }
    float s = 0.f, q = 0.f;
#pragma unroll
    for (int i = 0; i < 16; ++i) { s += __uint_as_float(t[i][0]); q += __uint_as_float(t[i][1]); }
    const float mean = s * (1.f / 640.f), var = fmaxf(q * (1.f / 640.f) - mean * mean, 0.f);
    const float den = ln_den(var), ri = 1.f / den;
    *(f32x4*)(lnp + lane * 4) = (f32x4){den, -rbf(mean), GC1 * ri, GC2 * ri * ri * ri};
#pragma unroll
    for (int v = 0; v < 2; ++v) *(f32x4*)(cbl + v * 256 + lane * 4) = ((cp[0][v] + cp[1][v]) + (cp[2][v] + cp[3][v])) + cp[4][v];
  }
  waitv<0>();
  BAR();
  __builtin_amdgcn_s_setprio(0);
  __builtin_amdgcn_s_sleep(32);
  sp_prefetch(wr, wsp, panel, wid, lane);
  __builtin_amdgcn_s_setprio(2);
#pragma nounroll
  for (unsigned spin = 0; spin < (1u << 24); ++spin) {
    if (__hip_atomic_load(cnt, __ATOMIC_RELAXED, __HIP_MEMORY_SCOPE_WORKGROUP) >= 4) break;
    __builtin_amdgcn_s_sleep(1);
  }
  asm volatile("" ::: "memory");
  float s, q;
  {
    const f32x2 t0 = *(const f32x2*)(st2 + lane * 2), t1 = *(const f32x2*)(st2 + (64 + lane) * 2), t2 = *(const f32x2*)(st2 + (128 + lane) * 2), t3 = *(const f32x2*)(st2 + (192 + lane) * 2);
    s = (t0[0] + t1[0]) + t2[0]; q = (t0[1] + t1[1]) + t2[1];
    if (cb < 6) { s += t3[0]; q += t3[1]; }
  }
  GAS u64* xch = (GAS u64*)(ws + OFF_XCH) + panel * 512;
  unsigned qb = __float_as_uint(q); if (qb == ~0u) qb = 0x7fc00000u;
  __hip_atomic_store(xch + cb * 64 + lane, ((u64)qb << 32) | __float_as_uint(s), __ATOMIC_RELAXED, __HIP_MEMORY_SCOPE_AGENT);
  TS(19);
  u64 v[8];
#pragma nounroll
  for (unsigned spin = 0; spin < (1u << 21); ++spin) {
    bool ok = true;
#pragma unroll
    for (int i = 0; i < 8; ++i) { v[i] = __hip_atomic_load(xch + i * 64 + lane, __ATOMIC_RELAXED, __HIP_MEMORY_SCOPE_AGENT); ok = ok && ((unsigned)(v[i] >> 32) != ~0u); }
    if (__all(ok)) break;
    __builtin_amdgcn_s_sleep(1);
  }
  s = 0.f; q = 0.f;
#pragma unroll
  for (int i = 0; i < 8; ++i) { s += __uint_as_float((unsigned)v[i]); q += __uint_as_float((unsigned)(v[i] >> 32)); }
  const float mean = s * (1.f / 864.f), var = fmaxf(q * (1.f / 864.f) - mean * mean, 0.f);
  const float ri = 1.f / ln_den(var);
  *(f32x2*)(fin + lane * 2) = (f32x2){ri, rbf(mean) * ri};
  __builtin_amdgcn_s_setprio(0);
  TS(20);
  BAR();
  __builtin_amdgcn_s_sleep(10);
  sp_ring(wsp, smem, panel, wid, lane);
  win_tail(ws, wsp, smem, panel, cb, wid, lane TPASS);
}

#define SP_LB 4
DI void phase_sp(char* __restrict__ ws, rsrc_t rs, const u16* __restrict__ bsp, char* smem, int wid, const u32x4 (&wr)[16 - K5_R][2] TARG) {
  const int tid = fresh_tid(), lane = tid & 63;
  const int xcd = blockIdx.x & 7, idx = blockIdx.x >> 3, panel = xcd * 4 + (idx >> 3), cb = idx & 7;
  const int kh = wid >> 2, up = wid & 3, u0 = unit0_of(cb) + 2 * up, nu = cb < 6 ? 7 : 6, g = lane >> 4, r = lane & 15;
  const unsigned dead[2] = {2 * up < nu ? 0u : DEAD, 2 * up + 1 < nu ? 0u : DEAD};
  const unsigned bvo[2] = {lane * 16 + dead[0], lane * 16 + 1024 + dead[1]};
  u16 bb[2];
#pragma unroll
  for (int j = 0; j < 2; ++j) bb[j] = bsp[panel * 64 + (2 * kh + j) * 16 + r];
  const char* al[2][2];
#pragma unroll
  for (int k = 0; k < 2; ++k)
#pragma unroll
    for (int j = 0; j < 2; ++j) al[k][j] = smem + kh * 8192 + (j ^ kh) * 4096 + r * 128 + ((4 * k + g) ^ (r >> 1)) * 16;
  constexpr int LB = SP_LB, PD = 3;
  bf16x8 bq[LB + 1][4], af[PD + 1][2]; f32x4 acc[2][4]; u32x4 wq[17];
  if (wid == 7) poll_ge(ws, C_Z2 + (panel >> 3) * 8 + cb, 32);
#pragma unroll
  for (int i = 0; i < 2; ++i) {
    const float b0 = bf2f(bb[0]), b1 = bf2f(bb[1]);
    acc[i][0] = (f32x4){b0, b0, b0, b0}; acc[i][1] = (f32x4){b1, b1, b1, b1}; acc[i][2] = (f32x4){}; acc[i][3] = (f32x4){};
  }
  if (kh && up < 3) waitv<2 * (16 - K5_R)>(); else waitv<0>();
  BAR();
  TS(23);
  const unsigned bso = OFF_Z2 + (kh * 2 * 54 + u0) * 1024;
#define SP_LD(s, j) (bq[(s) % (LB + 1)][j] = BF8(ldc16(rs, bvo[(j) & 1], bso + (sp_stage(s) * 4 + ((j) >> 1)) * 55296)))
#define SP_RD(t, k) (af[(t) % (PD + 1)][k] = *(const bf16x8*)(al[k][((t) >> 1) & 1] + (((t) >> 2) % K5_R) * 16384 + ((t) & 1) * 2048))
#pragma unroll
  for (int s = 0; s < LB; ++s)
#pragma unroll
    for (int j = 0; j < 4; ++j) SP_LD(s, j);
#pragma unroll
  for (int t = 0; t < PD; ++t) { SP_RD(t, 0); SP_RD(t, 1); }
  SCHED();
  sfor<64>([&](auto T) __attribute__((always_inline)) {
    constexpr int t = T, nx = t + PD, s = t >> 2, m = t & 3, tl = t + 4 * LB;
    if constexpr (nx == 16 || nx == 28 || nx == 44 || nx == 56) { BAR(); TS(32 + (nx == 28) + 2 * (nx == 44) + 3 * (nx == 56)); }
    constexpr int c = nx >= 16 && nx < 24 ? nx - 16 : nx >= 28 && nx < 34 ? nx - 20 : nx >= 44 && nx < 48 ? nx - 30 : -1;
    if constexpr (nx < 64) SP_RD(nx, 0);
    acc[0][m] = MFMA16(bq[s % (LB + 1)][0], af[t % (PD + 1)][0], acc[0][m]);
    acc[0][m] = MFMA16(bq[s % (LB + 1)][2], af[t % (PD + 1)][1], acc[0][m]); PIN(acc[0][m]);
    if constexpr (tl < 64) SP_LD(tl >> 2, m); else if constexpr (tl - 64 < 17) wq[tl - 64] = proj_ld<true>(rs, tl - 64, wid, lane);
    SCHED();
    if constexpr (nx < 64) SP_RD(nx, 1);
    acc[1][m] = MFMA16(bq[s % (LB + 1)][1], af[t % (PD + 1)][0], acc[1][m]);
    acc[1][m] = MFMA16(bq[s % (LB + 1)][3], af[t % (PD + 1)][1], acc[1][m]); PIN(acc[1][m]);
    if constexpr (c >= 0) *(u32x4*)(smem + ((c >> 1) % K5_R) * 16384 + wid * 2048 + (c & 1) * 1024 + lane * 16) = wr[c >> 1][c & 1];
    if constexpr (t == 63 && 4 * LB < 17) { _Pragma("unroll") for (int i = 4 * LB; i < 17; ++i) wq[i] = proj_ld<true>(rs, i, wid, lane); }
    SCHED();
  });
  TS(24);
  f32x4* exw = (f32x4*)(smem + 32768 + wid * 4096) + lane;
  const f32x4* exr = (const f32x4*)(smem + 32768 + (wid ^ 4) * 4096) + lane;
  u32x4 z[2];
#pragma unroll
  for (int i = 0; i < 2; ++i) z[i] = *(const u32x4*)(smem + L4_Z1 + (2 * up + i) * 2048 + kh * 1024 + lane * 16);
#pragma unroll
  for (int i = 0; i < 2; ++i)
#pragma unroll
    for (int j = 0; j < 2; ++j) exw[(i * 2 + j) * 64] = acc[i][2 + j];
  f32x4 zf[2][2];
#pragma unroll
  for (int i = 0; i < 2; ++i)
#pragma unroll
    for (int j = 0; j < 2; ++j) {
      zf[i][j] = (f32x4){bflo(z[i][2 * j]), bfhi(z[i][2 * j]), bflo(z[i][2 * j + 1]), bfhi(z[i][2 * j + 1])};
      acc[i][j] *= zf[i][j]; PIN(zf[i][j]); PIN(acc[i][j]);
    }
  BAR();
  f32x4 oth[2][2];
#pragma unroll
  for (int i = 0; i < 2; ++i)
#pragma unroll
    for (int j = 0; j < 2; ++j) oth[i][j] = exr[(i * 2 + j) * 64];
#pragma unroll
  for (int i = 0; i < 2; ++i)
#pragma unroll
    for (int j = 0; j < 2; ++j) {
      const f32x4 v = acc[i][j] + zf[i][j] * oth[i][j];
      stc8(rs, ((g >> 1) * 16 + r) * 16 + (g & 1) * 8 + dead[i], OFF_U + (panel * 4 + 2 * kh + j) * 27648 + (u0 + i) * 512, (u32x2){pack2(v[0], v[1]), pack2(v[2], v[3])});
    }
  TS(25);
  waitv<0>();
  BAR();
  TS(26);
  if (wid == 7) arrive(ws, C_U + panel, lane);
#pragma unroll
  for (int i = 0; i < 17; ++i) proj_park<true>(smem, i, wid, lane, wq[i]);
}

__global__ __launch_bounds__(512, 2) OCC2 void k_mega(char* __restrict__ ws, const u16* __restrict__ x, const u16* __restrict__ win,
                                                     const u16* __restrict__ wsp, const u16* __restrict__ bsp, const u16* __restrict__ wout,
                                                     const u16* __restrict__ cvec, const u16* __restrict__ wmod, u16* __restrict__ out) {
  extern __shared__ __attribute__((aligned(16))) char smem[];
  const int wid = __builtin_amdgcn_readfirstlane(threadIdx.x >> 6);
  const rsrc_t rs = __builtin_amdgcn_make_buffer_rsrc(ws, 0, WS_USED, 0x00020000);
  TDECL; TS(0);
  u32x2 res[3];
  phase_qkv(ws, rs, cvec, wmod, smem, wid TPASS);
  phase_attn(ws, rs, (const char*)wsp, win, wout, smem, wid TPASS);
  u32x4 wr[16 - K5_R][2];
  auto nohook = []() __attribute__((always_inline)) {};
  if (wid < 4) {
    bf16x8 b[40];
    proj_body<false, 0>(ws, rs, x, out, smem, wid, res, [&]() __attribute__((always_inline)) {
      const unsigned bo = win_boff(wid, fresh_tid() & 63);
#pragma unroll
      for (int k = 0; k < WIN_PRE; ++k) b[k] = WIN_LD(k);
    } TPASS);
    win_body<true>(ws, rs, wsp, smem, wid, b, wr TPASS);
  } else {
    proj_body<false, 1>(ws, rs, x, out, smem, wid, res, nohook TPASS);
    bf16x8 b[40];
    if (wid < 7) win_body<false>(ws, rs, wsp, smem, wid, b, wr TPASS);
    else win_agent(ws, rs, wsp, smem, wr TPASS);
  }
  phase_sp(ws, rs, bsp, smem, wid, wr TPASS);
  if (wid < 4) proj_body<true, 0>(ws, rs, x, out, smem, wid, res, nohook TPASS);
  else proj_body<true, 1>(ws, rs, x, out, smem, wid, res, nohook TPASS);
  TS(29);
  TDUMP();
}

extern "C" void kernel_launch(void* const* d_in, const int* in_sizes, int n_in, void* d_out, int out_size, void* d_ws, size_t ws_size,
                              hipStream_t stream) {
  const u16* x = (const u16*)d_in[0];
  const u16* wqkv = (const u16*)d_in[1];
  const u16* wo = (const u16*)d_in[2];
  const u16* win = (const u16*)d_in[4];
  const u16* wsp = (const u16*)d_in[5];
  const u16* bsp = (const u16*)d_in[6];
  const u16* wout = (const u16*)d_in[7];
  const u16* cvec = (const u16*)d_in[8];
  const u16* wmod = (const u16*)d_in[9];
  char* ws = (char*)d_ws;
  (void)hipFuncSetAttribute((const void*)k_mega, hipFuncAttributeMaxDynamicSharedMemorySize, 163840);
  k0_prep<<<K0_GRID, 256, 0, stream>>>(x, wqkv, wo, cvec, wmod, ws);
  k_mega<<<256, 512, 163840, stream>>>(ws, x, win, wsp, bsp, wout, cvec, wmod, (u16*)d_out);
}
```

```cpp
#include <hip/hip_runtime.h>
#include <hip/hip_bf16.h>
#include <stdint.h>
#include <utility>
#define W_PAGE 1

typedef __attribute__((ext_vector_type(8))) short bf16x8;
typedef __attribute__((ext_vector_type(4))) short bf16x4;
typedef __attribute__((ext_vector_type(2))) float f32x2;
typedef __attribute__((ext_vector_type(4))) float f32x4;
typedef __attribute__((ext_vector_type(16))) float f32x16;
typedef __attribute__((ext_vector_type(2))) unsigned u32x2;
typedef __attribute__((ext_vector_type(4))) unsigned u32x4;
typedef __attribute__((ext_vector_type(2))) __bf16 bf2_t;
typedef __attribute__((ext_vector_type(2))) _Float16 h2_t;
typedef unsigned short u16;
typedef unsigned long long u64;
#define LAS __attribute__((address_space(3)))
#define GAS __attribute__((address_space(1)))
#define DI __device__ __forceinline__

constexpr int D_ = 640, NQKV = 1920, F_ = 1728;

constexpr unsigned OFF_CNT = 0;
constexpr unsigned OFF_MOD = 65536;
constexpr unsigned OFF_MODP = OFF_MOD + 16384;
constexpr unsigned OFF_CBP = OFF_MODP + 40960;
constexpr unsigned OFF_XCH = OFF_CBP + 5 * 16384;
constexpr unsigned OFF_ST1 = OFF_XCH + 131072;
constexpr unsigned OFF_XHAT = OFF_ST1 + 262144;
constexpr unsigned OFF_WQKV = OFF_XHAT + 2621440;
constexpr unsigned OFF_WO = OFF_WQKV + 2457600;
constexpr unsigned OFF_WIN = OFF_WO + 819200;
constexpr unsigned OFF_WOUT = OFF_WIN + 2211840;
constexpr unsigned OFF_Q = OFF_WOUT + 1105920;
constexpr unsigned OFF_KV = OFF_Q + 32 * 81920;
constexpr unsigned OFF_O = OFF_KV + 32 * 163840;
constexpr unsigned OFF_X1S = OFF_O + 2621440;
constexpr unsigned OFF_Z2 = OFF_X1S + 2621440;
constexpr unsigned OFF_U = OFF_Z2 + 3538944;
constexpr unsigned WS_USED = OFF_U + 3538944;
constexpr int C_QKV = 0, C_MOD = 15  , C_ATT = 128, C_REL = 136, C_X1 = 144, C_U = 176, C_Z2 = 208  ;

#define STAMP(kid, k)
#define TDECL
#define TS(k)
#define TDUMP()
DI unsigned pack2(float lo, float hi) { return __builtin_bit_cast(unsigned, __builtin_convertvector((f32x2){lo, hi}, bf2_t)); }
DI unsigned pack2s(float lo, float hi) { unsigned r; asm("v_cvt_pk_bf16_f32 %0, %1, %2" : "=v"(r) : "v"(lo), "v"(hi)); return r; }
DI float opq(float x) { asm("" : "+v"(x)); return x; }
DI unsigned pack2h(float lo, float hi) { return __builtin_bit_cast(unsigned, __builtin_convertvector((f32x2){lo, hi}, h2_t)); }
DI float bflo(unsigned u) { return __uint_as_float(u << 16); }
DI float bfhi(unsigned u) { return __uint_as_float(u & 0xffff0000u); }
DI float rbf(float x) { return bflo(pack2(x, x)); }
DI float bf2f(u16 b) { return __uint_as_float((unsigned)b << 16); }
#define MFMA32(a, b, c) __builtin_amdgcn_mfma_f32_32x32x16_bf16((a), (b), (c), 0, 0, 0)
#define MFMA32K8(a, b, c) __builtin_amdgcn_mfma_f32_32x32x8bf16_1k((a), (b), (c), 0, 0, 0)
#define MFMA16(a, b, c) __builtin_amdgcn_mfma_f32_16x16x32_bf16((a), (b), (c), 0, 0, 0)
#define BAR() asm volatile("s_waitcnt lgkmcnt(0)\n\ts_barrier" ::: "memory")
#define SCHED() __builtin_amdgcn_sched_barrier(0)
#define PIN(x) asm volatile("" : "+v"(x))
#define OCC2 __attribute__((amdgpu_waves_per_eu(2, 2)))
#define BF8(x) __builtin_bit_cast(bf16x8, x)
#define BF4(x) __builtin_bit_cast(bf16x4, x)
template <int N> DI void waitv() { asm volatile("s_waitcnt vmcnt(%0)" ::"i"(N) : "memory"); }
template <class F, int... I> DI void sfor_impl(F&& f, std::integer_sequence<int, I...>) { (f(std::integral_constant<int, I>{}), ...); }
template <int N, class F> DI void sfor(F&& f) { sfor_impl(f, std::make_integer_sequence<int, N>{}); }
typedef short v4i16_t __attribute__((ext_vector_type(4)));
DI bf16x4 lds_tr(const char* p) { return __builtin_bit_cast(bf16x4, __builtin_amdgcn_ds_read_tr16_b64_v4i16((LAS v4i16_t*)p)); }
DI void glds16(const void* g, unsigned lds_base) {
  unsigned sv;
  asm volatile("s_mov_b32 %0, m0\n\ts_mov_b32 m0, %2\n\ts_nop 0\n\tglobal_load_lds_dwordx4 %1, off\n\ts_mov_b32 m0, %0" : "=&s"(sv) : "v"(g), "s"(lds_base) : "memory");
}
DI void glds16c(const void* g, unsigned lds_base) {
  unsigned sv;
  asm volatile("s_mov_b32 %0, m0\n\ts_mov_b32 m0, %2\n\ts_nop 0\n\tglobal_load_lds_dwordx4 %1, off sc1\n\ts_mov_b32 m0, %0" : "=&s"(sv) : "v"(g), "s"(lds_base) : "memory");
}
DI void glds4c(const void* g, unsigned lds_base) {
  unsigned sv;
  asm volatile("s_mov_b32 %0, m0\n\ts_mov_b32 m0, %2\n\ts_nop 0\n\tglobal_load_lds_dword %1, off sc1\n\ts_mov_b32 m0, %0" : "=&s"(sv) : "v"(g), "s"(lds_base) : "memory");
}
typedef __amdgpu_buffer_rsrc_t rsrc_t;
DI u32x4 ldc16(rsrc_t rs, unsigned voff, unsigned soff) { return __builtin_amdgcn_raw_buffer_load_b128(rs, voff, soff, 16); }
DI unsigned ldc4(rsrc_t rs, unsigned voff, unsigned soff) { return __builtin_amdgcn_raw_buffer_load_b32(rs, voff, soff, 16); }
DI void stc16(rsrc_t rs, unsigned voff, unsigned soff, u32x4 v) { __builtin_amdgcn_raw_buffer_store_b128(v, rs, voff, soff, 16); asm volatile("s_nop 1" ::"v"(v) : "memory"); }
DI void stc8(rsrc_t rs, unsigned voff, unsigned soff, u32x2 v) { __builtin_amdgcn_raw_buffer_store_b64(v, rs, voff, soff, 16); }
DI void stc4(rsrc_t rs, unsigned voff, unsigned soff, unsigned v) { __builtin_amdgcn_raw_buffer_store_b32(v, rs, voff, soff, 16); }
DI f32x4 ldc16f(rsrc_t rs, unsigned voff, unsigned soff) { return __builtin_bit_cast(f32x4, ldc16(rs, voff, soff)); }
DI void arrive(char* ws, int c, int lane) { if (lane == 0) __hip_atomic_fetch_add((GAS unsigned*)(ws + OFF_CNT + c * 256), 1u, __ATOMIC_RELAXED, __HIP_MEMORY_SCOPE_AGENT); }
DI void poll_ge(char* ws, int c, unsigned thr) {
  const GAS unsigned* p = (const GAS unsigned*)(ws + OFF_CNT + c * 256);
#pragma nounroll
  for (unsigned spin = 0; spin < (1u << 21); ++spin) {
    if (__all(__hip_atomic_load(p, __ATOMIC_RELAXED, __HIP_MEMORY_SCOPE_AGENT) >= thr)) break;
    __builtin_amdgcn_s_sleep(1);
  }
  asm volatile("" ::: "memory");
}
DI int fresh_tid() { int t = threadIdx.x; asm volatile("" : "+v"(t)); return t; }
DI void l2touch(const void* g, unsigned lds_scratch) {
  unsigned sv;
  asm volatile("s_mov_b32 %0, m0\n\ts_mov_b32 m0, %2\n\ts_nop 0\n\tglobal_load_lds_dword %1, off\n\ts_mov_b32 m0, %0" : "=&s"(sv) : "v"(g), "s"(lds_scratch) : "memory");
}
#define LDNT(p) __builtin_nontemporal_load(p)
DI float silu_bf(u16 c) { const float f = __uint_as_float((unsigned)c << 16); const float v = f / (1.f + __expf(-f)); return __uint_as_float(__builtin_bit_cast(unsigned, __builtin_convertvector((f32x2){v, v}, bf2_t)) << 16); }
DI unsigned lds_addr(const void* p) { return (unsigned)(uintptr_t)(const LAS void*)p; }
DI float xhalf(float v) {
  auto rr = __builtin_amdgcn_permlane32_swap(__float_as_uint(v), __float_as_uint(v), false, false);
  return __uint_as_float((threadIdx.x & 32) ? rr[0] : rr[1]);
}
DI float sum_g(float v) {
  auto a = __builtin_amdgcn_permlane16_swap(__float_as_uint(v), __float_as_uint(v), false, false);
  v = __uint_as_float(a[0]) + __uint_as_float(a[1]);
  auto b = __builtin_amdgcn_permlane32_swap(__float_as_uint(v), __float_as_uint(v), false, false);
  return __uint_as_float(b[0]) + __uint_as_float(b[1]);
}
template <int CTRL> DI float dpp_add(float v) { return v + __uint_as_float(__builtin_amdgcn_update_dpp(0u, __float_as_uint(v), CTRL, 0xf, 0xf, true)); }
DI float sum16(float v) {
  v = dpp_add<0x128>(v); v = dpp_add<0x124>(v); v = dpp_add<0x122>(v); return dpp_add<0x121>(v);
}
DI u32x4 modulate8(u32x4 v, f32x4 sc0, f32x4 sc1, f32x4 sh0, f32x4 sh1) {
  unsigned o0, o1, o2, o3; float t0, t1, t2, t3;
  asm volatile(
      "v_fma_mix_f32 %0, %8, %12, %20 op_sel_hi:[1,0,0]\n\tv_fma_mix_f32 %4, %8, %13, %21 op_sel:[1,0,0] op_sel_hi:[1,0,0]\n\t"
      "v_fma_mix_f32 %1, %9, %14, %22 op_sel_hi:[1,0,0]\n\tv_fma_mix_f32 %5, %9, %15, %23 op_sel:[1,0,0] op_sel_hi:[1,0,0]\n\t"
      "v_fma_mix_f32 %2, %10, %16, %24 op_sel_hi:[1,0,0]\n\tv_fma_mix_f32 %6, %10, %17, %25 op_sel:[1,0,0] op_sel_hi:[1,0,0]\n\t"
      "v_fma_mix_f32 %3, %11, %18, %26 op_sel_hi:[1,0,0]\n\tv_fma_mix_f32 %7, %11, %19, %27 op_sel:[1,0,0] op_sel_hi:[1,0,0]\n\t"
      "v_cvt_pk_bf16_f32 %0, %0, %4\n\tv_cvt_pk_bf16_f32 %1, %1, %5\n\tv_cvt_pk_bf16_f32 %2, %2, %6\n\tv_cvt_pk_bf16_f32 %3, %3, %7"
      : "=&v"(o0), "=&v"(o1), "=&v"(o2), "=&v"(o3), "=&v"(t0), "=&v"(t1), "=&v"(t2), "=&v"(t3)
      : "v"(v[0]), "v"(v[1]), "v"(v[2]), "v"(v[3]), "v"(sc0[0]), "v"(sc0[1]), "v"(sc0[2]), "v"(sc0[3]), "v"(sc1[0]), "v"(sc1[1]), "v"(sc1[2]), "v"(sc1[3]),
        "v"(sh0[0]), "v"(sh0[1]), "v"(sh0[2]), "v"(sh0[3]), "v"(sh1[0]), "v"(sh1[1]), "v"(sh1[2]), "v"(sh1[3]));
  return (u32x4){o0, o1, o2, o3};
}
DI float ln_den(float var) { return rbf(__builtin_sqrtf(rbf(rbf(var) + 1e-5f))); }
DI unsigned a16_off(int n, int r) { return (n >> 5) * 1024 + (((n & 31) >> 3) * 16 + r) * 16 + (n & 4) * 2; }
DI int unit0_of(int j) { return j < 6 ? 7 * j : 42 + 6 * (j - 6); }

DI void K0ST(char* p, u32x4 v) { *(u32x4*)p = v; }
constexpr int K0_MOD = 160, K0_LN = 128, K0_RL = 240, K0_WO = 50, K0_INIT = 12, K0_WARM = 32;
constexpr int K0_GRID = K0_WARM + K0_MOD + K0_LN + K0_RL + K0_WO + K0_INIT;

__global__ __launch_bounds__(256) void k0_prep(const u16* __restrict__ x, const u16* __restrict__ wqkv, const u16* __restrict__ wo, const u16* __restrict__ cvec,
                                               const u16* __restrict__ wmod, char* __restrict__ ws,
                                               const char* win, const char* wsp, const char* wout, const char* bsp, const char* out) {
  __shared__ __attribute__((aligned(16))) char smem[4 * 4608];
  const int tid = threadIdx.x, lane = tid & 63, wid = __builtin_amdgcn_readfirstlane(tid >> 6);
  int b = blockIdx.x;
  STAMP(0, 0);
  if (b < K0_WARM) {
    const unsigned i = (b >> 3) * 256 + tid, sc = lds_addr(smem) + wid * 256;
    {
      const char* p;
      if (i < 512) p = ws + min(i * 65536u, WS_USED - 4);
      else if (i < 640) p = wsp + (i - 512) * 65536u;
      else if (i < 680) p = win + min((i - 640) * 65536u, 2211840u - 4);
      else if (i < 700) p = wout + min((i - 680) * 65536u, 1105920u - 4);
      else if (i < 760) p = out + min((i - 700) * 65536u, 2621440u - 4);
      else if (i < 860) p = (const char*)wmod + min((i - 760) * 65536u, 4915200u - 4);
      else if (i < 920) p = (const char*)x + min((i - 860) * 65536u, 2621440u - 4);
      else p = bsp;
      l2touch(p, sc);
    }
    return;
  }
  b -= K0_WARM;
  if (b < K0_MOD) {
    const int cg = b >> 2, ks = b & 3, c = tid & 7, row0 = ks * 160 + (tid >> 3), col0 = cg * 64 + (cg >= 20 ? 640 : 0);
    u32x4 w[5]; u16 cv[5];
#pragma unroll
    for (int i = 0; i < 5; ++i) { w[i] = LDNT((const u32x4*)(wmod + (size_t)(row0 + 32 * i) * 3840 + col0 + c * 8)); cv[i] = cvec[row0 + 32 * i]; }
    float acc[8] = {0, 0, 0, 0, 0, 0, 0, 0};
#pragma unroll
    for (int i = 0; i < 5; ++i) {
      const float s = silu_bf(cv[i]);
#pragma unroll
      for (int e = 0; e < 4; ++e) { acc[2 * e] += s * bflo(w[i][e]); acc[2 * e + 1] += s * bfhi(w[i][e]); }
    }
#pragma unroll
    for (int e = 0; e < 8; ++e) acc[e] = sum_g(dpp_add<0x128>(acc[e]));
    float* red = (float*)smem;
    if (lane < 8) { *(f32x4*)(red + wid * 64 + lane * 8) = (f32x4){acc[0], acc[1], acc[2], acc[3]}; *(f32x4*)(red + wid * 64 + lane * 8 + 4) = (f32x4){acc[4], acc[5], acc[6], acc[7]}; }
    __syncthreads();
    if (tid < 64) ((float*)(ws + OFF_MODP))[ks * 2560 + cg * 64 + tid] = (red[tid] + red[64 + tid]) + (red[128 + tid] + red[192 + tid]);
    STAMP(0, 1); STAMP(0, 13);
    return;
  }
  b -= K0_MOD;
  if (b < K0_LN) {
    b = (b & 7) * 16 + (b >> 3);
    const int row = b * 16 + wid * 4 + (lane >> 4), c = lane & 15;
    const u16* xr = x + (size_t)row * D_ + c * 8;
    u32x4 v[5];
#pragma unroll
    for (int i = 0; i < 5; ++i) v[i] = *(const u32x4*)(xr + i * 128);
    float s = 0.f, q = 0.f;
#pragma unroll
    for (int i = 0; i < 5; ++i)
#pragma unroll
      for (int e = 0; e < 4; ++e) { const float a = bflo(v[i][e]), d = bfhi(v[i][e]); s += a + d; q += a * a + d * d; }
    s = sum16(s); q = sum16(q);
    const float mean = s * (1.f / 640.f); const float var = fmaxf(q * (1.f / 640.f) - mean * mean, 0.f);
    const float rinv = 1.f / ln_den(var), mb = rbf(mean) * rinv;
    char* dst = ws + OFF_XHAT + (size_t)row * 1280 + c * 16;
#pragma unroll
    for (int i = 0; i < 5; ++i) {
      u32x4 o;
#pragma unroll
      for (int e = 0; e < 4; ++e) o[e] = pack2h(bflo(v[i][e]) * rinv - mb, bfhi(v[i][e]) * rinv - mb);
      K0ST(dst + i * 256, o);
    }
    STAMP(0, 2); STAMP(0, 13);
    return;
  }
  b -= K0_LN;
  if (b < K0_RL) {
    const int kt = b / 12, G = b % 12, sec = G >> 2, gq = G & 3;
    u32x4 v[3];
#pragma unroll
    for (int i = 0; i < 3; ++i) { const int ci = min(i * 256 + tid, 639); v[i] = LDNT((const u32x4*)(wqkv + (size_t)(kt * 32 + ci / 20) * NQKV + G * 160 + (ci % 20) * 8)); }
#pragma unroll
    for (int i = 0; i < 3; ++i) { const int ci = min(i * 256 + tid, 639); *(u32x4*)(smem + (ci / 20) * 336 + (ci % 20) * 16) = v[i]; }
    __syncthreads();
    const int g = lane >> 4, q = (lane & 15) >> 2, p = lane & 3, rho = 16 * (g & 1) + 4 * p, qd = rho >> 3, h = (rho >> 2) & 1;
    for (int f = wid; f < 10; f += 4) {
      const int t = f >> 1, ks = f & 1;
      const int col = sec == 2 ? 32 * t + rho : t < 4 ? 20 * (2 * t + h) + 4 * qd : 20 * (4 * h + qd) + 16;
      const int T = sec == 2 ? 40 + gq * 5 + t : sec * 20 + (t < 4 ? 4 * gq + t : 16 + gq);
      const char* pp = smem + (16 * ks + 8 * (g >> 1) + q) * 336 + col * 2;
      const bf16x4 lo = lds_tr(pp), hi = lds_tr(pp + 4 * 336);
      K0ST(ws + OFF_WQKV + ((size_t)T * 40 + kt * 2 + ks) * 1024 + lane * 16, __builtin_bit_cast(u32x4, (bf16x8)__builtin_shufflevector(lo, hi, 0, 1, 2, 3, 4, 5, 6, 7)));
    }
    STAMP(0, 3); STAMP(0, 13);
    return;
  }
  b -= K0_RL;
  if (b < K0_WO) {
    const int job = b * 4 + wid, kt = job / 10, ng = job % 10, j8 = lane >> 3;
    char* t = smem + wid * 4608;
    u32x4 v[4];
#pragma unroll
    for (int i = 0; i < 4; ++i) {
      const int c = 4 * kt + i, row = c < 64 ? 20 * (c >> 1) + 4 * (c & 1) + (j8 & 3) + 8 * (j8 >> 2) : 20 * (2 * (c - 64) + (j8 >> 2)) + 16 + (j8 & 3);
      v[i] = LDNT((const u32x4*)(wo + (size_t)row * D_ + ng * 64 + (lane & 7) * 8));
    }
#pragma unroll
    for (int i = 0; i < 4; ++i) *(u32x4*)(t + (i * 8 + j8) * 144 + (lane & 7) * 16) = v[i];
    const char* pp = t + (8 * (lane >> 4) + ((lane & 15) >> 2)) * 144 + (lane & 3) * 8;
#pragma unroll
    for (int f = 0; f < 4; ++f) {
      const bf16x4 lo = lds_tr(pp + f * 32), hi = lds_tr(pp + 4 * 144 + f * 32);
      K0ST(ws + OFF_WO + ((size_t)(ng * 4 + f) * 20 + kt) * 1024 + lane * 16, __builtin_bit_cast(u32x4, (bf16x8)__builtin_shufflevector(lo, hi, 0, 1, 2, 3, 4, 5, 6, 7)));
    }
    return;
  }
  b -= K0_WO;
  {
    u32x4* p = (u32x4*)(ws + (b < 8 ? OFF_XCH + b * 16384 : OFF_CNT + (b - 8) * 16384)) + tid;
    const unsigned f = b < 8 ? ~0u : 0u;
#pragma unroll
    for (int i = 0; i < 4; ++i) p[i * 256] = (u32x4){f, f, f, f};
  }
}

constexpr float QSCALE = 0.22360679774997896f * 1.4426950408889634f;
#define K1W 3
constexpr int K1_W = K1W;

template <bool ISV>
DI void k1_mma(const char* __restrict__ bsrc, const char* smem, int lane, f32x16 (&acc)[4]) {
  bf16x8 bfr[40], af[4][4];
  const char* lds[4];
#pragma unroll
  for (int k = 0; k < 4; ++k) lds[k] = smem + (lane ^ (2 * k + (lane >> 5))) * 16;
#pragma unroll
  for (int k = 0; k < 4 * K1_W; ++k) bfr[k] = *(const bf16x8*)(bsrc + k * 1024);
#pragma unroll
  for (int m = 0; m < 4; ++m) acc[m] = (f32x16){};
  BAR();
  BAR();
  __builtin_amdgcn_s_setprio(1);
#define K1_RD1(s, k, m) (af[k][m] = *(const bf16x8*)(lds[k] + ((s) % 3) * 16384 + ((k) * 4 + (m)) * 1024))
#define K1_STEP(s, k, RD, rs, rk, LD) do { _Pragma("unroll") for (int m = 0; m < 4; ++m) { if (RD) K1_RD1(rs, rk, m); \
      if (LD) bfr[4 * ((s) + K1_W) + m] = *(const bf16x8*)(bsrc + (4 * ((s) + K1_W) + m) * 1024); \
      acc[m] = ISV ? MFMA32(af[k][m], bfr[(s) * 4 + (k)], acc[m]) : MFMA32(bfr[(s) * 4 + (k)], af[k][m], acc[m]); SCHED(); } } while (0)
#pragma unroll
  for (int m = 0; m < 4; ++m) K1_RD1(0, 0, m);
#pragma unroll
  for (int m = 0; m < 4; ++m) K1_RD1(0, 1, m);
  SCHED();
#pragma unroll
  for (int s = 0; s < 10; ++s) {
    K1_STEP(s, 0, true, s, 2, s + K1_W < 10);
    K1_STEP(s, 1, true, s, 3, false);
    if (s < 9) asm volatile("s_barrier" ::: "memory");
    K1_STEP(s, 2, s < 9, s + 1, 0, false);
    K1_STEP(s, 3, s < 9, s + 1, 1, false);
  }
  __builtin_amdgcn_s_setprio(0);
}

#define TARG
#define TPASS

DI void phase_qkv(char* __restrict__ ws, rsrc_t rs, const u16* __restrict__ cvec, const u16* __restrict__ wmod, char* smem, int wid TARG) {
  const int tid = fresh_tid(), lane = tid & 63;
  const int xcd = blockIdx.x & 7, idx = blockIdx.x >> 3;
  int ctr;
  if (idx >= 30) {
    ctr = C_MOD;
    const int sb = xcd * 2 + idx - 30, col0 = (sb < 8 ? 1280 : 3200 - 640) + sb * 80, c = tid % 10, rg = tid / 10;
    float* sil = (float*)smem; float* red = (float*)(smem + 4096);
    u32x4 w[13];
#pragma unroll
    for (int i = 0; i < 13; ++i) { const int row = min(rg + 50 * i, 639); w[i] = LDNT((const u32x4*)(wmod + (size_t)row * 3840 + col0 + c * 8)); }
    for (int i = tid; i < 660; i += 512) sil[i] = i < 640 ? silu_bf(cvec[i]) : 0.f;
    if (tid < 80) {
      const float* pp = (const float*)(ws + OFF_MODP) + 1280 + sb * 80 + tid;
      float v = rbf((pp[0] + pp[2560]) + (pp[5120] + pp[7680]));
      if (sb >= 8) v = rbf(1.f + v);
      stc4(rs, (1920 + sb * 80 + tid) * 4, OFF_MOD, __float_as_uint(v));
    }
    BAR();
    float acc[8] = {0, 0, 0, 0, 0, 0, 0, 0};
#pragma unroll
    for (int i = 0; i < 13; ++i) {
      const float s = sil[rg + 50 * i];
#pragma unroll
      for (int e = 0; e < 4; ++e) { acc[2 * e] += s * bflo(w[i][e]); acc[2 * e + 1] += s * bfhi(w[i][e]); }
    }
    if (rg < 50) { *(f32x4*)(red + rg * 80 + c * 8) = (f32x4){acc[0], acc[1], acc[2], acc[3]}; *(f32x4*)(red + rg * 80 + c * 8 + 4) = (f32x4){acc[4], acc[5], acc[6], acc[7]}; }
    BAR();
    if (tid < 80) {
      float v = 0.f;
#pragma unroll
      for (int i = 0; i < 50; ++i) v += red[i * 80 + tid];
      stc4(rs, (col0 + tid) * 4, OFF_MOD, __float_as_uint(rbf(v)));
    }
  } else {
    const int mt = xcd * 2 + (idx / 15), nt = idx % 15;
    ctr = C_QKV + nt;
    if (wid < 4) {
      float* cst = (float*)(smem + 49152);
      const int c = lane & 7, rr = lane >> 3;
      const char* src = ws + OFF_XHAT + (size_t)(mt * 128 + wid * 32 + rr) * 1280 + c * 16;
      char* wdst = smem + ((c >> 1) * 4 + wid) * 1024 + (c & 1) * 512 + (rr ^ c) * 16;
      u32x4 a[10][4];
      f32x4 c0[4], c1[4];
#pragma unroll
      for (int i = 0; i < 4; ++i) { c0[i] = *(const f32x4*)(ws + OFF_MODP + i * 10240 + tid * 16); if (wid == 0) c1[i] = *(const f32x4*)(ws + OFF_MODP + i * 10240 + 4096 + tid * 16); }
#pragma unroll
      for (int s = 0; s < K1_W; ++s)
#pragma unroll
        for (int m = 0; m < 4; ++m) a[s][m] = *(const u32x4*)(src + m * 10240 + s * 128);
      {
        f32x4 v = (c0[0] + c0[1]) + (c0[2] + c0[3]); const float one = tid >= 160 ? 1.f : 0.f;
        *(f32x4*)(cst + tid * 4) = (f32x4){rbf(one + rbf(v[0])), rbf(one + rbf(v[1])), rbf(one + rbf(v[2])), rbf(one + rbf(v[3]))};
        if (wid == 0) { v = (c1[0] + c1[1]) + (c1[2] + c1[3]); *(f32x4*)(cst + 1024 + tid * 4) = (f32x4){rbf(1.f + rbf(v[0])), rbf(1.f + rbf(v[1])), rbf(1.f + rbf(v[2])), rbf(1.f + rbf(v[3]))}; }
      }
      BAR();
      const float* cp = cst + c * 8;
      f32x4 kc[10][4];
#define K1_KC(s) do { kc[s][0] = *(const f32x4*)(cp + (s) * 64); kc[s][1] = *(const f32x4*)(cp + (s) * 64 + 4); kc[s][2] = *(const f32x4*)(cp + (s) * 64 + 640); kc[s][3] = *(const f32x4*)(cp + (s) * 64 + 644); } while (0)
#pragma unroll
      for (int s = 0; s < 6; ++s) K1_KC(s);
      SCHED();
      u32x4 o[2][4];
#define K1_XF(s, m) (o[(s) & 1][m] = modulate8(a[s][m], kc[s][2], kc[s][3], kc[s][0], kc[s][1]))
#define K1_WR(s, m) (*(u32x4*)(wdst + ((s) % 3) * 16384 + (m) * 128) = o[(s) & 1][m])
#pragma unroll
      for (int m = 0; m < 4; ++m) { K1_XF(0, m); K1_WR(0, m); }
      BAR();
      TS(1);
#pragma unroll
      for (int m = 0; m < 4; ++m) a[K1_W][m] = *(const u32x4*)(src + m * 10240 + K1_W * 128);
#pragma unroll
      for (int m = 0; m < 4; ++m) K1_XF(1, m);
      SCHED();
#pragma unroll
      for (int s = 1; s < 10; ++s) {
#pragma unroll
        for (int m = 0; m < 4; ++m) {
          if (m == 0) K1_WR(s, 0);
          if (m < 3) K1_WR(s, m + 1);
          if (s + K1_W < 10) a[s + K1_W][m] = *(const u32x4*)(src + m * 10240 + (s + K1_W) * 128);
          if (s < 5) kc[s + 5][m] = *(const f32x4*)(cp + (s + 5) * 64 + (m & 1) * 4 + (m >> 1) * 640);
          SCHED();
          if (s < 9) K1_XF(s + 1, m);
          SCHED();
        }
        asm volatile("" :: "v"(o[s & 1][0]), "v"(o[s & 1][1]), "v"(o[s & 1][2]), "v"(o[s & 1][3]));
        BAR();
      }
    } else {
      const int cw = wid - 4;
      const char* bsrc = ws + OFF_WQKV + (size_t)(nt * 4 + cw) * 40 * 1024 + lane * 16;
      f32x16 acc[4];
      const int r = lane & 31, h = lane >> 5;
      if (nt >= 10) {
        k1_mma<true>(bsrc, smem, lane, acc);
        TS(2);
        const unsigned c = (nt - 10) * 128 + cw * 32 + r, head = c / 20, d = c % 20;
        const unsigned so = OFF_KV + (mt >> 1) * 20480 + 10240 + (mt & 1) * 5120, vo = head * 163840u + (h * 20 + d) * 16;
#pragma unroll
        for (int m = 0; m < 4; ++m)
#pragma unroll
          for (int s = 0; s < 2; ++s)
            stc16(rs, vo + s * 640, so + m * 1280, (u32x4){pack2(acc[m][8 * s], acc[m][8 * s + 1]), pack2(acc[m][8 * s + 2], acc[m][8 * s + 3]), pack2(acc[m][8 * s + 4], acc[m][8 * s + 5]), pack2(acc[m][8 * s + 6], acc[m][8 * s + 7])});
      } else {
        k1_mma<false>(bsrc, smem, lane, acc);
        TS(2);
        const bool isQ = nt < 5; const float sc = isQ ? QSCALE : 1.f;
        const unsigned so = isQ ? OFF_Q + mt * 5120 : OFF_KV + (mt >> 1) * 20480 + (mt & 1) * 5120, hs = isQ ? 81920u : 163840u;
        const int n5 = isQ ? nt : nt - 5;
        if (n5 < 4) {
          const unsigned vo = (2 * (n5 * 4 + cw) + h) * hs + r * 16;
#pragma unroll
          for (int m = 0; m < 4; ++m)
#pragma unroll
            for (int s = 0; s < 2; ++s)
              stc16(rs, vo + s * 512, so + m * 1280, (u32x4){pack2(acc[m][8 * s] * sc, acc[m][8 * s + 1] * sc), pack2(acc[m][8 * s + 2] * sc, acc[m][8 * s + 3] * sc),
                                                           pack2(acc[m][8 * s + 4] * sc, acc[m][8 * s + 5] * sc), pack2(acc[m][8 * s + 6] * sc, acc[m][8 * s + 7] * sc)});
        } else {
          const unsigned vo = (8 * cw + 4 * h) * hs + 1024 + r * 8;
#pragma unroll
          for (int m = 0; m < 4; ++m)
#pragma unroll
            for (int qd = 0; qd < 4; ++qd)
              stc8(rs, vo, so + qd * hs + m * 1280, (u32x2){pack2(acc[m][4 * qd] * sc, acc[m][4 * qd + 1] * sc), pack2(acc[m][4 * qd + 2] * sc, acc[m][4 * qd + 3] * sc)});
        }
      }
    }
  }
  TS(3);
  waitv<0>();
  BAR();
  TS(4);
  if (wid == 7 && lane < 8) __hip_atomic_fetch_add((GAS unsigned*)(ws + OFF_CNT + (lane * 16 + ctr) * 256), 1u, __ATOMIC_RELAXED, __HIP_MEMORY_SCOPE_AGENT);
}

template <bool FINAL> DI constexpr int proj_pos(int f) { return FINAL ? (f < 96 ? 65536 + f * 1024 : (f - 96) * 1024) : 17408 + (f % 20 >= 10 ? 81920 : 0) + (f / 20 * 10 + f % 10) * 1024; }
template <bool FINAL> DI u32x4 proj_ld(rsrc_t rs, int i, int wid, int lane) {
  constexpr int NF = FINAL ? 135 : 100;
  return ldc16(rs, lane * 16, (FINAL ? OFF_WOUT : OFF_WO) + (((blockIdx.x >> 3) & 7) * NF + min(i * 8 + wid, NF - 1)) * 1024);
}
template <bool FINAL> DI void proj_park(char* smem, int i, int wid, int lane, u32x4 v) {
  constexpr int NF = FINAL ? 135 : 100;
  *(u32x4*)(smem + proj_pos<FINAL>(min(i * 8 + wid, NF - 1)) + lane * 16) = v;
}

DI void split3(float m, unsigned& w_h0, unsigned& w_h1) {
  const float n = -m; const float a = rbf(n), b = rbf(n - a), c = rbf(n - a - b);
  w_h0 = pack2(a, b); w_h1 = pack2(c, 0.f);
}

DI void phase_attn(char* __restrict__ ws, rsrc_t rs, const char* __restrict__ wsp, const u16* __restrict__ win, const u16* __restrict__ wout, char* smem, int wid TARG) {
  const int tid = fresh_tid(), lane = tid & 63;
  const int xcd = blockIdx.x & 7, idx = blockIdx.x >> 3, head = xcd * 4 + (idx >> 3), qb = idx & 7;
  const int r = lane & 31, h = lane >> 5, qg = wid & 3, kh = wid >> 2;
  if (wid == 7) {
    const int l7 = lane & 7;
    const int c = l7 == 0 ? head >> 3 : l7 == 1 ? 4 : l7 == 2 ? 5 + (head >> 3) : l7 == 3 ? 9 : l7 == 4 ? 10 + (20 * head) / 128 : l7 == 5 ? 10 + (20 * head + 19) / 128 : C_MOD;
    poll_ge(ws, xcd * 16 + c, 16);
  }
  BAR();
  TS(5);
  unsigned kvo = OFF_KV + head * 163840, qo = OFF_Q + head * 81920 + (qb * 8 + qg * 2) * 1280;
  const unsigned kconst = h == 0 ? 0x3f803f80u : 0x00003f80u;
  const unsigned so16 = (tid & 255) * 16, l16 = lane * 16, l2 = 1024 + r * 8 + h * 4;
  const int blk = blockIdx.x;
  u32x4 wq[13];
  for (int attempt = 0; attempt < 2; ++attempt) {
    asm volatile("" : "+s"(kvo), "+s"(qo));
    const char* kvs = ws + kvo + kh * 81920 + so16;
    const unsigned ldst = lds_addr(smem) + kh * 81920 + qg * 1024;
    u32x4 qA1w, qB1w, k1w; unsigned qA2w, qB2w, k2, m0sv;
    asm volatile(
        "buffer_load_dwordx4 %0, %7, %9, %10 offen sc1\n\tbuffer_load_dwordx4 %1, %7, %9, %10 offen offset:1280 sc1\n\tbuffer_load_dwordx4 %2, %7, %9, %11 offen sc1\n\t"
        "buffer_load_dword %3, %8, %9, %10 offen sc1\n\tbuffer_load_dword %4, %8, %9, %10 offen offset:1280 sc1\n\tbuffer_load_dword %5, %8, %9, %11 offen sc1\n\t"
        "s_mov_b32 %6, m0\n\ts_mov_b32 m0, %17\n\ts_nop 0\n\tglobal_load_lds_dwordx4 %12, off sc1\n\t"
        "s_add_u32 m0, %18, 0x1000\n\ts_nop 0\n\tglobal_load_lds_dwordx4 %13, off sc1\n\t"
        "s_add_u32 m0, %18, 0x2000\n\ts_nop 0\n\tglobal_load_lds_dwordx4 %14, off sc1\n\t"
        "s_add_u32 m0, %18, 0x3000\n\ts_nop 0\n\tglobal_load_lds_dwordx4 %15, off sc1\n\t"
        "s_add_u32 m0, %18, 0x4000\n\ts_nop 0\n\tglobal_load_lds_dwordx4 %16, off sc1\n\t"
        "s_mov_b32 m0, %6\n\ts_waitcnt vmcnt(5)"
        : "=&v"(qA1w), "=&v"(qB1w), "=&v"(k1w), "=&v"(qA2w), "=&v"(qB2w), "=&v"(k2), "=&s"(m0sv)
        : "v"(l16), "v"(l2), "s"(rs), "s"(qo), "s"(kvo), "v"(kvs + (wid == 0 ? 4096 : 0)), "v"(kvs + 4096), "v"(kvs + 8192), "v"(kvs + 12288), "v"(kvs + 16384),
          "s"(ldst + (wid == 0 ? 4096 : 0)), "s"(ldst)
        : "memory", "scc");
    if (wid == 0) { const unsigned f = (tid & 1) ? 0u : 0x3f803f80u; *(u32x4*)(smem + l16) = (u32x4){f, f, f, f}; }
    const bf16x8 qA1 = BF8(qA1w), qB1 = BF8(qB1w);
    bf16x8 k1 = BF8(k1w);
    TS(30);
    float mA = -INFINITY, mB = -INFINITY;
    {
      const bf16x4 qA2 = BF4(((u32x2){qA2w, 0u})), qB2 = BF4(((u32x2){qB2w, 0u}));
      const int nt = attempt ? 64 : 1;
      bf16x8 t1 = k1; unsigned t2 = k2;
      for (int t = 0; t < nt; ++t) {
        if (t) { const unsigned to = kvo + (t >> 3) * 20480 + (t & 7) * 1280; t1 = BF8(ldc16(rs, l16, to)); t2 = ldc4(rs, l2, to); }
        const bf16x4 t2v = BF4(((u32x2){t2, kconst}));
        f32x16 a = MFMA32(t1, qA1, (f32x16){}); a = MFMA32K8(t2v, qA2, a);
        f32x16 b = MFMA32(t1, qB1, (f32x16){}); b = MFMA32K8(t2v, qB2, b);
#pragma unroll
        for (int i = 0; i < 16; ++i) { mA = fmaxf(mA, a[i]); mB = fmaxf(mB, b[i]); }
      }
      mA = fmaxf(mA, xhalf(mA)); mB = fmaxf(mB, xhalf(mB));
    }
    unsigned a0, a1, b0, b1; split3(mA, a0, a1); split3(mB, b0, b1);
    const bf16x4 qA2 = BF4(((u32x2){qA2w, h ? a1 : a0})), qB2 = BF4(((u32x2){qB2w, h ? b1 : b0}));
    char* lh = smem + kh * 81920;
    TS(31);
    waitv<0>();
    BAR();
    TS(6);
    f32x16 oA = (f32x16){}, oB = (f32x16){};
    const char* kp1 = lh + l16;
    const char* kp2 = lh + l2;
    const bool vreal = r < 20;
    const char* vp = vreal ? lh + 10240 + (h * 20 + r) * 16 : smem + (r == 20 ? 0 : 16);
    const int vinc = vreal ? 1280 : 0, vjump = vreal ? 10240 : 0;
    if (kh) { k1 = *(const bf16x8*)kp1; k2 = *(const unsigned*)kp2; }
    bf16x8 kf1[2], vf[2][2]; u32x2 kf2[2];
    kf1[0] = k1; kf2[0] = (u32x2){k2, kconst}; kf2[1] = (u32x2){0u, kconst};
    vf[1][0] = (bf16x8){}; vf[1][1] = (bf16x8){};
    u32x4 pA0, pA1, pB0 = (u32x4){}, pB1 = (u32x4){};
    f32x4 e0 = (f32x4){}, e1;
    f32x16 sA = MFMA32(kf1[0], qA1, (f32x16){}), sB;
    sA = MFMA32K8(BF4(kf2[0]), qA2, sA);
#define EX2(x) __builtin_amdgcn_exp2f(x)
#define GRP(MF, S, i, EN, EP, P, w) do { MF; SCHED(); EN[0] = EX2(S[i]); P[w] = pack2(EP[0], EP[1]); EN[1] = EX2(S[i + 1]); P[w + 1] = pack2(EP[2], EP[3]); \
      EN[2] = EX2(S[i + 2]); EN[3] = EX2(S[i + 3]); PIN(P); PIN(EN); SCHED(); } while (0)
    const unsigned relsc = lds_addr(smem) + 768;
    auto group = [&](auto FIRST, int jump, int j) __attribute__((always_inline)) {
#pragma unroll
      for (int t = 0; t < 8; ++t) {
        const int c = t & 1, n = c ^ 1;
        if (FIRST) { if (t == 0) __builtin_amdgcn_s_setprio(3); if (t == 3) __builtin_amdgcn_s_setprio(2); if (t == 5) __builtin_amdgcn_s_setprio(1); if (t == 6) __builtin_amdgcn_s_setprio(0); if (t == 7) __builtin_amdgcn_s_setprio(3); }
        else {
          if (t == 4 && j == 2) __builtin_amdgcn_s_setprio(2);
          if (t == 3 && j == 3) {
            __builtin_amdgcn_s_setprio(1);
#pragma unroll
            for (int i = 0; i < 13; ++i) wq[i] = proj_ld<false>(rs, i, wid, lane);
          }
          if (t == 6 && j == 3) { __builtin_amdgcn_s_setprio(0); if (wid == 7) glds4c(ws + OFF_CNT + (C_REL + (lane & 7)) * 256, relsc); }
        }
        if (FIRST && t < 5) {
#pragma unroll
          for (int i = 5 + 3 * t; i < 8 + 3 * t; ++i) glds16c(kvs + i * 4096, ldst + i * 4096);
        }
        if (t == 7) {
          if (FIRST) { waitv<0>(); BAR(); }
          kp1 += jump; kp2 += jump;
        }
        SCHED();
        kf1[n] = *(const bf16x8*)(kp1 + ((t + 1) & 7) * 1280); kf2[n][0] = *(const unsigned*)(kp2 + ((t + 1) & 7) * 1280);
        GRP(sB = MFMA32(kf1[c], qB1, (f32x16){}), sA, 0, e1, e0, pB1, 2);
        vf[c][0] = *(const bf16x8*)vp; vf[c][1] = *(const bf16x8*)(vp + 640); vp += vinc;
        GRP(sB = MFMA32K8(BF4(kf2[c]), qB2, sB), sA, 4, e0, e1, pA0, 0);
        GRP(oB = MFMA32(vf[n][0], BF8(pB0), oB), sA, 8, e1, e0, pA0, 2);
        GRP(oB = MFMA32(vf[n][1], BF8(pB1), oB), sA, 12, e0, e1, pA1, 0);
        GRP(sA = MFMA32(kf1[n], qA1, (f32x16){}), sB, 0, e1, e0, pA1, 2);
        GRP(sA = MFMA32K8(BF4(kf2[n]), qA2, sA), sB, 4, e0, e1, pB0, 0);
        GRP(oA = MFMA32(vf[c][0], BF8(pA0), oA), sB, 8, e1, e0, pB0, 2);
        GRP(oA = MFMA32(vf[c][1], BF8(pA1), oA), sB, 12, e0, e1, pB1, 0);
      }
      vp += vjump;
    };
    group(std::true_type{}, 20480, 0);
    TS(7);
    const int j2 = (blk - 135) * 4 + qg;
    const bool side = kh == 0 && attempt == 0 && j2 < 270;
    char* stile = smem + 1024 + qg * 4096;
    const char* svec = smem + 81920 + qg * 256;
    const int kt = blk < 135 ? 4 * (blk % 5) + qg : j2 / 10, ng = blk < 135 ? blk / 5 : j2 % 10;
    if (side) {
      const u16* W = blk < 135 ? win : wout; const int N = blk < 135 ? F_ : D_;
      const int j8 = lane >> 3;
#pragma unroll
      for (int i = 0; i < 4; ++i) {
        const int row = kt * 32 + i * 8 + j8;
        glds16(W + (size_t)row * N + ng * 64 + ((lane & 7) ^ j8) * 8, lds_addr(stile) + i * 1024);
      }
      glds4c((const float*)(ws + OFF_MOD) + (4 - h) * 640 + (kt < 20 ? kt : 0) * 32 + r, lds_addr(svec));
    }
#pragma nounroll
    for (int j = 1; j < 4; ++j) {
      if (j == 2) {
        if (kh) l2touch(wsp + ((size_t)(blk * 4 + qg) * 64 + lane) * 128, lds_addr(smem) + 256);
        else if (side) {
          waitv<0>();
          const int g4 = lane >> 4, r4 = lane & 15, row = 8 * g4 + (r4 >> 2);
          const f32x4 sv0 = *(const f32x4*)(svec + (r4 & 1) * 128 + g4 * 32), sv1 = *(const f32x4*)(svec + (r4 & 1) * 128 + g4 * 32 + 16);
          const bf16x8 vec = BF8(((u32x4){pack2(sv0[0], sv0[1]), pack2(sv0[2], sv0[3]), pack2(sv1[0], sv1[1]), pack2(sv1[2], sv1[3])}));
          const unsigned nks = blk < 135 ? 20 : 27, dso = (blk < 135 ? OFF_WIN : OFF_WOUT) + (ng * 4 * nks + kt) * 1024;
          bf16x8 fr[4]; f32x4 pr[4];
#pragma unroll
          for (int f = 0; f < 4; ++f) {
            const int ch = 2 * f + ((r4 & 3) >> 1);
            const bf16x4 lo = lds_tr(stile + row * 128 + (ch ^ (row & 7)) * 16 + (r4 & 1) * 8), hi = lds_tr(stile + (row + 4) * 128 + (ch ^ ((row + 4) & 7)) * 16 + (r4 & 1) * 8);
            fr[f] = __builtin_shufflevector(lo, hi, 0, 1, 2, 3, 4, 5, 6, 7);
          }
#pragma unroll
          for (int f = 0; f < 4; ++f) { stc16(rs, l16, dso + f * nks * 1024, __builtin_bit_cast(u32x4, fr[f])); pr[f] = MFMA16(fr[f], vec, (f32x4){}); }
          if (r4 < 2) {
#pragma unroll
            for (int f = 0; f < 4; ++f) *(f32x4*)(stile + 3072 + r4 * 256 + f * 64 + g4 * 16) = pr[f];
          }
        }
      }
      if (j == 3 && kh == 0 && attempt == 0) { waitv<0>(); arrive(ws, C_REL + xcd, lane); }
      group(std::false_type{}, j < 3 ? 20480 : 0, j);
    }
    TS(8);
    pB1[2] = pack2(e0[0], e0[1]); pB1[3] = pack2(e0[2], e0[3]);
    oB = MFMA32(vf[1][0], BF8(pB0), oB); oB = MFMA32(vf[1][1], BF8(pB1), oB);
    {
      f32x4* ex = (f32x4*)(lh + 1024 + qg * 4096) + lane;
      const f32x16 give = kh ? oA : oB;
#pragma unroll
      for (int g = 0; g < 3; ++g) ex[g * 64] = (f32x4){give[4 * g], give[4 * g + 1], give[4 * g + 2], give[4 * g + 3]};
    }
    int* flag = (int*)(smem + 528);
    BAR();
    TS(9);
    float o[12];
    {
      const f32x4* ex = (const f32x4*)(smem + (kh ^ 1) * 81920 + 1024 + qg * 4096) + lane;
      const f32x16 keep = kh ? oB : oA;
#pragma unroll
      for (int g = 0; g < 3; ++g) { const f32x4 t = ex[g * 64];
#pragma unroll
        for (int e = 0; e < 4; ++e) o[4 * g + e] = keep[4 * g + e] + t[e]; }
    }
    const float lo = xhalf(o[8]); const float l = h ? o[8] : lo;
    const float rl = __builtin_amdgcn_rcpf(l);
    const int tok = qb * 256 + qg * 64 + kh * 32 + r;
    const unsigned vo = (tok >> 4) * 20480 + (tok & 15) * 16;
    stc16(rs, vo + h * 256, OFF_O + (head >> 1) * 1024 + (head & 1) * 512,
          (u32x4){pack2(o[0] * rl, o[1] * rl), pack2(o[2] * rl, o[3] * rl), pack2(o[4] * rl, o[5] * rl), pack2(o[6] * rl, o[7] * rl)});
    if (h == 0) stc8(rs, vo, OFF_O + 16384 + (head >> 3) * 1024 + ((head >> 1) & 3) * 256 + (head & 1) * 8, (u32x2){pack2(o[8] * rl, o[9] * rl), pack2(o[10] * rl, o[11] * rl)});
    if (wid == 4 && blk < 135 && attempt == 0 && lane < 32) {
      const char* pp = smem + 1024 + 3072 + lane * 16;
      const f32x4 v = (*(const f32x4*)pp + *(const f32x4*)(pp + 4096)) + (*(const f32x4*)(pp + 8192) + *(const f32x4*)(pp + 12288));
      stc16(rs, (lane >> 4) * 8192 + (lane & 15) * 16, OFF_CBP + (blk % 5) * 16384 + (blk / 5) * 256, __builtin_bit_cast(u32x4, v));
    }
    bool ok = (l > 1e-30f) && (l < 1e30f);
#pragma unroll
    for (int i = 0; i < 12; ++i) ok = ok && (fabsf(o[i]) < 3e38f);
    if (!ok && attempt == 0) *flag = 1;
    waitv<0>();
    BAR();
    const int redo = __builtin_amdgcn_readfirstlane(*(volatile LAS int*)flag);
    if (!redo) break;
    BAR();
  }
  TS(10);
  if (wid == 7) arrive(ws, C_ATT + qb, lane);
#pragma unroll
  for (int i = 0; i < 13; ++i) proj_park<false>(smem, i, wid, lane, wq[i]);
  if (!__all(*(volatile LAS unsigned*)(smem + 768 + (lane & 7) * 4) >= 128u)) poll_ge(ws, C_REL + (lane & 7), 128);
}

template <bool FINAL, int KH, class HOOK>
DI void proj_body(char* __restrict__ ws, rsrc_t rs, const u16* __restrict__ x, u16* __restrict__ out, char* smem, int wid, u32x2 (&res)[3], HOOK&& after_mma TARG) {
  const int lane = fresh_tid() & 63;
  const int xcd = blockIdx.x & 7, idx = blockIdx.x >> 3, panel = xcd * 4 + (idx >> 3), cb = idx & 7;
  constexpr int NKS = FINAL ? 27 : 20;
  constexpr int K0 = KH ? (NKS + 1) / 2 : 0, NK = KH ? NKS / 2 : (NKS + 1) / 2, NKEEP = KH ? 2 : 3, UK = KH ? 3 : 0, UG = KH ? 0 : 3;
  const int m = wid & 3, mt = panel * 4 + m, g = lane >> 4, r = lane & 15;
  f32x4 gate[NKEEP], cbv[NKEEP];
#pragma unroll
  for (int i = 0; i < NKEEP; ++i) {
    const int n = (cb * 5 + UK + i) * 16;
    if (!FINAL) { res[i] = *(const u32x2*)(x + (size_t)(mt * 16 + r) * D_ + n + g * 4); cbv[i] = ldc16f(rs, g * 16, OFF_MOD + (4 * 640 + n) * 4); }
    gate[i] = ldc16f(rs, g * 16, OFF_MOD + ((FINAL ? 5 : 2) * 640 + n) * 4);
  }
  if (wid == 7) poll_ge(ws, FINAL ? C_U + panel : C_ATT + (panel >> 2), FINAL ? 8 : 32);
  waitv<0>();
  BAR();
  TS(FINAL ? 27 : 11);
  bf16x8 a[NK];
#pragma unroll
  for (int k = 0; k < NK; ++k) a[k] = BF8(ldc16(rs, lane * 16, (FINAL ? OFF_U : OFF_O) + (mt * NKS + K0 + k) * 1024));
  SCHED();
  constexpr int PD = FINAL ? 5 : 6;
  const char* bl = smem + lane * 16;
  bf16x8 bf[PD + 1][5]; f32x4 acc[5];
#pragma unroll
  for (int u = 0; u < 5; ++u) acc[u] = (f32x4){};
#pragma unroll
  for (int k = 0; k < PD; ++k)
#pragma unroll
    for (int u = 0; u < 5; ++u) bf[k][u] = *(const bf16x8*)(bl + proj_pos<FINAL>(u * NKS + K0 + k));
  SCHED();
#pragma unroll
  for (int k = 0; k < NK; ++k) {
#pragma unroll
    for (int u = 0; u < 5; ++u) {
      if (k + PD < NK) bf[(k + PD) % (PD + 1)][u] = *(const bf16x8*)(bl + proj_pos<FINAL>(u * NKS + K0 + k + PD));
      acc[u] = MFMA16(bf[k % (PD + 1)][u], a[k], acc[u]);
      SCHED();
    }
  }
  TS(FINAL ? 28 : 12);
  f32x4* ex = (f32x4*)(smem + (FINAL ? 40960 : 68608)) + m * 5 * 64 + lane;
#pragma unroll
  for (int i = 0; i < 5 - NKEEP; ++i) ex[(UG + i) * 64] = acc[UG + i];
  f32x4 part[NKEEP];
#pragma unroll
  for (int i = 0; i < NKEEP; ++i) {
    part[i] = (f32x4){bflo(res[i][0]), bfhi(res[i][0]), bflo(res[i][1]), bfhi(res[i][1])} + gate[i] * acc[UK + i];
    PIN(part[i]);
  }
  SCHED();
  after_mma();
  BAR();
  f32x4 oth[NKEEP];
#pragma unroll
  for (int i = 0; i < NKEEP; ++i) oth[i] = ex[(UK + i) * 64];
  f32x4 sv = (f32x4){}, qv = (f32x4){};
#pragma unroll
  for (int i = 0; i < NKEEP; ++i) {
    const f32x4 v = part[i] + gate[i] * oth[i];
    const u32x2 o = (u32x2){pack2(v[0], v[1]), pack2(v[2], v[3])};
    if (FINAL) *(u32x2*)(out + (size_t)(mt * 16 + r) * D_ + (cb * 5 + UK + i) * 16 + g * 4) = o;
    else {
      res[i] = o; PIN(res[i]);
      const f32x4 w = v * cbv[i]; const int un = cb * 5 + UK + i;
      stc8(rs, ((g >> 1) * 16 + r) * 16 + (g & 1) * 8, OFF_X1S + mt * 20480 + un * 512, (u32x2){pack2(w[0], w[1]), pack2(w[2], w[3])});
      sv += v; qv += v * v;
    }
  }
  if (!FINAL) {
    const float s = sum_g((sv[0] + sv[1]) + (sv[2] + sv[3])), q = sum_g((qv[0] + qv[1]) + (qv[2] + qv[3]));
    if (g == 0) stc8(rs, r * 8, OFF_ST1 + ((panel * 16 + cb * 2 + KH) * 64 + m * 16) * 8, (u32x2){__float_as_uint(s), __float_as_uint(q)});
    TS(13);
    waitv<0>();
    BAR();
    TS(14);
    if (wid == 7) arrive(ws, C_X1 + panel, lane);
  }
}

constexpr float GC1 = -2.8853900817779268f * 0.7978845608028654f, GC2 = GC1 * 0.044715f;
DI float gelu_s(float a, float k1, float k3, float den) {
  const float e = __builtin_amdgcn_exp2f(a * __builtin_fmaf(a * a, k3, k1));
  return a * __builtin_amdgcn_rcpf(__builtin_fmaf(e, den, den));
}
template <int I0> DI void gelu8(f32x16& a, const f32x4& ln, float& s, float& q) {
  float t[8];
#pragma unroll
  for (int i = 0; i < 8; ++i) t[i] = opq(a[I0 + i] * a[I0 + i]);
  SCHED();
#pragma unroll
  for (int i = 0; i < 8; ++i) t[i] = opq(__builtin_fmaf(t[i], ln[3], ln[2]));
  SCHED();
#pragma unroll
  for (int i = 0; i < 8; ++i) t[i] = opq(a[I0 + i] * t[i]);
  SCHED();
#pragma unroll
  for (int i = 0; i < 8; ++i) t[i] = opq(__builtin_amdgcn_exp2f(t[i]));
  SCHED();
#pragma unroll
  for (int i = 0; i < 8; ++i) t[i] = opq(__builtin_fmaf(t[i], ln[0], ln[0]));
  SCHED();
#pragma unroll
  for (int i = 0; i < 8; ++i) t[i] = opq(__builtin_amdgcn_rcpf(t[i]));
  SCHED();
#pragma unroll
  for (int i = 0; i < 8; ++i) t[i] = opq(a[I0 + i] * t[i]);
  SCHED();
  float s2 = opq(t[0] + t[1]), q2 = opq(t[0] * t[0]);
  s = opq(s + t[2]); q = opq(__builtin_fmaf(t[1], t[1], q));
#pragma unroll
  for (int i = 2; i < 8; i += 2) {
    q2 = opq(__builtin_fmaf(t[i], t[i], q2)); s2 = opq(s2 + t[i + 1]); q = opq(__builtin_fmaf(t[i + 1], t[i + 1], q));
    if (i < 6) s = opq(s + t[i + 2]);
  }
  s = opq(s + s2); q = opq(q + q2);
#pragma unroll
  for (int i = 0; i < 8; ++i) a[I0 + i] = t[i];
  SCHED();
}
constexpr int L4_Z1 = 114688, L4_TR = L4_Z1 + 16384, L4_LN = L4_TR + 16384, L4_ST2 = L4_LN + 1024, L4_FIN = L4_ST2 + 2048, L4_CB = L4_FIN + 512, L4_CNT = L4_CB + 2048;
constexpr int K5_R = 7, WIN_PRE = 28;
constexpr unsigned DEAD = 0x80000000u;
DI int sp_stage(int s) { const int idx = blockIdx.x >> 3; return (s + (idx & 7) * 2) & 15; }
DI unsigned sp_voff(int wid, int lane, int i) { return ((wid & 3) * 16 + i * 8 + (lane >> 3)) * 4096 + (wid >> 2) * 128 + ((lane & 7) ^ (4 * i + (lane >> 4))) * 16; }
DI const char* sp_base(const u16* wsp, int panel, int s) { return (const char*)wsp + (size_t)panel * 262144 + sp_stage(s) * 256; }
DI void sp_prefetch(u32x4 (&wr)[16 - K5_R][2], const u16* __restrict__ wsp, int panel, int wid, int lane) {
  const unsigned v0 = sp_voff(wid, lane, 0), v1 = sp_voff(wid, lane, 1);
#pragma unroll
  for (int s = K5_R; s < 16; ++s) { wr[s - K5_R][0] = *(const u32x4*)(sp_base(wsp, panel, s) + v0); wr[s - K5_R][1] = *(const u32x4*)(sp_base(wsp, panel, s) + v1); }
  asm volatile("" ::: "memory");
}
DI void sp_ring(const u16* __restrict__ wsp, char* smem, int panel, int wid, int lane) {
  const unsigned v0 = sp_voff(wid, lane, 0), v1 = sp_voff(wid, lane, 1);
#pragma unroll
  for (int s = 0; s < K5_R; ++s) { glds16(sp_base(wsp, panel, s) + v0, lds_addr(smem) + wid * 2048 + s * 16384); glds16(sp_base(wsp, panel, s) + v1, lds_addr(smem) + wid * 2048 + s * 16384 + 1024); }
}
DI unsigned win_boff(int wid, int lane) {
  const int cb = (blockIdx.x >> 3) & 7, u0 = unit0_of(cb), r = lane & 31, hi = r >> 4;
  const int u = wid < 3 ? 54 + u0 + 2 * wid + hi : wid == 3 ? (hi ? 0 : 54) + u0 + 6 : u0 + 2 * wid - 8 + hi;
  return u * 20480 + (lane >> 5) * 256 + (r & 15) * 16 + (wid == 3 && cb >= 6 ? DEAD : 0u);
}
#define WIN_LD(k) BF8(ldc16(rs, bo + ((k) & 7) * 512, OFF_WIN + ((k) >> 3) * 4096))
DI void stage_x1s(char* __restrict__ ws, char* smem, int panel, int wid, int tid) {
  const char* src = ws + OFF_X1S + (size_t)panel * 81920 + tid * 16;
#pragma unroll
  for (int i = 0; i < 10; ++i) glds16c(src + i * 8192, lds_addr(smem) + wid * 1024 + i * 8192);
}
DI void win_tail(char* __restrict__ ws, const u16* __restrict__ wsp, char* smem, int panel, int cb, int wid, int lane TARG) {
  BAR();
  TS(22);
  if (wid == 7 && lane < 4) __hip_atomic_fetch_add((GAS unsigned*)(ws + OFF_CNT + (C_Z2 + lane * 8 + cb) * 256), 1u, __ATOMIC_RELAXED, __HIP_MEMORY_SCOPE_AGENT);
}

template <bool ZA>
DI void win_body(char* __restrict__ ws, rsrc_t rs, const u16* __restrict__ wsp, char* smem, int wid, bf16x8 (&b)[40], u32x4 (&wr)[16 - K5_R][2] TARG) {
  const int tid = fresh_tid(), lane = tid & 63, r = lane & 31, h = lane >> 5;
  const int xcd = blockIdx.x & 7, idx = blockIdx.x >> 3, panel = xcd * 4 + (idx >> 3), cb = idx & 7, u0 = unit0_of(cb);
  const bool zb = wid < 3;
  const int la = 2 * (wid & 3), lb = wid == 3 ? 6 : la + 1;
  constexpr int PRE = ZA ? WIN_PRE : 0;
  float* lnp = (float*)(smem + L4_LN); float* st2 = (float*)(smem + L4_ST2); float* fin = (float*)(smem + L4_FIN); float* cbl = (float*)(smem + L4_CB);
  if (ZA) __builtin_amdgcn_s_setprio(1);
  const unsigned bo = win_boff(wid, lane);
  BAR();
  TS(15);
  stage_x1s(ws, smem, panel, wid, tid);
#pragma unroll
  for (int k = PRE; k < PRE + 4; ++k) b[k] = WIN_LD(k);
  waitv<4>();
  BAR();
  TS(16);
#pragma unroll
  for (int k = PRE + 4; k < 32; ++k) b[k] = WIN_LD(k);
  constexpr int PD = 3;
  const char* al = smem + (r >> 4) * 20480 + h * 256 + (r & 15) * 16;
  bf16x8 af[PD + 1][2]; f32x16 acc[2];
#pragma unroll
  for (int k = 0; k < PD; ++k) { af[k][0] = *(const bf16x8*)(al + k * 512); af[k][1] = *(const bf16x8*)(al + 40960 + k * 512); }
  f32x4 ln[2];
#pragma unroll
  for (int m = 0; m < 2; ++m) ln[m] = *(const f32x4*)(lnp + (m * 32 + r) * 4);
#pragma unroll
  for (int qd = 0; qd < 4; ++qd) {
    const int o = (qd < 2 ? (ZA ? 112 : 0) + la * 16 : (zb ? 112 : 0) + lb * 16) + 8 * (qd & 1) + 4 * h;
    const f32x4 cs = *(const f32x4*)(cbl + o), bi = *(const f32x4*)(cbl + 256 + o);
#pragma unroll
    for (int m = 0; m < 2; ++m)
#pragma unroll
      for (int e = 0; e < 4; ++e) { const float c0 = __builtin_fmaf(ln[m][1], cs[e], ln[m][0] * bi[e]); acc[m][4 * qd + e] = ZA ? c0 : opq(c0); }
  }
  SCHED();
  const unsigned wvo[2] = {sp_voff(wid, lane, 0), sp_voff(wid, lane, 1)};
#pragma unroll
  for (int k = 0; k < 40; ++k)
#pragma unroll
    for (int m = 0; m < 2; ++m) {
      if (k + PD < 40) af[(k + PD) % (PD + 1)][m] = *(const bf16x8*)(al + m * 40960 + (k + PD) * 512);
      acc[m] = MFMA32(b[k], af[k % (PD + 1)][m], acc[m]);
      if (k < 4) b[32 + 2 * k + m] = WIN_LD(32 + 2 * k + m);
      if (ZA && k >= 22 && m == 0) wr[(k - 22) >> 1][k & 1] = *(const u32x4*)(sp_base(wsp, panel, K5_R + ((k - 22) >> 1)) + wvo[k & 1]);
      SCHED();
    }
  TS(17);
#define WIN_Z(m, i) gelu_s(acc[m][i], ln[m][2], ln[m][3], ln[m][0])
#define WIN_Z1(m, qd) (*(u32x2*)(smem + L4_Z1 + ((qd) < 2 ? la : lb) * 2048 + (m) * 1024 + ((2 * ((qd) & 1) + h) * 16 + (r & 15)) * 16 + (r >> 4) * 8) = \
      (u32x2){pack2(WIN_Z(m, 4 * (qd)), WIN_Z(m, 4 * (qd) + 1)), pack2(WIN_Z(m, 4 * (qd) + 2), WIN_Z(m, 4 * (qd) + 3))})
  if (ZA) {
    float s[2] = {0.f, 0.f}, q[2] = {0.f, 0.f};
    gelu8<0>(acc[0], ln[0], s[0], q[0]); gelu8<0>(acc[1], ln[1], s[1], q[1]);
    if (zb) { gelu8<8>(acc[0], ln[0], s[0], q[0]); gelu8<8>(acc[1], ln[1], s[1], q[1]); }
#pragma unroll
    for (int m = 0; m < 2; ++m) {
      s[m] = opq(s[m] + xhalf(s[m])); q[m] = opq(q[m] + xhalf(q[m]));
      if (h == 0) *(f32x2*)(st2 + (wid * 64 + m * 32 + r) * 2) = (f32x2){s[m], q[m]};
    }
    asm volatile("" ::: "memory");
    if (lane == 0) __hip_atomic_fetch_add((int*)(smem + L4_CNT), 1, __ATOMIC_RELAXED, __HIP_MEMORY_SCOPE_WORKGROUP);
    asm volatile("" ::: "memory");
    __builtin_amdgcn_s_setprio(0);
    TS(18);
    if (!zb) { WIN_Z1(0, 2); WIN_Z1(0, 3); WIN_Z1(1, 2); WIN_Z1(1, 3); }
  } else {
    WIN_Z1(0, 0); WIN_Z1(0, 1); WIN_Z1(0, 2); WIN_Z1(0, 3); WIN_Z1(1, 0); WIN_Z1(1, 1); WIN_Z1(1, 2); WIN_Z1(1, 3);
  }
  TS(20);
  BAR();
  TS(21);
  if (ZA) {
    char* tr = smem + L4_TR + wid * 4096;
#pragma unroll
    for (int m = 0; m < 2; ++m) {
      const f32x2 f = *(const f32x2*)(fin + (m * 32 + r) * 2);
#pragma unroll
      for (int qd = 0; qd < 4; ++qd)
        *(u32x2*)(tr + (qd >> 1) * 2048 + (m * 32 + r) * 32 + (2 * (qd & 1) + h) * 8) =
            (u32x2){pack2(acc[m][4 * qd] * f[0] - f[1], acc[m][4 * qd + 1] * f[0] - f[1]), pack2(acc[m][4 * qd + 2] * f[0] - f[1], acc[m][4 * qd + 3] * f[0] - f[1])};
    }
    const char* p = tr + (8 * (lane >> 4) + ((lane & 15) >> 2)) * 32 + (lane & 3) * 8;
    bf16x4 lo[2][2], hi[2][2];
#pragma unroll
    for (int x = 0; x < 2; ++x)
#pragma unroll
      for (int ks = 0; ks < 2; ++ks) { lo[x][ks] = lds_tr(p + x * 2048 + ks * 1024); hi[x][ks] = lds_tr(p + x * 2048 + ks * 1024 + 128); }
#pragma unroll
    for (int x = 0; x < 2; ++x)
#pragma unroll
      for (int ks = 0; ks < 2; ++ks)
        stc16(rs, lane * 16 + x * 1024 + (zb || (x == 0 && cb < 6) ? 0u : DEAD), OFF_Z2 + ((panel * 2 + ks) * 54 + u0 + la) * 1024,
              __builtin_bit_cast(u32x4, (bf16x8)__builtin_shufflevector(lo[x][ks], hi[x][ks], 0, 1, 2, 3, 4, 5, 6, 7)));
    waitv<0>();
    win_tail(ws, wsp, smem, panel, cb, wid, lane TPASS);
    sp_ring(wsp, smem, panel, wid, lane);
  } else {
    __builtin_amdgcn_s_sleep(10);
    sp_ring(wsp, smem, panel, wid, lane);
    win_tail(ws, wsp, smem, panel, cb, wid, lane TPASS);
    sp_prefetch(wr, wsp, panel, wid, lane);
  }
}

DI void win_agent(char* __restrict__ ws, rsrc_t rs, const u16* __restrict__ wsp, char* smem, u32x4 (&wr)[16 - K5_R][2] TARG) {
  const int tid = fresh_tid(), lane = tid & 63, wid = 7;
  const int xcd = blockIdx.x & 7, idx = blockIdx.x >> 3, panel = xcd * 4 + (idx >> 3), cb = idx & 7, u0 = unit0_of(cb);
  float* lnp = (float*)(smem + L4_LN); float* st2 = (float*)(smem + L4_ST2); float* fin = (float*)(smem + L4_FIN); float* cbl = (float*)(smem + L4_CB);
  int* cnt = (int*)(smem + L4_CNT);
  __builtin_amdgcn_s_setprio(2);
  if (lane == 0) *cnt = 0;
  poll_ge(ws, lane < 8 ? C_ATT + lane : C_X1 + panel, lane < 8 ? 32 : 8);
  BAR();
  stage_x1s(ws, smem, panel, wid, tid);
  {
    u32x2 t[16]; f32x4 cp[5][2];
#pragma unroll
    for (int i = 0; i < 16; ++i) t[i] = __builtin_amdgcn_raw_buffer_load_b64(rs, lane * 8, OFF_ST1 + (panel * 16 + i) * 512, 16);
    const unsigned co = lane < 28 ? (u0 * 16 + lane * 4) * 4 : ((54 + u0) * 16 + min(lane - 28, 27) * 4) * 4;
#pragma unroll
    for (int i = 0; i < 5; ++i) { cp[i][0] = ldc16f(rs, co, OFF_CBP + i * 16384); cp[i][1] = ldc16f(rs, co, OFF_CBP + i * 16384 + 8192); }
    float s = 0.f, q = 0.f;
#pragma unroll
    for (int i = 0; i < 16; ++i) { s += __uint_as_float(t[i][0]); q += __uint_as_float(t[i][1]); }
    const float mean = s * (1.f / 640.f), var = fmaxf(q * (1.f / 640.f) - mean * mean, 0.f);
    const float den = ln_den(var), ri = 1.f / den;
    *(f32x4*)(lnp + lane * 4) = (f32x4){den, -rbf(mean), GC1 * ri, GC2 * ri * ri * ri};
#pragma unroll
    for (int v = 0; v < 2; ++v) *(f32x4*)(cbl + v * 256 + lane * 4) = ((cp[0][v] + cp[1][v]) + (cp[2][v] + cp[3][v])) + cp[4][v];
  }
  waitv<0>();
  BAR();
  __builtin_amdgcn_s_setprio(0);
  __builtin_amdgcn_s_sleep(32);
  sp_prefetch(wr, wsp, panel, wid, lane);
  __builtin_amdgcn_s_setprio(2);
#pragma nounroll
  for (unsigned spin = 0; spin < (1u << 24); ++spin) {
    if (__hip_atomic_load(cnt, __ATOMIC_RELAXED, __HIP_MEMORY_SCOPE_WORKGROUP) >= 4) break;
    __builtin_amdgcn_s_sleep(1);
  }
  asm volatile("" ::: "memory");
  float s, q;
  {
    const f32x2 t0 = *(const f32x2*)(st2 + lane * 2), t1 = *(const f32x2*)(st2 + (64 + lane) * 2), t2 = *(const f32x2*)(st2 + (128 + lane) * 2), t3 = *(const f32x2*)(st2 + (192 + lane) * 2);
    s = (t0[0] + t1[0]) + t2[0]; q = (t0[1] + t1[1]) + t2[1];
    if (cb < 6) { s += t3[0]; q += t3[1]; }
  }
  GAS u64* xch = (GAS u64*)(ws + OFF_XCH) + panel * 512;
  unsigned qb = __float_as_uint(q); if (qb == ~0u) qb = 0x7fc00000u;
  __hip_atomic_store(xch + cb * 64 + lane, ((u64)qb << 32) | __float_as_uint(s), __ATOMIC_RELAXED, __HIP_MEMORY_SCOPE_AGENT);
  TS(19);
  u64 v[8];
#pragma nounroll
  for (unsigned spin = 0; spin < (1u << 21); ++spin) {
    bool ok = true;
#pragma unroll
    for (int i = 0; i < 8; ++i) { v[i] = __hip_atomic_load(xch + i * 64 + lane, __ATOMIC_RELAXED, __HIP_MEMORY_SCOPE_AGENT); ok = ok && ((unsigned)(v[i] >> 32) != ~0u); }
    if (__all(ok)) break;
    __builtin_amdgcn_s_sleep(1);
  }
  s = 0.f; q = 0.f;
#pragma unroll
  for (int i = 0; i < 8; ++i) { s += __uint_as_float((unsigned)v[i]); q += __uint_as_float((unsigned)(v[i] >> 32)); }
  const float mean = s * (1.f / 864.f), var = fmaxf(q * (1.f / 864.f) - mean * mean, 0.f);
  const float ri = 1.f / ln_den(var);
  *(f32x2*)(fin + lane * 2) = (f32x2){ri, rbf(mean) * ri};
  __builtin_amdgcn_s_setprio(0);
  TS(20);
  BAR();
  __builtin_amdgcn_s_sleep(10);
  sp_ring(wsp, smem, panel, wid, lane);
  win_tail(ws, wsp, smem, panel, cb, wid, lane TPASS);
}

#define SP_LB 4
DI void phase_sp(char* __restrict__ ws, rsrc_t rs, const u16* __restrict__ bsp, char* smem, int wid, const u32x4 (&wr)[16 - K5_R][2] TARG) {
  const int tid = fresh_tid(), lane = tid & 63;
  const int xcd = blockIdx.x & 7, idx = blockIdx.x >> 3, panel = xcd * 4 + (idx >> 3), cb = idx & 7;
  const int kh = wid >> 2, up = wid & 3, u0 = unit0_of(cb) + 2 * up, nu = cb < 6 ? 7 : 6, g = lane >> 4, r = lane & 15;
  const unsigned dead[2] = {2 * up < nu ? 0u : DEAD, 2 * up + 1 < nu ? 0u : DEAD};
  const unsigned bvo[2] = {lane * 16 + dead[0], lane * 16 + 1024 + dead[1]};
  u16 bb[2];
#pragma unroll
  for (int j = 0; j < 2; ++j) bb[j] = bsp[panel * 64 + (2 * kh + j) * 16 + r];
  const char* al[2][2];
#pragma unroll
  for (int k = 0; k < 2; ++k)
#pragma unroll
    for (int j = 0; j < 2; ++j) al[k][j] = smem + kh * 8192 + (j ^ kh) * 4096 + r * 128 + ((4 * k + g) ^ (r >> 1)) * 16;
  constexpr int LB = SP_LB, PD = 3;
  bf16x8 bq[LB + 1][4], af[PD + 1][2]; f32x4 acc[2][4]; u32x4 wq[17];
  if (wid == 7) poll_ge(ws, C_Z2 + (panel >> 3) * 8 + cb, 32);
#pragma unroll
  for (int i = 0; i < 2; ++i) {
    const float b0 = bf2f(bb[0]), b1 = bf2f(bb[1]);
    acc[i][0] = (f32x4){b0, b0, b0, b0}; acc[i][1] = (f32x4){b1, b1, b1, b1}; acc[i][2] = (f32x4){}; acc[i][3] = (f32x4){};
  }
  if (kh && up < 3) waitv<2 * (16 - K5_R)>(); else waitv<0>();
  BAR();
  TS(23);
  const unsigned bso = OFF_Z2 + (kh * 2 * 54 + u0) * 1024;
#define SP_LD(s, j) (bq[(s) % (LB + 1)][j] = BF8(ldc16(rs, bvo[(j) & 1], bso + (sp_stage(s) * 4 + ((j) >> 1)) * 55296)))
#define SP_RD(t, k) (af[(t) % (PD + 1)][k] = *(const bf16x8*)(al[k][((t) >> 1) & 1] + (((t) >> 2) % K5_R) * 16384 + ((t) & 1) * 2048))
#pragma unroll
  for (int s = 0; s < LB; ++s)
#pragma unroll
    for (int j = 0; j < 4; ++j) SP_LD(s, j);
#pragma unroll
  for (int t = 0; t < PD; ++t) { SP_RD(t, 0); SP_RD(t, 1); }
  SCHED();
  sfor<64>([&](auto T) __attribute__((always_inline)) {
    constexpr int t = T, nx = t + PD, s = t >> 2, m = t & 3, tl = t + 4 * LB;
    if constexpr (nx == 16 || nx == 28 || nx == 44 || nx == 56) { BAR(); TS(32 + (nx == 28) + 2 * (nx == 44) + 3 * (nx == 56)); }
    constexpr int c = nx >= 16 && nx < 24 ? nx - 16 : nx >= 28 && nx < 34 ? nx - 20 : nx >= 44 && nx < 48 ? nx - 30 : -1;
    if constexpr (nx < 64) SP_RD(nx, 0);
    acc[0][m] = MFMA16(bq[s % (LB + 1)][0], af[t % (PD + 1)][0], acc[0][m]);
    acc[0][m] = MFMA16(bq[s % (LB + 1)][2], af[t % (PD + 1)][1], acc[0][m]); PIN(acc[0][m]);
    if constexpr (tl < 64) SP_LD(tl >> 2, m); else if constexpr (tl - 64 < 17) wq[tl - 64] = proj_ld<true>(rs, tl - 64, wid, lane);
    SCHED();
    if constexpr (nx < 64) SP_RD(nx, 1);
    acc[1][m] = MFMA16(bq[s % (LB + 1)][1], af[t % (PD + 1)][0], acc[1][m]);
    acc[1][m] = MFMA16(bq[s % (LB + 1)][3], af[t % (PD + 1)][1], acc[1][m]); PIN(acc[1][m]);
    if constexpr (c >= 0) *(u32x4*)(smem + ((c >> 1) % K5_R) * 16384 + wid * 2048 + (c & 1) * 1024 + lane * 16) = wr[c >> 1][c & 1];
    if constexpr (t == 63 && 4 * LB < 17) { _Pragma("unroll") for (int i = 4 * LB; i < 17; ++i) wq[i] = proj_ld<true>(rs, i, wid, lane); }
    SCHED();
  });
  TS(24);
  f32x4* exw = (f32x4*)(smem + 32768 + wid * 4096) + lane;
  const f32x4* exr = (const f32x4*)(smem + 32768 + (wid ^ 4) * 4096) + lane;
  u32x4 z[2];
#pragma unroll
  for (int i = 0; i < 2; ++i) z[i] = *(const u32x4*)(smem + L4_Z1 + (2 * up + i) * 2048 + kh * 1024 + lane * 16);
#pragma unroll
  for (int i = 0; i < 2; ++i)
#pragma unroll
    for (int j = 0; j < 2; ++j) exw[(i * 2 + j) * 64] = acc[i][2 + j];
  f32x4 zf[2][2];
#pragma unroll
  for (int i = 0; i < 2; ++i)
#pragma unroll
    for (int j = 0; j < 2; ++j) {
      zf[i][j] = (f32x4){bflo(z[i][2 * j]), bfhi(z[i][2 * j]), bflo(z[i][2 * j + 1]), bfhi(z[i][2 * j + 1])};
      acc[i][j] *= zf[i][j]; PIN(zf[i][j]); PIN(acc[i][j]);
    }
  BAR();
  f32x4 oth[2][2];
#pragma unroll
  for (int i = 0; i < 2; ++i)
#pragma unroll
    for (int j = 0; j < 2; ++j) oth[i][j] = exr[(i * 2 + j) * 64];
#pragma unroll
  for (int i = 0; i < 2; ++i)
#pragma unroll
    for (int j = 0; j < 2; ++j) {
      const f32x4 v = acc[i][j] + zf[i][j] * oth[i][j];
      stc8(rs, ((g >> 1) * 16 + r) * 16 + (g & 1) * 8 + dead[i], OFF_U + (panel * 4 + 2 * kh + j) * 27648 + (u0 + i) * 512, (u32x2){pack2(v[0], v[1]), pack2(v[2], v[3])});
    }
  TS(25);
  waitv<0>();
  BAR();
  TS(26);
  if (wid == 7) arrive(ws, C_U + panel, lane);
#pragma unroll
  for (int i = 0; i < 17; ++i) proj_park<true>(smem, i, wid, lane, wq[i]);
}

extern "C" __global__ __launch_bounds__(512, 2) OCC2 void k_mega(char* __restrict__ ws, const u16* __restrict__ x, const u16* __restrict__ win,
                                                     const u16* __restrict__ wsp, const u16* __restrict__ bsp, const u16* __restrict__ wout,
                                                     const u16* __restrict__ cvec, const u16* __restrict__ wmod, u16* __restrict__ out) {
  extern __shared__ __attribute__((aligned(16))) char smem[];
  const int wid = __builtin_amdgcn_readfirstlane(threadIdx.x >> 6);
  const rsrc_t rs = __builtin_amdgcn_make_buffer_rsrc(ws, 0, WS_USED, 0x00020000);
  TDECL; TS(0);
  u32x2 res[3];
  phase_qkv(ws, rs, cvec, wmod, smem, wid TPASS);
  phase_attn(ws, rs, (const char*)wsp, win, wout, smem, wid TPASS);
  u32x4 wr[16 - K5_R][2];
  auto nohook = []() __attribute__((always_inline)) {};
  if (wid < 4) {
    bf16x8 b[40];
    proj_body<false, 0>(ws, rs, x, out, smem, wid, res, [&]() __attribute__((always_inline)) {
      const unsigned bo = win_boff(wid, fresh_tid() & 63);
#pragma unroll
      for (int k = 0; k < WIN_PRE; ++k) b[k] = WIN_LD(k);
    } TPASS);
    win_body<true>(ws, rs, wsp, smem, wid, b, wr TPASS);
  } else {
    proj_body<false, 1>(ws, rs, x, out, smem, wid, res, nohook TPASS);
    bf16x8 b[40];
    if (wid < 7) win_body<false>(ws, rs, wsp, smem, wid, b, wr TPASS);
    else win_agent(ws, rs, wsp, smem, wr TPASS);
  }
  phase_sp(ws, rs, bsp, smem, wid, wr TPASS);
  if (wid < 4) proj_body<true, 0>(ws, rs, x, out, smem, wid, res, nohook TPASS);
  else proj_body<true, 1>(ws, rs, x, out, smem, wid, res, nohook TPASS);
  TS(29);
  TDUMP();
}

extern "C" void kernel_launch(void* const* d_in, const int* in_sizes, int n_in, void* d_out, int out_size, void* d_ws, size_t ws_size,
                              hipStream_t stream) {
  const u16* x = (const u16*)d_in[0];
  const u16* wqkv = (const u16*)d_in[1];
  const u16* wo = (const u16*)d_in[2];
  const u16* win = (const u16*)d_in[4];
  const u16* wsp = (const u16*)d_in[5];
  const u16* bsp = (const u16*)d_in[6];
  const u16* wout = (const u16*)d_in[7];
  const u16* cvec = (const u16*)d_in[8];
  const u16* wmod = (const u16*)d_in[9];
  char* ws = (char*)d_ws;
  (void)hipFuncSetAttribute((const void*)k_mega, hipFuncAttributeMaxDynamicSharedMemorySize, 163840);
  k0_prep<<<K0_GRID, 256, 0, stream>>>(x, wqkv, wo, cvec, wmod, ws, (const char*)win, (const char*)wsp, (const char*)wout, (const char*)bsp, (const char*)d_out);
  k_mega<<<256, 512, 163840, stream>>>(ws, x, win, wsp, bsp, wout, cvec, wmod, (u16*)d_out);
}
```

```cpp
#include <hip/hip_runtime.h>
#include <hip/hip_bf16.h>
#include <stdint.h>
#include <utility>

typedef __attribute__((ext_vector_type(8))) short bf16x8;
typedef __attribute__((ext_vector_type(4))) short bf16x4;
typedef __attribute__((ext_vector_type(2))) float f32x2;
typedef __attribute__((ext_vector_type(4))) float f32x4;
typedef __attribute__((ext_vector_type(16))) float f32x16;
typedef __attribute__((ext_vector_type(2))) unsigned u32x2;
typedef __attribute__((ext_vector_type(4))) unsigned u32x4;
typedef __attribute__((ext_vector_type(2))) __bf16 bf2_t;
typedef __attribute__((ext_vector_type(2))) _Float16 h2_t;
typedef unsigned short u16;
typedef unsigned long long u64;
#define LAS __attribute__((address_space(3)))
#define GAS __attribute__((address_space(1)))
#define DI __device__ __forceinline__

constexpr int D_ = 640, NQKV = 1920, F_ = 1728;

constexpr unsigned OFF_CNT = 0;
constexpr unsigned OFF_MOD = 65536;
constexpr unsigned OFF_MODP = OFF_MOD + 16384;
constexpr unsigned OFF_CBP = OFF_MODP + 40960;
constexpr unsigned OFF_XCH = OFF_CBP + 5 * 16384;
constexpr unsigned OFF_ST1 = OFF_XCH + 131072;
constexpr unsigned OFF_XHAT = OFF_ST1 + 262144;
constexpr unsigned OFF_WQKV = OFF_XHAT + 2621440;
constexpr unsigned OFF_WO = OFF_WQKV + 2457600;
constexpr unsigned OFF_WIN = OFF_WO + 819200;
constexpr unsigned OFF_WOUT = OFF_WIN + 2211840;
constexpr unsigned OFF_Q = OFF_WOUT + 1105920;
constexpr unsigned OFF_KV = OFF_Q + 32 * 81920;
constexpr unsigned OFF_O = OFF_KV + 32 * 163840;
constexpr unsigned OFF_X1S = OFF_O + 2621440;
constexpr unsigned OFF_Z2 = OFF_X1S + 2621440;
constexpr unsigned OFF_U = OFF_Z2 + 3538944;
constexpr unsigned WS_USED = OFF_U + 3538944;
constexpr int C_QKV = 0, C_MOD = 15  , C_ATT = 128, C_REL = 136, C_X1 = 144, C_U = 176, C_Z2 = 208  ;

#define STAMP(kid, k)
#define TDECL
#define TS(k)
#define TDUMP()
DI unsigned pack2(float lo, float hi) { return __builtin_bit_cast(unsigned, __builtin_convertvector((f32x2){lo, hi}, bf2_t)); }
DI unsigned pack2s(float lo, float hi) { unsigned r; asm("v_cvt_pk_bf16_f32 %0, %1, %2" : "=v"(r) : "v"(lo), "v"(hi)); return r; }
DI float opq(float x) { asm("" : "+v"(x)); return x; }
DI unsigned pack2h(float lo, float hi) { return __builtin_bit_cast(unsigned, __builtin_convertvector((f32x2){lo, hi}, h2_t)); }
DI float bflo(unsigned u) { return __uint_as_float(u << 16); }
DI float bfhi(unsigned u) { return __uint_as_float(u & 0xffff0000u); }
DI float rbf(float x) { return bflo(pack2(x, x)); }
DI float bf2f(u16 b) { return __uint_as_float((unsigned)b << 16); }
#define MFMA32(a, b, c) __builtin_amdgcn_mfma_f32_32x32x16_bf16((a), (b), (c), 0, 0, 0)
#define MFMA32K8(a, b, c) __builtin_amdgcn_mfma_f32_32x32x8bf16_1k((a), (b), (c), 0, 0, 0)
#define MFMA16(a, b, c) __builtin_amdgcn_mfma_f32_16x16x32_bf16((a), (b), (c), 0, 0, 0)
#define BAR() asm volatile("s_waitcnt lgkmcnt(0)\n\ts_barrier" ::: "memory")
#define SCHED() __builtin_amdgcn_sched_barrier(0)
#define PIN(x) asm volatile("" : "+v"(x))
#define OCC2 __attribute__((amdgpu_waves_per_eu(2, 2)))
#define BF8(x) __builtin_bit_cast(bf16x8, x)
#define BF4(x) __builtin_bit_cast(bf16x4, x)
template <int N> DI void waitv() { asm volatile("s_waitcnt vmcnt(%0)" ::"i"(N) : "memory"); }
template <class F, int... I> DI void sfor_impl(F&& f, std::integer_sequence<int, I...>) { (f(std::integral_constant<int, I>{}), ...); }
template <int N, class F> DI void sfor(F&& f) { sfor_impl(f, std::make_integer_sequence<int, N>{}); }
typedef short v4i16_t __attribute__((ext_vector_type(4)));
DI bf16x4 lds_tr(const char* p) { return __builtin_bit_cast(bf16x4, __builtin_amdgcn_ds_read_tr16_b64_v4i16((LAS v4i16_t*)p)); }
DI void glds16(const void* g, unsigned lds_base) {
  unsigned sv;
  asm volatile("s_mov_b32 %0, m0\n\ts_mov_b32 m0, %2\n\ts_nop 0\n\tglobal_load_lds_dwordx4 %1, off\n\ts_mov_b32 m0, %0" : "=&s"(sv) : "v"(g), "s"(lds_base) : "memory");
}
DI void glds16c(const void* g, unsigned lds_base) {
  unsigned sv;
  asm volatile("s_mov_b32 %0, m0\n\ts_mov_b32 m0, %2\n\ts_nop 0\n\tglobal_load_lds_dwordx4 %1, off sc1\n\ts_mov_b32 m0, %0" : "=&s"(sv) : "v"(g), "s"(lds_base) : "memory");
}
DI void glds4c(const void* g, unsigned lds_base) {
  unsigned sv;
  asm volatile("s_mov_b32 %0, m0\n\ts_mov_b32 m0, %2\n\ts_nop 0\n\tglobal_load_lds_dword %1, off sc1\n\ts_mov_b32 m0, %0" : "=&s"(sv) : "v"(g), "s"(lds_base) : "memory");
}
typedef __amdgpu_buffer_rsrc_t rsrc_t;
DI u32x4 ldc16(rsrc_t rs, unsigned voff, unsigned soff) { return __builtin_amdgcn_raw_buffer_load_b128(rs, voff, soff, 16); }
DI unsigned ldc4(rsrc_t rs, unsigned voff, unsigned soff) { return __builtin_amdgcn_raw_buffer_load_b32(rs, voff, soff, 16); }
DI void stc16(rsrc_t rs, unsigned voff, unsigned soff, u32x4 v) { __builtin_amdgcn_raw_buffer_store_b128(v, rs, voff, soff, 16); asm volatile("s_nop 1" ::"v"(v) : "memory"); }
DI void stc8(rsrc_t rs, unsigned voff, unsigned soff, u32x2 v) { __builtin_amdgcn_raw_buffer_store_b64(v, rs, voff, soff, 16); }
DI void stc4(rsrc_t rs, unsigned voff, unsigned soff, unsigned v) { __builtin_amdgcn_raw_buffer_store_b32(v, rs, voff, soff, 16); }
DI f32x4 ldc16f(rsrc_t rs, unsigned voff, unsigned soff) { return __builtin_bit_cast(f32x4, ldc16(rs, voff, soff)); }
DI void arrive(char* ws, int c, int lane) { if (lane == 0) __hip_atomic_fetch_add((GAS unsigned*)(ws + OFF_CNT + c * 256), 1u, __ATOMIC_RELAXED, __HIP_MEMORY_SCOPE_AGENT); }
DI void poll_ge(char* ws, int c, unsigned thr) {
  const GAS unsigned* p = (const GAS unsigned*)(ws + OFF_CNT + c * 256);
#pragma nounroll
  for (unsigned spin = 0; spin < (1u << 21); ++spin) {
    if (__all(__hip_atomic_load(p, __ATOMIC_RELAXED, __HIP_MEMORY_SCOPE_AGENT) >= thr)) break;
    __builtin_amdgcn_s_sleep(1);
  }
  asm volatile("" ::: "memory");
}
DI int fresh_tid() { int t = threadIdx.x; asm volatile("" : "+v"(t)); return t; }
DI void l2touch(const void* g, unsigned lds_scratch) {
  unsigned sv;
  asm volatile("s_mov_b32 %0, m0\n\ts_mov_b32 m0, %2\n\ts_nop 0\n\tglobal_load_lds_dword %1, off\n\ts_mov_b32 m0, %0" : "=&s"(sv) : "v"(g), "s"(lds_scratch) : "memory");
}
#define LDNT(p) __builtin_nontemporal_load(p)
DI float silu_bf(u16 c) { const float f = __uint_as_float((unsigned)c << 16); const float v = f / (1.f + __expf(-f)); return __uint_as_float(__builtin_bit_cast(unsigned, __builtin_convertvector((f32x2){v, v}, bf2_t)) << 16); }
DI unsigned lds_addr(const void* p) { return (unsigned)(uintptr_t)(const LAS void*)p; }
DI float xhalf(float v) {
  auto rr = __builtin_amdgcn_permlane32_swap(__float_as_uint(v), __float_as_uint(v), false, false);
  return __uint_as_float((threadIdx.x & 32) ? rr[0] : rr[1]);
}
DI float sum_g(float v) {
  auto a = __builtin_amdgcn_permlane16_swap(__float_as_uint(v), __float_as_uint(v), false, false);
  v = __uint_as_float(a[0]) + __uint_as_float(a[1]);
  auto b = __builtin_amdgcn_permlane32_swap(__float_as_uint(v), __float_as_uint(v), false, false);
  return __uint_as_float(b[0]) + __uint_as_float(b[1]);
}
template <int CTRL> DI float dpp_add(float v) { return v + __uint_as_float(__builtin_amdgcn_update_dpp(0u, __float_as_uint(v), CTRL, 0xf, 0xf, true)); }
DI float sum16(float v) {
  v = dpp_add<0x128>(v); v = dpp_add<0x124>(v); v = dpp_add<0x122>(v); return dpp_add<0x121>(v);
}
DI u32x4 modulate8(u32x4 v, f32x4 sc0, f32x4 sc1, f32x4 sh0, f32x4 sh1) {
  unsigned o0, o1, o2, o3; float t0, t1, t2, t3;
  asm volatile(
      "v_fma_mix_f32 %0, %8, %12, %20 op_sel_hi:[1,0,0]\n\tv_fma_mix_f32 %4, %8, %13, %21 op_sel:[1,0,0] op_sel_hi:[1,0,0]\n\t"
      "v_fma_mix_f32 %1, %9, %14, %22 op_sel_hi:[1,0,0]\n\tv_fma_mix_f32 %5, %9, %15, %23 op_sel:[1,0,0] op_sel_hi:[1,0,0]\n\t"
      "v_fma_mix_f32 %2, %10, %16, %24 op_sel_hi:[1,0,0]\n\tv_fma_mix_f32 %6, %10, %17, %25 op_sel:[1,0,0] op_sel_hi:[1,0,0]\n\t"
      "v_fma_mix_f32 %3, %11, %18, %26 op_sel_hi:[1,0,0]\n\tv_fma_mix_f32 %7, %11, %19, %27 op_sel:[1,0,0] op_sel_hi:[1,0,0]\n\t"
      "v_cvt_pk_bf16_f32 %0, %0, %4\n\tv_cvt_pk_bf16_f32 %1, %1, %5\n\tv_cvt_pk_bf16_f32 %2, %2, %6\n\tv_cvt_pk_bf16_f32 %3, %3, %7"
      : "=&v"(o0), "=&v"(o1), "=&v"(o2), "=&v"(o3), "=&v"(t0), "=&v"(t1), "=&v"(t2), "=&v"(t3)
      : "v"(v[0]), "v"(v[1]), "v"(v[2]), "v"(v[3]), "v"(sc0[0]), "v"(sc0[1]), "v"(sc0[2]), "v"(sc0[3]), "v"(sc1[0]), "v"(sc1[1]), "v"(sc1[2]), "v"(sc1[3]),
        "v"(sh0[0]), "v"(sh0[1]), "v"(sh0[2]), "v"(sh0[3]), "v"(sh1[0]), "v"(sh1[1]), "v"(sh1[2]), "v"(sh1[3]));
  return (u32x4){o0, o1, o2, o3};
}
DI float ln_den(float var) { return rbf(__builtin_sqrtf(rbf(rbf(var) + 1e-5f))); }
DI unsigned a16_off(int n, int r) { return (n >> 5) * 1024 + (((n & 31) >> 3) * 16 + r) * 16 + (n & 4) * 2; }
DI int unit0_of(int j) { return j < 6 ? 7 * j : 42 + 6 * (j - 6); }

DI void K0ST(char* p, u32x4 v) { *(u32x4*)p = v; }
constexpr int K0_MOD = 160, K0_LN = 128, K0_RL = 240, K0_WO = 50, K0_INIT = 12;
constexpr int K0_GRID = K0_MOD + K0_LN + K0_RL + K0_WO + K0_INIT;

__global__ __launch_bounds__(256) void k0_prep(const u16* __restrict__ x, const u16* __restrict__ wqkv, const u16* __restrict__ wo, const u16* __restrict__ cvec,
                                               const u16* __restrict__ wmod, char* __restrict__ ws) {
  __shared__ __attribute__((aligned(16))) char smem[4 * 4608];
  const int tid = threadIdx.x, lane = tid & 63, wid = __builtin_amdgcn_readfirstlane(tid >> 6);
  int b = blockIdx.x;
  STAMP(0, 0);
  if (b < K0_MOD) {
    const int cg = b >> 2, ks = b & 3, c = tid & 7, row0 = ks * 160 + (tid >> 3), col0 = cg * 64 + (cg >= 20 ? 640 : 0);
    u32x4 w[5]; u16 cv[5];
#pragma unroll
    for (int i = 0; i < 5; ++i) { w[i] = LDNT((const u32x4*)(wmod + (size_t)(row0 + 32 * i) * 3840 + col0 + c * 8)); cv[i] = cvec[row0 + 32 * i]; }
    float acc[8] = {0, 0, 0, 0, 0, 0, 0, 0};
#pragma unroll
    for (int i = 0; i < 5; ++i) {
      const float s = silu_bf(cv[i]);
#pragma unroll
      for (int e = 0; e < 4; ++e) { acc[2 * e] += s * bflo(w[i][e]); acc[2 * e + 1] += s * bfhi(w[i][e]); }
    }
#pragma unroll
    for (int e = 0; e < 8; ++e) acc[e] = sum_g(dpp_add<0x128>(acc[e]));
    float* red = (float*)smem;
    if (lane < 8) { *(f32x4*)(red + wid * 64 + lane * 8) = (f32x4){acc[0], acc[1], acc[2], acc[3]}; *(f32x4*)(red + wid * 64 + lane * 8 + 4) = (f32x4){acc[4], acc[5], acc[6], acc[7]}; }
    __syncthreads();
    if (tid < 64) ((float*)(ws + OFF_MODP))[ks * 2560 + cg * 64 + tid] = (red[tid] + red[64 + tid]) + (red[128 + tid] + red[192 + tid]);
    STAMP(0, 1); STAMP(0, 13);
    return;
  }
  b -= K0_MOD;
  if (b < K0_LN) {
    b = (b & 7) * 16 + (b >> 3);
    const int row = b * 16 + wid * 4 + (lane >> 4), c = lane & 15;
    const u16* xr = x + (size_t)row * D_ + c * 8;
    u32x4 v[5];
#pragma unroll
    for (int i = 0; i < 5; ++i) v[i] = *(const u32x4*)(xr + i * 128);
    float s = 0.f, q = 0.f;
#pragma unroll
    for (int i = 0; i < 5; ++i)
#pragma unroll
      for (int e = 0; e < 4; ++e) { const float a = bflo(v[i][e]), d = bfhi(v[i][e]); s += a + d; q += a * a + d * d; }
    s = sum16(s); q = sum16(q);
    const float mean = s * (1.f / 640.f); const float var = fmaxf(q * (1.f / 640.f) - mean * mean, 0.f);
    const float rinv = 1.f / ln_den(var), mb = rbf(mean) * rinv;
    char* dst = ws + OFF_XHAT + (size_t)row * 1280 + c * 16;
#pragma unroll
    for (int i = 0; i < 5; ++i) {
      u32x4 o;
#pragma unroll
      for (int e = 0; e < 4; ++e) o[e] = pack2h(bflo(v[i][e]) * rinv - mb, bfhi(v[i][e]) * rinv - mb);
      K0ST(dst + i * 256, o);
    }
    STAMP(0, 2); STAMP(0, 13);
    return;
  }
  b -= K0_LN;
  if (b < K0_RL) {
    const int kt = b / 12, G = b % 12, sec = G >> 2, gq = G & 3;
    u32x4 v[3];
#pragma unroll
    for (int i = 0; i < 3; ++i) { const int ci = min(i * 256 + tid, 639); v[i] = LDNT((const u32x4*)(wqkv + (size_t)(kt * 32 + ci / 20) * NQKV + G * 160 + (ci % 20) * 8)); }
#pragma unroll
    for (int i = 0; i < 3; ++i) { const int ci = min(i * 256 + tid, 639); *(u32x4*)(smem + (ci / 20) * 336 + (ci % 20) * 16) = v[i]; }
    __syncthreads();
    const int g = lane >> 4, q = (lane & 15) >> 2, p = lane & 3;
    for (int f = wid; f < 10; f += 4) {
      const int t = f >> 1, x2 = f & 1;
      const int col = sec == 2 ? 16 * f + 4 * p : t < 4 ? 20 * (2 * t + (p >> 1)) + 8 * (p & 1) + 4 * x2 : 20 * (4 * x2 + p) + 16;
      const int T = sec == 2 ? 40 + gq * 5 + t : sec * 20 + (t < 4 ? 4 * gq + t : 16 + gq);
      const char* pp = smem + (8 * g + q) * 336 + col * 2;
      const bf16x4 lo = lds_tr(pp), hi = lds_tr(pp + 4 * 336);
      K0ST(ws + OFF_WQKV + (((size_t)T * 20 + kt) * 2 + x2) * 1024 + lane * 16, __builtin_bit_cast(u32x4, (bf16x8)__builtin_shufflevector(lo, hi, 0, 1, 2, 3, 4, 5, 6, 7)));
    }
    STAMP(0, 3); STAMP(0, 13);
    return;
  }
  b -= K0_RL;
  if (b < K0_WO) {
    const int job = b * 4 + wid, kt = job / 10, ng = job % 10, j8 = lane >> 3;
    char* t = smem + wid * 4608;
    u32x4 v[4];
#pragma unroll
    for (int i = 0; i < 4; ++i) {
      const int c = 4 * kt + i, row = c < 64 ? 20 * (c >> 1) + 4 * (c & 1) + (j8 & 3) + 8 * (j8 >> 2) : 20 * (2 * (c - 64) + (j8 >> 2)) + 16 + (j8 & 3);
      v[i] = LDNT((const u32x4*)(wo + (size_t)row * D_ + ng * 64 + (lane & 7) * 8));
    }
#pragma unroll
    for (int i = 0; i < 4; ++i) *(u32x4*)(t + (i * 8 + j8) * 144 + (lane & 7) * 16) = v[i];
    const char* pp = t + (8 * (lane >> 4) + ((lane & 15) >> 2)) * 144 + (lane & 3) * 8;
#pragma unroll
    for (int f = 0; f < 4; ++f) {
      const bf16x4 lo = lds_tr(pp + f * 32), hi = lds_tr(pp + 4 * 144 + f * 32);
      K0ST(ws + OFF_WO + ((size_t)(ng * 4 + f) * 20 + kt) * 1024 + lane * 16, __builtin_bit_cast(u32x4, (bf16x8)__builtin_shufflevector(lo, hi, 0, 1, 2, 3, 4, 5, 6, 7)));
    }
    return;
  }
  b -= K0_WO;
  {
    u32x4* p = (u32x4*)(ws + (b < 8 ? OFF_XCH + b * 16384 : OFF_CNT + (b - 8) * 16384)) + tid;
    const unsigned f = b < 8 ? ~0u : 0u;
#pragma unroll
    for (int i = 0; i < 4; ++i) p[i * 256] = (u32x4){f, f, f, f};
  }
}

constexpr float QSCALE = 0.22360679774997896f * 1.4426950408889634f;
#define K1W 3
constexpr int K1_W = K1W;

template <bool ISV>
DI void k1_mma(const char* __restrict__ bsrc, const char* smem, int lane, f32x4 (&acc)[8][2]) {
  constexpr int PD = 4;
  bf16x8 bfr[K1_W + 1][4], af[PD + 1][2];
  const char* lds[2];
#pragma unroll
  for (int k = 0; k < 2; ++k) lds[k] = smem + k * 8192 + (lane ^ (4 * k + (lane >> 4))) * 16;
#pragma unroll
  for (int i = 0; i < 4 * K1_W; ++i) bfr[i >> 2][i & 3] = *(const bf16x8*)(bsrc + i * 1024);
#pragma unroll
  for (int m = 0; m < 8; ++m) { acc[m][0] = (f32x4){}; acc[m][1] = (f32x4){}; }
  BAR();
  BAR();
  __builtin_amdgcn_s_setprio(1);
#define K1_RD(t, k) (af[(t) % (PD + 1)][k] = *(const bf16x8*)(lds[k] + (((t) >> 3) % 3) * 16384 + ((t) & 7) * 1024))
#define K1_MM(c, b, a) (c = ISV ? MFMA16(a, b, c) : MFMA16(b, a, c))
#pragma unroll
  for (int t = 0; t < PD; ++t) { K1_RD(t, 0); K1_RD(t, 1); }
  SCHED();
  sfor<80>([&](auto T) __attribute__((always_inline)) {
    constexpr int t = T, s = t >> 3, m = t & 7, nx = t + PD;
    if constexpr (nx < 80 && (nx & 7) == 0) asm volatile("s_barrier" ::: "memory");
    if constexpr (nx < 80) K1_RD(nx, 0);
    K1_MM(acc[m][0], bfr[s % (K1_W + 1)][0], af[t % (PD + 1)][0]); K1_MM(acc[m][0], bfr[s % (K1_W + 1)][2], af[t % (PD + 1)][1]); PIN(acc[m][0]);
    SCHED();
    if constexpr (nx < 80) K1_RD(nx, 1);
    if constexpr (s + K1_W < 10 && (m & 1)) bfr[(s + K1_W) % (K1_W + 1)][m >> 1] = *(const bf16x8*)(bsrc + ((s + K1_W) * 4 + (m >> 1)) * 1024);
    K1_MM(acc[m][1], bfr[s % (K1_W + 1)][1], af[t % (PD + 1)][0]); K1_MM(acc[m][1], bfr[s % (K1_W + 1)][3], af[t % (PD + 1)][1]); PIN(acc[m][1]);
    SCHED();
  });
  __builtin_amdgcn_s_setprio(0);
}

#define TARG
#define TPASS

DI void phase_qkv(char* __restrict__ ws, rsrc_t rs, const u16* __restrict__ cvec, const u16* __restrict__ wmod, char* smem, int wid TARG) {
  const int tid = fresh_tid(), lane = tid & 63;
  const int xcd = blockIdx.x & 7, idx = blockIdx.x >> 3;
  int ctr;
  if (idx >= 30) {
    ctr = C_MOD;
    const int sb = xcd * 2 + idx - 30, col0 = (sb < 8 ? 1280 : 3200 - 640) + sb * 80, c = tid % 10, rg = tid / 10;
    float* sil = (float*)smem; float* red = (float*)(smem + 4096);
    u32x4 w[13];
#pragma unroll
    for (int i = 0; i < 13; ++i) { const int row = min(rg + 50 * i, 639); w[i] = LDNT((const u32x4*)(wmod + (size_t)row * 3840 + col0 + c * 8)); }
    for (int i = tid; i < 660; i += 512) sil[i] = i < 640 ? silu_bf(cvec[i]) : 0.f;
    if (tid < 80) {
      const float* pp = (const float*)(ws + OFF_MODP) + 1280 + sb * 80 + tid;
      float v = rbf((pp[0] + pp[2560]) + (pp[5120] + pp[7680]));
      if (sb >= 8) v = rbf(1.f + v);
      stc4(rs, (1920 + sb * 80 + tid) * 4, OFF_MOD, __float_as_uint(v));
    }
    BAR();
    float acc[8] = {0, 0, 0, 0, 0, 0, 0, 0};
#pragma unroll
    for (int i = 0; i < 13; ++i) {
      const float s = sil[rg + 50 * i];
#pragma unroll
      for (int e = 0; e < 4; ++e) { acc[2 * e] += s * bflo(w[i][e]); acc[2 * e + 1] += s * bfhi(w[i][e]); }
    }
    if (rg < 50) { *(f32x4*)(red + rg * 80 + c * 8) = (f32x4){acc[0], acc[1], acc[2], acc[3]}; *(f32x4*)(red + rg * 80 + c * 8 + 4) = (f32x4){acc[4], acc[5], acc[6], acc[7]}; }
    BAR();
    if (tid < 80) {
      float v = 0.f;
#pragma unroll
      for (int i = 0; i < 50; ++i) v += red[i * 80 + tid];
      stc4(rs, (col0 + tid) * 4, OFF_MOD, __float_as_uint(rbf(v)));
    }
  } else {
    const int mt = xcd * 2 + (idx / 15), nt = idx % 15;
    ctr = C_QKV + nt;
    if (wid < 4) {
      float* cst = (float*)(smem + 49152);
      const int c = lane & 7, rr = lane >> 3;
      const char* src = ws + OFF_XHAT + (size_t)(mt * 128 + wid * 32 + rr) * 1280 + c * 16;
      char* wdst = smem + (c >> 2) * 8192 + wid * 2048 + (c & 3) * 256 + (rr ^ c) * 16;
      u32x4 a[10][4];
      f32x4 c0[4], c1[4];
#pragma unroll
      for (int i = 0; i < 4; ++i) { c0[i] = *(const f32x4*)(ws + OFF_MODP + i * 10240 + tid * 16); if (wid == 0) c1[i] = *(const f32x4*)(ws + OFF_MODP + i * 10240 + 4096 + tid * 16); }
#pragma unroll
      for (int s = 0; s < K1_W; ++s)
#pragma unroll
        for (int m = 0; m < 4; ++m) a[s][m] = *(const u32x4*)(src + m * 10240 + s * 128);
      {
        f32x4 v = (c0[0] + c0[1]) + (c0[2] + c0[3]); const float one = tid >= 160 ? 1.f : 0.f;
        *(f32x4*)(cst + tid * 4) = (f32x4){rbf(one + rbf(v[0])), rbf(one + rbf(v[1])), rbf(one + rbf(v[2])), rbf(one + rbf(v[3]))};
        if (wid == 0) { v = (c1[0] + c1[1]) + (c1[2] + c1[3]); *(f32x4*)(cst + 1024 + tid * 4) = (f32x4){rbf(1.f + rbf(v[0])), rbf(1.f + rbf(v[1])), rbf(1.f + rbf(v[2])), rbf(1.f + rbf(v[3]))}; }
      }
      BAR();
      const float* cp = cst + c * 8;
      f32x4 kc[10][4];
#define K1_KC(s) do { kc[s][0] = *(const f32x4*)(cp + (s) * 64); kc[s][1] = *(const f32x4*)(cp + (s) * 64 + 4); kc[s][2] = *(const f32x4*)(cp + (s) * 64 + 640); kc[s][3] = *(const f32x4*)(cp + (s) * 64 + 644); } while (0)
#pragma unroll
      for (int s = 0; s < 6; ++s) K1_KC(s);
      SCHED();
      u32x4 o[2][4];
#define K1_XF(s, m) (o[(s) & 1][m] = modulate8(a[s][m], kc[s][2], kc[s][3], kc[s][0], kc[s][1]))
#define K1_WR(s, m) (*(u32x4*)(wdst + ((s) % 3) * 16384 + ((m) >> 1) * 1024 + ((m) & 1) * 128) = o[(s) & 1][m])
#pragma unroll
      for (int m = 0; m < 4; ++m) { K1_XF(0, m); K1_WR(0, m); }
      BAR();
      TS(1);
#pragma unroll
      for (int m = 0; m < 4; ++m) a[K1_W][m] = *(const u32x4*)(src + m * 10240 + K1_W * 128);
#pragma unroll
      for (int m = 0; m < 4; ++m) K1_XF(1, m);
      SCHED();
#pragma unroll
      for (int s = 1; s < 10; ++s) {
#pragma unroll
        for (int m = 0; m < 4; ++m) {
          if (m == 0) K1_WR(s, 0);
          if (m < 3) K1_WR(s, m + 1);
          if (s + K1_W < 10) a[s + K1_W][m] = *(const u32x4*)(src + m * 10240 + (s + K1_W) * 128);
          if (s < 5) kc[s + 5][m] = *(const f32x4*)(cp + (s + 5) * 64 + (m & 1) * 4 + (m >> 1) * 640);
          SCHED();
          if (s < 9) K1_XF(s + 1, m);
          SCHED();
        }
        asm volatile("" :: "v"(o[s & 1][0]), "v"(o[s & 1][1]), "v"(o[s & 1][2]), "v"(o[s & 1][3]));
        BAR();
      }
    } else {
      const int cw = wid - 4;
      const char* bsrc = ws + OFF_WQKV + (size_t)(nt * 4 + cw) * 40 * 1024 + lane * 16;
      f32x4 acc[8][2];
      const int r = lane & 15, g = lane >> 4;
      if (nt >= 10) {
        k1_mma<true>(bsrc, smem, lane, acc);
        TS(2);
        const unsigned so = OFF_KV + (mt >> 1) * 20480 + 10240 + (mt & 1) * 5120;
#pragma unroll
        for (int x = 0; x < 2; ++x) {
          const unsigned c = (nt - 10) * 128 + cw * 32 + x * 16 + r, head = c / 20, d = c % 20;
          const unsigned vo = head * 163840u + ((g & 1) * 20 + d) * 16 + (g >> 1) * 8;
#pragma unroll
          for (int m = 0; m < 8; ++m) stc8(rs, vo + (m & 3) * 640, so + (m >> 2) * 2560, (u32x2){pack2(acc[m][x][0], acc[m][x][1]), pack2(acc[m][x][2], acc[m][x][3])});
        }
      } else {
        k1_mma<false>(bsrc, smem, lane, acc);
        TS(2);
        const bool isQ = nt < 5; const float sc = isQ ? QSCALE : 1.f;
        const unsigned so = isQ ? OFF_Q + mt * 5120 : OFF_KV + (mt >> 1) * 20480 + (mt & 1) * 5120, hs = isQ ? 81920u : 163840u;
        const int n5 = isQ ? nt : nt - 5;
        if (n5 < 4) {
          const unsigned vo = (2 * (n5 * 4 + cw) + (g >> 1)) * hs + (g & 1) * 512 + r * 16;
#pragma unroll
          for (int m = 0; m < 8; ++m) {
            const f32x4 v0 = acc[m][0] * sc, v1 = acc[m][1] * sc;
            stc16(rs, vo + (m & 1) * 256, so + (m >> 1) * 1280, (u32x4){pack2(v0[0], v0[1]), pack2(v0[2], v0[3]), pack2(v1[0], v1[1]), pack2(v1[2], v1[3])});
          }
        } else {
          const unsigned vo = (8 * cw + g) * hs + 1024 + r * 8;
#pragma unroll
          for (int m = 0; m < 8; ++m)
#pragma unroll
            for (int x = 0; x < 2; ++x) {
              const f32x4 v = acc[m][x] * sc;
              stc8(rs, vo + (m & 1) * 128, so + x * 4 * hs + (m >> 1) * 1280, (u32x2){pack2(v[0], v[1]), pack2(v[2], v[3])});
            }
        }
      }
    }
  }
  TS(3);
  waitv<0>();
  BAR();
  TS(4);
  if (wid == 7 && lane < 8) __hip_atomic_fetch_add((GAS unsigned*)(ws + OFF_CNT + (lane * 16 + ctr) * 256), 1u, __ATOMIC_RELAXED, __HIP_MEMORY_SCOPE_AGENT);
}

template <bool FINAL> DI constexpr int proj_pos(int f) { return FINAL ? (f < 96 ? 65536 + f * 1024 : (f - 96) * 1024) : 17408 + (f % 20 >= 10 ? 81920 : 0) + (f / 20 * 10 + f % 10) * 1024; }
template <bool FINAL> DI u32x4 proj_ld(rsrc_t rs, int i, int wid, int lane) {
  constexpr int NF = FINAL ? 135 : 100;
  return ldc16(rs, lane * 16, (FINAL ? OFF_WOUT : OFF_WO) + (((blockIdx.x >> 3) & 7) * NF + min(i * 8 + wid, NF - 1)) * 1024);
}
template <bool FINAL> DI void proj_park(char* smem, int i, int wid, int lane, u32x4 v) {
  constexpr int NF = FINAL ? 135 : 100;
  *(u32x4*)(smem + proj_pos<FINAL>(min(i * 8 + wid, NF - 1)) + lane * 16) = v;
}

DI void split3(float m, unsigned& w_h0, unsigned& w_h1) {
  const float n = -m; const float a = rbf(n), b = rbf(n - a), c = rbf(n - a - b);
  w_h0 = pack2(a, b); w_h1 = pack2(c, 0.f);
}

DI void phase_attn(char* __restrict__ ws, rsrc_t rs, const char* __restrict__ wsp, const u16* __restrict__ win, const u16* __restrict__ wout, char* smem, int wid TARG) {
  const int tid = fresh_tid(), lane = tid & 63;
  const int xcd = blockIdx.x & 7, idx = blockIdx.x >> 3, head = xcd * 4 + (idx >> 3), qb = idx & 7;
  const int r = lane & 31, h = lane >> 5, qg = wid & 3, kh = wid >> 2;
  if (wid == 7) {
    const int l7 = lane & 7;
    const int c = l7 == 0 ? head >> 3 : l7 == 1 ? 4 : l7 == 2 ? 5 + (head >> 3) : l7 == 3 ? 9 : l7 == 4 ? 10 + (20 * head) / 128 : l7 == 5 ? 10 + (20 * head + 19) / 128 : C_MOD;
    poll_ge(ws, xcd * 16 + c, 16);
  }
  BAR();
  TS(5);
  unsigned kvo = OFF_KV + head * 163840, qo = OFF_Q + head * 81920 + (qb * 8 + qg * 2) * 1280;
  const unsigned kconst = h == 0 ? 0x3f803f80u : 0x00003f80u;
  const unsigned so16 = (tid & 255) * 16, l16 = lane * 16, l2 = 1024 + r * 8 + h * 4;
  const int blk = blockIdx.x;
  u32x4 wq[13];
  for (int attempt = 0; attempt < 2; ++attempt) {
    asm volatile("" : "+s"(kvo), "+s"(qo));
    const char* kvs = ws + kvo + kh * 81920 + so16;
    const unsigned ldst = lds_addr(smem) + kh * 81920 + qg * 1024;
    u32x4 qA1w, qB1w, k1w; unsigned qA2w, qB2w, k2, m0sv;
    asm volatile(
        "buffer_load_dwordx4 %0, %7, %9, %10 offen sc1\n\tbuffer_load_dwordx4 %1, %7, %9, %10 offen offset:1280 sc1\n\tbuffer_load_dwordx4 %2, %7, %9, %11 offen sc1\n\t"
        "buffer_load_dword %3, %8, %9, %10 offen sc1\n\tbuffer_load_dword %4, %8, %9, %10 offen offset:1280 sc1\n\tbuffer_load_dword %5, %8, %9, %11 offen sc1\n\t"
        "s_mov_b32 %6, m0\n\ts_mov_b32 m0, %17\n\ts_nop 0\n\tglobal_load_lds_dwordx4 %12, off sc1\n\t"
        "s_add_u32 m0, %18, 0x1000\n\ts_nop 0\n\tglobal_load_lds_dwordx4 %13, off sc1\n\t"
        "s_add_u32 m0, %18, 0x2000\n\ts_nop 0\n\tglobal_load_lds_dwordx4 %14, off sc1\n\t"
        "s_add_u32 m0, %18, 0x3000\n\ts_nop 0\n\tglobal_load_lds_dwordx4 %15, off sc1\n\t"
        "s_add_u32 m0, %18, 0x4000\n\ts_nop 0\n\tglobal_load_lds_dwordx4 %16, off sc1\n\t"
        "s_mov_b32 m0, %6\n\ts_waitcnt vmcnt(5)"
        : "=&v"(qA1w), "=&v"(qB1w), "=&v"(k1w), "=&v"(qA2w), "=&v"(qB2w), "=&v"(k2), "=&s"(m0sv)
        : "v"(l16), "v"(l2), "s"(rs), "s"(qo), "s"(kvo), "v"(kvs + (wid == 0 ? 4096 : 0)), "v"(kvs + 4096), "v"(kvs + 8192), "v"(kvs + 12288), "v"(kvs + 16384),
          "s"(ldst + (wid == 0 ? 4096 : 0)), "s"(ldst)
        : "memory", "scc");
    if (wid == 0) { const unsigned f = (tid & 1) ? 0u : 0x3f803f80u; *(u32x4*)(smem + l16) = (u32x4){f, f, f, f}; }
    const bf16x8 qA1 = BF8(qA1w), qB1 = BF8(qB1w);
    bf16x8 k1 = BF8(k1w);
    TS(30);
    float mA = -INFINITY, mB = -INFINITY;
    {
      const bf16x4 qA2 = BF4(((u32x2){qA2w, 0u})), qB2 = BF4(((u32x2){qB2w, 0u}));
      const int nt = attempt ? 64 : 1;
      bf16x8 t1 = k1; unsigned t2 = k2;
      for (int t = 0; t < nt; ++t) {
        if (t) { const unsigned to = kvo + (t >> 3) * 20480 + (t & 7) * 1280; t1 = BF8(ldc16(rs, l16, to)); t2 = ldc4(rs, l2, to); }
        const bf16x4 t2v = BF4(((u32x2){t2, kconst}));
        f32x16 a = MFMA32(t1, qA1, (f32x16){}); a = MFMA32K8(t2v, qA2, a);
        f32x16 b = MFMA32(t1, qB1, (f32x16){}); b = MFMA32K8(t2v, qB2, b);
#pragma unroll
        for (int i = 0; i < 16; ++i) { mA = fmaxf(mA, a[i]); mB = fmaxf(mB, b[i]); }
      }
      mA = fmaxf(mA, xhalf(mA)); mB = fmaxf(mB, xhalf(mB));
    }
    unsigned a0, a1, b0, b1; split3(mA, a0, a1); split3(mB, b0, b1);
    const bf16x4 qA2 = BF4(((u32x2){qA2w, h ? a1 : a0})), qB2 = BF4(((u32x2){qB2w, h ? b1 : b0}));
    char* lh = smem + kh * 81920;
    TS(31);
    waitv<0>();
    BAR();
    TS(6);
    f32x16 oA = (f32x16){}, oB = (f32x16){};
    const char* kp1 = lh + l16;
    const char* kp2 = lh + l2;
    const bool vreal = r < 20;
    const char* vp = vreal ? lh + 10240 + (h * 20 + r) * 16 : smem + (r == 20 ? 0 : 16);
    const int vinc = vreal ? 1280 : 0, vjump = vreal ? 10240 : 0;
    if (kh) { k1 = *(const bf16x8*)kp1; k2 = *(const unsigned*)kp2; }
    bf16x8 kf1[2], vf[2][2]; u32x2 kf2[2];
    kf1[0] = k1; kf2[0] = (u32x2){k2, kconst}; kf2[1] = (u32x2){0u, kconst};
    vf[1][0] = (bf16x8){}; vf[1][1] = (bf16x8){};
    u32x4 pA0, pA1, pB0 = (u32x4){}, pB1 = (u32x4){};
    f32x4 e0 = (f32x4){}, e1;
    f32x16 sA = MFMA32(kf1[0], qA1, (f32x16){}), sB;
    sA = MFMA32K8(BF4(kf2[0]), qA2, sA);
#define EX2(x) __builtin_amdgcn_exp2f(x)
#define GRP(MF, S, i, EN, EP, P, w) do { MF; SCHED(); EN[0] = EX2(S[i]); P[w] = pack2(EP[0], EP[1]); EN[1] = EX2(S[i + 1]); P[w + 1] = pack2(EP[2], EP[3]); \
      EN[2] = EX2(S[i + 2]); EN[3] = EX2(S[i + 3]); PIN(P); PIN(EN); SCHED(); } while (0)
    const unsigned relsc = lds_addr(smem) + 768;
    auto group = [&](auto FIRST, int jump, int j) __attribute__((always_inline)) {
#pragma unroll
      for (int t = 0; t < 8; ++t) {
        const int c = t & 1, n = c ^ 1;
        if (FIRST) { if (t == 0) __builtin_amdgcn_s_setprio(3); if (t == 3) __builtin_amdgcn_s_setprio(2); if (t == 5) __builtin_amdgcn_s_setprio(1); if (t == 6) __builtin_amdgcn_s_setprio(0); if (t == 7) __builtin_amdgcn_s_setprio(3); }
        else {
          if (t == 4 && j == 2) __builtin_amdgcn_s_setprio(2);
          if (t == 3 && j == 3) {
            __builtin_amdgcn_s_setprio(1);
#pragma unroll
            for (int i = 0; i < 13; ++i) wq[i] = proj_ld<false>(rs, i, wid, lane);
          }
          if (t == 6 && j == 3) { __builtin_amdgcn_s_setprio(0); if (wid == 7) glds4c(ws + OFF_CNT + (C_REL + (lane & 7)) * 256, relsc); }
        }
        if (FIRST && t < 5) {
#pragma unroll
          for (int i = 5 + 3 * t; i < 8 + 3 * t; ++i) glds16c(kvs + i * 4096, ldst + i * 4096);
        }
        if (t == 7) {
          if (FIRST) { waitv<0>(); BAR(); }
          kp1 += jump; kp2 += jump;
        }
        SCHED();
        kf1[n] = *(const bf16x8*)(kp1 + ((t + 1) & 7) * 1280); kf2[n][0] = *(const unsigned*)(kp2 + ((t + 1) & 7) * 1280);
        GRP(sB = MFMA32(kf1[c], qB1, (f32x16){}), sA, 0, e1, e0, pB1, 2);
        vf[c][0] = *(const bf16x8*)vp; vf[c][1] = *(const bf16x8*)(vp + 640); vp += vinc;
        GRP(sB = MFMA32K8(BF4(kf2[c]), qB2, sB), sA, 4, e0, e1, pA0, 0);
        GRP(oB = MFMA32(vf[n][0], BF8(pB0), oB), sA, 8, e1, e0, pA0, 2);
        GRP(oB = MFMA32(vf[n][1], BF8(pB1), oB), sA, 12, e0, e1, pA1, 0);
        GRP(sA = MFMA32(kf1[n], qA1, (f32x16){}), sB, 0, e1, e0, pA1, 2);
        GRP(sA = MFMA32K8(BF4(kf2[n]), qA2, sA), sB, 4, e0, e1, pB0, 0);
        GRP(oA = MFMA32(vf[c][0], BF8(pA0), oA), sB, 8, e1, e0, pB0, 2);
        GRP(oA = MFMA32(vf[c][1], BF8(pA1), oA), sB, 12, e0, e1, pB1, 0);
      }
      vp += vjump;
    };
    group(std::true_type{}, 20480, 0);
    TS(7);
    const int j2 = (blk - 135) * 4 + qg;
    const bool side = kh == 0 && attempt == 0 && j2 < 270;
    char* stile = smem + 1024 + qg * 4096;
    const char* svec = smem + 81920 + qg * 256;
    const int kt = blk < 135 ? 4 * (blk % 5) + qg : j2 / 10, ng = blk < 135 ? blk / 5 : j2 % 10;
    if (side) {
      const u16* W = blk < 135 ? win : wout; const int N = blk < 135 ? F_ : D_;
      const int j8 = lane >> 3;
#pragma unroll
      for (int i = 0; i < 4; ++i) {
        const int row = kt * 32 + i * 8 + j8;
        glds16(W + (size_t)row * N + ng * 64 + ((lane & 7) ^ j8) * 8, lds_addr(stile) + i * 1024);
      }
      glds4c((const float*)(ws + OFF_MOD) + (4 - h) * 640 + (kt < 20 ? kt : 0) * 32 + r, lds_addr(svec));
    }
#pragma nounroll
    for (int j = 1; j < 4; ++j) {
      if (j == 2) {
        if (kh) l2touch(wsp + ((size_t)(blk * 4 + qg) * 64 + lane) * 128, lds_addr(smem) + 256);
        else if (side) {
          waitv<0>();
          const int g4 = lane >> 4, r4 = lane & 15, row = 8 * g4 + (r4 >> 2);
          const f32x4 sv0 = *(const f32x4*)(svec + (r4 & 1) * 128 + g4 * 32), sv1 = *(const f32x4*)(svec + (r4 & 1) * 128 + g4 * 32 + 16);
          const bf16x8 vec = BF8(((u32x4){pack2(sv0[0], sv0[1]), pack2(sv0[2], sv0[3]), pack2(sv1[0], sv1[1]), pack2(sv1[2], sv1[3])}));
          const unsigned nks = blk < 135 ? 20 : 27, dso = (blk < 135 ? OFF_WIN : OFF_WOUT) + (ng * 4 * nks + kt) * 1024;
          bf16x8 fr[4]; f32x4 pr[4];
#pragma unroll
          for (int f = 0; f < 4; ++f) {
            const int ch = 2 * f + ((r4 & 3) >> 1);
            const bf16x4 lo = lds_tr(stile + row * 128 + (ch ^ (row & 7)) * 16 + (r4 & 1) * 8), hi = lds_tr(stile + (row + 4) * 128 + (ch ^ ((row + 4) & 7)) * 16 + (r4 & 1) * 8);
            fr[f] = __builtin_shufflevector(lo, hi, 0, 1, 2, 3, 4, 5, 6, 7);
          }
#pragma unroll
          for (int f = 0; f < 4; ++f) { stc16(rs, l16, dso + f * nks * 1024, __builtin_bit_cast(u32x4, fr[f])); pr[f] = MFMA16(fr[f], vec, (f32x4){}); }
          if (r4 < 2) {
#pragma unroll
            for (int f = 0; f < 4; ++f) *(f32x4*)(stile + 3072 + r4 * 256 + f * 64 + g4 * 16) = pr[f];
          }
        }
      }
      if (j == 3 && kh == 0 && attempt == 0) { waitv<0>(); arrive(ws, C_REL + xcd, lane); }
      group(std::false_type{}, j < 3 ? 20480 : 0, j);
    }
    TS(8);
    pB1[2] = pack2(e0[0], e0[1]); pB1[3] = pack2(e0[2], e0[3]);
    oB = MFMA32(vf[1][0], BF8(pB0), oB); oB = MFMA32(vf[1][1], BF8(pB1), oB);
    {
      f32x4* ex = (f32x4*)(lh + 1024 + qg * 4096) + lane;
      const f32x16 give = kh ? oA : oB;
#pragma unroll
      for (int g = 0; g < 3; ++g) ex[g * 64] = (f32x4){give[4 * g], give[4 * g + 1], give[4 * g + 2], give[4 * g + 3]};
    }
    int* flag = (int*)(smem + 528);
    BAR();
    TS(9);
    float o[12];
    {
      const f32x4* ex = (const f32x4*)(smem + (kh ^ 1) * 81920 + 1024 + qg * 4096) + lane;
      const f32x16 keep = kh ? oB : oA;
#pragma unroll
      for (int g = 0; g < 3; ++g) { const f32x4 t = ex[g * 64];
#pragma unroll
        for (int e = 0; e < 4; ++e) o[4 * g + e] = keep[4 * g + e] + t[e]; }
    }
    const float lo = xhalf(o[8]); const float l = h ? o[8] : lo;
    const float rl = __builtin_amdgcn_rcpf(l);
    const int tok = qb * 256 + qg * 64 + kh * 32 + r;
    const unsigned vo = (tok >> 4) * 20480 + (tok & 15) * 16;
    stc16(rs, vo + h * 256, OFF_O + (head >> 1) * 1024 + (head & 1) * 512,
          (u32x4){pack2(o[0] * rl, o[1] * rl), pack2(o[2] * rl, o[3] * rl), pack2(o[4] * rl, o[5] * rl), pack2(o[6] * rl, o[7] * rl)});
    if (h == 0) stc8(rs, vo, OFF_O + 16384 + (head >> 3) * 1024 + ((head >> 1) & 3) * 256 + (head & 1) * 8, (u32x2){pack2(o[8] * rl, o[9] * rl), pack2(o[10] * rl, o[11] * rl)});
    if (wid == 4 && blk < 135 && attempt == 0 && lane < 32) {
      const char* pp = smem + 1024 + 3072 + lane * 16;
      const f32x4 v = (*(const f32x4*)pp + *(const f32x4*)(pp + 4096)) + (*(const f32x4*)(pp + 8192) + *(const f32x4*)(pp + 12288));
      stc16(rs, (lane >> 4) * 8192 + (lane & 15) * 16, OFF_CBP + (blk % 5) * 16384 + (blk / 5) * 256, __builtin_bit_cast(u32x4, v));
    }
    bool ok = (l > 1e-30f) && (l < 1e30f);
#pragma unroll
    for (int i = 0; i < 12; ++i) ok = ok && (fabsf(o[i]) < 3e38f);
    if (!ok && attempt == 0) *flag = 1;
    waitv<0>();
    BAR();
    const int redo = __builtin_amdgcn_readfirstlane(*(volatile LAS int*)flag);
    if (!redo) break;
    BAR();
  }
  TS(10);
  if (wid == 7) arrive(ws, C_ATT + qb, lane);
#pragma unroll
  for (int i = 0; i < 13; ++i) proj_park<false>(smem, i, wid, lane, wq[i]);
  if (!__all(*(volatile LAS unsigned*)(smem + 768 + (lane & 7) * 4) >= 128u)) poll_ge(ws, C_REL + (lane & 7), 128);
}

template <bool FINAL, int KH, class HOOK>
DI void proj_body(char* __restrict__ ws, rsrc_t rs, const u16* __restrict__ x, u16* __restrict__ out, char* smem, int wid, u32x2 (&res)[3], HOOK&& after_mma TARG) {
  const int lane = fresh_tid() & 63;
  const int xcd = blockIdx.x & 7, idx = blockIdx.x >> 3, panel = xcd * 4 + (idx >> 3), cb = idx & 7;
  constexpr int NKS = FINAL ? 27 : 20;
  constexpr int K0 = KH ? (NKS + 1) / 2 : 0, NK = KH ? NKS / 2 : (NKS + 1) / 2, NKEEP = KH ? 2 : 3, UK = KH ? 3 : 0, UG = KH ? 0 : 3;
  const int m = wid & 3, mt = panel * 4 + m, g = lane >> 4, r = lane & 15;
  f32x4 gate[NKEEP], cbv[NKEEP];
#pragma unroll
  for (int i = 0; i < NKEEP; ++i) {
    const int n = (cb * 5 + UK + i) * 16;
    if (!FINAL) { res[i] = *(const u32x2*)(x + (size_t)(mt * 16 + r) * D_ + n + g * 4); cbv[i] = ldc16f(rs, g * 16, OFF_MOD + (4 * 640 + n) * 4); }
    gate[i] = ldc16f(rs, g * 16, OFF_MOD + ((FINAL ? 5 : 2) * 640 + n) * 4);
  }
  if (wid == 7) poll_ge(ws, FINAL ? C_U + panel : C_ATT + (panel >> 2), FINAL ? 8 : 32);
  waitv<0>();
  BAR();
  TS(FINAL ? 27 : 11);
  bf16x8 a[NK];
#pragma unroll
  for (int k = 0; k < NK; ++k) a[k] = BF8(ldc16(rs, lane * 16, (FINAL ? OFF_U : OFF_O) + (mt * NKS + K0 + k) * 1024));
  SCHED();
  constexpr int PD = FINAL ? 5 : 6;
  const char* bl = smem + lane * 16;
  bf16x8 bf[PD + 1][5]; f32x4 acc[5];
#pragma unroll
  for (int u = 0; u < 5; ++u) acc[u] = (f32x4){};
#pragma unroll
  for (int k = 0; k < PD; ++k)
#pragma unroll
    for (int u = 0; u < 5; ++u) bf[k][u] = *(const bf16x8*)(bl + proj_pos<FINAL>(u * NKS + K0 + k));
  SCHED();
#pragma unroll
  for (int k = 0; k < NK; ++k) {
#pragma unroll
    for (int u = 0; u < 5; ++u) {
      if (k + PD < NK) bf[(k + PD) % (PD + 1)][u] = *(const bf16x8*)(bl + proj_pos<FINAL>(u * NKS + K0 + k + PD));
      acc[u] = MFMA16(bf[k % (PD + 1)][u], a[k], acc[u]);
      SCHED();
    }
  }
  TS(FINAL ? 28 : 12);
  f32x4* ex = (f32x4*)(smem + (FINAL ? 40960 : 68608)) + m * 5 * 64 + lane;
#pragma unroll
  for (int i = 0; i < 5 - NKEEP; ++i) ex[(UG + i) * 64] = acc[UG + i];
  f32x4 part[NKEEP];
#pragma unroll
  for (int i = 0; i < NKEEP; ++i) {
    part[i] = (f32x4){bflo(res[i][0]), bfhi(res[i][0]), bflo(res[i][1]), bfhi(res[i][1])} + gate[i] * acc[UK + i];
    PIN(part[i]);
  }
  SCHED();
  after_mma();
  BAR();
  f32x4 oth[NKEEP];
#pragma unroll
  for (int i = 0; i < NKEEP; ++i) oth[i] = ex[(UK + i) * 64];
  f32x4 sv = (f32x4){}, qv = (f32x4){};
#pragma unroll
  for (int i = 0; i < NKEEP; ++i) {
    const f32x4 v = part[i] + gate[i] * oth[i];
    const u32x2 o = (u32x2){pack2(v[0], v[1]), pack2(v[2], v[3])};
    if (FINAL) *(u32x2*)(out + (size_t)(mt * 16 + r) * D_ + (cb * 5 + UK + i) * 16 + g * 4) = o;
    else {
      res[i] = o; PIN(res[i]);
      const f32x4 w = v * cbv[i]; const int un = cb * 5 + UK + i;
      stc8(rs, ((g >> 1) * 16 + r) * 16 + (g & 1) * 8, OFF_X1S + mt * 20480 + un * 512, (u32x2){pack2(w[0], w[1]), pack2(w[2], w[3])});
      sv += v; qv += v * v;
    }
  }
  if (!FINAL) {
    const float s = sum_g((sv[0] + sv[1]) + (sv[2] + sv[3])), q = sum_g((qv[0] + qv[1]) + (qv[2] + qv[3]));
    if (g == 0) stc8(rs, r * 8, OFF_ST1 + ((panel * 16 + cb * 2 + KH) * 64 + m * 16) * 8, (u32x2){__float_as_uint(s), __float_as_uint(q)});
    TS(13);
    waitv<0>();
    BAR();
    TS(14);
    if (wid == 7) arrive(ws, C_X1 + panel, lane);
  }
}

constexpr float GC1 = -2.8853900817779268f * 0.7978845608028654f, GC2 = GC1 * 0.044715f;
DI float gelu_s(float a, float k1, float k3, float den) {
  const float e = __builtin_amdgcn_exp2f(a * __builtin_fmaf(a * a, k3, k1));
  return a * __builtin_amdgcn_rcpf(__builtin_fmaf(e, den, den));
}
DI void gelu8(f32x4& a0, const f32x4& l0, f32x4& a1, const f32x4& l1) {
  float t[8];
#define G8(expr0, expr1) do { _Pragma("unroll") for (int i = 0; i < 4; ++i) { t[i] = opq(expr0); } _Pragma("unroll") for (int i = 0; i < 4; ++i) { t[4 + i] = opq(expr1); } SCHED(); } while (0)
  G8(a0[i] * a0[i], a1[i] * a1[i]);
  G8(__builtin_fmaf(t[i], l0[3], l0[2]), __builtin_fmaf(t[4 + i], l1[3], l1[2]));
  G8(a0[i] * t[i], a1[i] * t[4 + i]);
  G8(__builtin_amdgcn_exp2f(t[i]), __builtin_amdgcn_exp2f(t[4 + i]));
  G8(__builtin_fmaf(t[i], l0[0], l0[0]), __builtin_fmaf(t[4 + i], l1[0], l1[0]));
  G8(__builtin_amdgcn_rcpf(t[i]), __builtin_amdgcn_rcpf(t[4 + i]));
  G8(a0[i] * t[i], a1[i] * t[4 + i]);
  a0 = (f32x4){t[0], t[1], t[2], t[3]}; a1 = (f32x4){t[4], t[5], t[6], t[7]};
}
constexpr int L4_Z1 = 114688, L4_TR = L4_Z1 + 16384, L4_LN = L4_TR + 16384, L4_ST2 = L4_LN + 1024, L4_FIN = L4_ST2 + 3584, L4_CB = L4_FIN + 512, L4_CNT = L4_CB + 2048;
constexpr int K5_R = 7;
constexpr unsigned DEAD = 0x80000000u;
DI int sp_stage(int s) { const int idx = blockIdx.x >> 3; return (s + (idx & 7) * 2) & 15; }
DI unsigned sp_voff(int wid, int lane, int i) { return ((wid & 3) * 16 + i * 8 + (lane >> 3)) * 4096 + (wid >> 2) * 128 + ((lane & 7) ^ (4 * i + (lane >> 4))) * 16; }
DI const char* sp_base(const u16* wsp, int panel, int s) { return (const char*)wsp + (size_t)panel * 262144 + sp_stage(s) * 256; }
DI void sp_prefetch(u32x4 (&wr)[16 - K5_R][2], const u16* __restrict__ wsp, int panel, int wid, int lane) {
  const unsigned v0 = sp_voff(wid, lane, 0), v1 = sp_voff(wid, lane, 1);
#pragma unroll
  for (int s = K5_R; s < 16; ++s) { wr[s - K5_R][0] = *(const u32x4*)(sp_base(wsp, panel, s) + v0); wr[s - K5_R][1] = *(const u32x4*)(sp_base(wsp, panel, s) + v1); }
  asm volatile("" ::: "memory");
}
template <int S0, int S1>
DI void sp_ring(const u16* __restrict__ wsp, char* smem, int panel, int wid, int lane) {
  const unsigned v0 = sp_voff(wid, lane, 0), v1 = sp_voff(wid, lane, 1);
#pragma unroll
  for (int s = S0; s < S1; ++s) { glds16(sp_base(wsp, panel, s) + v0, lds_addr(smem) + wid * 2048 + s * 16384); glds16(sp_base(wsp, panel, s) + v1, lds_addr(smem) + wid * 2048 + s * 16384 + 1024); }
}
DI unsigned win_boff(int wid, int lane) { const int cb = (blockIdx.x >> 3) & 7; return lane * 16 + (wid == 6 && cb >= 6 ? DEAD : 0u); }
DI unsigned win_soff(int wid, bool z2) { return OFF_WIN + ((z2 ? 54 : 0) + unit0_of((blockIdx.x >> 3) & 7) + wid) * 20480; }
#define WIN_LD(so, k) BF8(ldc16(rs, bo + ((k) & 3) * 1024, (so) + ((k) >> 2) * 4096))
DI void win_prefetch(rsrc_t rs, int wid, bf16x8 (&b2)[20]) {
  const unsigned bo = win_boff(wid, fresh_tid() & 63), so = win_soff(wid, true);
#pragma unroll
  for (int k = 0; k < 20; ++k) b2[k] = WIN_LD(so, k);
}
template <class HOOK>
DI void win_mma(const char* al, const bf16x8 (&b)[20], f32x4 (&acc)[4], HOOK&& hook) {
  constexpr int PD = 4;
  bf16x8 af[PD + 1][2];
#define WIN_RD(i) do { af[(i) % (PD + 1)][0] = *(const bf16x8*)(al + ((i) & 3) * 20480 + ((i) >> 2) * 2048); af[(i) % (PD + 1)][1] = *(const bf16x8*)(al + ((i) & 3) * 20480 + ((i) >> 2) * 2048 + 1024); } while (0)
#pragma unroll
  for (int i = 0; i < PD; ++i) WIN_RD(i);
  SCHED();
  sfor<40>([&](auto I) __attribute__((always_inline)) {
    constexpr int i = I, m = i & 3, k = (i >> 2) * 2;
    if constexpr (i + PD < 40) WIN_RD(i + PD);
    acc[m] = MFMA16(b[k], af[i % (PD + 1)][0], acc[m]);
    acc[m] = MFMA16(b[k + 1], af[i % (PD + 1)][1], acc[m]); PIN(acc[m]);
    hook(I);
    SCHED();
  });
}
DI void stage_x1s(char* __restrict__ ws, char* smem, int panel, int wid, int tid) {
  const char* src = ws + OFF_X1S + (size_t)panel * 81920 + tid * 16;
#pragma unroll
  for (int i = 0; i < 10; ++i) glds16c(src + i * 8192, lds_addr(smem) + wid * 1024 + i * 8192);
}
DI void win_tail(char* __restrict__ ws, const u16* __restrict__ wsp, char* smem, int panel, int cb, int wid, int lane TARG) {
  BAR();
  TS(22);
  if (wid == 7 && lane < 4) __hip_atomic_fetch_add((GAS unsigned*)(ws + OFF_CNT + (C_Z2 + lane * 8 + cb) * 256), 1u, __ATOMIC_RELAXED, __HIP_MEMORY_SCOPE_AGENT);
}

template <bool EARLY>
DI void win_body(char* __restrict__ ws, rsrc_t rs, const u16* __restrict__ wsp, char* smem, int wid, const bf16x8 (&b2)[20], u32x4 (&wr)[16 - K5_R][2] TARG) {
  const int tid = fresh_tid(), lane = tid & 63, g = lane >> 4, r = lane & 15;
  const int xcd = blockIdx.x & 7, idx = blockIdx.x >> 3, panel = xcd * 4 + (idx >> 3), cb = idx & 7, u0 = unit0_of(cb);
  float* lnp = (float*)(smem + L4_LN); float* st2 = (float*)(smem + L4_ST2); float* fin = (float*)(smem + L4_FIN); float* cbl = (float*)(smem + L4_CB);
  int* flg = (int*)(smem + L4_CNT);
  __builtin_amdgcn_s_setprio(1);
  const unsigned bo = win_boff(wid, lane), so1 = win_soff(wid, false);
  bf16x8 b1[20];
  BAR();
  TS(15);
  stage_x1s(ws, smem, panel, wid, tid);
#pragma unroll
  for (int k = 0; k < 4; ++k) b1[k] = WIN_LD(so1, k);
  waitv<4>();
  BAR();
  TS(16);
  f32x4 ln[4], z2[4], z1[4];
#pragma unroll
  for (int m = 0; m < 4; ++m) ln[m] = *(const f32x4*)(lnp + (m * 16 + r) * 4);
  {
    const f32x4 cs2 = *(const f32x4*)(cbl + 112 + wid * 16 + g * 4), bi2 = *(const f32x4*)(cbl + 256 + 112 + wid * 16 + g * 4);
    const f32x4 cs1 = *(const f32x4*)(cbl + wid * 16 + g * 4), bi1 = *(const f32x4*)(cbl + 256 + wid * 16 + g * 4);
#pragma unroll
    for (int m = 0; m < 4; ++m) { z2[m] = ln[m][1] * cs2 + ln[m][0] * bi2; z1[m] = ln[m][1] * cs1 + ln[m][0] * bi1; PIN(z1[m]); }
  }
  win_mma(smem + lane * 16, b2, z2, [&](auto I) __attribute__((always_inline)) { constexpr int i = I; if constexpr (i < 32 && (i & 1)) b1[4 + i / 2] = WIN_LD(so1, 4 + i / 2); });
  TS(17);
  gelu8(z2[0], ln[0], z2[1], ln[1]); gelu8(z2[2], ln[2], z2[3], ln[3]);
  {
    float s[4], q[4];
#pragma unroll
    for (int m = 0; m < 4; ++m) { s[m] = opq(opq(z2[m][0] + z2[m][1]) + opq(z2[m][2] + z2[m][3])); q[m] = opq(__builtin_fmaf(z2[m][0], z2[m][0], opq(z2[m][1] * z2[m][1])) + opq(__builtin_fmaf(z2[m][2], z2[m][2], opq(z2[m][3] * z2[m][3])))); }
    auto pa = __builtin_amdgcn_permlane32_swap(__float_as_uint(s[0]), __float_as_uint(s[2]), false, false);
    auto pb = __builtin_amdgcn_permlane32_swap(__float_as_uint(s[1]), __float_as_uint(s[3]), false, false);
    auto pc = __builtin_amdgcn_permlane32_swap(__float_as_uint(q[0]), __float_as_uint(q[2]), false, false);
    auto pd = __builtin_amdgcn_permlane32_swap(__float_as_uint(q[1]), __float_as_uint(q[3]), false, false);
    const float sa = opq(__uint_as_float(pa[0]) + __uint_as_float(pa[1])), sb = opq(__uint_as_float(pb[0]) + __uint_as_float(pb[1]));
    const float qa = opq(__uint_as_float(pc[0]) + __uint_as_float(pc[1])), qb = opq(__uint_as_float(pd[0]) + __uint_as_float(pd[1]));
    auto ps = __builtin_amdgcn_permlane16_swap(__float_as_uint(sa), __float_as_uint(sb), false, false);
    auto pq = __builtin_amdgcn_permlane16_swap(__float_as_uint(qa), __float_as_uint(qb), false, false);
    *(f32x2*)(st2 + (wid * 64 + lane) * 2) = (f32x2){opq(__uint_as_float(ps[0]) + __uint_as_float(ps[1])), opq(__uint_as_float(pq[0]) + __uint_as_float(pq[1]))};
  }
  asm volatile("" ::: "memory");
  if (lane == 0) __hip_atomic_fetch_add(flg, 1, __ATOMIC_RELAXED, __HIP_MEMORY_SCOPE_WORKGROUP);
  asm volatile("" ::: "memory");
  __builtin_amdgcn_s_setprio(0);
  TS(18);
  const unsigned wvo[2] = {sp_voff(wid, lane, 0), sp_voff(wid, lane, 1)};
  win_mma(smem + lane * 16, b1, z1, [&](auto I) __attribute__((always_inline)) {
    constexpr int i = I, j = i < 8 ? i : (i & 1) ? -1 : 4 + i / 2;
    if constexpr (EARLY && j >= 0 && j < 18) wr[j >> 1][j & 1] = *(const u32x4*)(sp_base(wsp, panel, K5_R + (j >> 1)) + wvo[j & 1]);
  });
  gelu8(z1[0], ln[0], z1[1], ln[1]); gelu8(z1[2], ln[2], z1[3], ln[3]);
#pragma unroll
  for (int mm = 0; mm < 2; ++mm)
    *(u32x4*)(smem + L4_Z1 + wid * 2048 + mm * 1024 + lane * 16) = (u32x4){pack2(z1[2 * mm][0], z1[2 * mm][1]), pack2(z1[2 * mm][2], z1[2 * mm][3]), pack2(z1[2 * mm + 1][0], z1[2 * mm + 1][1]), pack2(z1[2 * mm + 1][2], z1[2 * mm + 1][3])};
  TS(20);
  BAR();
  TS(21);
  char* tr = smem + L4_TR + wid * 2048;
#pragma unroll
  for (int m = 0; m < 4; ++m) {
    const f32x2 f = *(const f32x2*)(fin + (m * 16 + r) * 2);
    const f32x4 v = z2[m] * f[0] - f[1];
    *(u32x2*)(tr + (m * 16 + r) * 32 + g * 8) = (u32x2){pack2(v[0], v[1]), pack2(v[2], v[3])};
  }
  {
    const char* p = tr + (8 * g + (r >> 2)) * 32 + (r & 3) * 8;
    const bf16x4 lo0 = lds_tr(p), hi0 = lds_tr(p + 128), lo1 = lds_tr(p + 1024), hi1 = lds_tr(p + 1152);
    stc16(rs, bo, OFF_Z2 + (panel * 2 * 54 + u0 + wid) * 1024, __builtin_bit_cast(u32x4, (bf16x8)__builtin_shufflevector(lo0, hi0, 0, 1, 2, 3, 4, 5, 6, 7)));
    stc16(rs, bo, OFF_Z2 + ((panel * 2 + 1) * 54 + u0 + wid) * 1024, __builtin_bit_cast(u32x4, (bf16x8)__builtin_shufflevector(lo1, hi1, 0, 1, 2, 3, 4, 5, 6, 7)));
  }
  SCHED();
  sp_ring<0, 2>(wsp, smem, panel, wid, lane);
  waitv<4>();
  win_tail(ws, wsp, smem, panel, cb, wid, lane TPASS);
  sp_ring<2, 5>(wsp, smem, panel, wid, lane);
  if (!EARLY) sp_prefetch(wr, wsp, panel, wid, lane);
}

DI void win_agent(char* __restrict__ ws, rsrc_t rs, const u16* __restrict__ wsp, char* smem, u32x4 (&wr)[16 - K5_R][2] TARG) {
  const int tid = fresh_tid(), lane = tid & 63, wid = 7;
  const int xcd = blockIdx.x & 7, idx = blockIdx.x >> 3, panel = xcd * 4 + (idx >> 3), cb = idx & 7, u0 = unit0_of(cb);
  float* lnp = (float*)(smem + L4_LN); float* st2 = (float*)(smem + L4_ST2); float* fin = (float*)(smem + L4_FIN); float* cbl = (float*)(smem + L4_CB);
  int* cnt = (int*)(smem + L4_CNT);
  __builtin_amdgcn_s_setprio(2);
  if (lane == 0) *cnt = 0;
  poll_ge(ws, lane < 8 ? C_ATT + lane : C_X1 + panel, lane < 8 ? 32 : 8);
  BAR();
  stage_x1s(ws, smem, panel, wid, tid);
  {
    u32x2 t[16]; f32x4 cp[5][2];
#pragma unroll
    for (int i = 0; i < 16; ++i) t[i] = __builtin_amdgcn_raw_buffer_load_b64(rs, lane * 8, OFF_ST1 + (panel * 16 + i) * 512, 16);
    const unsigned co = lane < 28 ? (u0 * 16 + lane * 4) * 4 : ((54 + u0) * 16 + min(lane - 28, 27) * 4) * 4;
#pragma unroll
    for (int i = 0; i < 5; ++i) { cp[i][0] = ldc16f(rs, co, OFF_CBP + i * 16384); cp[i][1] = ldc16f(rs, co, OFF_CBP + i * 16384 + 8192); }
    float s = 0.f, q = 0.f;
#pragma unroll
    for (int i = 0; i < 16; ++i) { s += __uint_as_float(t[i][0]); q += __uint_as_float(t[i][1]); }
    const float mean = s * (1.f / 640.f), var = fmaxf(q * (1.f / 640.f) - mean * mean, 0.f);
    const float den = ln_den(var), ri = 1.f / den;
    *(f32x4*)(lnp + lane * 4) = (f32x4){den, -rbf(mean), GC1 * ri, GC2 * ri * ri * ri};
#pragma unroll
    for (int v = 0; v < 2; ++v) *(f32x4*)(cbl + v * 256 + lane * 4) = ((cp[0][v] + cp[1][v]) + (cp[2][v] + cp[3][v])) + cp[4][v];
  }
  waitv<0>();
  BAR();
  __builtin_amdgcn_s_setprio(0);
  __builtin_amdgcn_s_sleep(8);
  sp_prefetch(wr, wsp, panel, wid, lane);
#pragma nounroll
  for (int w = 0; w < 8; ++w) sp_ring<5, 7>(wsp, smem, panel, w, lane);
  __builtin_amdgcn_s_setprio(2);
#pragma nounroll
  for (unsigned spin = 0; spin < (1u << 24); ++spin) {
    if (__hip_atomic_load(cnt, __ATOMIC_RELAXED, __HIP_MEMORY_SCOPE_WORKGROUP) >= 7) break;
    __builtin_amdgcn_s_sleep(1);
  }
  asm volatile("" ::: "memory");
  float s, q;
  {
    f32x2 t[7];
#pragma unroll
    for (int i = 0; i < 7; ++i) t[i] = *(const f32x2*)(st2 + (i * 64 + lane) * 2);
    s = ((t[0][0] + t[1][0]) + (t[2][0] + t[3][0])) + (t[4][0] + t[5][0]); q = ((t[0][1] + t[1][1]) + (t[2][1] + t[3][1])) + (t[4][1] + t[5][1]);
    if (cb < 6) { s += t[6][0]; q += t[6][1]; }
  }
  GAS u64* xch = (GAS u64*)(ws + OFF_XCH) + panel * 512;
  unsigned qb = __float_as_uint(q); if (qb == ~0u) qb = 0x7fc00000u;
  __hip_atomic_store(xch + cb * 64 + lane, ((u64)qb << 32) | __float_as_uint(s), __ATOMIC_RELAXED, __HIP_MEMORY_SCOPE_AGENT);
  TS(19);
  u64 v[8];
#pragma nounroll
  for (unsigned spin = 0; spin < (1u << 21); ++spin) {
    bool ok = true;
#pragma unroll
    for (int i = 0; i < 8; ++i) { v[i] = __hip_atomic_load(xch + i * 64 + lane, __ATOMIC_RELAXED, __HIP_MEMORY_SCOPE_AGENT); ok = ok && ((unsigned)(v[i] >> 32) != ~0u); }
    if (__all(ok)) break;
    __builtin_amdgcn_s_sleep(1);
  }
  s = 0.f; q = 0.f;
#pragma unroll
  for (int i = 0; i < 8; ++i) { s += __uint_as_float((unsigned)v[i]); q += __uint_as_float((unsigned)(v[i] >> 32)); }
  const float mean = s * (1.f / 864.f), var = fmaxf(q * (1.f / 864.f) - mean * mean, 0.f);
  const float ri = 1.f / ln_den(var);
  *(f32x2*)(fin + lane * 2) = (f32x2){ri, rbf(mean) * ri};
  __builtin_amdgcn_s_setprio(0);
  TS(20);
  BAR();
  __builtin_amdgcn_s_sleep(6);
  sp_ring<0, 5>(wsp, smem, panel, wid, lane);
  win_tail(ws, wsp, smem, panel, cb, wid, lane TPASS);
}

#define SP_LB 4
DI void phase_sp(char* __restrict__ ws, rsrc_t rs, const u16* __restrict__ bsp, char* smem, int wid, const u32x4 (&wr)[16 - K5_R][2] TARG) {
  const int tid = fresh_tid(), lane = tid & 63;
  const int xcd = blockIdx.x & 7, idx = blockIdx.x >> 3, panel = xcd * 4 + (idx >> 3), cb = idx & 7;
  const int kh = wid >> 2, up = wid & 3, u0 = unit0_of(cb) + 2 * up, nu = cb < 6 ? 7 : 6, g = lane >> 4, r = lane & 15;
  const unsigned dead[2] = {2 * up < nu ? 0u : DEAD, 2 * up + 1 < nu ? 0u : DEAD};
  const unsigned bvo[2] = {lane * 16 + dead[0], lane * 16 + 1024 + dead[1]};
  u16 bb[2];
#pragma unroll
  for (int j = 0; j < 2; ++j) bb[j] = bsp[panel * 64 + (2 * kh + j) * 16 + r];
  const char* al[2][2];
#pragma unroll
  for (int k = 0; k < 2; ++k)
#pragma unroll
    for (int j = 0; j < 2; ++j) al[k][j] = smem + kh * 8192 + (j ^ kh) * 4096 + r * 128 + ((4 * k + g) ^ (r >> 1)) * 16;
  constexpr int LB = SP_LB, PD = 3;
  bf16x8 bq[LB + 1][4], af[PD + 1][2]; f32x4 acc[2][4]; u32x4 wq[17];
  if (wid == 7) poll_ge(ws, C_Z2 + (panel >> 3) * 8 + cb, 32);
#pragma unroll
  for (int i = 0; i < 2; ++i) {
    const float b0 = bf2f(bb[0]), b1 = bf2f(bb[1]);
    acc[i][0] = (f32x4){b0, b0, b0, b0}; acc[i][1] = (f32x4){b1, b1, b1, b1}; acc[i][2] = (f32x4){}; acc[i][3] = (f32x4){};
  }
  if (kh && up < 3) waitv<2 * (16 - K5_R)>(); else waitv<0>();
  BAR();
  TS(23);
  const unsigned bso = OFF_Z2 + (kh * 2 * 54 + u0) * 1024;
#define SP_LD(s, j) (bq[(s) % (LB + 1)][j] = BF8(ldc16(rs, bvo[(j) & 1], bso + (sp_stage(s) * 4 + ((j) >> 1)) * 55296)))
#define SP_RD(t, k) (af[(t) % (PD + 1)][k] = *(const bf16x8*)(al[k][((t) >> 1) & 1] + (((t) >> 2) % K5_R) * 16384 + ((t) & 1) * 2048))
#pragma unroll
  for (int s = 0; s < LB; ++s)
#pragma unroll
    for (int j = 0; j < 4; ++j) SP_LD(s, j);
#pragma unroll
  for (int t = 0; t < PD; ++t) { SP_RD(t, 0); SP_RD(t, 1); }
  SCHED();
  sfor<64>([&](auto T) __attribute__((always_inline)) {
    constexpr int t = T, nx = t + PD, s = t >> 2, m = t & 3, tl = t + 4 * LB;
    if constexpr (nx == 16 || nx == 28 || nx == 44 || nx == 56) { BAR(); TS(32 + (nx == 28) + 2 * (nx == 44) + 3 * (nx == 56)); }
    constexpr int c = nx >= 16 && nx < 24 ? nx - 16 : nx >= 28 && nx < 34 ? nx - 20 : nx >= 44 && nx < 48 ? nx - 30 : -1;
    if constexpr (nx < 64) SP_RD(nx, 0);
    acc[0][m] = MFMA16(bq[s % (LB + 1)][0], af[t % (PD + 1)][0], acc[0][m]);
    acc[0][m] = MFMA16(bq[s % (LB + 1)][2], af[t % (PD + 1)][1], acc[0][m]); PIN(acc[0][m]);
    if constexpr (tl < 64) SP_LD(tl >> 2, m); else if constexpr (tl - 64 < 17) wq[tl - 64] = proj_ld<true>(rs, tl - 64, wid, lane);
    SCHED();
    if constexpr (nx < 64) SP_RD(nx, 1);
    acc[1][m] = MFMA16(bq[s % (LB + 1)][1], af[t % (PD + 1)][0], acc[1][m]);
    acc[1][m] = MFMA16(bq[s % (LB + 1)][3], af[t % (PD + 1)][1], acc[1][m]); PIN(acc[1][m]);
    if constexpr (c >= 0) *(u32x4*)(smem + ((c >> 1) % K5_R) * 16384 + wid * 2048 + (c & 1) * 1024 + lane * 16) = wr[c >> 1][c & 1];
    if constexpr (t == 63 && 4 * LB < 17) { _Pragma("unroll") for (int i = 4 * LB; i < 17; ++i) wq[i] = proj_ld<true>(rs, i, wid, lane); }
    SCHED();
  });
  TS(24);
  f32x4* exw = (f32x4*)(smem + 32768 + wid * 4096) + lane;
  const f32x4* exr = (const f32x4*)(smem + 32768 + (wid ^ 4) * 4096) + lane;
  u32x4 z[2];
#pragma unroll
  for (int i = 0; i < 2; ++i) z[i] = *(const u32x4*)(smem + L4_Z1 + (2 * up + i) * 2048 + kh * 1024 + lane * 16);
#pragma unroll
  for (int i = 0; i < 2; ++i)
#pragma unroll
    for (int j = 0; j < 2; ++j) exw[(i * 2 + j) * 64] = acc[i][2 + j];
  f32x4 zf[2][2];
#pragma unroll
  for (int i = 0; i < 2; ++i)
#pragma unroll
    for (int j = 0; j < 2; ++j) {
      zf[i][j] = (f32x4){bflo(z[i][2 * j]), bfhi(z[i][2 * j]), bflo(z[i][2 * j + 1]), bfhi(z[i][2 * j + 1])};
      acc[i][j] *= zf[i][j]; PIN(zf[i][j]); PIN(acc[i][j]);
    }
  BAR();
  f32x4 oth[2][2];
#pragma unroll
  for (int i = 0; i < 2; ++i)
#pragma unroll
    for (int j = 0; j < 2; ++j) oth[i][j] = exr[(i * 2 + j) * 64];
#pragma unroll
  for (int i = 0; i < 2; ++i)
#pragma unroll
    for (int j = 0; j < 2; ++j) {
      const f32x4 v = acc[i][j] + zf[i][j] * oth[i][j];
      stc8(rs, ((g >> 1) * 16 + r) * 16 + (g & 1) * 8 + dead[i], OFF_U + (panel * 4 + 2 * kh + j) * 27648 + (u0 + i) * 512, (u32x2){pack2(v[0], v[1]), pack2(v[2], v[3])});
    }
  TS(25);
  waitv<0>();
  BAR();
  TS(26);
  if (wid == 7) arrive(ws, C_U + panel, lane);
#pragma unroll
  for (int i = 0; i < 17; ++i) proj_park<true>(smem, i, wid, lane, wq[i]);
}

__global__ __launch_bounds__(512, 2) OCC2 void k_mega(char* __restrict__ ws, const u16* __restrict__ x, const u16* __restrict__ win,
                                                     const u16* __restrict__ wsp, const u16* __restrict__ bsp, const u16* __restrict__ wout,
                                                     const u16* __restrict__ cvec, const u16* __restrict__ wmod, u16* __restrict__ out) {
  extern __shared__ __attribute__((aligned(16))) char smem[];
  const int wid = __builtin_amdgcn_readfirstlane(threadIdx.x >> 6);
  const rsrc_t rs = __builtin_amdgcn_make_buffer_rsrc(ws, 0, WS_USED, 0x00020000);
  TDECL; TS(0);
  u32x2 res[3];
  phase_qkv(ws, rs, cvec, wmod, smem, wid TPASS);
  phase_attn(ws, rs, (const char*)wsp, win, wout, smem, wid TPASS);
  u32x4 wr[16 - K5_R][2];
  auto nohook = []() __attribute__((always_inline)) {};
  if (wid < 4) {
    bf16x8 b2[20];
    proj_body<false, 0>(ws, rs, x, out, smem, wid, res, [&]() __attribute__((always_inline)) { win_prefetch(rs, wid, b2); } TPASS);
    win_body<true>(ws, rs, wsp, smem, wid, b2, wr TPASS);
  } else {
    proj_body<false, 1>(ws, rs, x, out, smem, wid, res, nohook TPASS);
    if (wid == 7) win_agent(ws, rs, wsp, smem, wr TPASS);
    else { bf16x8 b2[20]; win_prefetch(rs, wid, b2); win_body<false>(ws, rs, wsp, smem, wid, b2, wr TPASS); }
  }
  phase_sp(ws, rs, bsp, smem, wid, wr TPASS);
  if (wid < 4) proj_body<true, 0>(ws, rs, x, out, smem, wid, res, nohook TPASS);
  else proj_body<true, 1>(ws, rs, x, out, smem, wid, res, nohook TPASS);
  TS(29);
  TDUMP();
}

extern "C" void kernel_launch(void* const* d_in, const int* in_sizes, int n_in, void* d_out, int out_size, void* d_ws, size_t ws_size,
                              hipStream_t stream) {
  const u16* x = (const u16*)d_in[0];
  const u16* wqkv = (const u16*)d_in[1];
  const u16* wo = (const u16*)d_in[2];
  const u16* win = (const u16*)d_in[4];
  const u16* wsp = (const u16*)d_in[5];
  const u16* bsp = (const u16*)d_in[6];
  const u16* wout = (const u16*)d_in[7];
  const u16* cvec = (const u16*)d_in[8];
  const u16* wmod = (const u16*)d_in[9];
  char* ws = (char*)d_ws;
  (void)hipFuncSetAttribute((const void*)k_mega, hipFuncAttributeMaxDynamicSharedMemorySize, 163840);
  k0_prep<<<K0_GRID, 256, 0, stream>>>(x, wqkv, wo, cvec, wmod, ws);
  k_mega<<<256, 512, 163840, stream>>>(ws, x, win, wsp, bsp, wout, cvec, wmod, (u16*)d_out);
}
```

```cpp
#include <hip/hip_runtime.h>
#include <hip/hip_bf16.h>
#include <stdint.h>
#include <utility>
#define PA 60
#define PB 61

typedef __attribute__((ext_vector_type(8))) short bf16x8;
typedef __attribute__((ext_vector_type(4))) short bf16x4;
typedef __attribute__((ext_vector_type(2))) float f32x2;
typedef __attribute__((ext_vector_type(4))) float f32x4;
typedef __attribute__((ext_vector_type(16))) float f32x16;
typedef __attribute__((ext_vector_type(2))) unsigned u32x2;
typedef __attribute__((ext_vector_type(4))) unsigned u32x4;
typedef __attribute__((ext_vector_type(2))) __bf16 bf2_t;
typedef __attribute__((ext_vector_type(2))) _Float16 h2_t;
typedef unsigned short u16;
typedef unsigned long long u64;
#define LAS __attribute__((address_space(3)))
#define GAS __attribute__((address_space(1)))
#define DI __device__ __forceinline__

constexpr int D_ = 640, NQKV = 1920, F_ = 1728;

constexpr unsigned OFF_CNT = 0;
constexpr unsigned OFF_MOD = 65536;
constexpr unsigned OFF_MODP = OFF_MOD + 16384;
constexpr unsigned OFF_CBP = OFF_MODP + 40960;
constexpr unsigned OFF_XCH = OFF_CBP + 5 * 16384;
constexpr unsigned OFF_ST1 = OFF_XCH + 131072;
constexpr unsigned OFF_XHAT = OFF_ST1 + 262144;
constexpr unsigned OFF_WQKV = OFF_XHAT + 2621440;
constexpr unsigned OFF_WO = OFF_WQKV + 2457600;
constexpr unsigned OFF_WIN = OFF_WO + 819200;
constexpr unsigned OFF_WOUT = OFF_WIN + 2211840;
constexpr unsigned OFF_Q = OFF_WOUT + 1105920;
constexpr unsigned OFF_KV = OFF_Q + 32 * 81920;
constexpr unsigned OFF_O = OFF_KV + 32 * 163840;
constexpr unsigned OFF_X1S = OFF_O + 2621440;
constexpr unsigned OFF_Z2 = OFF_X1S + 2621440;
constexpr unsigned OFF_U = OFF_Z2 + 3538944;
constexpr unsigned WS_USED = OFF_U + 3538944;
constexpr int C_QKV = 0, C_MOD = 15  , C_ATT = 128, C_REL = 136, C_X1 = 144, C_U = 176, C_Z2 = 208  ;

#define STAMP(kid, k)
#define TDECL u64 tsv_ = 0
#define TS(k) do { if ((k) == PA) tsv_ -= __builtin_amdgcn_s_memrealtime(); if ((k) == PB) tsv_ += __builtin_amdgcn_s_memrealtime(); } while (0)
#define TDUMP() do { if (blockIdx.x == 3 && wid == 4) { const u64 e_ = __builtin_amdgcn_s_memrealtime() + 8 * tsv_; for (int i_ = 0; i_ < 200000; ++i_) { if (__builtin_amdgcn_s_memrealtime() >= e_) break; __builtin_amdgcn_s_sleep(8); } } } while (0)
DI unsigned pack2(float lo, float hi) { return __builtin_bit_cast(unsigned, __builtin_convertvector((f32x2){lo, hi}, bf2_t)); }
DI unsigned pack2s(float lo, float hi) { unsigned r; asm("v_cvt_pk_bf16_f32 %0, %1, %2" : "=v"(r) : "v"(lo), "v"(hi)); return r; }
DI float opq(float x) { asm("" : "+v"(x)); return x; }
DI f32x4 fma4s(f32x4 a, f32x4 b, f32x4 c) { return (f32x4){opq(__builtin_fmaf(a[0], b[0], c[0])), opq(__builtin_fmaf(a[1], b[1], c[1])), opq(__builtin_fmaf(a[2], b[2], c[2])), opq(__builtin_fmaf(a[3], b[3], c[3]))}; }
DI f32x4 mul4s(f32x4 a, f32x4 b) { return (f32x4){opq(a[0] * b[0]), opq(a[1] * b[1]), opq(a[2] * b[2]), opq(a[3] * b[3])}; }
DI unsigned pack2h(float lo, float hi) { return __builtin_bit_cast(unsigned, __builtin_convertvector((f32x2){lo, hi}, h2_t)); }
DI float bflo(unsigned u) { return __uint_as_float(u << 16); }
DI float bfhi(unsigned u) { return __uint_as_float(u & 0xffff0000u); }
DI float rbf(float x) { return bflo(pack2(x, x)); }
DI float bf2f(u16 b) { return __uint_as_float((unsigned)b << 16); }
#define MFMA32(a, b, c) __builtin_amdgcn_mfma_f32_32x32x16_bf16((a), (b), (c), 0, 0, 0)
#define MFMA32K8(a, b, c) __builtin_amdgcn_mfma_f32_32x32x8bf16_1k((a), (b), (c), 0, 0, 0)
#define MFMA16(a, b, c) __builtin_amdgcn_mfma_f32_16x16x32_bf16((a), (b), (c), 0, 0, 0)
#define BAR() asm volatile("s_waitcnt lgkmcnt(0)\n\ts_barrier" ::: "memory")
#define SCHED() __builtin_amdgcn_sched_barrier(0)
#define PIN(x) asm volatile("" : "+v"(x))
#define OCC2 __attribute__((amdgpu_waves_per_eu(2, 2)))
#define BF8(x) __builtin_bit_cast(bf16x8, x)
#define BF4(x) __builtin_bit_cast(bf16x4, x)
template <int N> DI void waitv() { asm volatile("s_waitcnt vmcnt(%0)" ::"i"(N) : "memory"); }
template <class F, int... I> DI void sfor_impl(F&& f, std::integer_sequence<int, I...>) { (f(std::integral_constant<int, I>{}), ...); }
template <int N, class F> DI void sfor(F&& f) { sfor_impl(f, std::make_integer_sequence<int, N>{}); }
typedef short v4i16_t __attribute__((ext_vector_type(4)));
DI bf16x4 lds_tr(const char* p) { return __builtin_bit_cast(bf16x4, __builtin_amdgcn_ds_read_tr16_b64_v4i16((LAS v4i16_t*)p)); }
DI void glds16(const void* g, unsigned lds_base) {
  unsigned sv;
  asm volatile("s_mov_b32 %0, m0\n\ts_mov_b32 m0, %2\n\ts_nop 0\n\tglobal_load_lds_dwordx4 %1, off\n\ts_mov_b32 m0, %0" : "=&s"(sv) : "v"(g), "s"(lds_base) : "memory");
}
DI void glds16c(const void* g, unsigned lds_base) {
  unsigned sv;
  asm volatile("s_mov_b32 %0, m0\n\ts_mov_b32 m0, %2\n\ts_nop 0\n\tglobal_load_lds_dwordx4 %1, off sc1\n\ts_mov_b32 m0, %0" : "=&s"(sv) : "v"(g), "s"(lds_base) : "memory");
}
DI void glds4c(const void* g, unsigned lds_base) {
  unsigned sv;
  asm volatile("s_mov_b32 %0, m0\n\ts_mov_b32 m0, %2\n\ts_nop 0\n\tglobal_load_lds_dword %1, off sc1\n\ts_mov_b32 m0, %0" : "=&s"(sv) : "v"(g), "s"(lds_base) : "memory");
}
typedef __amdgpu_buffer_rsrc_t rsrc_t;
DI u32x4 ldc16(rsrc_t rs, unsigned voff, unsigned soff) { return __builtin_amdgcn_raw_buffer_load_b128(rs, voff, soff, 16); }
DI unsigned ldc4(rsrc_t rs, unsigned voff, unsigned soff) { return __builtin_amdgcn_raw_buffer_load_b32(rs, voff, soff, 16); }
DI void stc16(rsrc_t rs, unsigned voff, unsigned soff, u32x4 v) { __builtin_amdgcn_raw_buffer_store_b128(v, rs, voff, soff, 16); asm volatile("s_nop 1" ::"v"(v) : "memory"); }
DI void stc8(rsrc_t rs, unsigned voff, unsigned soff, u32x2 v) { __builtin_amdgcn_raw_buffer_store_b64(v, rs, voff, soff, 16); }
DI void stc4(rsrc_t rs, unsigned voff, unsigned soff, unsigned v) { __builtin_amdgcn_raw_buffer_store_b32(v, rs, voff, soff, 16); }
DI f32x4 ldc16f(rsrc_t rs, unsigned voff, unsigned soff) { return __builtin_bit_cast(f32x4, ldc16(rs, voff, soff)); }
DI void arrive(char* ws, int c, int lane) { if (lane == 0) __hip_atomic_fetch_add((GAS unsigned*)(ws + OFF_CNT + c * 256), 1u, __ATOMIC_RELAXED, __HIP_MEMORY_SCOPE_AGENT); }
DI void poll_ge(char* ws, int c, unsigned thr) {
  const GAS unsigned* p = (const GAS unsigned*)(ws + OFF_CNT + c * 256);
#pragma nounroll
  for (unsigned spin = 0; spin < (1u << 21); ++spin) {
    if (__all(__hip_atomic_load(p, __ATOMIC_RELAXED, __HIP_MEMORY_SCOPE_AGENT) >= thr)) break;
    __builtin_amdgcn_s_sleep(1);
  }
  asm volatile("" ::: "memory");
}
DI int fresh_tid() { int t = threadIdx.x; asm volatile("" : "+v"(t)); return t; }
DI void l2touch(const void* g, unsigned lds_scratch) {
  unsigned sv;
  asm volatile("s_mov_b32 %0, m0\n\ts_mov_b32 m0, %2\n\ts_nop 0\n\tglobal_load_lds_dword %1, off\n\ts_mov_b32 m0, %0" : "=&s"(sv) : "v"(g), "s"(lds_scratch) : "memory");
}
#define LDNT(p) __builtin_nontemporal_load(p)
DI float silu_bf(u16 c) { const float f = __uint_as_float((unsigned)c << 16); const float v = f / (1.f + __expf(-f)); return __uint_as_float(__builtin_bit_cast(unsigned, __builtin_convertvector((f32x2){v, v}, bf2_t)) << 16); }
DI unsigned lds_addr(const void* p) { return (unsigned)(uintptr_t)(const LAS void*)p; }
DI float xhalf(float v) {
  auto rr = __builtin_amdgcn_permlane32_swap(__float_as_uint(v), __float_as_uint(v), false, false);
  return __uint_as_float((threadIdx.x & 32) ? rr[0] : rr[1]);
}
DI float sum_g(float v) {
  auto a = __builtin_amdgcn_permlane16_swap(__float_as_uint(v), __float_as_uint(v), false, false);
  v = __uint_as_float(a[0]) + __uint_as_float(a[1]);
  auto b = __builtin_amdgcn_permlane32_swap(__float_as_uint(v), __float_as_uint(v), false, false);
  return __uint_as_float(b[0]) + __uint_as_float(b[1]);
}
template <int CTRL> DI float dpp_add(float v) { return v + __uint_as_float(__builtin_amdgcn_update_dpp(0u, __float_as_uint(v), CTRL, 0xf, 0xf, true)); }
DI float sum16(float v) {
  v = dpp_add<0x128>(v); v = dpp_add<0x124>(v); v = dpp_add<0x122>(v); return dpp_add<0x121>(v);
}
DI u32x4 modulate8(u32x4 v, f32x4 sc0, f32x4 sc1, f32x4 sh0, f32x4 sh1) {
  unsigned o0, o1, o2, o3; float t0, t1, t2, t3;
  asm volatile(
      "v_fma_mix_f32 %0, %8, %12, %20 op_sel_hi:[1,0,0]\n\tv_fma_mix_f32 %4, %8, %13, %21 op_sel:[1,0,0] op_sel_hi:[1,0,0]\n\t"
      "v_fma_mix_f32 %1, %9, %14, %22 op_sel_hi:[1,0,0]\n\tv_fma_mix_f32 %5, %9, %15, %23 op_sel:[1,0,0] op_sel_hi:[1,0,0]\n\t"
      "v_fma_mix_f32 %2, %10, %16, %24 op_sel_hi:[1,0,0]\n\tv_fma_mix_f32 %6, %10, %17, %25 op_sel:[1,0,0] op_sel_hi:[1,0,0]\n\t"
      "v_fma_mix_f32 %3, %11, %18, %26 op_sel_hi:[1,0,0]\n\tv_fma_mix_f32 %7, %11, %19, %27 op_sel:[1,0,0] op_sel_hi:[1,0,0]\n\t"
      "v_cvt_pk_bf16_f32 %0, %0, %4\n\tv_cvt_pk_bf16_f32 %1, %1, %5\n\tv_cvt_pk_bf16_f32 %2, %2, %6\n\tv_cvt_pk_bf16_f32 %3, %3, %7"
      : "=&v"(o0), "=&v"(o1), "=&v"(o2), "=&v"(o3), "=&v"(t0), "=&v"(t1), "=&v"(t2), "=&v"(t3)
      : "v"(v[0]), "v"(v[1]), "v"(v[2]), "v"(v[3]), "v"(sc0[0]), "v"(sc0[1]), "v"(sc0[2]), "v"(sc0[3]), "v"(sc1[0]), "v"(sc1[1]), "v"(sc1[2]), "v"(sc1[3]),
        "v"(sh0[0]), "v"(sh0[1]), "v"(sh0[2]), "v"(sh0[3]), "v"(sh1[0]), "v"(sh1[1]), "v"(sh1[2]), "v"(sh1[3]));
  return (u32x4){o0, o1, o2, o3};
}
DI float ln_den(float var) { return rbf(__builtin_sqrtf(rbf(rbf(var) + 1e-5f))); }
DI unsigned a16_off(int n, int r) { return (n >> 5) * 1024 + (((n & 31) >> 3) * 16 + r) * 16 + (n & 4) * 2; }
DI int unit0_of(int j) { return j < 6 ? 7 * j : 42 + 6 * (j - 6); }

DI void K0ST(char* p, u32x4 v) { *(u32x4*)p = v; }
constexpr int K0_MOD = 160, K0_LN = 128, K0_RL = 240, K0_WO = 50, K0_INIT = 12;
constexpr int K0_GRID = K0_MOD + K0_LN + K0_RL + K0_WO + K0_INIT;

__global__ __launch_bounds__(256) void k0_prep(const u16* __restrict__ x, const u16* __restrict__ wqkv, const u16* __restrict__ wo, const u16* __restrict__ cvec,
                                               const u16* __restrict__ wmod, char* __restrict__ ws) {
  __shared__ __attribute__((aligned(16))) char smem[4 * 4608];
  const int tid = threadIdx.x, lane = tid & 63, wid = __builtin_amdgcn_readfirstlane(tid >> 6);
  int b = blockIdx.x;
  STAMP(0, 0);
  if (b < K0_MOD) {
    const int cg = b >> 2, ks = b & 3, c = tid & 7, row0 = ks * 160 + (tid >> 3), col0 = cg * 64 + (cg >= 20 ? 640 : 0);
    u32x4 w[5]; u16 cv[5];
#pragma unroll
    for (int i = 0; i < 5; ++i) { w[i] = LDNT((const u32x4*)(wmod + (size_t)(row0 + 32 * i) * 3840 + col0 + c * 8)); cv[i] = cvec[row0 + 32 * i]; }
    float acc[8] = {0, 0, 0, 0, 0, 0, 0, 0};
#pragma unroll
    for (int i = 0; i < 5; ++i) {
      const float s = silu_bf(cv[i]);
#pragma unroll
      for (int e = 0; e < 4; ++e) { acc[2 * e] += s * bflo(w[i][e]); acc[2 * e + 1] += s * bfhi(w[i][e]); }
    }
#pragma unroll
    for (int e = 0; e < 8; ++e) acc[e] = sum_g(dpp_add<0x128>(acc[e]));
    float* red = (float*)smem;
    if (lane < 8) { *(f32x4*)(red + wid * 64 + lane * 8) = (f32x4){acc[0], acc[1], acc[2], acc[3]}; *(f32x4*)(red + wid * 64 + lane * 8 + 4) = (f32x4){acc[4], acc[5], acc[6], acc[7]}; }
    __syncthreads();
    if (tid < 64) ((float*)(ws + OFF_MODP))[ks * 2560 + cg * 64 + tid] = (red[tid] + red[64 + tid]) + (red[128 + tid] + red[192 + tid]);
    STAMP(0, 1); STAMP(0, 13);
    return;
  }
  b -= K0_MOD;
  if (b < K0_LN) {
    b = (b & 7) * 16 + (b >> 3);
    const int row = b * 16 + wid * 4 + (lane >> 4), c = lane & 15;
    const u16* xr = x + (size_t)row * D_ + c * 8;
    u32x4 v[5];
#pragma unroll
    for (int i = 0; i < 5; ++i) v[i] = *(const u32x4*)(xr + i * 128);
    float s = 0.f, q = 0.f;
#pragma unroll
    for (int i = 0; i < 5; ++i)
#pragma unroll
      for (int e = 0; e < 4; ++e) { const float a = bflo(v[i][e]), d = bfhi(v[i][e]); s += a + d; q += a * a + d * d; }
    s = sum16(s); q = sum16(q);
    const float mean = s * (1.f / 640.f); const float var = fmaxf(q * (1.f / 640.f) - mean * mean, 0.f);
    const float rinv = 1.f / ln_den(var), mb = rbf(mean) * rinv;
    char* dst = ws + OFF_XHAT + (size_t)row * 1280 + c * 16;
#pragma unroll
    for (int i = 0; i < 5; ++i) {
      u32x4 o;
#pragma unroll
      for (int e = 0; e < 4; ++e) o[e] = pack2h(bflo(v[i][e]) * rinv - mb, bfhi(v[i][e]) * rinv - mb);
      K0ST(dst + i * 256, o);
    }
    STAMP(0, 2); STAMP(0, 13);
    return;
  }
  b -= K0_LN;
  if (b < K0_RL) {
    const int kt = b / 12, G = b % 12, sec = G >> 2, gq = G & 3;
    u32x4 v[3];
#pragma unroll
    for (int i = 0; i < 3; ++i) { const int ci = min(i * 256 + tid, 639); v[i] = LDNT((const u32x4*)(wqkv + (size_t)(kt * 32 + ci / 20) * NQKV + G * 160 + (ci % 20) * 8)); }
#pragma unroll
    for (int i = 0; i < 3; ++i) { const int ci = min(i * 256 + tid, 639); *(u32x4*)(smem + (ci / 20) * 336 + (ci % 20) * 16) = v[i]; }
    __syncthreads();
    const int g = lane >> 4, q = (lane & 15) >> 2, p = lane & 3;
    for (int f = wid; f < 10; f += 4) {
      const int t = f >> 1, x2 = f & 1;
      const int col = sec == 2 ? 16 * f + 4 * p : t < 4 ? 20 * (2 * t + (p >> 1)) + 8 * (p & 1) + 4 * x2 : 20 * (4 * x2 + p) + 16;
      const int T = sec == 2 ? 40 + gq * 5 + t : sec * 20 + (t < 4 ? 4 * gq + t : 16 + gq);
      const char* pp = smem + (8 * g + q) * 336 + col * 2;
      const bf16x4 lo = lds_tr(pp), hi = lds_tr(pp + 4 * 336);
      K0ST(ws + OFF_WQKV + (((size_t)T * 20 + kt) * 2 + x2) * 1024 + lane * 16, __builtin_bit_cast(u32x4, (bf16x8)__builtin_shufflevector(lo, hi, 0, 1, 2, 3, 4, 5, 6, 7)));
    }
    STAMP(0, 3); STAMP(0, 13);
    return;
  }
  b -= K0_RL;
  if (b < K0_WO) {
    const int job = b * 4 + wid, kt = job / 10, ng = job % 10, j8 = lane >> 3;
    char* t = smem + wid * 4608;
    u32x4 v[4];
#pragma unroll
    for (int i = 0; i < 4; ++i) {
      const int c = 4 * kt + i, row = c < 64 ? 20 * (c >> 1) + 4 * (c & 1) + (j8 & 3) + 8 * (j8 >> 2) : 20 * (2 * (c - 64) + (j8 >> 2)) + 16 + (j8 & 3);
      v[i] = LDNT((const u32x4*)(wo + (size_t)row * D_ + ng * 64 + (lane & 7) * 8));
    }
#pragma unroll
    for (int i = 0; i < 4; ++i) *(u32x4*)(t + (i * 8 + j8) * 144 + (lane & 7) * 16) = v[i];
    const char* pp = t + (8 * (lane >> 4) + ((lane & 15) >> 2)) * 144 + (lane & 3) * 8;
#pragma unroll
    for (int f = 0; f < 4; ++f) {
      const bf16x4 lo = lds_tr(pp + f * 32), hi = lds_tr(pp + 4 * 144 + f * 32);
      K0ST(ws + OFF_WO + ((size_t)(ng * 4 + f) * 20 + kt) * 1024 + lane * 16, __builtin_bit_cast(u32x4, (bf16x8)__builtin_shufflevector(lo, hi, 0, 1, 2, 3, 4, 5, 6, 7)));
    }
    return;
  }
  b -= K0_WO;
  {
    u32x4* p = (u32x4*)(ws + (b < 8 ? OFF_XCH + b * 16384 : OFF_CNT + (b - 8) * 16384)) + tid;
    const unsigned f = b < 8 ? ~0u : 0u;
#pragma unroll
    for (int i = 0; i < 4; ++i) p[i * 256] = (u32x4){f, f, f, f};
  }
}

constexpr float QSCALE = 0.22360679774997896f * 1.4426950408889634f;
#define K1W 3
#define K1WB 5
#define K1WB0 2
constexpr int K1_W = K1W, K1_WB = K1WB, K1_WB0 = K1WB0;

template <bool ISV>
DI void k1_mma(const char* __restrict__ bsrc, const char* smem, int lane, f32x4 (&acc)[8][2]) {
  constexpr int PD = 4;
  bf16x8 bfr[K1_WB + 1][4], af[PD + 1][2];
  const char* lds[2];
#pragma unroll
  for (int k = 0; k < 2; ++k) lds[k] = smem + k * 8192 + (lane ^ (4 * k + (lane >> 4))) * 16;
#pragma unroll
  for (int i = 0; i < 4 * K1_WB0; ++i) bfr[i >> 2][i & 3] = *(const bf16x8*)(bsrc + i * 1024);
#pragma unroll
  for (int m = 0; m < 8; ++m) { acc[m][0] = (f32x4){}; acc[m][1] = (f32x4){}; }
  BAR();
#pragma unroll
  for (int i = 4 * K1_WB0; i < 4 * K1_WB; ++i) bfr[i >> 2][i & 3] = *(const bf16x8*)(bsrc + i * 1024);
  BAR();
  __builtin_amdgcn_s_setprio(1);
#define K1_RD(t, k) (af[(t) % (PD + 1)][k] = *(const bf16x8*)(lds[k] + (((t) >> 3) % 3) * 16384 + ((t) & 7) * 1024))
#define K1_MM(c, b, a) (c = ISV ? MFMA16(a, b, c) : MFMA16(b, a, c))
#pragma unroll
  for (int t = 0; t < PD; ++t) { K1_RD(t, 0); K1_RD(t, 1); }
  SCHED();
  sfor<80>([&](auto T) __attribute__((always_inline)) {
    constexpr int t = T, s = t >> 3, m = t & 7, nx = t + PD;
    if constexpr (nx < 80 && (nx & 7) == 0) asm volatile("s_barrier" ::: "memory");
    if constexpr (nx < 80) K1_RD(nx, 0);
    K1_MM(acc[m][0], bfr[s % (K1_WB + 1)][0], af[t % (PD + 1)][0]); K1_MM(acc[m][0], bfr[s % (K1_WB + 1)][2], af[t % (PD + 1)][1]); PIN(acc[m][0]);
    SCHED();
    if constexpr (nx < 80) K1_RD(nx, 1);
    if constexpr (s + K1_WB < 10 && (m & 1)) bfr[(s + K1_WB) % (K1_WB + 1)][m >> 1] = *(const bf16x8*)(bsrc + ((s + K1_WB) * 4 + (m >> 1)) * 1024);
    K1_MM(acc[m][1], bfr[s % (K1_WB + 1)][1], af[t % (PD + 1)][0]); K1_MM(acc[m][1], bfr[s % (K1_WB + 1)][3], af[t % (PD + 1)][1]); PIN(acc[m][1]);
    SCHED();
  });
  __builtin_amdgcn_s_setprio(0);
}

#define TARG , u64& tsv_
#define TPASS , tsv_

DI void phase_qkv(char* __restrict__ ws, rsrc_t rs, const u16* __restrict__ cvec, const u16* __restrict__ wmod, char* smem, int wid TARG) {
  const int tid = fresh_tid(), lane = tid & 63;
  const int xcd = blockIdx.x & 7, idx = blockIdx.x >> 3;
  int ctr;
  if ((xcd & 1) && idx >= 28) {
    ctr = C_MOD;
    const int sb = (xcd >> 1) * 4 + idx - 28, col0 = (sb < 8 ? 1280 : 3200 - 640) + sb * 80, c = tid % 10, rg = tid / 10;
    float* sil = (float*)smem; float* red = (float*)(smem + 4096);
    u32x4 w[13];
#pragma unroll
    for (int i = 0; i < 13; ++i) { const int row = min(rg + 50 * i, 639); w[i] = LDNT((const u32x4*)(wmod + (size_t)row * 3840 + col0 + c * 8)); }
    for (int i = tid; i < 660; i += 512) sil[i] = i < 640 ? silu_bf(cvec[i]) : 0.f;
    if (tid < 80) {
      const float* pp = (const float*)(ws + OFF_MODP) + 1280 + sb * 80 + tid;
      float v = rbf((pp[0] + pp[2560]) + (pp[5120] + pp[7680]));
      if (sb >= 8) v = rbf(1.f + v);
      stc4(rs, (1920 + sb * 80 + tid) * 4, OFF_MOD, __float_as_uint(v));
    }
    BAR();
    float acc[8] = {0, 0, 0, 0, 0, 0, 0, 0};
#pragma unroll
    for (int i = 0; i < 13; ++i) {
      const float s = sil[rg + 50 * i];
#pragma unroll
      for (int e = 0; e < 4; ++e) { acc[2 * e] += s * bflo(w[i][e]); acc[2 * e + 1] += s * bfhi(w[i][e]); }
    }
    if (rg < 50) { *(f32x4*)(red + rg * 80 + c * 8) = (f32x4){acc[0], acc[1], acc[2], acc[3]}; *(f32x4*)(red + rg * 80 + c * 8 + 4) = (f32x4){acc[4], acc[5], acc[6], acc[7]}; }
    BAR();
    if (tid < 80) {
      float v = 0.f;
#pragma unroll
      for (int i = 0; i < 50; ++i) v += red[i * 80 + tid];
      stc4(rs, (col0 + tid) * 4, OFF_MOD, __float_as_uint(rbf(v)));
    }
  } else {
    const int mt = (xcd >> 1) * 4 + (idx & 3), nt = (xcd & 1) * 8 + (idx >> 2);
    ctr = C_QKV + nt;
    if (wid < 4) {
      float* cst = (float*)(smem + 49152);
      const int c = lane & 7, rr = lane >> 3;
      const char* src = ws + OFF_XHAT + (size_t)(mt * 128 + wid * 32 + rr) * 1280 + c * 16;
      char* wdst = smem + (c >> 2) * 8192 + wid * 2048 + (c & 3) * 256 + (rr ^ c) * 16;
      u32x4 a[10][4];
      f32x4 c0[4], c1[4];
#pragma unroll
      for (int i = 0; i < 4; ++i) { c0[i] = *(const f32x4*)(ws + OFF_MODP + i * 10240 + tid * 16); if (wid == 0) c1[i] = *(const f32x4*)(ws + OFF_MODP + i * 10240 + 4096 + tid * 16); }
#pragma unroll
      for (int s = 0; s < K1_W; ++s)
#pragma unroll
        for (int m = 0; m < 4; ++m) a[s][m] = *(const u32x4*)(src + m * 10240 + s * 128);
      {
        f32x4 v = (c0[0] + c0[1]) + (c0[2] + c0[3]); const float one = tid >= 160 ? 1.f : 0.f;
        *(f32x4*)(cst + tid * 4) = (f32x4){rbf(one + rbf(v[0])), rbf(one + rbf(v[1])), rbf(one + rbf(v[2])), rbf(one + rbf(v[3]))};
        if (wid == 0) { v = (c1[0] + c1[1]) + (c1[2] + c1[3]); *(f32x4*)(cst + 1024 + tid * 4) = (f32x4){rbf(1.f + rbf(v[0])), rbf(1.f + rbf(v[1])), rbf(1.f + rbf(v[2])), rbf(1.f + rbf(v[3]))}; }
      }
      BAR();
      const float* cp = cst + c * 8;
      f32x4 kc[10][4];
#define K1_KC(s) do { kc[s][0] = *(const f32x4*)(cp + (s) * 64); kc[s][1] = *(const f32x4*)(cp + (s) * 64 + 4); kc[s][2] = *(const f32x4*)(cp + (s) * 64 + 640); kc[s][3] = *(const f32x4*)(cp + (s) * 64 + 644); } while (0)
#pragma unroll
      for (int s = 0; s < 6; ++s) K1_KC(s);
      SCHED();
      u32x4 o[2][4];
#define K1_XF(s, m) (o[(s) & 1][m] = modulate8(a[s][m], kc[s][2], kc[s][3], kc[s][0], kc[s][1]))
#define K1_WR(s, m) (*(u32x4*)(wdst + ((s) % 3) * 16384 + ((m) >> 1) * 1024 + ((m) & 1) * 128) = o[(s) & 1][m])
#pragma unroll
      for (int m = 0; m < 4; ++m) { K1_XF(0, m); K1_WR(0, m); }
      BAR();
      TS(1);
#pragma unroll
      for (int m = 0; m < 4; ++m) a[K1_W][m] = *(const u32x4*)(src + m * 10240 + K1_W * 128);
#pragma unroll
      for (int m = 0; m < 4; ++m) K1_XF(1, m);
      SCHED();
#pragma unroll
      for (int s = 1; s < 10; ++s) {
#pragma unroll
        for (int m = 0; m < 4; ++m) {
          if (m == 0) K1_WR(s, 0);
          if (m < 3) K1_WR(s, m + 1);
          if (s + K1_W < 10) a[s + K1_W][m] = *(const u32x4*)(src + m * 10240 + (s + K1_W) * 128);
          if (s < 5) kc[s + 5][m] = *(const f32x4*)(cp + (s + 5) * 64 + (m & 1) * 4 + (m >> 1) * 640);
          SCHED();
          if (s < 9) K1_XF(s + 1, m);
          SCHED();
        }
        asm volatile("" :: "v"(o[s & 1][0]), "v"(o[s & 1][1]), "v"(o[s & 1][2]), "v"(o[s & 1][3]));
        BAR();
      }
    } else {
      const int cw = wid - 4;
      const char* bsrc = ws + OFF_WQKV + (size_t)(nt * 4 + cw) * 40 * 1024 + lane * 16;
      f32x4 acc[8][2];
      const int r = lane & 15, g = lane >> 4;
      if (nt >= 10) {
        k1_mma<true>(bsrc, smem, lane, acc);
        TS(2);
        const unsigned so = OFF_KV + (mt >> 1) * 20480 + 10240 + (mt & 1) * 5120;
#pragma unroll
        for (int x = 0; x < 2; ++x) {
          const unsigned c = (nt - 10) * 128 + cw * 32 + x * 16 + r, head = c / 20, d = c % 20;
          const unsigned vo = head * 163840u + ((g & 1) * 20 + d) * 16 + (g >> 1) * 8;
#pragma unroll
          for (int m = 0; m < 8; ++m) stc8(rs, vo + (m & 3) * 640, so + (m >> 2) * 2560, (u32x2){pack2(acc[m][x][0], acc[m][x][1]), pack2(acc[m][x][2], acc[m][x][3])});
        }
      } else {
        k1_mma<false>(bsrc, smem, lane, acc);
        TS(2);
        const bool isQ = nt < 5; const float sc = isQ ? QSCALE : 1.f;
        const unsigned so = isQ ? OFF_Q + mt * 5120 : OFF_KV + (mt >> 1) * 20480 + (mt & 1) * 5120, hs = isQ ? 81920u : 163840u;
        const int n5 = isQ ? nt : nt - 5;
        if (n5 < 4) {
          const unsigned vo = (2 * (n5 * 4 + cw) + (g >> 1)) * hs + (g & 1) * 512 + r * 16;
#pragma unroll
          for (int m = 0; m < 8; ++m) {
            const f32x4 v0 = acc[m][0] * sc, v1 = acc[m][1] * sc;
            stc16(rs, vo + (m & 1) * 256, so + (m >> 1) * 1280, (u32x4){pack2(v0[0], v0[1]), pack2(v0[2], v0[3]), pack2(v1[0], v1[1]), pack2(v1[2], v1[3])});
          }
        } else {
          const unsigned vo = (8 * cw + g) * hs + 1024 + r * 8;
#pragma unroll
          for (int m = 0; m < 8; ++m)
#pragma unroll
            for (int x = 0; x < 2; ++x) {
              const f32x4 v = acc[m][x] * sc;
              stc8(rs, vo + (m & 1) * 128, so + x * 4 * hs + (m >> 1) * 1280, (u32x2){pack2(v[0], v[1]), pack2(v[2], v[3])});
            }
        }
      }
    }
  }
  TS(3);
  waitv<0>();
  BAR();
  TS(4);
  if (wid == 7 && lane < 8) __hip_atomic_fetch_add((GAS unsigned*)(ws + OFF_CNT + (lane * 16 + ctr) * 256), 1u, __ATOMIC_RELAXED, __HIP_MEMORY_SCOPE_AGENT);
}

template <bool FINAL> DI constexpr int proj_pos(int f) { return FINAL ? (f < 96 ? 65536 + f * 1024 : (f - 96) * 1024) : 17408 + (f % 20 >= 10 ? 81920 : 0) + (f / 20 * 10 + f % 10) * 1024; }
template <bool FINAL> DI u32x4 proj_ld(rsrc_t rs, int i, int wid, int lane) {
  constexpr int NF = FINAL ? 135 : 100;
  return ldc16(rs, lane * 16, (FINAL ? OFF_WOUT : OFF_WO) + (((blockIdx.x >> 3) & 7) * NF + min(i * 8 + wid, NF - 1)) * 1024);
}
template <bool FINAL> DI void proj_park(char* smem, int i, int wid, int lane, u32x4 v) {
  constexpr int NF = FINAL ? 135 : 100;
  *(u32x4*)(smem + proj_pos<FINAL>(min(i * 8 + wid, NF - 1)) + lane * 16) = v;
}

DI void split3(float m, unsigned& w_h0, unsigned& w_h1) {
  const float n = -m; const float a = rbf(n), b = rbf(n - a), c = rbf(n - a - b);
  w_h0 = pack2(a, b); w_h1 = pack2(c, 0.f);
}

DI void phase_attn(char* __restrict__ ws, rsrc_t rs, const char* __restrict__ wsp, const u16* __restrict__ win, const u16* __restrict__ wout, char* smem, int wid TARG) {
  const int tid = fresh_tid(), lane = tid & 63;
  const int xcd = blockIdx.x & 7, idx = blockIdx.x >> 3, head = xcd * 4 + (idx >> 3), qb = idx & 7;
  const int r = lane & 31, h = lane >> 5, qg = wid & 3, kh = wid >> 2;
  if (wid == 7) {
    const int l7 = lane & 7;
    const int c = l7 == 0 ? head >> 3 : l7 == 1 ? 4 : l7 == 2 ? 5 + (head >> 3) : l7 == 3 ? 9 : l7 == 4 ? 10 + (20 * head) / 128 : l7 == 5 ? 10 + (20 * head + 19) / 128 : C_MOD;
    poll_ge(ws, xcd * 16 + c, 16);
  }
  BAR();
  TS(5);
  unsigned kvo = OFF_KV + head * 163840, qo = OFF_Q + head * 81920 + (qb * 8 + qg * 2) * 1280;
  const unsigned kconst = h == 0 ? 0x3f803f80u : 0x00003f80u;
  const unsigned so16 = (tid & 255) * 16, l16 = lane * 16, l2 = 1024 + r * 8 + h * 4;
  const int blk = blockIdx.x;
  u32x4 wq[13];
  for (int attempt = 0; attempt < 2; ++attempt) {
    asm volatile("" : "+s"(kvo), "+s"(qo));
    const char* kvs = ws + kvo + kh * 81920 + so16;
    const unsigned ldst = lds_addr(smem) + kh * 81920 + qg * 1024;
    u32x4 qA1w, qB1w, k1w; unsigned qA2w, qB2w, k2, m0sv;
    asm volatile(
        "buffer_load_dwordx4 %0, %7, %9, %10 offen sc1\n\tbuffer_load_dwordx4 %1, %7, %9, %10 offen offset:1280 sc1\n\tbuffer_load_dwordx4 %2, %7, %9, %11 offen sc1\n\t"
        "buffer_load_dword %3, %8, %9, %10 offen sc1\n\tbuffer_load_dword %4, %8, %9, %10 offen offset:1280 sc1\n\tbuffer_load_dword %5, %8, %9, %11 offen sc1\n\t"
        "s_mov_b32 %6, m0\n\ts_mov_b32 m0, %17\n\ts_nop 0\n\tglobal_load_lds_dwordx4 %12, off sc1\n\t"
        "s_add_u32 m0, %18, 0x1000\n\ts_nop 0\n\tglobal_load_lds_dwordx4 %13, off sc1\n\t"
        "s_add_u32 m0, %18, 0x2000\n\ts_nop 0\n\tglobal_load_lds_dwordx4 %14, off sc1\n\t"
        "s_add_u32 m0, %18, 0x3000\n\ts_nop 0\n\tglobal_load_lds_dwordx4 %15, off sc1\n\t"
        "s_add_u32 m0, %18, 0x4000\n\ts_nop 0\n\tglobal_load_lds_dwordx4 %16, off sc1\n\t"
        "s_mov_b32 m0, %6\n\ts_waitcnt vmcnt(5)"
        : "=&v"(qA1w), "=&v"(qB1w), "=&v"(k1w), "=&v"(qA2w), "=&v"(qB2w), "=&v"(k2), "=&s"(m0sv)
        : "v"(l16), "v"(l2), "s"(rs), "s"(qo), "s"(kvo), "v"(kvs + (wid == 0 ? 4096 : 0)), "v"(kvs + 4096), "v"(kvs + 8192), "v"(kvs + 12288), "v"(kvs + 16384),
          "s"(ldst + (wid == 0 ? 4096 : 0)), "s"(ldst)
        : "memory", "scc");
    if (wid == 0) { const unsigned f = (tid & 1) ? 0u : 0x3f803f80u; *(u32x4*)(smem + l16) = (u32x4){f, f, f, f}; }
    const bf16x8 qA1 = BF8(qA1w), qB1 = BF8(qB1w);
    bf16x8 k1 = BF8(k1w);
    TS(30);
    float mA = -INFINITY, mB = -INFINITY;
    {
      const bf16x4 qA2 = BF4(((u32x2){qA2w, 0u})), qB2 = BF4(((u32x2){qB2w, 0u}));
      const int nt = attempt ? 64 : 1;
      bf16x8 t1 = k1; unsigned t2 = k2;
      for (int t = 0; t < nt; ++t) {
        if (t) { const unsigned to = kvo + (t >> 3) * 20480 + (t & 7) * 1280; t1 = BF8(ldc16(rs, l16, to)); t2 = ldc4(rs, l2, to); }
        const bf16x4 t2v = BF4(((u32x2){t2, kconst}));
        f32x16 a = MFMA32(t1, qA1, (f32x16){}); a = MFMA32K8(t2v, qA2, a);
        f32x16 b = MFMA32(t1, qB1, (f32x16){}); b = MFMA32K8(t2v, qB2, b);
#pragma unroll
        for (int i = 0; i < 16; ++i) { mA = fmaxf(mA, a[i]); mB = fmaxf(mB, b[i]); }
      }
      mA = fmaxf(mA, xhalf(mA)); mB = fmaxf(mB, xhalf(mB));
    }
    unsigned a0, a1, b0, b1; split3(mA, a0, a1); split3(mB, b0, b1);
    const bf16x4 qA2 = BF4(((u32x2){qA2w, h ? a1 : a0})), qB2 = BF4(((u32x2){qB2w, h ? b1 : b0}));
    char* lh = smem + kh * 81920;
    TS(31);
    waitv<0>();
    BAR();
    TS(6);
    f32x16 oA = (f32x16){}, oB = (f32x16){};
    const char* kp1 = lh + l16;
    const char* kp2 = lh + l2;
    const bool vreal = r < 20;
    const char* vp = vreal ? lh + 10240 + (h * 20 + r) * 16 : smem + (r == 20 ? 0 : 16);
    const int vinc = vreal ? 1280 : 0, vjump = vreal ? 10240 : 0;
    if (kh) { k1 = *(const bf16x8*)kp1; k2 = *(const unsigned*)kp2; }
    bf16x8 kf1[2], vf[2][2]; u32x2 kf2[2];
    kf1[0] = k1; kf2[0] = (u32x2){k2, kconst}; kf2[1] = (u32x2){0u, kconst};
    vf[1][0] = (bf16x8){}; vf[1][1] = (bf16x8){};
    u32x4 pA0, pA1, pB0 = (u32x4){}, pB1 = (u32x4){};
    f32x4 e0 = (f32x4){}, e1;
    f32x16 sA = MFMA32(kf1[0], qA1, (f32x16){}), sB;
    sA = MFMA32K8(BF4(kf2[0]), qA2, sA);
#define EX2(x) __builtin_amdgcn_exp2f(x)
#define GRP(MF, S, i, EN, EP, P, w) do { MF; SCHED(); EN[0] = EX2(S[i]); P[w] = pack2(EP[0], EP[1]); EN[1] = EX2(S[i + 1]); P[w + 1] = pack2(EP[2], EP[3]); \
      EN[2] = EX2(S[i + 2]); EN[3] = EX2(S[i + 3]); PIN(P); PIN(EN); SCHED(); } while (0)
    const unsigned relsc = lds_addr(smem) + 768;
    auto group = [&](auto FIRST, int jump, int j) __attribute__((always_inline)) {
#pragma unroll
      for (int t = 0; t < 8; ++t) {
        const int c = t & 1, n = c ^ 1;
        if (FIRST) { if (t == 0) __builtin_amdgcn_s_setprio(3); if (t == 3) __builtin_amdgcn_s_setprio(2); if (t == 5) __builtin_amdgcn_s_setprio(1); if (t == 6) __builtin_amdgcn_s_setprio(0); if (t == 7) __builtin_amdgcn_s_setprio(3); }
        else {
          if (t == 4 && j == 2) __builtin_amdgcn_s_setprio(2);
          if (t == 3 && j == 3) {
            __builtin_amdgcn_s_setprio(1);
#pragma unroll
            for (int i = 0; i < 13; ++i) wq[i] = proj_ld<false>(rs, i, wid, lane);
          }
          if (t == 6 && j == 3) { __builtin_amdgcn_s_setprio(0); if (wid == 7) glds4c(ws + OFF_CNT + (C_REL + (lane & 7)) * 256, relsc); }
        }
        if (FIRST && t < 5) {
#pragma unroll
          for (int i = 5 + 3 * t; i < 8 + 3 * t; ++i) glds16c(kvs + i * 4096, ldst + i * 4096);
        }
        if (t == 7) {
          if (FIRST) { waitv<0>(); BAR(); }
          kp1 += jump; kp2 += jump;
        }
        SCHED();
        kf1[n] = *(const bf16x8*)(kp1 + ((t + 1) & 7) * 1280); kf2[n][0] = *(const unsigned*)(kp2 + ((t + 1) & 7) * 1280);
        GRP(sB = MFMA32(kf1[c], qB1, (f32x16){}), sA, 0, e1, e0, pB1, 2);
        vf[c][0] = *(const bf16x8*)vp; vf[c][1] = *(const bf16x8*)(vp + 640); vp += vinc;
        GRP(sB = MFMA32K8(BF4(kf2[c]), qB2, sB), sA, 4, e0, e1, pA0, 0);
        GRP(oB = MFMA32(vf[n][0], BF8(pB0), oB), sA, 8, e1, e0, pA0, 2);
        GRP(oB = MFMA32(vf[n][1], BF8(pB1), oB), sA, 12, e0, e1, pA1, 0);
        GRP(sA = MFMA32(kf1[n], qA1, (f32x16){}), sB, 0, e1, e0, pA1, 2);
        GRP(sA = MFMA32K8(BF4(kf2[n]), qA2, sA), sB, 4, e0, e1, pB0, 0);
        GRP(oA = MFMA32(vf[c][0], BF8(pA0), oA), sB, 8, e1, e0, pB0, 2);
        GRP(oA = MFMA32(vf[c][1], BF8(pA1), oA), sB, 12, e0, e1, pB1, 0);
      }
      vp += vjump;
    };
    group(std::true_type{}, 20480, 0);
    TS(7);
    const int j2 = (blk - 135) * 4 + qg;
    const bool side = kh == 0 && attempt == 0 && j2 < 270;
    char* stile = smem + 1024 + qg * 4096;
    const char* svec = smem + 81920 + qg * 256;
    const int kt = blk < 135 ? 4 * (blk % 5) + qg : j2 / 10, ng = blk < 135 ? blk / 5 : j2 % 10;
    if (side) {
      const u16* W = blk < 135 ? win : wout; const int N = blk < 135 ? F_ : D_;
      const int j8 = lane >> 3;
#pragma unroll
      for (int i = 0; i < 4; ++i) {
        const int row = kt * 32 + i * 8 + j8;
        glds16(W + (size_t)row * N + ng * 64 + ((lane & 7) ^ j8) * 8, lds_addr(stile) + i * 1024);
      }
      glds4c((const float*)(ws + OFF_MOD) + (4 - h) * 640 + (kt < 20 ? kt : 0) * 32 + r, lds_addr(svec));
    }
#pragma nounroll
    for (int j = 1; j < 4; ++j) {
      if (j == 2) {
        if (kh) l2touch(wsp + ((size_t)(blk * 4 + qg) * 64 + lane) * 128, lds_addr(smem) + 256);
        else if (side) {
          waitv<0>();
          const int g4 = lane >> 4, r4 = lane & 15, row = 8 * g4 + (r4 >> 2);
          const f32x4 sv0 = *(const f32x4*)(svec + (r4 & 1) * 128 + g4 * 32), sv1 = *(const f32x4*)(svec + (r4 & 1) * 128 + g4 * 32 + 16);
          const bf16x8 vec = BF8(((u32x4){pack2(sv0[0], sv0[1]), pack2(sv0[2], sv0[3]), pack2(sv1[0], sv1[1]), pack2(sv1[2], sv1[3])}));
          const unsigned nks = blk < 135 ? 20 : 27, dso = (blk < 135 ? OFF_WIN : OFF_WOUT) + (ng * 4 * nks + kt) * 1024;
          bf16x8 fr[4]; f32x4 pr[4];
#pragma unroll
          for (int f = 0; f < 4; ++f) {
            const int ch = 2 * f + ((r4 & 3) >> 1);
            const bf16x4 lo = lds_tr(stile + row * 128 + (ch ^ (row & 7)) * 16 + (r4 & 1) * 8), hi = lds_tr(stile + (row + 4) * 128 + (ch ^ ((row + 4) & 7)) * 16 + (r4 & 1) * 8);
            fr[f] = __builtin_shufflevector(lo, hi, 0, 1, 2, 3, 4, 5, 6, 7);
          }
#pragma unroll
          for (int f = 0; f < 4; ++f) { stc16(rs, l16, dso + f * nks * 1024, __builtin_bit_cast(u32x4, fr[f])); pr[f] = MFMA16(fr[f], vec, (f32x4){}); }
          if (r4 < 2) {
#pragma unroll
            for (int f = 0; f < 4; ++f) *(f32x4*)(stile + 3072 + r4 * 256 + f * 64 + g4 * 16) = pr[f];
          }
        }
      }
      if (j == 3 && kh == 0 && attempt == 0) { waitv<0>(); arrive(ws, C_REL + xcd, lane); }
      group(std::false_type{}, j < 3 ? 20480 : 0, j);
    }
    TS(8);
    pB1[2] = pack2(e0[0], e0[1]); pB1[3] = pack2(e0[2], e0[3]);
    oB = MFMA32(vf[1][0], BF8(pB0), oB); oB = MFMA32(vf[1][1], BF8(pB1), oB);
    {
      f32x4* ex = (f32x4*)(lh + 1024 + qg * 4096) + lane;
      const f32x16 give = kh ? oA : oB;
#pragma unroll
      for (int g = 0; g < 3; ++g) ex[g * 64] = (f32x4){give[4 * g], give[4 * g + 1], give[4 * g + 2], give[4 * g + 3]};
    }
    int* flag = (int*)(smem + 528);
    BAR();
    TS(9);
    float o[12];
    {
      const f32x4* ex = (const f32x4*)(smem + (kh ^ 1) * 81920 + 1024 + qg * 4096) + lane;
      const f32x16 keep = kh ? oB : oA;
#pragma unroll
      for (int g = 0; g < 3; ++g) { const f32x4 t = ex[g * 64];
#pragma unroll
        for (int e = 0; e < 4; ++e) o[4 * g + e] = keep[4 * g + e] + t[e]; }
    }
    const float lo = xhalf(o[8]); const float l = h ? o[8] : lo;
    const float rl = __builtin_amdgcn_rcpf(l);
    const int tok = qb * 256 + qg * 64 + kh * 32 + r;
    const unsigned vo = (tok >> 4) * 20480 + (tok & 15) * 16;
    stc16(rs, vo + h * 256, OFF_O + (head >> 1) * 1024 + (head & 1) * 512,
          (u32x4){pack2(o[0] * rl, o[1] * rl), pack2(o[2] * rl, o[3] * rl), pack2(o[4] * rl, o[5] * rl), pack2(o[6] * rl, o[7] * rl)});
    if (h == 0) stc8(rs, vo, OFF_O + 16384 + (head >> 3) * 1024 + ((head >> 1) & 3) * 256 + (head & 1) * 8, (u32x2){pack2(o[8] * rl, o[9] * rl), pack2(o[10] * rl, o[11] * rl)});
    if (wid == 4 && blk < 135 && attempt == 0 && lane < 32) {
      const char* pp = smem + 1024 + 3072 + lane * 16;
      const f32x4 v = (*(const f32x4*)pp + *(const f32x4*)(pp + 4096)) + (*(const f32x4*)(pp + 8192) + *(const f32x4*)(pp + 12288));
      stc16(rs, (lane >> 4) * 8192 + (lane & 15) * 16, OFF_CBP + (blk % 5) * 16384 + (blk / 5) * 256, __builtin_bit_cast(u32x4, v));
    }
    bool ok = (l > 1e-30f) && (l < 1e30f);
#pragma unroll
    for (int i = 0; i < 12; ++i) ok = ok && (fabsf(o[i]) < 3e38f);
    if (!ok && attempt == 0) *flag = 1;
    waitv<0>();
    BAR();
    const int redo = __builtin_amdgcn_readfirstlane(*(volatile LAS int*)flag);
    if (!redo) break;
    BAR();
  }
  TS(10);
  if (wid == 7) arrive(ws, C_ATT + qb, lane);
#pragma unroll
  for (int i = 0; i < 13; ++i) proj_park<false>(smem, i, wid, lane, wq[i]);
  if (!__all(*(volatile LAS unsigned*)(smem + 768 + (lane & 7) * 4) >= 128u)) poll_ge(ws, C_REL + (lane & 7), 128);
}

template <bool FINAL, int KH, class HOOK>
DI void proj_body(char* __restrict__ ws, rsrc_t rs, const u16* __restrict__ x, u16* __restrict__ out, char* smem, int wid, u32x2 (&res)[3], HOOK&& after_mma TARG) {
  const int lane = fresh_tid() & 63;
  const int xcd = blockIdx.x & 7, idx = blockIdx.x >> 3, panel = xcd * 4 + (idx >> 3), cb = idx & 7;
  constexpr int NKS = FINAL ? 27 : 20;
  constexpr int K0 = KH ? (NKS + 1) / 2 : 0, NK = KH ? NKS / 2 : (NKS + 1) / 2, NKEEP = KH ? 2 : 3, UK = KH ? 3 : 0, UG = KH ? 0 : 3;
  const int m = wid & 3, mt = panel * 4 + m, g = lane >> 4, r = lane & 15;
  f32x4 gate[NKEEP], cbv[NKEEP];
#pragma unroll
  for (int i = 0; i < NKEEP; ++i) {
    const int n = (cb * 5 + UK + i) * 16;
    if (!FINAL) { res[i] = *(const u32x2*)(x + (size_t)(mt * 16 + r) * D_ + n + g * 4); cbv[i] = ldc16f(rs, g * 16, OFF_MOD + (4 * 640 + n) * 4); }
    gate[i] = ldc16f(rs, g * 16, OFF_MOD + ((FINAL ? 5 : 2) * 640 + n) * 4);
  }
  if (wid == 7) poll_ge(ws, FINAL ? C_U + panel : C_ATT + (panel >> 2), FINAL ? 8 : 32);
  waitv<0>();
  BAR();
  TS(FINAL ? 27 : 11);
  bf16x8 a[NK];
#pragma unroll
  for (int k = 0; k < NK; ++k) a[k] = BF8(ldc16(rs, lane * 16, (FINAL ? OFF_U : OFF_O) + (mt * NKS + K0 + k) * 1024));
  SCHED();
  constexpr int PD = FINAL ? 5 : 6;
  const char* bl = smem + lane * 16;
  bf16x8 bf[PD + 1][5]; f32x4 acc[5];
#pragma unroll
  for (int u = 0; u < 5; ++u) acc[u] = (f32x4){};
#pragma unroll
  for (int k = 0; k < PD; ++k)
#pragma unroll
    for (int u = 0; u < 5; ++u) bf[k][u] = *(const bf16x8*)(bl + proj_pos<FINAL>(u * NKS + K0 + k));
  SCHED();
#pragma unroll
  for (int k = 0; k < NK; ++k) {
#pragma unroll
    for (int u = 0; u < 5; ++u) {
      if (k + PD < NK) bf[(k + PD) % (PD + 1)][u] = *(const bf16x8*)(bl + proj_pos<FINAL>(u * NKS + K0 + k + PD));
      acc[u] = MFMA16(bf[k % (PD + 1)][u], a[k], acc[u]);
      SCHED();
    }
  }
  TS(FINAL ? 28 : 12);
  f32x4* ex = (f32x4*)(smem + (FINAL ? 40960 : 68608)) + m * 5 * 64 + lane;
#pragma unroll
  for (int i = 0; i < 5 - NKEEP; ++i) ex[(UG + i) * 64] = acc[UG + i];
  f32x4 part[NKEEP];
#pragma unroll
  for (int i = 0; i < NKEEP; ++i) {
    part[i] = fma4s(gate[i], acc[UK + i], (f32x4){bflo(res[i][0]), bfhi(res[i][0]), bflo(res[i][1]), bfhi(res[i][1])});
    PIN(part[i]);
  }
  SCHED();
  after_mma();
  BAR();
  f32x4 oth[NKEEP];
#pragma unroll
  for (int i = 0; i < NKEEP; ++i) oth[i] = ex[(UK + i) * 64];
  f32x4 sv = (f32x4){}, qv = (f32x4){};
#pragma unroll
  for (int i = 0; i < NKEEP; ++i) {
    const f32x4 v = part[i] + gate[i] * oth[i];
    const u32x2 o = (u32x2){pack2(v[0], v[1]), pack2(v[2], v[3])};
    if (FINAL) *(u32x2*)(out + (size_t)(mt * 16 + r) * D_ + (cb * 5 + UK + i) * 16 + g * 4) = o;
    else {
      res[i] = o; PIN(res[i]);
      const f32x4 w = v * cbv[i]; const int un = cb * 5 + UK + i;
      stc8(rs, ((g >> 1) * 16 + r) * 16 + (g & 1) * 8, OFF_X1S + mt * 20480 + un * 512, (u32x2){pack2(w[0], w[1]), pack2(w[2], w[3])});
      sv += v; qv += v * v;
    }
  }
  if (!FINAL) {
    const float s = sum_g((sv[0] + sv[1]) + (sv[2] + sv[3])), q = sum_g((qv[0] + qv[1]) + (qv[2] + qv[3]));
    if (g == 0) stc8(rs, r * 8, OFF_ST1 + ((panel * 16 + cb * 2 + KH) * 64 + m * 16) * 8, (u32x2){__float_as_uint(s), __float_as_uint(q)});
    TS(13);
    waitv<0>();
    BAR();
    TS(14);
    if (wid == 7) arrive(ws, C_X1 + panel, lane);
  }
}

constexpr float GC1 = -2.8853900817779268f * 0.7978845608028654f, GC2 = GC1 * 0.044715f;
DI float gelu_s(float a, float k1, float k3, float den) {
  const float e = __builtin_amdgcn_exp2f(a * __builtin_fmaf(a * a, k3, k1));
  return a * __builtin_amdgcn_rcpf(__builtin_fmaf(e, den, den));
}
DI void gelu8(f32x4& a0, const f32x4& l0, f32x4& a1, const f32x4& l1) {
  float t[8];
#define G8(expr0, expr1) do { _Pragma("unroll") for (int i = 0; i < 4; ++i) { t[i] = opq(expr0); } _Pragma("unroll") for (int i = 0; i < 4; ++i) { t[4 + i] = opq(expr1); } SCHED(); } while (0)
  G8(a0[i] * a0[i], a1[i] * a1[i]);
  G8(__builtin_fmaf(t[i], l0[3], l0[2]), __builtin_fmaf(t[4 + i], l1[3], l1[2]));
  G8(a0[i] * t[i], a1[i] * t[4 + i]);
  G8(__builtin_amdgcn_exp2f(t[i]), __builtin_amdgcn_exp2f(t[4 + i]));
  G8(__builtin_fmaf(t[i], l0[0], l0[0]), __builtin_fmaf(t[4 + i], l1[0], l1[0]));
  G8(__builtin_amdgcn_rcpf(t[i]), __builtin_amdgcn_rcpf(t[4 + i]));
  G8(a0[i] * t[i], a1[i] * t[4 + i]);
  a0 = (f32x4){t[0], t[1], t[2], t[3]}; a1 = (f32x4){t[4], t[5], t[6], t[7]};
}
constexpr int L4_Z1 = 114688, L4_TR = L4_Z1 + 16384, L4_LN = L4_TR + 16384, L4_ST2 = L4_LN + 1024, L4_FIN = L4_ST2 + 3584, L4_CB = L4_FIN + 512, L4_CNT = L4_CB + 2048;
constexpr int K5_R = 7;
constexpr unsigned DEAD = 0x80000000u;
DI int sp_stage(int s) { const int idx = blockIdx.x >> 3; return (s + (idx & 7) * 2) & 15; }
DI unsigned sp_voff(int wid, int lane, int i) { return ((wid & 3) * 16 + i * 8 + (lane >> 3)) * 4096 + (wid >> 2) * 128 + ((lane & 7) ^ (4 * i + (lane >> 4))) * 16; }
DI const char* sp_base(const u16* wsp, int panel, int s) { return (const char*)wsp + (size_t)panel * 262144 + sp_stage(s) * 256; }
DI void sp_prefetch(u32x4 (&wr)[16 - K5_R][2], const u16* __restrict__ wsp, int panel, int wid, int lane) {
  const unsigned v0 = sp_voff(wid, lane, 0), v1 = sp_voff(wid, lane, 1);
#pragma unroll
  for (int s = K5_R; s < 16; ++s) { wr[s - K5_R][0] = *(const u32x4*)(sp_base(wsp, panel, s) + v0); wr[s - K5_R][1] = *(const u32x4*)(sp_base(wsp, panel, s) + v1); }
  asm volatile("" ::: "memory");
}
template <int S0, int S1>
DI void sp_ring(const u16* __restrict__ wsp, char* smem, int panel, int wid, int lane) {
  const unsigned v0 = sp_voff(wid, lane, 0), v1 = sp_voff(wid, lane, 1);
#pragma unroll
  for (int s = S0; s < S1; ++s) { glds16(sp_base(wsp, panel, s) + v0, lds_addr(smem) + wid * 2048 + s * 16384); glds16(sp_base(wsp, panel, s) + v1, lds_addr(smem) + wid * 2048 + s * 16384 + 1024); }
}
DI unsigned win_boff(int wid, int lane) { const int cb = (blockIdx.x >> 3) & 7; return lane * 16 + (wid == 6 && cb >= 6 ? DEAD : 0u); }
DI unsigned win_soff(int wid, bool z2) { return OFF_WIN + ((z2 ? 54 : 0) + unit0_of((blockIdx.x >> 3) & 7) + wid) * 20480; }
#define WIN_LD(so, k) BF8(ldc16(rs, bo + ((k) & 3) * 1024, (so) + ((k) >> 2) * 4096))
DI void win_prefetch(rsrc_t rs, int wid, bf16x8 (&b2)[20]) {
  const unsigned bo = win_boff(wid, fresh_tid() & 63), so = win_soff(wid, true);
#pragma unroll
  for (int k = 0; k < 20; ++k) b2[k] = WIN_LD(so, k);
}
template <class HOOK>
DI void win_mma(const char* al, const bf16x8 (&b)[20], f32x4 (&acc)[4], HOOK&& hook) {
  constexpr int PD = 4;
  bf16x8 af[PD + 1][2];
#define WIN_RD(i) do { af[(i) % (PD + 1)][0] = *(const bf16x8*)(al + ((i) & 3) * 20480 + ((i) >> 2) * 2048); af[(i) % (PD + 1)][1] = *(const bf16x8*)(al + ((i) & 3) * 20480 + ((i) >> 2) * 2048 + 1024); } while (0)
#pragma unroll
  for (int i = 0; i < PD; ++i) WIN_RD(i);
  SCHED();
  sfor<40>([&](auto I) __attribute__((always_inline)) {
    constexpr int i = I, m = i & 3, k = (i >> 2) * 2;
    if constexpr (i + PD < 40) WIN_RD(i + PD);
    acc[m] = MFMA16(b[k], af[i % (PD + 1)][0], acc[m]);
    acc[m] = MFMA16(b[k + 1], af[i % (PD + 1)][1], acc[m]); PIN(acc[m]);
    hook(I);
    SCHED();
  });
}
DI void stage_x1s(char* __restrict__ ws, char* smem, int panel, int wid, int tid) {
  const char* src = ws + OFF_X1S + (size_t)panel * 81920 + tid * 16;
#pragma unroll
  for (int i = 0; i < 10; ++i) glds16c(src + i * 8192, lds_addr(smem) + wid * 1024 + i * 8192);
}
DI void win_tail(char* __restrict__ ws, const u16* __restrict__ wsp, char* smem, int panel, int cb, int wid, int lane TARG) {
  BAR();
  TS(22);
  if (wid == 7 && lane < 4) __hip_atomic_fetch_add((GAS unsigned*)(ws + OFF_CNT + (C_Z2 + lane * 8 + cb) * 256), 1u, __ATOMIC_RELAXED, __HIP_MEMORY_SCOPE_AGENT);
}

template <bool EARLY>
DI void win_body(char* __restrict__ ws, rsrc_t rs, const u16* __restrict__ wsp, char* smem, int wid, const bf16x8 (&b2)[20], u32x4 (&wr)[16 - K5_R][2] TARG) {
  const int tid = fresh_tid(), lane = tid & 63, g = lane >> 4, r = lane & 15;
  const int xcd = blockIdx.x & 7, idx = blockIdx.x >> 3, panel = xcd * 4 + (idx >> 3), cb = idx & 7, u0 = unit0_of(cb);
  float* lnp = (float*)(smem + L4_LN); float* st2 = (float*)(smem + L4_ST2); float* fin = (float*)(smem + L4_FIN); float* cbl = (float*)(smem + L4_CB);
  int* flg = (int*)(smem + L4_CNT);
  __builtin_amdgcn_s_setprio(1);
  const unsigned bo = win_boff(wid, lane), so1 = win_soff(wid, false);
  bf16x8 b1[20];
  BAR();
  TS(15);
  stage_x1s(ws, smem, panel, wid, tid);
#pragma unroll
  for (int k = 0; k < 4; ++k) b1[k] = WIN_LD(so1, k);
  waitv<4>();
  BAR();
  TS(16);
  f32x4 ln[4], z2[4], z1[4];
#pragma unroll
  for (int m = 0; m < 4; ++m) ln[m] = *(const f32x4*)(lnp + (m * 16 + r) * 4);
  {
    const f32x4 cs2 = *(const f32x4*)(cbl + 112 + wid * 16 + g * 4), bi2 = *(const f32x4*)(cbl + 256 + 112 + wid * 16 + g * 4);
    const f32x4 cs1 = *(const f32x4*)(cbl + wid * 16 + g * 4), bi1 = *(const f32x4*)(cbl + 256 + wid * 16 + g * 4);
#pragma unroll
    for (int m = 0; m < 4; ++m) {
#pragma unroll
      for (int e = 0; e < 4; ++e) { z2[m][e] = opq(__builtin_fmaf(ln[m][1], cs2[e], opq(ln[m][0] * bi2[e]))); z1[m][e] = opq(__builtin_fmaf(ln[m][1], cs1[e], opq(ln[m][0] * bi1[e]))); }
      PIN(z1[m]);
    }
  }
  win_mma(smem + lane * 16, b2, z2, [&](auto I) __attribute__((always_inline)) { constexpr int i = I; if constexpr (i < 32 && (i & 1)) b1[4 + i / 2] = WIN_LD(so1, 4 + i / 2); });
  TS(17);
  gelu8(z2[0], ln[0], z2[1], ln[1]); gelu8(z2[2], ln[2], z2[3], ln[3]);
  {
    float s[4], q[4];
#pragma unroll
    for (int m = 0; m < 4; ++m) { s[m] = opq(opq(z2[m][0] + z2[m][1]) + opq(z2[m][2] + z2[m][3])); q[m] = opq(__builtin_fmaf(z2[m][0], z2[m][0], opq(z2[m][1] * z2[m][1])) + opq(__builtin_fmaf(z2[m][2], z2[m][2], opq(z2[m][3] * z2[m][3])))); }
    auto pa = __builtin_amdgcn_permlane32_swap(__float_as_uint(s[0]), __float_as_uint(s[2]), false, false);
    auto pb = __builtin_amdgcn_permlane32_swap(__float_as_uint(s[1]), __float_as_uint(s[3]), false, false);
    auto pc = __builtin_amdgcn_permlane32_swap(__float_as_uint(q[0]), __float_as_uint(q[2]), false, false);
    auto pd = __builtin_amdgcn_permlane32_swap(__float_as_uint(q[1]), __float_as_uint(q[3]), false, false);
    const float sa = opq(__uint_as_float(pa[0]) + __uint_as_float(pa[1])), sb = opq(__uint_as_float(pb[0]) + __uint_as_float(pb[1]));
    const float qa = opq(__uint_as_float(pc[0]) + __uint_as_float(pc[1])), qb = opq(__uint_as_float(pd[0]) + __uint_as_float(pd[1]));
    auto ps = __builtin_amdgcn_permlane16_swap(__float_as_uint(sa), __float_as_uint(sb), false, false);
    auto pq = __builtin_amdgcn_permlane16_swap(__float_as_uint(qa), __float_as_uint(qb), false, false);
    *(f32x2*)(st2 + (wid * 64 + lane) * 2) = (f32x2){opq(__uint_as_float(ps[0]) + __uint_as_float(ps[1])), opq(__uint_as_float(pq[0]) + __uint_as_float(pq[1]))};
  }
  asm volatile("" ::: "memory");
  if (lane == 0) __hip_atomic_fetch_add(flg, 1, __ATOMIC_RELAXED, __HIP_MEMORY_SCOPE_WORKGROUP);
  asm volatile("" ::: "memory");
  __builtin_amdgcn_s_setprio(0);
  TS(18);
  const unsigned wvo[2] = {sp_voff(wid, lane, 0), sp_voff(wid, lane, 1)};
  win_mma(smem + lane * 16, b1, z1, [&](auto I) __attribute__((always_inline)) {
    constexpr int i = I, j = i < 8 ? i : (i & 1) ? -1 : 4 + i / 2;
    if constexpr (EARLY && j >= 0 && j < 18) wr[j >> 1][j & 1] = *(const u32x4*)(sp_base(wsp, panel, K5_R + (j >> 1)) + wvo[j & 1]);
  });
  gelu8(z1[0], ln[0], z1[1], ln[1]); gelu8(z1[2], ln[2], z1[3], ln[3]);
#pragma unroll
  for (int mm = 0; mm < 2; ++mm)
    *(u32x4*)(smem + L4_Z1 + wid * 2048 + mm * 1024 + lane * 16) = (u32x4){pack2(z1[2 * mm][0], z1[2 * mm][1]), pack2(z1[2 * mm][2], z1[2 * mm][3]), pack2(z1[2 * mm + 1][0], z1[2 * mm + 1][1]), pack2(z1[2 * mm + 1][2], z1[2 * mm + 1][3])};
  TS(20);
  BAR();
  TS(21);
  char* tr = smem + L4_TR + wid * 2048;
#pragma unroll
  for (int m = 0; m < 4; ++m) {
    const f32x2 f = *(const f32x2*)(fin + (m * 16 + r) * 2);
    const f32x4 v = z2[m] * f[0] - f[1];
    *(u32x2*)(tr + (m * 16 + r) * 32 + g * 8) = (u32x2){pack2(v[0], v[1]), pack2(v[2], v[3])};
  }
  {
    const char* p = tr + (8 * g + (r >> 2)) * 32 + (r & 3) * 8;
    const bf16x4 lo0 = lds_tr(p), hi0 = lds_tr(p + 128), lo1 = lds_tr(p + 1024), hi1 = lds_tr(p + 1152);
    stc16(rs, bo, OFF_Z2 + (panel * 2 * 54 + u0 + wid) * 1024, __builtin_bit_cast(u32x4, (bf16x8)__builtin_shufflevector(lo0, hi0, 0, 1, 2, 3, 4, 5, 6, 7)));
    stc16(rs, bo, OFF_Z2 + ((panel * 2 + 1) * 54 + u0 + wid) * 1024, __builtin_bit_cast(u32x4, (bf16x8)__builtin_shufflevector(lo1, hi1, 0, 1, 2, 3, 4, 5, 6, 7)));
  }
  SCHED();
  waitv<0>();
  win_tail(ws, wsp, smem, panel, cb, wid, lane TPASS);
  sp_ring<0, 5>(wsp, smem, panel, wid, lane);
  if (!EARLY) sp_prefetch(wr, wsp, panel, wid, lane);
}

DI void win_agent(char* __restrict__ ws, rsrc_t rs, const u16* __restrict__ wsp, char* smem, u32x4 (&wr)[16 - K5_R][2] TARG) {
  const int tid = fresh_tid(), lane = tid & 63, wid = 7;
  const int xcd = blockIdx.x & 7, idx = blockIdx.x >> 3, panel = xcd * 4 + (idx >> 3), cb = idx & 7, u0 = unit0_of(cb);
  float* lnp = (float*)(smem + L4_LN); float* st2 = (float*)(smem + L4_ST2); float* fin = (float*)(smem + L4_FIN); float* cbl = (float*)(smem + L4_CB);
  int* cnt = (int*)(smem + L4_CNT);
  __builtin_amdgcn_s_setprio(2);
  if (lane == 0) *cnt = 0;
  poll_ge(ws, lane < 8 ? C_ATT + lane : C_X1 + panel, lane < 8 ? 32 : 8);
  BAR();
  stage_x1s(ws, smem, panel, wid, tid);
  {
    u32x2 t[16]; f32x4 cp[5][2];
#pragma unroll
    for (int i = 0; i < 16; ++i) t[i] = __builtin_amdgcn_raw_buffer_load_b64(rs, lane * 8, OFF_ST1 + (panel * 16 + i) * 512, 16);
    const unsigned co = lane < 28 ? (u0 * 16 + lane * 4) * 4 : ((54 + u0) * 16 + min(lane - 28, 27) * 4) * 4;
#pragma unroll
    for (int i = 0; i < 5; ++i) { cp[i][0] = ldc16f(rs, co, OFF_CBP + i * 16384); cp[i][1] = ldc16f(rs, co, OFF_CBP + i * 16384 + 8192); }
    float s = 0.f, q = 0.f;
#pragma unroll
    for (int i = 0; i < 16; ++i) { s += __uint_as_float(t[i][0]); q += __uint_as_float(t[i][1]); }
    const float mean = s * (1.f / 640.f), var = fmaxf(q * (1.f / 640.f) - mean * mean, 0.f);
    const float den = ln_den(var), ri = 1.f / den;
    *(f32x4*)(lnp + lane * 4) = (f32x4){den, -rbf(mean), GC1 * ri, GC2 * ri * ri * ri};
#pragma unroll
    for (int v = 0; v < 2; ++v) *(f32x4*)(cbl + v * 256 + lane * 4) = ((cp[0][v] + cp[1][v]) + (cp[2][v] + cp[3][v])) + cp[4][v];
  }
  waitv<0>();
  BAR();
  __builtin_amdgcn_s_setprio(0);
  __builtin_amdgcn_s_sleep(8);
  sp_prefetch(wr, wsp, panel, wid, lane);
#pragma nounroll
  for (int w = 0; w < 8; ++w) sp_ring<5, 7>(wsp, smem, panel, w, lane);
  __builtin_amdgcn_s_setprio(2);
#pragma nounroll
  for (unsigned spin = 0; spin < (1u << 24); ++spin) {
    if (__hip_atomic_load(cnt, __ATOMIC_RELAXED, __HIP_MEMORY_SCOPE_WORKGROUP) >= 7) break;
    __builtin_amdgcn_s_sleep(1);
  }
  asm volatile("" ::: "memory");
  float s, q;
  {
    f32x2 t[7];
#pragma unroll
    for (int i = 0; i < 7; ++i) t[i] = *(const f32x2*)(st2 + (i * 64 + lane) * 2);
    s = t[0][0]; q = t[0][1];
#pragma unroll
    for (int i = 1; i < 6; ++i) { s = opq(s + t[i][0]); q = opq(q + t[i][1]); }
    if (cb < 6) { s = opq(s + t[6][0]); q = opq(q + t[6][1]); }
  }
  GAS u64* xch = (GAS u64*)(ws + OFF_XCH) + panel * 512;
  unsigned qb = __float_as_uint(q); if (qb == ~0u) qb = 0x7fc00000u;
  __hip_atomic_store(xch + cb * 64 + lane, ((u64)qb << 32) | __float_as_uint(s), __ATOMIC_RELAXED, __HIP_MEMORY_SCOPE_AGENT);
  TS(19);
  u64 v[8];
#pragma nounroll
  for (unsigned spin = 0; spin < (1u << 21); ++spin) {
    bool ok = true;
#pragma unroll
    for (int i = 0; i < 8; ++i) { v[i] = __hip_atomic_load(xch + i * 64 + lane, __ATOMIC_RELAXED, __HIP_MEMORY_SCOPE_AGENT); ok = ok && ((unsigned)(v[i] >> 32) != ~0u); }
    if (__all(ok)) break;
    __builtin_amdgcn_s_sleep(1);
  }
  s = 0.f; q = 0.f;
#pragma unroll
  for (int i = 0; i < 8; ++i) { s = opq(s + __uint_as_float((unsigned)v[i])); q = opq(q + __uint_as_float((unsigned)(v[i] >> 32))); }
  const float mean = opq(s * (1.f / 864.f)), var = fmaxf(opq(q * (1.f / 864.f)) - mean * mean, 0.f);
  const float ri = 1.f / ln_den(var);
  *(f32x2*)(fin + lane * 2) = (f32x2){ri, rbf(mean) * ri};
  __builtin_amdgcn_s_setprio(0);
  TS(20);
  BAR();
  __builtin_amdgcn_s_sleep(6);
  sp_ring<0, 5>(wsp, smem, panel, wid, lane);
  win_tail(ws, wsp, smem, panel, cb, wid, lane TPASS);
}

#define SP_LB 4
DI void phase_sp(char* __restrict__ ws, rsrc_t rs, const u16* __restrict__ bsp, char* smem, int wid, const u32x4 (&wr)[16 - K5_R][2] TARG) {
  const int tid = fresh_tid(), lane = tid & 63;
  const int xcd = blockIdx.x & 7, idx = blockIdx.x >> 3, panel = xcd * 4 + (idx >> 3), cb = idx & 7;
  const int kh = wid >> 2, up = wid & 3, u0 = unit0_of(cb) + 2 * up, nu = cb < 6 ? 7 : 6, g = lane >> 4, r = lane & 15;
  const unsigned dead[2] = {2 * up < nu ? 0u : DEAD, 2 * up + 1 < nu ? 0u : DEAD};
  const unsigned bvo[2] = {lane * 16 + dead[0], lane * 16 + 1024 + dead[1]};
  u16 bb[2];
#pragma unroll
  for (int j = 0; j < 2; ++j) bb[j] = bsp[panel * 64 + (2 * kh + j) * 16 + r];
  const char* al[2][2];
#pragma unroll
  for (int k = 0; k < 2; ++k)
#pragma unroll
    for (int j = 0; j < 2; ++j) al[k][j] = smem + kh * 8192 + (j ^ kh) * 4096 + r * 128 + ((4 * k + g) ^ (r >> 1)) * 16;
  constexpr int LB = SP_LB, PD = 3;
  bf16x8 bq[LB + 1][4], af[PD + 1][2]; f32x4 acc[2][4]; u32x4 wq[17];
  if (wid == 7) poll_ge(ws, C_Z2 + (panel >> 3) * 8 + cb, 32);
#pragma unroll
  for (int i = 0; i < 2; ++i) {
    const float b0 = bf2f(bb[0]), b1 = bf2f(bb[1]);
    acc[i][0] = (f32x4){b0, b0, b0, b0}; acc[i][1] = (f32x4){b1, b1, b1, b1}; acc[i][2] = (f32x4){}; acc[i][3] = (f32x4){};
  }
  if (kh && up < 3) waitv<2 * (16 - K5_R)>(); else waitv<0>();
  BAR();
  TS(23);
  const unsigned bso = OFF_Z2 + (kh * 2 * 54 + u0) * 1024;
#define SP_LD(s, j) (bq[(s) % (LB + 1)][j] = BF8(ldc16(rs, bvo[(j) & 1], bso + (sp_stage(s) * 4 + ((j) >> 1)) * 55296)))
#define SP_RD(t, k) (af[(t) % (PD + 1)][k] = *(const bf16x8*)(al[k][((t) >> 1) & 1] + (((t) >> 2) % K5_R) * 16384 + ((t) & 1) * 2048))
#pragma unroll
  for (int s = 0; s < LB; ++s)
#pragma unroll
    for (int j = 0; j < 4; ++j) SP_LD(s, j);
#pragma unroll
  for (int t = 0; t < PD; ++t) { SP_RD(t, 0); SP_RD(t, 1); }
  SCHED();
  sfor<64>([&](auto T) __attribute__((always_inline)) {
    constexpr int t = T, nx = t + PD, s = t >> 2, m = t & 3, tl = t + 4 * LB;
    if constexpr (nx == 16 || nx == 28 || nx == 44 || nx == 56) { BAR(); TS(32 + (nx == 28) + 2 * (nx == 44) + 3 * (nx == 56)); }
    constexpr int c = nx >= 16 && nx < 24 ? nx - 16 : nx >= 28 && nx < 34 ? nx - 20 : nx >= 44 && nx < 48 ? nx - 30 : -1;
    if constexpr (nx < 64) SP_RD(nx, 0);
    acc[0][m] = MFMA16(bq[s % (LB + 1)][0], af[t % (PD + 1)][0], acc[0][m]);
    acc[0][m] = MFMA16(bq[s % (LB + 1)][2], af[t % (PD + 1)][1], acc[0][m]); PIN(acc[0][m]);
    if constexpr (tl < 64) SP_LD(tl >> 2, m); else if constexpr (tl - 64 < 17) wq[tl - 64] = proj_ld<true>(rs, tl - 64, wid, lane);
    SCHED();
    if constexpr (nx < 64) SP_RD(nx, 1);
    acc[1][m] = MFMA16(bq[s % (LB + 1)][1], af[t % (PD + 1)][0], acc[1][m]);
    acc[1][m] = MFMA16(bq[s % (LB + 1)][3], af[t % (PD + 1)][1], acc[1][m]); PIN(acc[1][m]);
    if constexpr (c >= 0) *(u32x4*)(smem + ((c >> 1) % K5_R) * 16384 + wid * 2048 + (c & 1) * 1024 + lane * 16) = wr[c >> 1][c & 1];
    if constexpr (t == 63 && 4 * LB < 17) { _Pragma("unroll") for (int i = 4 * LB; i < 17; ++i) wq[i] = proj_ld<true>(rs, i, wid, lane); }
    SCHED();
  });
  TS(24);
  f32x4* exw = (f32x4*)(smem + 32768 + wid * 4096) + lane;
  const f32x4* exr = (const f32x4*)(smem + 32768 + (wid ^ 4) * 4096) + lane;
  u32x4 z[2];
#pragma unroll
  for (int i = 0; i < 2; ++i) z[i] = *(const u32x4*)(smem + L4_Z1 + (2 * up + i) * 2048 + kh * 1024 + lane * 16);
#pragma unroll
  for (int i = 0; i < 2; ++i)
#pragma unroll
    for (int j = 0; j < 2; ++j) exw[(i * 2 + j) * 64] = acc[i][2 + j];
  f32x4 zf[2][2];
#pragma unroll
  for (int i = 0; i < 2; ++i)
#pragma unroll
    for (int j = 0; j < 2; ++j) {
      zf[i][j] = (f32x4){bflo(z[i][2 * j]), bfhi(z[i][2 * j]), bflo(z[i][2 * j + 1]), bfhi(z[i][2 * j + 1])};
      acc[i][j] = mul4s(acc[i][j], zf[i][j]); PIN(zf[i][j]); PIN(acc[i][j]);
    }
  BAR();
  f32x4 oth[2][2];
#pragma unroll
  for (int i = 0; i < 2; ++i)
#pragma unroll
    for (int j = 0; j < 2; ++j) oth[i][j] = exr[(i * 2 + j) * 64];
#pragma unroll
  for (int i = 0; i < 2; ++i)
#pragma unroll
    for (int j = 0; j < 2; ++j) {
      const f32x4 v = acc[i][j] + zf[i][j] * oth[i][j];
      stc8(rs, ((g >> 1) * 16 + r) * 16 + (g & 1) * 8 + dead[i], OFF_U + (panel * 4 + 2 * kh + j) * 27648 + (u0 + i) * 512, (u32x2){pack2(v[0], v[1]), pack2(v[2], v[3])});
    }
  TS(25);
  waitv<0>();
  BAR();
  TS(26);
  if (wid == 7) arrive(ws, C_U + panel, lane);
#pragma unroll
  for (int i = 0; i < 17; ++i) proj_park<true>(smem, i, wid, lane, wq[i]);
}

__global__ __launch_bounds__(512, 2) OCC2 void k_mega(char* __restrict__ ws, const u16* __restrict__ x, const u16* __restrict__ win,
                                                     const u16* __restrict__ wsp, const u16* __restrict__ bsp, const u16* __restrict__ wout,
                                                     const u16* __restrict__ cvec, const u16* __restrict__ wmod, u16* __restrict__ out) {
  extern __shared__ __attribute__((aligned(16))) char smem[];
  const int wid = __builtin_amdgcn_readfirstlane(threadIdx.x >> 6);
  const rsrc_t rs = __builtin_amdgcn_make_buffer_rsrc(ws, 0, WS_USED, 0x00020000);
  TDECL; TS(0);
  u32x2 res[3];
  phase_qkv(ws, rs, cvec, wmod, smem, wid TPASS);
  phase_attn(ws, rs, (const char*)wsp, win, wout, smem, wid TPASS);
  u32x4 wr[16 - K5_R][2];
  auto nohook = []() __attribute__((always_inline)) {};
  if (wid < 4) {
    bf16x8 b2[20];
    proj_body<false, 0>(ws, rs, x, out, smem, wid, res, [&]() __attribute__((always_inline)) { win_prefetch(rs, wid, b2); } TPASS);
    win_body<true>(ws, rs, wsp, smem, wid, b2, wr TPASS);
  } else {
    proj_body<false, 1>(ws, rs, x, out, smem, wid, res, nohook TPASS);
    if (wid == 7) win_agent(ws, rs, wsp, smem, wr TPASS);
    else { bf16x8 b2[20]; win_prefetch(rs, wid, b2); win_body<false>(ws, rs, wsp, smem, wid, b2, wr TPASS); }
  }
  phase_sp(ws, rs, bsp, smem, wid, wr TPASS);
  if (wid < 4) proj_body<true, 0>(ws, rs, x, out, smem, wid, res, nohook TPASS);
  else proj_body<true, 1>(ws, rs, x, out, smem, wid, res, nohook TPASS);
  TS(29);
  TDUMP();
}

extern "C" void kernel_launch(void* const* d_in, const int* in_sizes, int n_in, void* d_out, int out_size, void* d_ws, size_t ws_size,
                              hipStream_t stream) {
  const u16* x = (const u16*)d_in[0];
  const u16* wqkv = (const u16*)d_in[1];
  const u16* wo = (const u16*)d_in[2];
  const u16* win = (const u16*)d_in[4];
  const u16* wsp = (const u16*)d_in[5];
  const u16* bsp = (const u16*)d_in[6];
  const u16* wout = (const u16*)d_in[7];
  const u16* cvec = (const u16*)d_in[8];
  const u16* wmod = (const u16*)d_in[9];
  char* ws = (char*)d_ws;
  (void)hipFuncSetAttribute((const void*)k_mega, hipFuncAttributeMaxDynamicSharedMemorySize, 163840);
  k0_prep<<<K0_GRID, 256, 0, stream>>>(x, wqkv, wo, cvec, wmod, ws);
  k_mega<<<256, 512, 163840, stream>>>(ws, x, win, wsp, bsp, wout, cvec, wmod, (u16*)d_out);
}
```

```cpp
#include <hip/hip_runtime.h>
#include <hip/hip_bf16.h>
#include <stdint.h>
#include <utility>

typedef __attribute__((ext_vector_type(8))) short bf16x8;
typedef __attribute__((ext_vector_type(4))) short bf16x4;
typedef __attribute__((ext_vector_type(2))) float f32x2;
typedef __attribute__((ext_vector_type(4))) float f32x4;
typedef __attribute__((ext_vector_type(16))) float f32x16;
typedef __attribute__((ext_vector_type(2))) unsigned u32x2;
typedef __attribute__((ext_vector_type(4))) unsigned u32x4;
typedef __attribute__((ext_vector_type(2))) __bf16 bf2_t;
typedef __attribute__((ext_vector_type(2))) _Float16 h2_t;
typedef unsigned short u16;
typedef unsigned long long u64;
#define LAS __attribute__((address_space(3)))
#define GAS __attribute__((address_space(1)))
#define DI __device__ __forceinline__

constexpr int D_ = 640, NQKV = 1920, F_ = 1728;

constexpr unsigned OFF_CNT = 0;
constexpr unsigned OFF_MOD = 65536;
constexpr unsigned OFF_MODP = OFF_MOD + 16384;
constexpr unsigned OFF_CBP = OFF_MODP + 40960;
constexpr unsigned OFF_XCH = OFF_CBP + 5 * 16384;
constexpr unsigned OFF_ST1 = OFF_XCH + 131072;
constexpr unsigned OFF_XHAT = OFF_ST1 + 262144;
constexpr unsigned OFF_WQKV = OFF_XHAT + 2621440;
constexpr unsigned OFF_WO = OFF_WQKV + 2457600;
constexpr unsigned OFF_WIN = OFF_WO + 819200;
constexpr unsigned OFF_WOUT = OFF_WIN + 2211840;
constexpr unsigned OFF_Q = OFF_WOUT + 1105920;
constexpr unsigned OFF_KV = OFF_Q + 32 * 81920;
constexpr unsigned OFF_O = OFF_KV + 32 * 163840;
constexpr unsigned OFF_X1S = OFF_O + 2621440;
constexpr unsigned OFF_Z2 = OFF_X1S + 2621440;
constexpr unsigned OFF_U = OFF_Z2 + 3538944;
constexpr unsigned WS_USED = OFF_U + 3538944;
constexpr int C_QKV = 0, C_MOD = 15  , C_ATT = 128, C_REL = 136, C_X1 = 144, C_U = 176, C_Z2 = 208  ;

#define STAMP(kid, k)
#define TDECL
#define TS(k)
#define TDUMP()
DI unsigned pack2(float lo, float hi) { return __builtin_bit_cast(unsigned, __builtin_convertvector((f32x2){lo, hi}, bf2_t)); }
DI unsigned pack2s(float lo, float hi) { unsigned r; asm("v_cvt_pk_bf16_f32 %0, %1, %2" : "=v"(r) : "v"(lo), "v"(hi)); return r; }
DI float opq(float x) { asm("" : "+v"(x)); return x; }
DI f32x4 fma4s(f32x4 a, f32x4 b, f32x4 c) { return (f32x4){opq(__builtin_fmaf(a[0], b[0], c[0])), opq(__builtin_fmaf(a[1], b[1], c[1])), opq(__builtin_fmaf(a[2], b[2], c[2])), opq(__builtin_fmaf(a[3], b[3], c[3]))}; }
DI f32x4 mul4s(f32x4 a, f32x4 b) { return (f32x4){opq(a[0] * b[0]), opq(a[1] * b[1]), opq(a[2] * b[2]), opq(a[3] * b[3])}; }
DI unsigned pack2h(float lo, float hi) { return __builtin_bit_cast(unsigned, __builtin_convertvector((f32x2){lo, hi}, h2_t)); }
DI float bflo(unsigned u) { return __uint_as_float(u << 16); }
DI float bfhi(unsigned u) { return __uint_as_float(u & 0xffff0000u); }
DI float rbf(float x) { return bflo(pack2(x, x)); }
DI float bf2f(u16 b) { return __uint_as_float((unsigned)b << 16); }
#define MFMA32(a, b, c) __builtin_amdgcn_mfma_f32_32x32x16_bf16((a), (b), (c), 0, 0, 0)
#define MFMA32K8(a, b, c) __builtin_amdgcn_mfma_f32_32x32x8bf16_1k((a), (b), (c), 0, 0, 0)
#define MFMA16(a, b, c) __builtin_amdgcn_mfma_f32_16x16x32_bf16((a), (b), (c), 0, 0, 0)
#define BAR() asm volatile("s_waitcnt lgkmcnt(0)\n\ts_barrier" ::: "memory")
#define SCHED() __builtin_amdgcn_sched_barrier(0)
#define PIN(x) asm volatile("" : "+v"(x))
#define OCC2 __attribute__((amdgpu_waves_per_eu(2, 2)))
#define BF8(x) __builtin_bit_cast(bf16x8, x)
#define BF4(x) __builtin_bit_cast(bf16x4, x)
template <int N> DI void waitv() { asm volatile("s_waitcnt vmcnt(%0)" ::"i"(N) : "memory"); }
template <class F, int... I> DI void sfor_impl(F&& f, std::integer_sequence<int, I...>) { (f(std::integral_constant<int, I>{}), ...); }
template <int N, class F> DI void sfor(F&& f) { sfor_impl(f, std::make_integer_sequence<int, N>{}); }
typedef short v4i16_t __attribute__((ext_vector_type(4)));
DI bf16x4 lds_tr(const char* p) { return __builtin_bit_cast(bf16x4, __builtin_amdgcn_ds_read_tr16_b64_v4i16((LAS v4i16_t*)p)); }
DI void glds16(const void* g, unsigned lds_base) {
  unsigned sv;
  asm volatile("s_mov_b32 %0, m0\n\ts_mov_b32 m0, %2\n\ts_nop 0\n\tglobal_load_lds_dwordx4 %1, off\n\ts_mov_b32 m0, %0" : "=&s"(sv) : "v"(g), "s"(lds_base) : "memory");
}
DI void glds16c(const void* g, unsigned lds_base) {
  unsigned sv;
  asm volatile("s_mov_b32 %0, m0\n\ts_mov_b32 m0, %2\n\ts_nop 0\n\tglobal_load_lds_dwordx4 %1, off sc1\n\ts_mov_b32 m0, %0" : "=&s"(sv) : "v"(g), "s"(lds_base) : "memory");
}
DI void glds4c(const void* g, unsigned lds_base) {
  unsigned sv;
  asm volatile("s_mov_b32 %0, m0\n\ts_mov_b32 m0, %2\n\ts_nop 0\n\tglobal_load_lds_dword %1, off sc1\n\ts_mov_b32 m0, %0" : "=&s"(sv) : "v"(g), "s"(lds_base) : "memory");
}
typedef __amdgpu_buffer_rsrc_t rsrc_t;
DI u32x4 ldc16(rsrc_t rs, unsigned voff, unsigned soff) { return __builtin_amdgcn_raw_buffer_load_b128(rs, voff, soff, 16); }
DI unsigned ldc4(rsrc_t rs, unsigned voff, unsigned soff) { return __builtin_amdgcn_raw_buffer_load_b32(rs, voff, soff, 16); }
DI void stc16(rsrc_t rs, unsigned voff, unsigned soff, u32x4 v) { __builtin_amdgcn_raw_buffer_store_b128(v, rs, voff, soff, 16); asm volatile("s_nop 1" ::"v"(v) : "memory"); }
DI void stc8(rsrc_t rs, unsigned voff, unsigned soff, u32x2 v) { __builtin_amdgcn_raw_buffer_store_b64(v, rs, voff, soff, 16); }
DI void stc4(rsrc_t rs, unsigned voff, unsigned soff, unsigned v) { __builtin_amdgcn_raw_buffer_store_b32(v, rs, voff, soff, 16); }
DI f32x4 ldc16f(rsrc_t rs, unsigned voff, unsigned soff) { return __builtin_bit_cast(f32x4, ldc16(rs, voff, soff)); }
DI void arrive(char* ws, int c, int lane) { if (lane == 0) __hip_atomic_fetch_add((GAS unsigned*)(ws + OFF_CNT + c * 256), 1u, __ATOMIC_RELAXED, __HIP_MEMORY_SCOPE_AGENT); }
DI void poll_ge(char* ws, int c, unsigned thr) {
  const GAS unsigned* p = (const GAS unsigned*)(ws + OFF_CNT + c * 256);
#pragma nounroll
  for (unsigned spin = 0; spin < (1u << 21); ++spin) {
    if (__all(__hip_atomic_load(p, __ATOMIC_RELAXED, __HIP_MEMORY_SCOPE_AGENT) >= thr)) break;
    __builtin_amdgcn_s_sleep(1);
  }
  asm volatile("" ::: "memory");
}
DI int fresh_tid() { int t = threadIdx.x; asm volatile("" : "+v"(t)); return t; }
DI void l2touch(const void* g, unsigned lds_scratch) {
  unsigned sv;
  asm volatile("s_mov_b32 %0, m0\n\ts_mov_b32 m0, %2\n\ts_nop 0\n\tglobal_load_lds_dword %1, off\n\ts_mov_b32 m0, %0" : "=&s"(sv) : "v"(g), "s"(lds_scratch) : "memory");
}
#define LDNT(p) __builtin_nontemporal_load(p)
DI float silu_bf(u16 c) { const float f = __uint_as_float((unsigned)c << 16); const float v = f / (1.f + __expf(-f)); return __uint_as_float(__builtin_bit_cast(unsigned, __builtin_convertvector((f32x2){v, v}, bf2_t)) << 16); }
DI unsigned lds_addr(const void* p) { return (unsigned)(uintptr_t)(const LAS void*)p; }
DI float xhalf(float v) {
  auto rr = __builtin_amdgcn_permlane32_swap(__float_as_uint(v), __float_as_uint(v), false, false);
  return __uint_as_float((threadIdx.x & 32) ? rr[0] : rr[1]);
}
DI float sum_g(float v) {
  auto a = __builtin_amdgcn_permlane16_swap(__float_as_uint(v), __float_as_uint(v), false, false);
  v = __uint_as_float(a[0]) + __uint_as_float(a[1]);
  auto b = __builtin_amdgcn_permlane32_swap(__float_as_uint(v), __float_as_uint(v), false, false);
  return __uint_as_float(b[0]) + __uint_as_float(b[1]);
}
template <int CTRL> DI float dpp_add(float v) { return v + __uint_as_float(__builtin_amdgcn_update_dpp(0u, __float_as_uint(v), CTRL, 0xf, 0xf, true)); }
DI float sum16(float v) {
  v = dpp_add<0x128>(v); v = dpp_add<0x124>(v); v = dpp_add<0x122>(v); return dpp_add<0x121>(v);
}
DI u32x4 modulate8(u32x4 v, f32x4 sc0, f32x4 sc1, f32x4 sh0, f32x4 sh1) {
  unsigned o0, o1, o2, o3; float t0, t1, t2, t3;
  asm volatile(
      "v_fma_mix_f32 %0, %8, %12, %20 op_sel_hi:[1,0,0]\n\tv_fma_mix_f32 %4, %8, %13, %21 op_sel:[1,0,0] op_sel_hi:[1,0,0]\n\t"
      "v_fma_mix_f32 %1, %9, %14, %22 op_sel_hi:[1,0,0]\n\tv_fma_mix_f32 %5, %9, %15, %23 op_sel:[1,0,0] op_sel_hi:[1,0,0]\n\t"
      "v_fma_mix_f32 %2, %10, %16, %24 op_sel_hi:[1,0,0]\n\tv_fma_mix_f32 %6, %10, %17, %25 op_sel:[1,0,0] op_sel_hi:[1,0,0]\n\t"
      "v_fma_mix_f32 %3, %11, %18, %26 op_sel_hi:[1,0,0]\n\tv_fma_mix_f32 %7, %11, %19, %27 op_sel:[1,0,0] op_sel_hi:[1,0,0]\n\t"
      "v_cvt_pk_bf16_f32 %0, %0, %4\n\tv_cvt_pk_bf16_f32 %1, %1, %5\n\tv_cvt_pk_bf16_f32 %2, %2, %6\n\tv_cvt_pk_bf16_f32 %3, %3, %7"
      : "=&v"(o0), "=&v"(o1), "=&v"(o2), "=&v"(o3), "=&v"(t0), "=&v"(t1), "=&v"(t2), "=&v"(t3)
      : "v"(v[0]), "v"(v[1]), "v"(v[2]), "v"(v[3]), "v"(sc0[0]), "v"(sc0[1]), "v"(sc0[2]), "v"(sc0[3]), "v"(sc1[0]), "v"(sc1[1]), "v"(sc1[2]), "v"(sc1[3]),
        "v"(sh0[0]), "v"(sh0[1]), "v"(sh0[2]), "v"(sh0[3]), "v"(sh1[0]), "v"(sh1[1]), "v"(sh1[2]), "v"(sh1[3]));
  return (u32x4){o0, o1, o2, o3};
}
DI float ln_den(float var) { return rbf(__builtin_sqrtf(rbf(rbf(var) + 1e-5f))); }
DI unsigned a16_off(int n, int r) { return (n >> 5) * 1024 + (((n & 31) >> 3) * 16 + r) * 16 + (n & 4) * 2; }
DI int unit0_of(int j) { return j < 6 ? 7 * j : 42 + 6 * (j - 6); }

DI void K0ST(char* p, u32x4 v) { *(u32x4*)p = v; }
constexpr int K0_MOD = 160, K0_LN = 128, K0_RL = 240, K0_WO = 50, K0_INIT = 12;
constexpr int K0_GRID = K0_MOD + K0_LN + K0_RL + K0_WO + K0_INIT;

__global__ __launch_bounds__(256) void k0_prep(const u16* __restrict__ x, const u16* __restrict__ wqkv, const u16* __restrict__ wo, const u16* __restrict__ cvec,
                                               const u16* __restrict__ wmod, char* __restrict__ ws) {
  __shared__ __attribute__((aligned(16))) char smem[4 * 4608];
  const int tid = threadIdx.x, lane = tid & 63, wid = __builtin_amdgcn_readfirstlane(tid >> 6);
  int b = blockIdx.x;
  STAMP(0, 0);
  if (b < K0_MOD) {
    const int cg = b >> 2, ks = b & 3, c = tid & 7, row0 = ks * 160 + (tid >> 3), col0 = cg * 64 + (cg >= 20 ? 640 : 0);
    u32x4 w[5]; u16 cv[5];
#pragma unroll
    for (int i = 0; i < 5; ++i) { w[i] = LDNT((const u32x4*)(wmod + (size_t)(row0 + 32 * i) * 3840 + col0 + c * 8)); cv[i] = cvec[row0 + 32 * i]; }
    float acc[8] = {0, 0, 0, 0, 0, 0, 0, 0};
#pragma unroll
    for (int i = 0; i < 5; ++i) {
      const float s = silu_bf(cv[i]);
#pragma unroll
      for (int e = 0; e < 4; ++e) { acc[2 * e] += s * bflo(w[i][e]); acc[2 * e + 1] += s * bfhi(w[i][e]); }
    }
#pragma unroll
    for (int e = 0; e < 8; ++e) acc[e] = sum_g(dpp_add<0x128>(acc[e]));
    float* red = (float*)smem;
    if (lane < 8) { *(f32x4*)(red + wid * 64 + lane * 8) = (f32x4){acc[0], acc[1], acc[2], acc[3]}; *(f32x4*)(red + wid * 64 + lane * 8 + 4) = (f32x4){acc[4], acc[5], acc[6], acc[7]}; }
    __syncthreads();
    if (tid < 64) ((float*)(ws + OFF_MODP))[ks * 2560 + cg * 64 + tid] = (red[tid] + red[64 + tid]) + (red[128 + tid] + red[192 + tid]);
    STAMP(0, 1); STAMP(0, 13);
    return;
  }
  b -= K0_MOD;
  if (b < K0_LN) {
    b = (b & 7) * 16 + (b >> 3);
    const int row = b * 16 + wid * 4 + (lane >> 4), c = lane & 15;
    const u16* xr = x + (size_t)row * D_ + c * 8;
    u32x4 v[5];
#pragma unroll
    for (int i = 0; i < 5; ++i) v[i] = *(const u32x4*)(xr + i * 128);
    float s = 0.f, q = 0.f;
#pragma unroll
    for (int i = 0; i < 5; ++i)
#pragma unroll
      for (int e = 0; e < 4; ++e) { const float a = bflo(v[i][e]), d = bfhi(v[i][e]); s += a + d; q += a * a + d * d; }
    s = sum16(s); q = sum16(q);
    const float mean = s * (1.f / 640.f); const float var = fmaxf(q * (1.f / 640.f) - mean * mean, 0.f);
    const float rinv = 1.f / ln_den(var), mb = rbf(mean) * rinv;
    char* dst = ws + OFF_XHAT + (size_t)row * 1280 + c * 16;
#pragma unroll
    for (int i = 0; i < 5; ++i) {
      u32x4 o;
#pragma unroll
      for (int e = 0; e < 4; ++e) o[e] = pack2h(bflo(v[i][e]) * rinv - mb, bfhi(v[i][e]) * rinv - mb);
      K0ST(dst + i * 256, o);
    }
    STAMP(0, 2); STAMP(0, 13);
    return;
  }
  b -= K0_LN;
  if (b < K0_RL) {
    const int kt = b / 12, G = b % 12, sec = G >> 2, gq = G & 3;
    u32x4 v[3];
#pragma unroll
    for (int i = 0; i < 3; ++i) { const int ci = min(i * 256 + tid, 639); v[i] = LDNT((const u32x4*)(wqkv + (size_t)(kt * 32 + ci / 20) * NQKV + G * 160 + (ci % 20) * 8)); }
#pragma unroll
    for (int i = 0; i < 3; ++i) { const int ci = min(i * 256 + tid, 639); *(u32x4*)(smem + (ci / 20) * 336 + (ci % 20) * 16) = v[i]; }
    __syncthreads();
    const int g = lane >> 4, q = (lane & 15) >> 2, p = lane & 3;
    for (int f = wid; f < 10; f += 4) {
      const int t = f >> 1, x2 = f & 1;
      const int col = sec == 2 ? 16 * f + 4 * p : t < 4 ? 20 * (2 * t + (p >> 1)) + 8 * (p & 1) + 4 * x2 : 20 * (4 * x2 + p) + 16;
      const int T = sec == 2 ? 40 + gq * 5 + t : sec * 20 + (t < 4 ? 4 * gq + t : 16 + gq);
      const char* pp = smem + (8 * g + q) * 336 + col * 2;
      const bf16x4 lo = lds_tr(pp), hi = lds_tr(pp + 4 * 336);
      K0ST(ws + OFF_WQKV + (((size_t)T * 20 + kt) * 2 + x2) * 1024 + lane * 16, __builtin_bit_cast(u32x4, (bf16x8)__builtin_shufflevector(lo, hi, 0, 1, 2, 3, 4, 5, 6, 7)));
    }
    STAMP(0, 3); STAMP(0, 13);
    return;
  }
  b -= K0_RL;
  if (b < K0_WO) {
    const int job = b * 4 + wid, kt = job / 10, ng = job % 10, j8 = lane >> 3;
    char* t = smem + wid * 4608;
    u32x4 v[4];
#pragma unroll
    for (int i = 0; i < 4; ++i) {
      const int c = 4 * kt + i, row = c < 64 ? 20 * (c >> 1) + 4 * (c & 1) + (j8 & 3) + 8 * (j8 >> 2) : 20 * (2 * (c - 64) + (j8 >> 2)) + 16 + (j8 & 3);
      v[i] = LDNT((const u32x4*)(wo + (size_t)row * D_ + ng * 64 + (lane & 7) * 8));
    }
#pragma unroll
    for (int i = 0; i < 4; ++i) *(u32x4*)(t + (i * 8 + j8) * 144 + (lane & 7) * 16) = v[i];
    const char* pp = t + (8 * (lane >> 4) + ((lane & 15) >> 2)) * 144 + (lane & 3) * 8;
#pragma unroll
    for (int f = 0; f < 4; ++f) {
      const bf16x4 lo = lds_tr(pp + f * 32), hi = lds_tr(pp + 4 * 144 + f * 32);
      K0ST(ws + OFF_WO + ((size_t)(ng * 4 + f) * 20 + kt) * 1024 + lane * 16, __builtin_bit_cast(u32x4, (bf16x8)__builtin_shufflevector(lo, hi, 0, 1, 2, 3, 4, 5, 6, 7)));
    }
    return;
  }
  b -= K0_WO;
  {
    u32x4* p = (u32x4*)(ws + (b < 8 ? OFF_XCH + b * 16384 : OFF_CNT + (b - 8) * 16384)) + tid;
    const unsigned f = b < 8 ? ~0u : 0u;
#pragma unroll
    for (int i = 0; i < 4; ++i) p[i * 256] = (u32x4){f, f, f, f};
  }
}

constexpr float QSCALE = 0.22360679774997896f * 1.4426950408889634f;
#define K1W 3
#define K1WB 5
#define K1WB0 2
constexpr int K1_W = K1W, K1_WB = K1WB, K1_WB0 = K1WB0;

template <bool ISV>
DI void k1_mma(const char* __restrict__ bsrc, const char* smem, int lane, f32x4 (&acc)[8][2]) {
  constexpr int PD = 4;
  bf16x8 bfr[K1_WB + 1][4], af[PD + 1][2];
  const char* lds[2];
#pragma unroll
  for (int k = 0; k < 2; ++k) lds[k] = smem + k * 8192 + (lane ^ (4 * k + (lane >> 4))) * 16;
#pragma unroll
  for (int i = 0; i < 4 * K1_WB0; ++i) bfr[i >> 2][i & 3] = *(const bf16x8*)(bsrc + i * 1024);
#pragma unroll
  for (int m = 0; m < 8; ++m) { acc[m][0] = (f32x4){}; acc[m][1] = (f32x4){}; }
  BAR();
#pragma unroll
  for (int i = 4 * K1_WB0; i < 4 * K1_WB; ++i) bfr[i >> 2][i & 3] = *(const bf16x8*)(bsrc + i * 1024);
  BAR();
  __builtin_amdgcn_s_setprio(1);
#define K1_RD(t, k) (af[(t) % (PD + 1)][k] = *(const bf16x8*)(lds[k] + (((t) >> 3) % 3) * 16384 + ((t) & 7) * 1024))
#define K1_MM(c, b, a) (c = ISV ? MFMA16(a, b, c) : MFMA16(b, a, c))
#pragma unroll
  for (int t = 0; t < PD; ++t) { K1_RD(t, 0); K1_RD(t, 1); }
  SCHED();
  sfor<80>([&](auto T) __attribute__((always_inline)) {
    constexpr int t = T, s = t >> 3, m = t & 7, nx = t + PD;
    if constexpr (nx < 80 && (nx & 7) == 0) asm volatile("s_barrier" ::: "memory");
    if constexpr (nx < 80) K1_RD(nx, 0);
    K1_MM(acc[m][0], bfr[s % (K1_WB + 1)][0], af[t % (PD + 1)][0]); K1_MM(acc[m][0], bfr[s % (K1_WB + 1)][2], af[t % (PD + 1)][1]); PIN(acc[m][0]);
    SCHED();
    if constexpr (nx < 80) K1_RD(nx, 1);
    if constexpr (s + K1_WB < 10 && (m & 1)) bfr[(s + K1_WB) % (K1_WB + 1)][m >> 1] = *(const bf16x8*)(bsrc + ((s + K1_WB) * 4 + (m >> 1)) * 1024);
    K1_MM(acc[m][1], bfr[s % (K1_WB + 1)][1], af[t % (PD + 1)][0]); K1_MM(acc[m][1], bfr[s % (K1_WB + 1)][3], af[t % (PD + 1)][1]); PIN(acc[m][1]);
    SCHED();
  });
  __builtin_amdgcn_s_setprio(0);
}

#define TARG
#define TPASS

DI void phase_qkv(char* __restrict__ ws, rsrc_t rs, const u16* __restrict__ cvec, const u16* __restrict__ wmod, char* smem, int wid TARG) {
  const int tid = fresh_tid(), lane = tid & 63;
  const int xcd = blockIdx.x & 7, idx = blockIdx.x >> 3;
  int ctr;
  if ((xcd & 1) && idx >= 28) {
    ctr = C_MOD;
    const int sb = (xcd >> 1) * 4 + idx - 28, col0 = (sb < 8 ? 1280 : 3200 - 640) + sb * 80, c = tid % 10, rg = tid / 10;
    float* sil = (float*)smem; float* red = (float*)(smem + 4096);
    u32x4 w[13];
#pragma unroll
    for (int i = 0; i < 13; ++i) { const int row = min(rg + 50 * i, 639); w[i] = LDNT((const u32x4*)(wmod + (size_t)row * 3840 + col0 + c * 8)); }
    for (int i = tid; i < 660; i += 512) sil[i] = i < 640 ? silu_bf(cvec[i]) : 0.f;
    if (tid < 80) {
      const float* pp = (const float*)(ws + OFF_MODP) + 1280 + sb * 80 + tid;
      float v = rbf((pp[0] + pp[2560]) + (pp[5120] + pp[7680]));
      if (sb >= 8) v = rbf(1.f + v);
      stc4(rs, (1920 + sb * 80 + tid) * 4, OFF_MOD, __float_as_uint(v));
    }
    BAR();
    float acc[8] = {0, 0, 0, 0, 0, 0, 0, 0};
#pragma unroll
    for (int i = 0; i < 13; ++i) {
      const float s = sil[rg + 50 * i];
#pragma unroll
      for (int e = 0; e < 4; ++e) { acc[2 * e] += s * bflo(w[i][e]); acc[2 * e + 1] += s * bfhi(w[i][e]); }
    }
    if (rg < 50) { *(f32x4*)(red + rg * 80 + c * 8) = (f32x4){acc[0], acc[1], acc[2], acc[3]}; *(f32x4*)(red + rg * 80 + c * 8 + 4) = (f32x4){acc[4], acc[5], acc[6], acc[7]}; }
    BAR();
    if (tid < 80) {
      float v = 0.f;
#pragma unroll
      for (int i = 0; i < 50; ++i) v += red[i * 80 + tid];
      stc4(rs, (col0 + tid) * 4, OFF_MOD, __float_as_uint(rbf(v)));
    }
  } else {
    const int mt = (xcd >> 1) * 4 + (idx & 3), nt = (xcd & 1) * 8 + (idx >> 2);
    ctr = C_QKV + nt;
    if (wid < 4) {
      float* cst = (float*)(smem + 49152);
      const int c = lane & 7, rr = lane >> 3;
      const char* src = ws + OFF_XHAT + (size_t)(mt * 128 + wid * 32 + rr) * 1280 + c * 16;
      char* wdst = smem + (c >> 2) * 8192 + wid * 2048 + (c & 3) * 256 + (rr ^ c) * 16;
      u32x4 a[10][4];
      f32x4 c0[4], c1[4];
#pragma unroll
      for (int i = 0; i < 4; ++i) { c0[i] = *(const f32x4*)(ws + OFF_MODP + i * 10240 + tid * 16); if (wid == 0) c1[i] = *(const f32x4*)(ws + OFF_MODP + i * 10240 + 4096 + tid * 16); }
#pragma unroll
      for (int s = 0; s < K1_W; ++s)
#pragma unroll
        for (int m = 0; m < 4; ++m) a[s][m] = *(const u32x4*)(src + m * 10240 + s * 128);
      {
        f32x4 v = (c0[0] + c0[1]) + (c0[2] + c0[3]); const float one = tid >= 160 ? 1.f : 0.f;
        *(f32x4*)(cst + tid * 4) = (f32x4){rbf(one + rbf(v[0])), rbf(one + rbf(v[1])), rbf(one + rbf(v[2])), rbf(one + rbf(v[3]))};
        if (wid == 0) { v = (c1[0] + c1[1]) + (c1[2] + c1[3]); *(f32x4*)(cst + 1024 + tid * 4) = (f32x4){rbf(1.f + rbf(v[0])), rbf(1.f + rbf(v[1])), rbf(1.f + rbf(v[2])), rbf(1.f + rbf(v[3]))}; }
      }
      BAR();
      const float* cp = cst + c * 8;
      f32x4 kc[10][4];
#define K1_KC(s) do { kc[s][0] = *(const f32x4*)(cp + (s) * 64); kc[s][1] = *(const f32x4*)(cp + (s) * 64 + 4); kc[s][2] = *(const f32x4*)(cp + (s) * 64 + 640); kc[s][3] = *(const f32x4*)(cp + (s) * 64 + 644); } while (0)
#pragma unroll
      for (int s = 0; s < 6; ++s) K1_KC(s);
      SCHED();
      u32x4 o[2][4];
#define K1_XF(s, m) (o[(s) & 1][m] = modulate8(a[s][m], kc[s][2], kc[s][3], kc[s][0], kc[s][1]))
#define K1_WR(s, m) (*(u32x4*)(wdst + ((s) % 3) * 16384 + ((m) >> 1) * 1024 + ((m) & 1) * 128) = o[(s) & 1][m])
#pragma unroll
      for (int m = 0; m < 4; ++m) { K1_XF(0, m); K1_WR(0, m); }
      BAR();
      TS(1);
#pragma unroll
      for (int m = 0; m < 4; ++m) a[K1_W][m] = *(const u32x4*)(src + m * 10240 + K1_W * 128);
#pragma unroll
      for (int m = 0; m < 4; ++m) K1_XF(1, m);
      SCHED();
#pragma unroll
      for (int s = 1; s < 10; ++s) {
#pragma unroll
        for (int m = 0; m < 4; ++m) {
          if (m == 0) K1_WR(s, 0);
          if (m < 3) K1_WR(s, m + 1);
          if (s + K1_W < 10) a[s + K1_W][m] = *(const u32x4*)(src + m * 10240 + (s + K1_W) * 128);
          if (s < 5) kc[s + 5][m] = *(const f32x4*)(cp + (s + 5) * 64 + (m & 1) * 4 + (m >> 1) * 640);
          SCHED();
          if (s < 9) K1_XF(s + 1, m);
          SCHED();
        }
        asm volatile("" :: "v"(o[s & 1][0]), "v"(o[s & 1][1]), "v"(o[s & 1][2]), "v"(o[s & 1][3]));
        BAR();
      }
    } else {
      const int cw = wid - 4;
      const char* bsrc = ws + OFF_WQKV + (size_t)(nt * 4 + cw) * 40 * 1024 + lane * 16;
      f32x4 acc[8][2];
      const int r = lane & 15, g = lane >> 4;
      if (nt >= 10) {
        k1_mma<true>(bsrc, smem, lane, acc);
        TS(2);
        const unsigned so = OFF_KV + (mt >> 1) * 20480 + 10240 + (mt & 1) * 5120;
#pragma unroll
        for (int x = 0; x < 2; ++x) {
          const unsigned c = (nt - 10) * 128 + cw * 32 + x * 16 + r, head = c / 20, d = c % 20;
          const unsigned vo = head * 163840u + ((g & 1) * 20 + d) * 16 + (g >> 1) * 8;
#pragma unroll
          for (int m = 0; m < 8; ++m) stc8(rs, vo + (m & 3) * 640, so + (m >> 2) * 2560, (u32x2){pack2(acc[m][x][0], acc[m][x][1]), pack2(acc[m][x][2], acc[m][x][3])});
        }
      } else {
        k1_mma<false>(bsrc, smem, lane, acc);
        TS(2);
        const bool isQ = nt < 5; const float sc = isQ ? QSCALE : 1.f;
        const unsigned so = isQ ? OFF_Q + mt * 5120 : OFF_KV + (mt >> 1) * 20480 + (mt & 1) * 5120, hs = isQ ? 81920u : 163840u;
        const int n5 = isQ ? nt : nt - 5;
        if (n5 < 4) {
          const unsigned vo = (2 * (n5 * 4 + cw) + (g >> 1)) * hs + (g & 1) * 512 + r * 16;
#pragma unroll
          for (int m = 0; m < 8; ++m) {
            const f32x4 v0 = acc[m][0] * sc, v1 = acc[m][1] * sc;
            stc16(rs, vo + (m & 1) * 256, so + (m >> 1) * 1280, (u32x4){pack2(v0[0], v0[1]), pack2(v0[2], v0[3]), pack2(v1[0], v1[1]), pack2(v1[2], v1[3])});
          }
        } else {
          const unsigned vo = (8 * cw + g) * hs + 1024 + r * 8;
#pragma unroll
          for (int m = 0; m < 8; ++m)
#pragma unroll
            for (int x = 0; x < 2; ++x) {
              const f32x4 v = acc[m][x] * sc;
              stc8(rs, vo + (m & 1) * 128, so + x * 4 * hs + (m >> 1) * 1280, (u32x2){pack2(v[0], v[1]), pack2(v[2], v[3])});
            }
        }
      }
    }
  }
  TS(3);
  waitv<0>();
  BAR();
  TS(4);
  if (wid == 7 && lane < 8) __hip_atomic_fetch_add((GAS unsigned*)(ws + OFF_CNT + (lane * 16 + ctr) * 256), 1u, __ATOMIC_RELAXED, __HIP_MEMORY_SCOPE_AGENT);
}

template <bool FINAL> DI constexpr int proj_pos(int f) { return FINAL ? (f < 96 ? 65536 + f * 1024 : (f - 96) * 1024) : 17408 + (f % 20 >= 10 ? 81920 : 0) + (f / 20 * 10 + f % 10) * 1024; }
template <bool FINAL> DI u32x4 proj_ld(rsrc_t rs, int i, int wid, int lane) {
  constexpr int NF = FINAL ? 135 : 100;
  return ldc16(rs, lane * 16, (FINAL ? OFF_WOUT : OFF_WO) + (((blockIdx.x >> 3) & 7) * NF + min(i * 8 + wid, NF - 1)) * 1024);
}
template <bool FINAL> DI void proj_park(char* smem, int i, int wid, int lane, u32x4 v) {
  constexpr int NF = FINAL ? 135 : 100;
  *(u32x4*)(smem + proj_pos<FINAL>(min(i * 8 + wid, NF - 1)) + lane * 16) = v;
}

DI void split3(float m, unsigned& w_h0, unsigned& w_h1) {
  const float n = -m; const float a = rbf(n), b = rbf(n - a), c = rbf(n - a - b);
  w_h0 = pack2(a, b); w_h1 = pack2(c, 0.f);
}

DI void phase_attn(char* __restrict__ ws, rsrc_t rs, const char* __restrict__ wsp, const u16* __restrict__ win, const u16* __restrict__ wout, char* smem, int wid TARG) {
  const int tid = fresh_tid(), lane = tid & 63;
  const int xcd = blockIdx.x & 7, idx = blockIdx.x >> 3, head = xcd * 4 + (idx >> 3), qb = idx & 7;
  const int r = lane & 31, h = lane >> 5, qg = wid & 3, kh = wid >> 2;
  if (wid == 7) {
    const int l7 = lane & 7;
    const int c = l7 == 0 ? head >> 3 : l7 == 1 ? 4 : l7 == 2 ? 5 + (head >> 3) : l7 == 3 ? 9 : l7 == 4 ? 10 + (20 * head) / 128 : l7 == 5 ? 10 + (20 * head + 19) / 128 : C_MOD;
    poll_ge(ws, xcd * 16 + c, 16);
  }
  BAR();
  TS(5);
  unsigned kvo = OFF_KV + head * 163840, qo = OFF_Q + head * 81920 + (qb * 8 + qg * 2) * 1280;
  const unsigned kconst = h == 0 ? 0x3f803f80u : 0x00003f80u;
  const unsigned so16 = (tid & 255) * 16, l16 = lane * 16, l2 = 1024 + r * 8 + h * 4;
  const int blk = blockIdx.x;
  u32x4 wq[13];
  for (int attempt = 0; attempt < 2; ++attempt) {
    asm volatile("" : "+s"(kvo), "+s"(qo));
    const char* kvs = ws + kvo + kh * 81920 + so16;
    const unsigned ldst = lds_addr(smem) + kh * 81920 + qg * 1024;
    u32x4 qA1w, qB1w, k1w; unsigned qA2w, qB2w, k2, m0sv;
    asm volatile(
        "buffer_load_dwordx4 %0, %7, %9, %10 offen sc1\n\tbuffer_load_dwordx4 %1, %7, %9, %10 offen offset:1280 sc1\n\tbuffer_load_dwordx4 %2, %7, %9, %11 offen sc1\n\t"
        "buffer_load_dword %3, %8, %9, %10 offen sc1\n\tbuffer_load_dword %4, %8, %9, %10 offen offset:1280 sc1\n\tbuffer_load_dword %5, %8, %9, %11 offen sc1\n\t"
        "s_mov_b32 %6, m0\n\ts_mov_b32 m0, %17\n\ts_nop 0\n\tglobal_load_lds_dwordx4 %12, off sc1\n\t"
        "s_add_u32 m0, %18, 0x1000\n\ts_nop 0\n\tglobal_load_lds_dwordx4 %13, off sc1\n\t"
        "s_add_u32 m0, %18, 0x2000\n\ts_nop 0\n\tglobal_load_lds_dwordx4 %14, off sc1\n\t"
        "s_add_u32 m0, %18, 0x3000\n\ts_nop 0\n\tglobal_load_lds_dwordx4 %15, off sc1\n\t"
        "s_add_u32 m0, %18, 0x4000\n\ts_nop 0\n\tglobal_load_lds_dwordx4 %16, off sc1\n\t"
        "s_mov_b32 m0, %6\n\ts_waitcnt vmcnt(5)"
        : "=&v"(qA1w), "=&v"(qB1w), "=&v"(k1w), "=&v"(qA2w), "=&v"(qB2w), "=&v"(k2), "=&s"(m0sv)
        : "v"(l16), "v"(l2), "s"(rs), "s"(qo), "s"(kvo), "v"(kvs + (wid == 0 ? 4096 : 0)), "v"(kvs + 4096), "v"(kvs + 8192), "v"(kvs + 12288), "v"(kvs + 16384),
          "s"(ldst + (wid == 0 ? 4096 : 0)), "s"(ldst)
        : "memory", "scc");
    if (wid == 0) { const unsigned f = (tid & 1) ? 0u : 0x3f803f80u; *(u32x4*)(smem + l16) = (u32x4){f, f, f, f}; }
    const bf16x8 qA1 = BF8(qA1w), qB1 = BF8(qB1w);
    bf16x8 k1 = BF8(k1w);
    TS(30);
    float mA = -INFINITY, mB = -INFINITY;
    {
      const bf16x4 qA2 = BF4(((u32x2){qA2w, 0u})), qB2 = BF4(((u32x2){qB2w, 0u}));
      const int nt = attempt ? 64 : 1;
      bf16x8 t1 = k1; unsigned t2 = k2;
      for (int t = 0; t < nt; ++t) {
        if (t) { const unsigned to = kvo + (t >> 3) * 20480 + (t & 7) * 1280; t1 = BF8(ldc16(rs, l16, to)); t2 = ldc4(rs, l2, to); }
        const bf16x4 t2v = BF4(((u32x2){t2, kconst}));
        f32x16 a = MFMA32(t1, qA1, (f32x16){}); a = MFMA32K8(t2v, qA2, a);
        f32x16 b = MFMA32(t1, qB1, (f32x16){}); b = MFMA32K8(t2v, qB2, b);
#pragma unroll
        for (int i = 0; i < 16; ++i) { mA = fmaxf(mA, a[i]); mB = fmaxf(mB, b[i]); }
      }
      mA = fmaxf(mA, xhalf(mA)); mB = fmaxf(mB, xhalf(mB));
    }
    unsigned a0, a1, b0, b1; split3(mA, a0, a1); split3(mB, b0, b1);
    const bf16x4 qA2 = BF4(((u32x2){qA2w, h ? a1 : a0})), qB2 = BF4(((u32x2){qB2w, h ? b1 : b0}));
    char* lh = smem + kh * 81920;
    TS(31);
    waitv<0>();
    BAR();
    TS(6);
    f32x16 oA = (f32x16){}, oB = (f32x16){};
    const char* kp1 = lh + l16;
    const char* kp2 = lh + l2;
    const bool vreal = r < 20;
    const char* vp = vreal ? lh + 10240 + (h * 20 + r) * 16 : smem + (r == 20 ? 0 : 16);
    const int vinc = vreal ? 1280 : 0, vjump = vreal ? 10240 : 0;
    if (kh) { k1 = *(const bf16x8*)kp1; k2 = *(const unsigned*)kp2; }
    bf16x8 kf1[2], vf[2][2]; u32x2 kf2[2];
    kf1[0] = k1; kf2[0] = (u32x2){k2, kconst}; kf2[1] = (u32x2){0u, kconst};
    vf[1][0] = (bf16x8){}; vf[1][1] = (bf16x8){};
    u32x4 pA0, pA1, pB0 = (u32x4){}, pB1 = (u32x4){};
    f32x4 e0 = (f32x4){}, e1;
    f32x16 sA = MFMA32(kf1[0], qA1, (f32x16){}), sB;
    sA = MFMA32K8(BF4(kf2[0]), qA2, sA);
#define EX2(x) __builtin_amdgcn_exp2f(x)
#define GRP(MF, S, i, EN, EP, P, w) do { MF; SCHED(); EN[0] = EX2(S[i]); P[w] = pack2(EP[0], EP[1]); EN[1] = EX2(S[i + 1]); P[w + 1] = pack2(EP[2], EP[3]); \
      EN[2] = EX2(S[i + 2]); EN[3] = EX2(S[i + 3]); PIN(P); PIN(EN); SCHED(); } while (0)
    const unsigned relsc = lds_addr(smem) + 768;
    auto group = [&](auto FIRST, int jump, int j) __attribute__((always_inline)) {
#pragma unroll
      for (int t = 0; t < 8; ++t) {
        const int c = t & 1, n = c ^ 1;
        if (FIRST) { if (t == 0) __builtin_amdgcn_s_setprio(3); if (t == 3) __builtin_amdgcn_s_setprio(2); if (t == 5) __builtin_amdgcn_s_setprio(1); if (t == 6) __builtin_amdgcn_s_setprio(0); if (t == 7) __builtin_amdgcn_s_setprio(3); }
        else {
          if (t == 4 && j == 2) __builtin_amdgcn_s_setprio(2);
          if (t == 3 && j == 3) {
            __builtin_amdgcn_s_setprio(1);
#pragma unroll
            for (int i = 0; i < 13; ++i) wq[i] = proj_ld<false>(rs, i, wid, lane);
          }
          if (t == 6 && j == 3) { __builtin_amdgcn_s_setprio(0); if (wid == 7) glds4c(ws + OFF_CNT + (C_REL + (lane & 7)) * 256, relsc); }
        }
        if (FIRST && t < 5) {
#pragma unroll
          for (int i = 5 + 3 * t; i < 8 + 3 * t; ++i) glds16c(kvs + i * 4096, ldst + i * 4096);
        }
        if (t == 7) {
          if (FIRST) { waitv<0>(); BAR(); }
          kp1 += jump; kp2 += jump;
        }
        SCHED();
        kf1[n] = *(const bf16x8*)(kp1 + ((t + 1) & 7) * 1280); kf2[n][0] = *(const unsigned*)(kp2 + ((t + 1) & 7) * 1280);
        GRP(sB = MFMA32(kf1[c], qB1, (f32x16){}), sA, 0, e1, e0, pB1, 2);
        vf[c][0] = *(const bf16x8*)vp; vf[c][1] = *(const bf16x8*)(vp + 640); vp += vinc;
        GRP(sB = MFMA32K8(BF4(kf2[c]), qB2, sB), sA, 4, e0, e1, pA0, 0);
        GRP(oB = MFMA32(vf[n][0], BF8(pB0), oB), sA, 8, e1, e0, pA0, 2);
        GRP(oB = MFMA32(vf[n][1], BF8(pB1), oB), sA, 12, e0, e1, pA1, 0);
        GRP(sA = MFMA32(kf1[n], qA1, (f32x16){}), sB, 0, e1, e0, pA1, 2);
        GRP(sA = MFMA32K8(BF4(kf2[n]), qA2, sA), sB, 4, e0, e1, pB0, 0);
        GRP(oA = MFMA32(vf[c][0], BF8(pA0), oA), sB, 8, e1, e0, pB0, 2);
        GRP(oA = MFMA32(vf[c][1], BF8(pA1), oA), sB, 12, e0, e1, pB1, 0);
      }
      vp += vjump;
    };
    group(std::true_type{}, 20480, 0);
    TS(7);
    const int j2 = (blk - 135) * 4 + qg;
    const bool side = kh == 0 && attempt == 0 && j2 < 270;
    char* stile = smem + 1024 + qg * 4096;
    const char* svec = smem + 81920 + qg * 256;
    const int kt = blk < 135 ? 4 * (blk % 5) + qg : j2 / 10, ng = blk < 135 ? blk / 5 : j2 % 10;
    if (side) {
      const u16* W = blk < 135 ? win : wout; const int N = blk < 135 ? F_ : D_;
      const int j8 = lane >> 3;
#pragma unroll
      for (int i = 0; i < 4; ++i) {
        const int row = kt * 32 + i * 8 + j8;
        glds16(W + (size_t)row * N + ng * 64 + ((lane & 7) ^ j8) * 8, lds_addr(stile) + i * 1024);
      }
      glds4c((const float*)(ws + OFF_MOD) + (4 - h) * 640 + (kt < 20 ? kt : 0) * 32 + r, lds_addr(svec));
    }
#pragma nounroll
    for (int j = 1; j < 4; ++j) {
      if (j == 2) {
        if (kh) l2touch(wsp + ((size_t)(blk * 4 + qg) * 64 + lane) * 128, lds_addr(smem) + 256);
        else if (side) {
          waitv<0>();
          const int g4 = lane >> 4, r4 = lane & 15, row = 8 * g4 + (r4 >> 2);
          const f32x4 sv0 = *(const f32x4*)(svec + (r4 & 1) * 128 + g4 * 32), sv1 = *(const f32x4*)(svec + (r4 & 1) * 128 + g4 * 32 + 16);
          const bf16x8 vec = BF8(((u32x4){pack2(sv0[0], sv0[1]), pack2(sv0[2], sv0[3]), pack2(sv1[0], sv1[1]), pack2(sv1[2], sv1[3])}));
          const unsigned nks = blk < 135 ? 20 : 27, dso = (blk < 135 ? OFF_WIN : OFF_WOUT) + (ng * 4 * nks + kt) * 1024;
          bf16x8 fr[4]; f32x4 pr[4];
#pragma unroll
          for (int f = 0; f < 4; ++f) {
            const int ch = 2 * f + ((r4 & 3) >> 1);
            const bf16x4 lo = lds_tr(stile + row * 128 + (ch ^ (row & 7)) * 16 + (r4 & 1) * 8), hi = lds_tr(stile + (row + 4) * 128 + (ch ^ ((row + 4) & 7)) * 16 + (r4 & 1) * 8);
            fr[f] = __builtin_shufflevector(lo, hi, 0, 1, 2, 3, 4, 5, 6, 7);
          }
#pragma unroll
          for (int f = 0; f < 4; ++f) { stc16(rs, l16, dso + f * nks * 1024, __builtin_bit_cast(u32x4, fr[f])); pr[f] = MFMA16(fr[f], vec, (f32x4){}); }
          if (r4 < 2) {
#pragma unroll
            for (int f = 0; f < 4; ++f) *(f32x4*)(stile + 3072 + r4 * 256 + f * 64 + g4 * 16) = pr[f];
          }
        }
      }
      if (j == 3 && kh == 0 && attempt == 0) { waitv<0>(); arrive(ws, C_REL + xcd, lane); }
      group(std::false_type{}, j < 3 ? 20480 : 0, j);
    }
    TS(8);
    pB1[2] = pack2(e0[0], e0[1]); pB1[3] = pack2(e0[2], e0[3]);
    oB = MFMA32(vf[1][0], BF8(pB0), oB); oB = MFMA32(vf[1][1], BF8(pB1), oB);
    {
      f32x4* ex = (f32x4*)(lh + 1024 + qg * 4096) + lane;
      const f32x16 give = kh ? oA : oB;
#pragma unroll
      for (int g = 0; g < 3; ++g) ex[g * 64] = (f32x4){give[4 * g], give[4 * g + 1], give[4 * g + 2], give[4 * g + 3]};
    }
    int* flag = (int*)(smem + 528);
    BAR();
    TS(9);
    float o[12];
    {
      const f32x4* ex = (const f32x4*)(smem + (kh ^ 1) * 81920 + 1024 + qg * 4096) + lane;
      const f32x16 keep = kh ? oB : oA;
#pragma unroll
      for (int g = 0; g < 3; ++g) { const f32x4 t = ex[g * 64];
#pragma unroll
        for (int e = 0; e < 4; ++e) o[4 * g + e] = keep[4 * g + e] + t[e]; }
    }
    const float lo = xhalf(o[8]); const float l = h ? o[8] : lo;
    const float rl = __builtin_amdgcn_rcpf(l);
    const int tok = qb * 256 + qg * 64 + kh * 32 + r;
    const unsigned vo = (tok >> 4) * 20480 + (tok & 15) * 16;
    stc16(rs, vo + h * 256, OFF_O + (head >> 1) * 1024 + (head & 1) * 512,
          (u32x4){pack2(o[0] * rl, o[1] * rl), pack2(o[2] * rl, o[3] * rl), pack2(o[4] * rl, o[5] * rl), pack2(o[6] * rl, o[7] * rl)});
    if (h == 0) stc8(rs, vo, OFF_O + 16384 + (head >> 3) * 1024 + ((head >> 1) & 3) * 256 + (head & 1) * 8, (u32x2){pack2(o[8] * rl, o[9] * rl), pack2(o[10] * rl, o[11] * rl)});
    if (wid == 4 && blk < 135 && attempt == 0 && lane < 32) {
      const char* pp = smem + 1024 + 3072 + lane * 16;
      const f32x4 v = (*(const f32x4*)pp + *(const f32x4*)(pp + 4096)) + (*(const f32x4*)(pp + 8192) + *(const f32x4*)(pp + 12288));
      stc16(rs, (lane >> 4) * 8192 + (lane & 15) * 16, OFF_CBP + (blk % 5) * 16384 + (blk / 5) * 256, __builtin_bit_cast(u32x4, v));
    }
    bool ok = (l > 1e-30f) && (l < 1e30f);
#pragma unroll
    for (int i = 0; i < 12; ++i) ok = ok && (fabsf(o[i]) < 3e38f);
    if (!ok && attempt == 0) *flag = 1;
    waitv<0>();
    BAR();
    const int redo = __builtin_amdgcn_readfirstlane(*(volatile LAS int*)flag);
    if (!redo) break;
    BAR();
  }
  TS(10);
  if (wid == 7) arrive(ws, C_ATT + qb, lane);
#pragma unroll
  for (int i = 0; i < 13; ++i) proj_park<false>(smem, i, wid, lane, wq[i]);
  if (!__all(*(volatile LAS unsigned*)(smem + 768 + (lane & 7) * 4) >= 128u)) poll_ge(ws, C_REL + (lane & 7), 128);
}

template <bool FINAL, int KH, class HOOK>
DI void proj_body(char* __restrict__ ws, rsrc_t rs, const u16* __restrict__ x, u16* __restrict__ out, char* smem, int wid, u32x2 (&res)[3], HOOK&& after_mma TARG) {
  const int lane = fresh_tid() & 63;
  const int xcd = blockIdx.x & 7, idx = blockIdx.x >> 3, panel = xcd * 4 + (idx >> 3), cb = idx & 7;
  constexpr int NKS = FINAL ? 27 : 20;
  constexpr int K0 = KH ? (NKS + 1) / 2 : 0, NK = KH ? NKS / 2 : (NKS + 1) / 2, NKEEP = KH ? 2 : 3, UK = KH ? 3 : 0, UG = KH ? 0 : 3;
  const int m = wid & 3, mt = panel * 4 + m, g = lane >> 4, r = lane & 15;
  f32x4 gate[NKEEP], cbv[NKEEP];
#pragma unroll
  for (int i = 0; i < NKEEP; ++i) {
    const int n = (cb * 5 + UK + i) * 16;
    if (!FINAL) { res[i] = *(const u32x2*)(x + (size_t)(mt * 16 + r) * D_ + n + g * 4); cbv[i] = ldc16f(rs, g * 16, OFF_MOD + (4 * 640 + n) * 4); }
    gate[i] = ldc16f(rs, g * 16, OFF_MOD + ((FINAL ? 5 : 2) * 640 + n) * 4);
  }
  if (wid == 7) poll_ge(ws, FINAL ? C_U + panel : C_ATT + (panel >> 2), FINAL ? 8 : 32);
  waitv<0>();
  BAR();
  TS(FINAL ? 27 : 11);
  bf16x8 a[NK];
#pragma unroll
  for (int k = 0; k < NK; ++k) a[k] = BF8(ldc16(rs, lane * 16, (FINAL ? OFF_U : OFF_O) + (mt * NKS + K0 + k) * 1024));
  SCHED();
  constexpr int PD = FINAL ? 5 : 6;
  const char* bl = smem + lane * 16;
  bf16x8 bf[PD + 1][5]; f32x4 acc[5];
#pragma unroll
  for (int u = 0; u < 5; ++u) acc[u] = (f32x4){};
#pragma unroll
  for (int k = 0; k < PD; ++k)
#pragma unroll
    for (int u = 0; u < 5; ++u) bf[k][u] = *(const bf16x8*)(bl + proj_pos<FINAL>(u * NKS + K0 + k));
  SCHED();
#pragma unroll
  for (int k = 0; k < NK; ++k) {
#pragma unroll
    for (int u = 0; u < 5; ++u) {
      if (k + PD < NK) bf[(k + PD) % (PD + 1)][u] = *(const bf16x8*)(bl + proj_pos<FINAL>(u * NKS + K0 + k + PD));
      acc[u] = MFMA16(bf[k % (PD + 1)][u], a[k], acc[u]);
      SCHED();
    }
  }
  TS(FINAL ? 28 : 12);
  f32x4* ex = (f32x4*)(smem + (FINAL ? 40960 : 68608)) + m * 5 * 64 + lane;
#pragma unroll
  for (int i = 0; i < 5 - NKEEP; ++i) ex[(UG + i) * 64] = acc[UG + i];
  f32x4 part[NKEEP];
#pragma unroll
  for (int i = 0; i < NKEEP; ++i) {
    part[i] = fma4s(gate[i], acc[UK + i], (f32x4){bflo(res[i][0]), bfhi(res[i][0]), bflo(res[i][1]), bfhi(res[i][1])});
    PIN(part[i]);
  }
  SCHED();
  after_mma();
  BAR();
  f32x4 oth[NKEEP];
#pragma unroll
  for (int i = 0; i < NKEEP; ++i) oth[i] = ex[(UK + i) * 64];
  f32x4 sv = (f32x4){}, qv = (f32x4){};
#pragma unroll
  for (int i = 0; i < NKEEP; ++i) {
    const f32x4 v = part[i] + gate[i] * oth[i];
    const u32x2 o = (u32x2){pack2(v[0], v[1]), pack2(v[2], v[3])};
    if (FINAL) *(u32x2*)(out + (size_t)(mt * 16 + r) * D_ + (cb * 5 + UK + i) * 16 + g * 4) = o;
    else {
      res[i] = o; PIN(res[i]);
      const f32x4 w = v * cbv[i]; const int un = cb * 5 + UK + i;
      stc8(rs, ((g >> 1) * 16 + r) * 16 + (g & 1) * 8, OFF_X1S + mt * 20480 + un * 512, (u32x2){pack2(w[0], w[1]), pack2(w[2], w[3])});
      sv += v; qv += v * v;
    }
  }
  if (!FINAL) {
    const float s = sum_g((sv[0] + sv[1]) + (sv[2] + sv[3])), q = sum_g((qv[0] + qv[1]) + (qv[2] + qv[3]));
    if (g == 0) stc8(rs, r * 8, OFF_ST1 + ((panel * 16 + cb * 2 + KH) * 64 + m * 16) * 8, (u32x2){__float_as_uint(s), __float_as_uint(q)});
    TS(13);
    waitv<0>();
    BAR();
    TS(14);
    if (wid == 7) arrive(ws, C_X1 + panel, lane);
  }
}

constexpr float GC1 = -2.8853900817779268f * 0.7978845608028654f, GC2 = GC1 * 0.044715f;
DI float gelu_s(float a, float k1, float k3, float den) {
  const float e = __builtin_amdgcn_exp2f(a * __builtin_fmaf(a * a, k3, k1));
  return a * __builtin_amdgcn_rcpf(__builtin_fmaf(e, den, den));
}
DI void gelu8(f32x4& a0, const f32x4& l0, f32x4& a1, const f32x4& l1) {
  float t[8];
#define G8(expr0, expr1) do { _Pragma("unroll") for (int i = 0; i < 4; ++i) { t[i] = opq(expr0); } _Pragma("unroll") for (int i = 0; i < 4; ++i) { t[4 + i] = opq(expr1); } SCHED(); } while (0)
  G8(a0[i] * a0[i], a1[i] * a1[i]);
  G8(__builtin_fmaf(t[i], l0[3], l0[2]), __builtin_fmaf(t[4 + i], l1[3], l1[2]));
  G8(a0[i] * t[i], a1[i] * t[4 + i]);
  G8(__builtin_amdgcn_exp2f(t[i]), __builtin_amdgcn_exp2f(t[4 + i]));
  G8(__builtin_fmaf(t[i], l0[0], l0[0]), __builtin_fmaf(t[4 + i], l1[0], l1[0]));
  G8(__builtin_amdgcn_rcpf(t[i]), __builtin_amdgcn_rcpf(t[4 + i]));
  G8(a0[i] * t[i], a1[i] * t[4 + i]);
  a0 = (f32x4){t[0], t[1], t[2], t[3]}; a1 = (f32x4){t[4], t[5], t[6], t[7]};
}
constexpr int L4_Z1 = 114688, L4_TR = L4_Z1 + 16384, L4_LN = L4_TR + 16384, L4_ST2 = L4_LN + 1024, L4_FIN = L4_ST2 + 3584, L4_CB = L4_FIN + 512, L4_CNT = L4_CB + 2048;
constexpr int K5_R = 7;
constexpr unsigned DEAD = 0x80000000u;
DI int sp_stage(int s) { const int idx = blockIdx.x >> 3; return (s + (idx & 7) * 2) & 15; }
DI unsigned sp_voff(int wid, int lane, int i) { return ((wid & 3) * 16 + i * 8 + (lane >> 3)) * 4096 + (wid >> 2) * 128 + ((lane & 7) ^ (4 * i + (lane >> 4))) * 16; }
DI const char* sp_base(const u16* wsp, int panel, int s) { return (const char*)wsp + (size_t)panel * 262144 + sp_stage(s) * 256; }
DI void sp_prefetch(u32x4 (&wr)[16 - K5_R][2], const u16* __restrict__ wsp, int panel, int wid, int lane) {
  const unsigned v0 = sp_voff(wid, lane, 0), v1 = sp_voff(wid, lane, 1);
#pragma unroll
  for (int s = K5_R; s < 16; ++s) { wr[s - K5_R][0] = *(const u32x4*)(sp_base(wsp, panel, s) + v0); wr[s - K5_R][1] = *(const u32x4*)(sp_base(wsp, panel, s) + v1); }
  asm volatile("" ::: "memory");
}
template <int S0, int S1>
DI void sp_ring(const u16* __restrict__ wsp, char* smem, int panel, int wid, int lane) {
  const unsigned v0 = sp_voff(wid, lane, 0), v1 = sp_voff(wid, lane, 1);
#pragma unroll
  for (int s = S0; s < S1; ++s) { glds16(sp_base(wsp, panel, s) + v0, lds_addr(smem) + wid * 2048 + s * 16384); glds16(sp_base(wsp, panel, s) + v1, lds_addr(smem) + wid * 2048 + s * 16384 + 1024); }
}
DI unsigned win_boff(int wid, int lane) { const int cb = (blockIdx.x >> 3) & 7; return lane * 16 + (wid == 6 && cb >= 6 ? DEAD : 0u); }
DI unsigned win_soff(int wid, bool z2) { return OFF_WIN + ((z2 ? 54 : 0) + unit0_of((blockIdx.x >> 3) & 7) + wid) * 20480; }
#define WIN_LD(so, k) BF8(ldc16(rs, bo + ((k) & 3) * 1024, (so) + ((k) >> 2) * 4096))
DI void win_prefetch(rsrc_t rs, int wid, bf16x8 (&b2)[20]) {
  const unsigned bo = win_boff(wid, fresh_tid() & 63), so = win_soff(wid, true);
#pragma unroll
  for (int k = 0; k < 20; ++k) b2[k] = WIN_LD(so, k);
}
template <class HOOK>
DI void win_mma(const char* al, const bf16x8 (&b)[20], f32x4 (&acc)[4], HOOK&& hook) {
  constexpr int PD = 4;
  bf16x8 af[PD + 1][2];
#define WIN_RD(i) do { af[(i) % (PD + 1)][0] = *(const bf16x8*)(al + ((i) & 3) * 20480 + ((i) >> 2) * 2048); af[(i) % (PD + 1)][1] = *(const bf16x8*)(al + ((i) & 3) * 20480 + ((i) >> 2) * 2048 + 1024); } while (0)
#pragma unroll
  for (int i = 0; i < PD; ++i) WIN_RD(i);
  SCHED();
  sfor<40>([&](auto I) __attribute__((always_inline)) {
    constexpr int i = I, m = i & 3, k = (i >> 2) * 2;
    if constexpr (i + PD < 40) WIN_RD(i + PD);
    acc[m] = MFMA16(b[k], af[i % (PD + 1)][0], acc[m]);
    acc[m] = MFMA16(b[k + 1], af[i % (PD + 1)][1], acc[m]); PIN(acc[m]);
    hook(I);
    SCHED();
  });
}
DI void stage_x1s(char* __restrict__ ws, char* smem, int panel, int wid, int tid) {
  const char* src = ws + OFF_X1S + (size_t)panel * 81920 + tid * 16;
#pragma unroll
  for (int i = 0; i < 10; ++i) glds16c(src + i * 8192, lds_addr(smem) + wid * 1024 + i * 8192);
}
DI void win_tail(char* __restrict__ ws, const u16* __restrict__ wsp, char* smem, int panel, int cb, int wid, int lane TARG) {
  BAR();
  TS(22);
  if (wid == 7 && lane < 4) __hip_atomic_fetch_add((GAS unsigned*)(ws + OFF_CNT + (C_Z2 + lane * 8 + cb) * 256), 1u, __ATOMIC_RELAXED, __HIP_MEMORY_SCOPE_AGENT);
}

template <bool EARLY>
DI void win_body(char* __restrict__ ws, rsrc_t rs, const u16* __restrict__ wsp, char* smem, int wid, const bf16x8 (&b2)[20], u32x4 (&wr)[16 - K5_R][2] TARG) {
  const int tid = fresh_tid(), lane = tid & 63, g = lane >> 4, r = lane & 15;
  const int xcd = blockIdx.x & 7, idx = blockIdx.x >> 3, panel = xcd * 4 + (idx >> 3), cb = idx & 7, u0 = unit0_of(cb);
  float* lnp = (float*)(smem + L4_LN); float* st2 = (float*)(smem + L4_ST2); float* fin = (float*)(smem + L4_FIN); float* cbl = (float*)(smem + L4_CB);
  int* flg = (int*)(smem + L4_CNT);
  __builtin_amdgcn_s_setprio(1);
  const unsigned bo = win_boff(wid, lane), so1 = win_soff(wid, false);
  bf16x8 b1[20];
  BAR();
  TS(15);
  stage_x1s(ws, smem, panel, wid, tid);
#pragma unroll
  for (int k = 0; k < 4; ++k) b1[k] = WIN_LD(so1, k);
  waitv<4>();
  BAR();
  TS(16);
  f32x4 ln[4], z2[4], z1[4];
#pragma unroll
  for (int m = 0; m < 4; ++m) ln[m] = *(const f32x4*)(lnp + (m * 16 + r) * 4);
  {
    const f32x4 cs2 = *(const f32x4*)(cbl + 112 + wid * 16 + g * 4), bi2 = *(const f32x4*)(cbl + 256 + 112 + wid * 16 + g * 4);
    const f32x4 cs1 = *(const f32x4*)(cbl + wid * 16 + g * 4), bi1 = *(const f32x4*)(cbl + 256 + wid * 16 + g * 4);
#pragma unroll
    for (int m = 0; m < 4; ++m) {
#pragma unroll
      for (int e = 0; e < 4; ++e) { z2[m][e] = opq(__builtin_fmaf(ln[m][1], cs2[e], opq(ln[m][0] * bi2[e]))); z1[m][e] = opq(__builtin_fmaf(ln[m][1], cs1[e], opq(ln[m][0] * bi1[e]))); }
      PIN(z1[m]);
    }
  }
  win_mma(smem + lane * 16, b2, z2, [&](auto I) __attribute__((always_inline)) { constexpr int i = I; if constexpr (i < 32 && (i & 1)) b1[4 + i / 2] = WIN_LD(so1, 4 + i / 2); });
  TS(17);
  gelu8(z2[0], ln[0], z2[1], ln[1]); gelu8(z2[2], ln[2], z2[3], ln[3]);
  {
    float s[4], q[4];
#pragma unroll
    for (int m = 0; m < 4; ++m) { s[m] = opq(opq(z2[m][0] + z2[m][1]) + opq(z2[m][2] + z2[m][3])); q[m] = opq(__builtin_fmaf(z2[m][0], z2[m][0], opq(z2[m][1] * z2[m][1])) + opq(__builtin_fmaf(z2[m][2], z2[m][2], opq(z2[m][3] * z2[m][3])))); }
    auto pa = __builtin_amdgcn_permlane32_swap(__float_as_uint(s[0]), __float_as_uint(s[2]), false, false);
    auto pb = __builtin_amdgcn_permlane32_swap(__float_as_uint(s[1]), __float_as_uint(s[3]), false, false);
    auto pc = __builtin_amdgcn_permlane32_swap(__float_as_uint(q[0]), __float_as_uint(q[2]), false, false);
    auto pd = __builtin_amdgcn_permlane32_swap(__float_as_uint(q[1]), __float_as_uint(q[3]), false, false);
    const float sa = opq(__uint_as_float(pa[0]) + __uint_as_float(pa[1])), sb = opq(__uint_as_float(pb[0]) + __uint_as_float(pb[1]));
    const float qa = opq(__uint_as_float(pc[0]) + __uint_as_float(pc[1])), qb = opq(__uint_as_float(pd[0]) + __uint_as_float(pd[1]));
    auto ps = __builtin_amdgcn_permlane16_swap(__float_as_uint(sa), __float_as_uint(sb), false, false);
    auto pq = __builtin_amdgcn_permlane16_swap(__float_as_uint(qa), __float_as_uint(qb), false, false);
    *(f32x2*)(st2 + (wid * 64 + lane) * 2) = (f32x2){opq(__uint_as_float(ps[0]) + __uint_as_float(ps[1])), opq(__uint_as_float(pq[0]) + __uint_as_float(pq[1]))};
  }
  asm volatile("" ::: "memory");
  if (lane == 0) __hip_atomic_fetch_add(flg, 1, __ATOMIC_RELAXED, __HIP_MEMORY_SCOPE_WORKGROUP);
  asm volatile("" ::: "memory");
  __builtin_amdgcn_s_setprio(0);
  TS(18);
  const unsigned wvo[2] = {sp_voff(wid, lane, 0), sp_voff(wid, lane, 1)};
  win_mma(smem + lane * 16, b1, z1, [&](auto I) __attribute__((always_inline)) {
    constexpr int i = I, j = i < 8 ? i : (i & 1) ? -1 : 4 + i / 2;
    if constexpr (EARLY && j >= 0 && j < 18) wr[j >> 1][j & 1] = *(const u32x4*)(sp_base(wsp, panel, K5_R + (j >> 1)) + wvo[j & 1]);
  });
  gelu8(z1[0], ln[0], z1[1], ln[1]); gelu8(z1[2], ln[2], z1[3], ln[3]);
#pragma unroll
  for (int mm = 0; mm < 2; ++mm)
    *(u32x4*)(smem + L4_Z1 + wid * 2048 + mm * 1024 + lane * 16) = (u32x4){pack2(z1[2 * mm][0], z1[2 * mm][1]), pack2(z1[2 * mm][2], z1[2 * mm][3]), pack2(z1[2 * mm + 1][0], z1[2 * mm + 1][1]), pack2(z1[2 * mm + 1][2], z1[2 * mm + 1][3])};
  TS(20);
  BAR();
  TS(21);
  char* tr = smem + L4_TR + wid * 2048;
#pragma unroll
  for (int m = 0; m < 4; ++m) {
    const f32x2 f = *(const f32x2*)(fin + (m * 16 + r) * 2);
    const f32x4 v = z2[m] * f[0] - f[1];
    *(u32x2*)(tr + (m * 16 + r) * 32 + g * 8) = (u32x2){pack2(v[0], v[1]), pack2(v[2], v[3])};
  }
  {
    const char* p = tr + (8 * g + (r >> 2)) * 32 + (r & 3) * 8;
    const bf16x4 lo0 = lds_tr(p), hi0 = lds_tr(p + 128), lo1 = lds_tr(p + 1024), hi1 = lds_tr(p + 1152);
    stc16(rs, bo, OFF_Z2 + (panel * 2 * 54 + u0 + wid) * 1024, __builtin_bit_cast(u32x4, (bf16x8)__builtin_shufflevector(lo0, hi0, 0, 1, 2, 3, 4, 5, 6, 7)));
    stc16(rs, bo, OFF_Z2 + ((panel * 2 + 1) * 54 + u0 + wid) * 1024, __builtin_bit_cast(u32x4, (bf16x8)__builtin_shufflevector(lo1, hi1, 0, 1, 2, 3, 4, 5, 6, 7)));
  }
  SCHED();
  waitv<0>();
  win_tail(ws, wsp, smem, panel, cb, wid, lane TPASS);
  sp_ring<0, 5>(wsp, smem, panel, wid, lane);
  if (!EARLY) sp_prefetch(wr, wsp, panel, wid, lane);
}

DI void win_agent(char* __restrict__ ws, rsrc_t rs, const u16* __restrict__ wsp, char* smem, u32x4 (&wr)[16 - K5_R][2] TARG) {
  const int tid = fresh_tid(), lane = tid & 63, wid = 7;
  const int xcd = blockIdx.x & 7, idx = blockIdx.x >> 3, panel = xcd * 4 + (idx >> 3), cb = idx & 7, u0 = unit0_of(cb);
  float* lnp = (float*)(smem + L4_LN); float* st2 = (float*)(smem + L4_ST2); float* fin = (float*)(smem + L4_FIN); float* cbl = (float*)(smem + L4_CB);
  int* cnt = (int*)(smem + L4_CNT);
  __builtin_amdgcn_s_setprio(2);
  if (lane == 0) *cnt = 0;
  poll_ge(ws, lane < 8 ? C_ATT + lane : C_X1 + panel, lane < 8 ? 32 : 8);
  BAR();
  stage_x1s(ws, smem, panel, wid, tid);
  {
    u32x2 t[16]; f32x4 cp[5][2];
#pragma unroll
    for (int i = 0; i < 16; ++i) t[i] = __builtin_amdgcn_raw_buffer_load_b64(rs, lane * 8, OFF_ST1 + (panel * 16 + i) * 512, 16);
    const unsigned co = lane < 28 ? (u0 * 16 + lane * 4) * 4 : ((54 + u0) * 16 + min(lane - 28, 27) * 4) * 4;
#pragma unroll
    for (int i = 0; i < 5; ++i) { cp[i][0] = ldc16f(rs, co, OFF_CBP + i * 16384); cp[i][1] = ldc16f(rs, co, OFF_CBP + i * 16384 + 8192); }
    float s = 0.f, q = 0.f;
#pragma unroll
    for (int i = 0; i < 16; ++i) { s += __uint_as_float(t[i][0]); q += __uint_as_float(t[i][1]); }
    const float mean = s * (1.f / 640.f), var = fmaxf(q * (1.f / 640.f) - mean * mean, 0.f);
    const float den = ln_den(var), ri = 1.f / den;
    *(f32x4*)(lnp + lane * 4) = (f32x4){den, -rbf(mean), GC1 * ri, GC2 * ri * ri * ri};
#pragma unroll
    for (int v = 0; v < 2; ++v) *(f32x4*)(cbl + v * 256 + lane * 4) = ((cp[0][v] + cp[1][v]) + (cp[2][v] + cp[3][v])) + cp[4][v];
  }
  waitv<0>();
  BAR();
  __builtin_amdgcn_s_setprio(0);
  __builtin_amdgcn_s_sleep(8);
  sp_prefetch(wr, wsp, panel, wid, lane);
#pragma nounroll
  for (int w = 0; w < 8; ++w) sp_ring<5, 7>(wsp, smem, panel, w, lane);
  __builtin_amdgcn_s_setprio(2);
#pragma nounroll
  for (unsigned spin = 0; spin < (1u << 24); ++spin) {
    if (__hip_atomic_load(cnt, __ATOMIC_RELAXED, __HIP_MEMORY_SCOPE_WORKGROUP) >= 7) break;
    __builtin_amdgcn_s_sleep(1);
  }
  asm volatile("" ::: "memory");
  float s, q;
  {
    f32x2 t[7];
#pragma unroll
    for (int i = 0; i < 7; ++i) t[i] = *(const f32x2*)(st2 + (i * 64 + lane) * 2);
    s = t[0][0]; q = t[0][1];
#pragma unroll
    for (int i = 1; i < 6; ++i) { s = opq(s + t[i][0]); q = opq(q + t[i][1]); }
    if (cb < 6) { s = opq(s + t[6][0]); q = opq(q + t[6][1]); }
  }
  GAS u64* xch = (GAS u64*)(ws + OFF_XCH) + panel * 512;
  unsigned qb = __float_as_uint(q); if (qb == ~0u) qb = 0x7fc00000u;
  __hip_atomic_store(xch + cb * 64 + lane, ((u64)qb << 32) | __float_as_uint(s), __ATOMIC_RELAXED, __HIP_MEMORY_SCOPE_AGENT);
  TS(19);
  u64 v[8];
#pragma nounroll
  for (unsigned spin = 0; spin < (1u << 21); ++spin) {
    bool ok = true;
#pragma unroll
    for (int i = 0; i < 8; ++i) { v[i] = __hip_atomic_load(xch + i * 64 + lane, __ATOMIC_RELAXED, __HIP_MEMORY_SCOPE_AGENT); ok = ok && ((unsigned)(v[i] >> 32) != ~0u); }
    if (__all(ok)) break;
    __builtin_amdgcn_s_sleep(1);
  }
  s = 0.f; q = 0.f;
#pragma unroll
  for (int i = 0; i < 8; ++i) { s = opq(s + __uint_as_float((unsigned)v[i])); q = opq(q + __uint_as_float((unsigned)(v[i] >> 32))); }
  const float mean = opq(s * (1.f / 864.f)), var = fmaxf(opq(q * (1.f / 864.f)) - mean * mean, 0.f);
  const float ri = 1.f / ln_den(var);
  *(f32x2*)(fin + lane * 2) = (f32x2){ri, rbf(mean) * ri};
  __builtin_amdgcn_s_setprio(0);
  TS(20);
  BAR();
  __builtin_amdgcn_s_sleep(6);
  sp_ring<0, 5>(wsp, smem, panel, wid, lane);
  win_tail(ws, wsp, smem, panel, cb, wid, lane TPASS);
}

#define SP_LB 4
DI void phase_sp(char* __restrict__ ws, rsrc_t rs, const u16* __restrict__ bsp, char* smem, int wid, const u32x4 (&wr)[16 - K5_R][2] TARG) {
  const int tid = fresh_tid(), lane = tid & 63;
  const int xcd = blockIdx.x & 7, idx = blockIdx.x >> 3, panel = xcd * 4 + (idx >> 3), cb = idx & 7;
  const int kh = wid >> 2, up = wid & 3, u0 = unit0_of(cb) + 2 * up, nu = cb < 6 ? 7 : 6, g = lane >> 4, r = lane & 15;
  const unsigned dead[2] = {2 * up < nu ? 0u : DEAD, 2 * up + 1 < nu ? 0u : DEAD};
  const unsigned bvo[2] = {lane * 16 + dead[0], lane * 16 + 1024 + dead[1]};
  u16 bb[2];
#pragma unroll
  for (int j = 0; j < 2; ++j) bb[j] = bsp[panel * 64 + (2 * kh + j) * 16 + r];
  const char* al[2][2];
#pragma unroll
  for (int k = 0; k < 2; ++k)
#pragma unroll
    for (int j = 0; j < 2; ++j) al[k][j] = smem + kh * 8192 + (j ^ kh) * 4096 + r * 128 + ((4 * k + g) ^ (r >> 1)) * 16;
  constexpr int LB = SP_LB, PD = 3;
  bf16x8 bq[LB + 1][4], af[PD + 1][2]; f32x4 acc[2][4]; u32x4 wq[17];
  if (wid == 7) poll_ge(ws, C_Z2 + (panel >> 3) * 8 + cb, 32);
#pragma unroll
  for (int i = 0; i < 2; ++i) {
    const float b0 = bf2f(bb[0]), b1 = bf2f(bb[1]);
    acc[i][0] = (f32x4){b0, b0, b0, b0}; acc[i][1] = (f32x4){b1, b1, b1, b1}; acc[i][2] = (f32x4){}; acc[i][3] = (f32x4){};
  }
  if (kh && up < 3) waitv<2 * (16 - K5_R)>(); else waitv<0>();
  BAR();
  TS(23);
  const unsigned bso = OFF_Z2 + (kh * 2 * 54 + u0) * 1024;
#define SP_LD(s, j) (bq[(s) % (LB + 1)][j] = BF8(ldc16(rs, bvo[(j) & 1], bso + (sp_stage(s) * 4 + ((j) >> 1)) * 55296)))
#define SP_RD(t, k) (af[(t) % (PD + 1)][k] = *(const bf16x8*)(al[k][((t) >> 1) & 1] + (((t) >> 2) % K5_R) * 16384 + ((t) & 1) * 2048))
#pragma unroll
  for (int s = 0; s < LB; ++s)
#pragma unroll
    for (int j = 0; j < 4; ++j) SP_LD(s, j);
#pragma unroll
  for (int t = 0; t < PD; ++t) { SP_RD(t, 0); SP_RD(t, 1); }
  SCHED();
  sfor<64>([&](auto T) __attribute__((always_inline)) {
    constexpr int t = T, nx = t + PD, s = t >> 2, m = t & 3, tl = t + 4 * LB;
    if constexpr (nx == 16 || nx == 28 || nx == 44 || nx == 56) { BAR(); TS(32 + (nx == 28) + 2 * (nx == 44) + 3 * (nx == 56)); }
    constexpr int c = nx >= 16 && nx < 24 ? nx - 16 : nx >= 28 && nx < 34 ? nx - 20 : nx >= 44 && nx < 48 ? nx - 30 : -1;
    if constexpr (nx < 64) SP_RD(nx, 0);
    acc[0][m] = MFMA16(bq[s % (LB + 1)][0], af[t % (PD + 1)][0], acc[0][m]);
    acc[0][m] = MFMA16(bq[s % (LB + 1)][2], af[t % (PD + 1)][1], acc[0][m]); PIN(acc[0][m]);
    if constexpr (tl < 64) SP_LD(tl >> 2, m); else if constexpr (tl - 64 < 17) wq[tl - 64] = proj_ld<true>(rs, tl - 64, wid, lane);
    SCHED();
    if constexpr (nx < 64) SP_RD(nx, 1);
    acc[1][m] = MFMA16(bq[s % (LB + 1)][1], af[t % (PD + 1)][0], acc[1][m]);
    acc[1][m] = MFMA16(bq[s % (LB + 1)][3], af[t % (PD + 1)][1], acc[1][m]); PIN(acc[1][m]);
    if constexpr (c >= 0) *(u32x4*)(smem + ((c >> 1) % K5_R) * 16384 + wid * 2048 + (c & 1) * 1024 + lane * 16) = wr[c >> 1][c & 1];
    if constexpr (t == 63 && 4 * LB < 17) { _Pragma("unroll") for (int i = 4 * LB; i < 17; ++i) wq[i] = proj_ld<true>(rs, i, wid, lane); }
    SCHED();
  });
  TS(24);
  f32x4* exw = (f32x4*)(smem + 32768 + wid * 4096) + lane;
  const f32x4* exr = (const f32x4*)(smem + 32768 + (wid ^ 4) * 4096) + lane;
  u32x4 z[2];
#pragma unroll
  for (int i = 0; i < 2; ++i) z[i] = *(const u32x4*)(smem + L4_Z1 + (2 * up + i) * 2048 + kh * 1024 + lane * 16);
#pragma unroll
  for (int i = 0; i < 2; ++i)
#pragma unroll
    for (int j = 0; j < 2; ++j) exw[(i * 2 + j) * 64] = acc[i][2 + j];
  f32x4 zf[2][2];
#pragma unroll
  for (int i = 0; i < 2; ++i)
#pragma unroll
    for (int j = 0; j < 2; ++j) {
      zf[i][j] = (f32x4){bflo(z[i][2 * j]), bfhi(z[i][2 * j]), bflo(z[i][2 * j + 1]), bfhi(z[i][2 * j + 1])};
      acc[i][j] = mul4s(acc[i][j], zf[i][j]); PIN(zf[i][j]); PIN(acc[i][j]);
    }
  BAR();
  f32x4 oth[2][2];
#pragma unroll
  for (int i = 0; i < 2; ++i)
#pragma unroll
    for (int j = 0; j < 2; ++j) oth[i][j] = exr[(i * 2 + j) * 64];
#pragma unroll
  for (int i = 0; i < 2; ++i)
#pragma unroll
    for (int j = 0; j < 2; ++j) {
      const f32x4 v = acc[i][j] + zf[i][j] * oth[i][j];
      stc8(rs, ((g >> 1) * 16 + r) * 16 + (g & 1) * 8 + dead[i], OFF_U + (panel * 4 + 2 * kh + j) * 27648 + (u0 + i) * 512, (u32x2){pack2(v[0], v[1]), pack2(v[2], v[3])});
    }
  TS(25);
  waitv<0>();
  BAR();
  TS(26);
  if (wid == 7) arrive(ws, C_U + panel, lane);
#pragma unroll
  for (int i = 0; i < 17; ++i) proj_park<true>(smem, i, wid, lane, wq[i]);
}

__global__ __launch_bounds__(512, 2) OCC2 void k_mega(char* __restrict__ ws, const u16* __restrict__ x, const u16* __restrict__ win,
                                                     const u16* __restrict__ wsp, const u16* __restrict__ bsp, const u16* __restrict__ wout,
                                                     const u16* __restrict__ cvec, const u16* __restrict__ wmod, u16* __restrict__ out) {
  extern __shared__ __attribute__((aligned(16))) char smem[];
  const int wid = __builtin_amdgcn_readfirstlane(threadIdx.x >> 6);
  const rsrc_t rs = __builtin_amdgcn_make_buffer_rsrc(ws, 0, WS_USED, 0x00020000);
  TDECL; TS(0);
  u32x2 res[3];
  phase_qkv(ws, rs, cvec, wmod, smem, wid TPASS);
  phase_attn(ws, rs, (const char*)wsp, win, wout, smem, wid TPASS);
  u32x4 wr[16 - K5_R][2];
  auto nohook = []() __attribute__((always_inline)) {};
  if (wid < 4) {
    bf16x8 b2[20];
    proj_body<false, 0>(ws, rs, x, out, smem, wid, res, [&]() __attribute__((always_inline)) { win_prefetch(rs, wid, b2); } TPASS);
    win_body<true>(ws, rs, wsp, smem, wid, b2, wr TPASS);
  } else {
    proj_body<false, 1>(ws, rs, x, out, smem, wid, res, nohook TPASS);
    if (wid == 7) win_agent(ws, rs, wsp, smem, wr TPASS);
    else { bf16x8 b2[20]; win_prefetch(rs, wid, b2); win_body<false>(ws, rs, wsp, smem, wid, b2, wr TPASS); }
  }
  phase_sp(ws, rs, bsp, smem, wid, wr TPASS);
  if (wid < 4) proj_body<true, 0>(ws, rs, x, out, smem, wid, res, nohook TPASS);
  else proj_body<true, 1>(ws, rs, x, out, smem, wid, res, nohook TPASS);
  TS(29);
  TDUMP();
}

extern "C" void kernel_launch(void* const* d_in, const int* in_sizes, int n_in, void* d_out, int out_size, void* d_ws, size_t ws_size,
                              hipStream_t stream) {
  const u16* x = (const u16*)d_in[0];
  const u16* wqkv = (const u16*)d_in[1];
  const u16* wo = (const u16*)d_in[2];
  const u16* win = (const u16*)d_in[4];
  const u16* wsp = (const u16*)d_in[5];
  const u16* bsp = (const u16*)d_in[6];
  const u16* wout = (const u16*)d_in[7];
  const u16* cvec = (const u16*)d_in[8];
  const u16* wmod = (const u16*)d_in[9];
  char* ws = (char*)d_ws;
  (void)hipFuncSetAttribute((const void*)k_mega, hipFuncAttributeMaxDynamicSharedMemorySize, 163840);
  k0_prep<<<K0_GRID, 256, 0, stream>>>(x, wqkv, wo, cvec, wmod, ws);
  k_mega<<<256, 512, 163840, stream>>>(ws, x, win, wsp, bsp, wout, cvec, wmod, (u16*)d_out);
}
```

```cpp
#include <hip/hip_runtime.h>
#include <hip/hip_bf16.h>
#include <stdint.h>
#include <utility>

typedef __attribute__((ext_vector_type(8))) short bf16x8;
typedef __attribute__((ext_vector_type(4))) short bf16x4;
typedef __attribute__((ext_vector_type(2))) float f32x2;
typedef __attribute__((ext_vector_type(4))) float f32x4;
typedef __attribute__((ext_vector_type(16))) float f32x16;
typedef __attribute__((ext_vector_type(2))) unsigned u32x2;
typedef __attribute__((ext_vector_type(4))) unsigned u32x4;
typedef __attribute__((ext_vector_type(2))) __bf16 bf2_t;
typedef __attribute__((ext_vector_type(2))) _Float16 h2_t;
typedef unsigned short u16;
typedef unsigned long long u64;
#define LAS __attribute__((address_space(3)))
#define GAS __attribute__((address_space(1)))
#define DI __device__ __forceinline__

constexpr int D_ = 640, NQKV = 1920, F_ = 1728;

constexpr unsigned OFF_CNT = 0;
constexpr unsigned OFF_MOD = 65536;
constexpr unsigned OFF_MODP = OFF_MOD + 16384;
constexpr unsigned OFF_CBP = OFF_MODP + 61440;
constexpr unsigned OFF_XCH = OFF_CBP + 5 * 16384;
constexpr unsigned OFF_ST1 = OFF_XCH + 131072;
constexpr unsigned OFF_XHAT = OFF_ST1 + 262144;
constexpr unsigned OFF_WQKV = OFF_XHAT + 2621440;
constexpr unsigned OFF_WO = OFF_WQKV + 2457600;
constexpr unsigned OFF_WIN = OFF_WO + 819200;
constexpr unsigned OFF_WOUT = OFF_WIN + 2211840;
constexpr unsigned OFF_Q = OFF_WOUT + 1105920;
constexpr unsigned OFF_KV = OFF_Q + 32 * 81920;
constexpr unsigned OFF_O = OFF_KV + 32 * 163840;
constexpr unsigned OFF_X1S = OFF_O + 2621440;
constexpr unsigned OFF_Z2 = OFF_X1S + 2621440;
constexpr unsigned OFF_U = OFF_Z2 + 3538944;
constexpr unsigned WS_USED = OFF_U + 3538944;
constexpr int C_QKV = 0, C_MOD = 15  , C_ATT = 128, C_REL = 136, C_X1 = 144, C_U = 176, C_Z2 = 208  ;

#define STAMP(kid, k)
#define TDECL
#define TS(k)
#define TDUMP()
DI unsigned pack2(float lo, float hi) { return __builtin_bit_cast(unsigned, __builtin_convertvector((f32x2){lo, hi}, bf2_t)); }
DI unsigned pack2s(float lo, float hi) { unsigned r; asm("v_cvt_pk_bf16_f32 %0, %1, %2" : "=v"(r) : "v"(lo), "v"(hi)); return r; }
DI float opq(float x) { asm("" : "+v"(x)); return x; }
DI f32x4 fma4s(f32x4 a, f32x4 b, f32x4 c) { return (f32x4){opq(__builtin_fmaf(a[0], b[0], c[0])), opq(__builtin_fmaf(a[1], b[1], c[1])), opq(__builtin_fmaf(a[2], b[2], c[2])), opq(__builtin_fmaf(a[3], b[3], c[3]))}; }
DI f32x4 mul4s(f32x4 a, f32x4 b) { return (f32x4){opq(a[0] * b[0]), opq(a[1] * b[1]), opq(a[2] * b[2]), opq(a[3] * b[3])}; }
DI unsigned pack2h(float lo, float hi) { return __builtin_bit_cast(unsigned, __builtin_convertvector((f32x2){lo, hi}, h2_t)); }
DI float bflo(unsigned u) { return __uint_as_float(u << 16); }
DI float bfhi(unsigned u) { return __uint_as_float(u & 0xffff0000u); }
DI float rbf(float x) { return bflo(pack2(x, x)); }
DI float bf2f(u16 b) { return __uint_as_float((unsigned)b << 16); }
#define MFMA32(a, b, c) __builtin_amdgcn_mfma_f32_32x32x16_bf16((a), (b), (c), 0, 0, 0)
#define MFMA32K8(a, b, c) __builtin_amdgcn_mfma_f32_32x32x8bf16_1k((a), (b), (c), 0, 0, 0)
#define MFMA16(a, b, c) __builtin_amdgcn_mfma_f32_16x16x32_bf16((a), (b), (c), 0, 0, 0)
#define BAR() asm volatile("s_waitcnt lgkmcnt(0)\n\ts_barrier" ::: "memory")
#define SCHED() __builtin_amdgcn_sched_barrier(0)
#define PIN(x) asm volatile("" : "+v"(x))
#define OCC2 __attribute__((amdgpu_waves_per_eu(2, 2)))
#define BF8(x) __builtin_bit_cast(bf16x8, x)
#define BF4(x) __builtin_bit_cast(bf16x4, x)
template <int N> DI void waitv() { asm volatile("s_waitcnt vmcnt(%0)" ::"i"(N) : "memory"); }
template <class F, int... I> DI void sfor_impl(F&& f, std::integer_sequence<int, I...>) { (f(std::integral_constant<int, I>{}), ...); }
template <int N, class F> DI void sfor(F&& f) { sfor_impl(f, std::make_integer_sequence<int, N>{}); }
typedef short v4i16_t __attribute__((ext_vector_type(4)));
DI bf16x4 lds_tr(const char* p) { return __builtin_bit_cast(bf16x4, __builtin_amdgcn_ds_read_tr16_b64_v4i16((LAS v4i16_t*)p)); }
DI void glds16(const void* g, unsigned lds_base) {
  unsigned sv;
  asm volatile("s_mov_b32 %0, m0\n\ts_mov_b32 m0, %2\n\ts_nop 0\n\tglobal_load_lds_dwordx4 %1, off\n\ts_mov_b32 m0, %0" : "=&s"(sv) : "v"(g), "s"(lds_base) : "memory");
}
DI void glds16c(const void* g, unsigned lds_base) {
  unsigned sv;
  asm volatile("s_mov_b32 %0, m0\n\ts_mov_b32 m0, %2\n\ts_nop 0\n\tglobal_load_lds_dwordx4 %1, off sc1\n\ts_mov_b32 m0, %0" : "=&s"(sv) : "v"(g), "s"(lds_base) : "memory");
}
DI void glds4c(const void* g, unsigned lds_base) {
  unsigned sv;
  asm volatile("s_mov_b32 %0, m0\n\ts_mov_b32 m0, %2\n\ts_nop 0\n\tglobal_load_lds_dword %1, off sc1\n\ts_mov_b32 m0, %0" : "=&s"(sv) : "v"(g), "s"(lds_base) : "memory");
}
typedef __amdgpu_buffer_rsrc_t rsrc_t;
DI u32x4 ldc16(rsrc_t rs, unsigned voff, unsigned soff) { return __builtin_amdgcn_raw_buffer_load_b128(rs, voff, soff, 16); }
DI unsigned ldc4(rsrc_t rs, unsigned voff, unsigned soff) { return __builtin_amdgcn_raw_buffer_load_b32(rs, voff, soff, 16); }
DI void stc16(rsrc_t rs, unsigned voff, unsigned soff, u32x4 v) { __builtin_amdgcn_raw_buffer_store_b128(v, rs, voff, soff, 16); asm volatile("s_nop 1" ::"v"(v) : "memory"); }
DI void stc8(rsrc_t rs, unsigned voff, unsigned soff, u32x2 v) { __builtin_amdgcn_raw_buffer_store_b64(v, rs, voff, soff, 16); }
DI void stc4(rsrc_t rs, unsigned voff, unsigned soff, unsigned v) { __builtin_amdgcn_raw_buffer_store_b32(v, rs, voff, soff, 16); }
DI f32x4 ldc16f(rsrc_t rs, unsigned voff, unsigned soff) { return __builtin_bit_cast(f32x4, ldc16(rs, voff, soff)); }
DI void arrive(char* ws, int c, int lane) { if (lane == 0) __hip_atomic_fetch_add((GAS unsigned*)(ws + OFF_CNT + c * 256), 1u, __ATOMIC_RELAXED, __HIP_MEMORY_SCOPE_AGENT); }
DI void poll_ge(char* ws, int c, unsigned thr) {
  const GAS unsigned* p = (const GAS unsigned*)(ws + OFF_CNT + c * 256);
#pragma nounroll
  for (unsigned spin = 0; spin < (1u << 21); ++spin) {
    if (__all(__hip_atomic_load(p, __ATOMIC_RELAXED, __HIP_MEMORY_SCOPE_AGENT) >= thr)) break;
    __builtin_amdgcn_s_sleep(1);
  }
  asm volatile("" ::: "memory");
}
DI int fresh_tid() { int t = threadIdx.x; asm volatile("" : "+v"(t)); return t; }
DI void l2touch(const void* g, unsigned lds_scratch) {
  unsigned sv;
  asm volatile("s_mov_b32 %0, m0\n\ts_mov_b32 m0, %2\n\ts_nop 0\n\tglobal_load_lds_dword %1, off\n\ts_mov_b32 m0, %0" : "=&s"(sv) : "v"(g), "s"(lds_scratch) : "memory");
}
#define LDNT(p) __builtin_nontemporal_load(p)
DI float silu_bf(u16 c) { const float f = __uint_as_float((unsigned)c << 16); const float v = f / (1.f + __expf(-f)); return __uint_as_float(__builtin_bit_cast(unsigned, __builtin_convertvector((f32x2){v, v}, bf2_t)) << 16); }
DI unsigned lds_addr(const void* p) { return (unsigned)(uintptr_t)(const LAS void*)p; }
DI float xhalf(float v) {
  auto rr = __builtin_amdgcn_permlane32_swap(__float_as_uint(v), __float_as_uint(v), false, false);
  return __uint_as_float((threadIdx.x & 32) ? rr[0] : rr[1]);
}
DI float sum_g(float v) {
  auto a = __builtin_amdgcn_permlane16_swap(__float_as_uint(v), __float_as_uint(v), false, false);
  v = __uint_as_float(a[0]) + __uint_as_float(a[1]);
  auto b = __builtin_amdgcn_permlane32_swap(__float_as_uint(v), __float_as_uint(v), false, false);
  return __uint_as_float(b[0]) + __uint_as_float(b[1]);
}
template <int CTRL> DI float dpp_add(float v) { return v + __uint_as_float(__builtin_amdgcn_update_dpp(0u, __float_as_uint(v), CTRL, 0xf, 0xf, true)); }
DI float sum16(float v) {
  v = dpp_add<0x128>(v); v = dpp_add<0x124>(v); v = dpp_add<0x122>(v); return dpp_add<0x121>(v);
}
DI u32x4 modulate8(u32x4 v, f32x4 sc0, f32x4 sc1, f32x4 sh0, f32x4 sh1) {
  unsigned o0, o1, o2, o3; float t0, t1, t2, t3;
  asm volatile(
      "v_fma_mix_f32 %0, %8, %12, %20 op_sel_hi:[1,0,0]\n\tv_fma_mix_f32 %4, %8, %13, %21 op_sel:[1,0,0] op_sel_hi:[1,0,0]\n\t"
      "v_fma_mix_f32 %1, %9, %14, %22 op_sel_hi:[1,0,0]\n\tv_fma_mix_f32 %5, %9, %15, %23 op_sel:[1,0,0] op_sel_hi:[1,0,0]\n\t"
      "v_fma_mix_f32 %2, %10, %16, %24 op_sel_hi:[1,0,0]\n\tv_fma_mix_f32 %6, %10, %17, %25 op_sel:[1,0,0] op_sel_hi:[1,0,0]\n\t"
      "v_fma_mix_f32 %3, %11, %18, %26 op_sel_hi:[1,0,0]\n\tv_fma_mix_f32 %7, %11, %19, %27 op_sel:[1,0,0] op_sel_hi:[1,0,0]\n\t"
      "v_cvt_pk_bf16_f32 %0, %0, %4\n\tv_cvt_pk_bf16_f32 %1, %1, %5\n\tv_cvt_pk_bf16_f32 %2, %2, %6\n\tv_cvt_pk_bf16_f32 %3, %3, %7"
      : "=&v"(o0), "=&v"(o1), "=&v"(o2), "=&v"(o3), "=&v"(t0), "=&v"(t1), "=&v"(t2), "=&v"(t3)
      : "v"(v[0]), "v"(v[1]), "v"(v[2]), "v"(v[3]), "v"(sc0[0]), "v"(sc0[1]), "v"(sc0[2]), "v"(sc0[3]), "v"(sc1[0]), "v"(sc1[1]), "v"(sc1[2]), "v"(sc1[3]),
        "v"(sh0[0]), "v"(sh0[1]), "v"(sh0[2]), "v"(sh0[3]), "v"(sh1[0]), "v"(sh1[1]), "v"(sh1[2]), "v"(sh1[3]));
  return (u32x4){o0, o1, o2, o3};
}
DI float ln_den(float var) { return rbf(__builtin_sqrtf(rbf(rbf(var) + 1e-5f))); }
DI unsigned a16_off(int n, int r) { return (n >> 5) * 1024 + (((n & 31) >> 3) * 16 + r) * 16 + (n & 4) * 2; }
DI int unit0_of(int j) { return j < 6 ? 7 * j : 42 + 6 * (j - 6); }

DI void K0ST(char* p, u32x4 v) { *(u32x4*)p = v; }
constexpr int K0_MOD = 240, K0_LN = 128, K0_RL = 240, K0_WO = 50, K0_INIT = 12;
constexpr int K0_GRID = K0_MOD + K0_LN + K0_RL + K0_WO + K0_INIT;

__global__ __launch_bounds__(256) void k0_prep(const u16* __restrict__ x, const u16* __restrict__ wqkv, const u16* __restrict__ wo, const u16* __restrict__ cvec,
                                               const u16* __restrict__ wmod, char* __restrict__ ws) {
  __shared__ __attribute__((aligned(16))) char smem[4 * 4608];
  const int tid = threadIdx.x, lane = tid & 63, wid = __builtin_amdgcn_readfirstlane(tid >> 6);
  int b = blockIdx.x;
  STAMP(0, 0);
  if (b < K0_MOD) {
    const int cg = b >> 2, ks = b & 3, c = tid & 7, row0 = ks * 160 + (tid >> 3), col0 = cg * 64;
    u32x4 w[5]; u16 cv[5];
#pragma unroll
    for (int i = 0; i < 5; ++i) { w[i] = LDNT((const u32x4*)(wmod + (size_t)(row0 + 32 * i) * 3840 + col0 + c * 8)); cv[i] = cvec[row0 + 32 * i]; }
    float acc[8] = {0, 0, 0, 0, 0, 0, 0, 0};
#pragma unroll
    for (int i = 0; i < 5; ++i) {
      const float s = silu_bf(cv[i]);
#pragma unroll
      for (int e = 0; e < 4; ++e) { acc[2 * e] += s * bflo(w[i][e]); acc[2 * e + 1] += s * bfhi(w[i][e]); }
    }
#pragma unroll
    for (int e = 0; e < 8; ++e) acc[e] = sum_g(dpp_add<0x128>(acc[e]));
    float* red = (float*)smem;
    if (lane < 8) { *(f32x4*)(red + wid * 64 + lane * 8) = (f32x4){acc[0], acc[1], acc[2], acc[3]}; *(f32x4*)(red + wid * 64 + lane * 8 + 4) = (f32x4){acc[4], acc[5], acc[6], acc[7]}; }
    __syncthreads();
    if (tid < 64) ((float*)(ws + OFF_MODP))[ks * 3840 + cg * 64 + tid] = (red[tid] + red[64 + tid]) + (red[128 + tid] + red[192 + tid]);
    STAMP(0, 1); STAMP(0, 13);
    return;
  }
  b -= K0_MOD;
  if (b < K0_LN) {
    b = (b & 7) * 16 + (b >> 3);
    const int row = b * 16 + wid * 4 + (lane >> 4), c = lane & 15;
    const u16* xr = x + (size_t)row * D_ + c * 8;
    u32x4 v[5];
#pragma unroll
    for (int i = 0; i < 5; ++i) v[i] = *(const u32x4*)(xr + i * 128);
    float s = 0.f, q = 0.f;
#pragma unroll
    for (int i = 0; i < 5; ++i)
#pragma unroll
      for (int e = 0; e < 4; ++e) { const float a = bflo(v[i][e]), d = bfhi(v[i][e]); s += a + d; q += a * a + d * d; }
    s = sum16(s); q = sum16(q);
    const float mean = s * (1.f / 640.f); const float var = fmaxf(q * (1.f / 640.f) - mean * mean, 0.f);
    const float rinv = 1.f / ln_den(var), mb = rbf(mean) * rinv;
    char* dst = ws + OFF_XHAT + (size_t)row * 1280 + c * 16;
#pragma unroll
    for (int i = 0; i < 5; ++i) {
      u32x4 o;
#pragma unroll
      for (int e = 0; e < 4; ++e) o[e] = pack2h(bflo(v[i][e]) * rinv - mb, bfhi(v[i][e]) * rinv - mb);
      K0ST(dst + i * 256, o);
    }
    STAMP(0, 2); STAMP(0, 13);
    return;
  }
  b -= K0_LN;
  if (b < K0_RL) {
    const int kt = b / 12, G = b % 12, sec = G >> 2, gq = G & 3;
    u32x4 v[3];
#pragma unroll
    for (int i = 0; i < 3; ++i) { const int ci = min(i * 256 + tid, 639); v[i] = LDNT((const u32x4*)(wqkv + (size_t)(kt * 32 + ci / 20) * NQKV + G * 160 + (ci % 20) * 8)); }
#pragma unroll
    for (int i = 0; i < 3; ++i) { const int ci = min(i * 256 + tid, 639); *(u32x4*)(smem + (ci / 20) * 336 + (ci % 20) * 16) = v[i]; }
    __syncthreads();
    const int g = lane >> 4, q = (lane & 15) >> 2, p = lane & 3;
    for (int f = wid; f < 10; f += 4) {
      const int t = f >> 1, x2 = f & 1;
      const int col = sec == 2 ? 16 * f + 4 * p : t < 4 ? 20 * (2 * t + (p >> 1)) + 8 * (p & 1) + 4 * x2 : 20 * (4 * x2 + p) + 16;
      const int T = sec == 2 ? 40 + gq * 5 + t : sec * 20 + (t < 4 ? 4 * gq + t : 16 + gq);
      const char* pp = smem + (8 * g + q) * 336 + col * 2;
      const bf16x4 lo = lds_tr(pp), hi = lds_tr(pp + 4 * 336);
      K0ST(ws + OFF_WQKV + (((size_t)T * 20 + kt) * 2 + x2) * 1024 + lane * 16, __builtin_bit_cast(u32x4, (bf16x8)__builtin_shufflevector(lo, hi, 0, 1, 2, 3, 4, 5, 6, 7)));
    }
    STAMP(0, 3); STAMP(0, 13);
    return;
  }
  b -= K0_RL;
  if (b < K0_WO) {
    const int job = b * 4 + wid, kt = job / 10, ng = job % 10, j8 = lane >> 3;
    char* t = smem + wid * 4608;
    u32x4 v[4];
#pragma unroll
    for (int i = 0; i < 4; ++i) {
      const int c = 4 * kt + i, row = c < 64 ? 20 * (c >> 1) + 4 * (c & 1) + (j8 & 3) + 8 * (j8 >> 2) : 20 * (2 * (c - 64) + (j8 >> 2)) + 16 + (j8 & 3);
      v[i] = LDNT((const u32x4*)(wo + (size_t)row * D_ + ng * 64 + (lane & 7) * 8));
    }
#pragma unroll
    for (int i = 0; i < 4; ++i) *(u32x4*)(t + (i * 8 + j8) * 144 + (lane & 7) * 16) = v[i];
    const char* pp = t + (8 * (lane >> 4) + ((lane & 15) >> 2)) * 144 + (lane & 3) * 8;
#pragma unroll
    for (int f = 0; f < 4; ++f) {
      const bf16x4 lo = lds_tr(pp + f * 32), hi = lds_tr(pp + 4 * 144 + f * 32);
      K0ST(ws + OFF_WO + ((size_t)(ng * 4 + f) * 20 + kt) * 1024 + lane * 16, __builtin_bit_cast(u32x4, (bf16x8)__builtin_shufflevector(lo, hi, 0, 1, 2, 3, 4, 5, 6, 7)));
    }
    return;
  }
  b -= K0_WO;
  {
    u32x4* p = (u32x4*)(ws + (b < 8 ? OFF_XCH + b * 16384 : OFF_CNT + (b - 8) * 16384)) + tid;
    const unsigned f = b < 8 ? ~0u : 0u;
#pragma unroll
    for (int i = 0; i < 4; ++i) p[i * 256] = (u32x4){f, f, f, f};
  }
}

constexpr float QSCALE = 0.22360679774997896f * 1.4426950408889634f;
#define K1W 3
#define K1WB 5
#define K1WB0 2
constexpr int K1_W = K1W, K1_WB = K1WB, K1_WB0 = K1WB0;

template <bool ISV>
DI void k1_mma(const char* __restrict__ bsrc, const char* smem, int lane, f32x4 (&acc)[8][2]) {
  constexpr int PD = 4;
  bf16x8 bfr[K1_WB + 1][4], af[PD + 1][2];
  const char* lds[2];
#pragma unroll
  for (int k = 0; k < 2; ++k) lds[k] = smem + k * 8192 + (lane ^ (4 * k + (lane >> 4))) * 16;
#pragma unroll
  for (int i = 0; i < 4 * K1_WB0; ++i) bfr[i >> 2][i & 3] = *(const bf16x8*)(bsrc + i * 1024);
#pragma unroll
  for (int m = 0; m < 8; ++m) { acc[m][0] = (f32x4){}; acc[m][1] = (f32x4){}; }
  BAR();
#pragma unroll
  for (int i = 4 * K1_WB0; i < 4 * K1_WB; ++i) bfr[i >> 2][i & 3] = *(const bf16x8*)(bsrc + i * 1024);
  BAR();
  __builtin_amdgcn_s_setprio(1);
#define K1_RD(t, k) (af[(t) % (PD + 1)][k] = *(const bf16x8*)(lds[k] + (((t) >> 3) % 3) * 16384 + ((t) & 7) * 1024))
#define K1_MM(c, b, a) (c = ISV ? MFMA16(a, b, c) : MFMA16(b, a, c))
#pragma unroll
  for (int t = 0; t < PD; ++t) { K1_RD(t, 0); K1_RD(t, 1); }
  SCHED();
  sfor<80>([&](auto T) __attribute__((always_inline)) {
    constexpr int t = T, s = t >> 3, m = t & 7, nx = t + PD;
    if constexpr (nx < 80 && (nx & 7) == 0) asm volatile("s_barrier" ::: "memory");
    if constexpr (nx < 80) K1_RD(nx, 0);
    K1_MM(acc[m][0], bfr[s % (K1_WB + 1)][0], af[t % (PD + 1)][0]); K1_MM(acc[m][0], bfr[s % (K1_WB + 1)][2], af[t % (PD + 1)][1]); PIN(acc[m][0]);
    SCHED();
    if constexpr (nx < 80) K1_RD(nx, 1);
    if constexpr (s + K1_WB < 10 && (m & 1)) bfr[(s + K1_WB) % (K1_WB + 1)][m >> 1] = *(const bf16x8*)(bsrc + ((s + K1_WB) * 4 + (m >> 1)) * 1024);
    K1_MM(acc[m][1], bfr[s % (K1_WB + 1)][1], af[t % (PD + 1)][0]); K1_MM(acc[m][1], bfr[s % (K1_WB + 1)][3], af[t % (PD + 1)][1]); PIN(acc[m][1]);
    SCHED();
  });
  __builtin_amdgcn_s_setprio(0);
}

#define TARG
#define TPASS

DI void phase_qkv(char* __restrict__ ws, rsrc_t rs, char* smem, int wid TARG) {
  const int tid = fresh_tid(), lane = tid & 63;
  const int xcd = blockIdx.x & 7, idx = blockIdx.x >> 3;
  int ctr;
  if ((xcd & 1) && idx >= 28) {
    ctr = C_MOD;
    if (tid < 160) {
      const int col = 1280 + ((xcd >> 1) * 4 + idx - 28) * 160 + tid;
      const float* pp = (const float*)(ws + OFF_MODP) + col;
      float v = rbf((pp[0] + pp[3840]) + (pp[7680] + pp[11520]));
      if (col / 640 == 4) v = rbf(1.f + v);
      stc4(rs, col * 4, OFF_MOD, __float_as_uint(v));
    }
  } else {
    const int mt = (xcd >> 1) * 4 + (idx & 3), nt = (xcd & 1) * 8 + (idx >> 2);
    ctr = C_QKV + nt;
    if (wid < 4) {
      float* cst = (float*)(smem + 49152);
      const int c = lane & 7, rr = lane >> 3;
      const char* src = ws + OFF_XHAT + (size_t)(mt * 128 + wid * 32 + rr) * 1280 + c * 16;
      char* wdst = smem + (c >> 2) * 8192 + wid * 2048 + (c & 3) * 256 + (rr ^ c) * 16;
      u32x4 a[10][4];
      f32x4 c0[4], c1[4];
#pragma unroll
      for (int i = 0; i < 4; ++i) { c0[i] = *(const f32x4*)(ws + OFF_MODP + i * 15360 + tid * 16); if (wid == 0) c1[i] = *(const f32x4*)(ws + OFF_MODP + i * 15360 + 4096 + tid * 16); }
#pragma unroll
      for (int s = 0; s < K1_W; ++s)
#pragma unroll
        for (int m = 0; m < 4; ++m) a[s][m] = *(const u32x4*)(src + m * 10240 + s * 128);
      {
        f32x4 v = (c0[0] + c0[1]) + (c0[2] + c0[3]); const float one = tid >= 160 ? 1.f : 0.f;
        *(f32x4*)(cst + tid * 4) = (f32x4){rbf(one + rbf(v[0])), rbf(one + rbf(v[1])), rbf(one + rbf(v[2])), rbf(one + rbf(v[3]))};
        if (wid == 0) { v = (c1[0] + c1[1]) + (c1[2] + c1[3]); *(f32x4*)(cst + 1024 + tid * 4) = (f32x4){rbf(1.f + rbf(v[0])), rbf(1.f + rbf(v[1])), rbf(1.f + rbf(v[2])), rbf(1.f + rbf(v[3]))}; }
      }
      BAR();
      const float* cp = cst + c * 8;
      f32x4 kc[10][4];
#define K1_KC(s) do { kc[s][0] = *(const f32x4*)(cp + (s) * 64); kc[s][1] = *(const f32x4*)(cp + (s) * 64 + 4); kc[s][2] = *(const f32x4*)(cp + (s) * 64 + 640); kc[s][3] = *(const f32x4*)(cp + (s) * 64 + 644); } while (0)
#pragma unroll
      for (int s = 0; s < 6; ++s) K1_KC(s);
      SCHED();
      u32x4 o[2][4];
#define K1_XF(s, m) (o[(s) & 1][m] = modulate8(a[s][m], kc[s][2], kc[s][3], kc[s][0], kc[s][1]))
#define K1_WR(s, m) (*(u32x4*)(wdst + ((s) % 3) * 16384 + ((m) >> 1) * 1024 + ((m) & 1) * 128) = o[(s) & 1][m])
#pragma unroll
      for (int m = 0; m < 4; ++m) { K1_XF(0, m); K1_WR(0, m); }
      BAR();
      TS(1);
#pragma unroll
      for (int m = 0; m < 4; ++m) a[K1_W][m] = *(const u32x4*)(src + m * 10240 + K1_W * 128);
#pragma unroll
      for (int m = 0; m < 4; ++m) K1_XF(1, m);
      SCHED();
#pragma unroll
      for (int s = 1; s < 10; ++s) {
#pragma unroll
        for (int m = 0; m < 4; ++m) {
          if (m == 0) K1_WR(s, 0);
          if (m < 3) K1_WR(s, m + 1);
          if (s + K1_W < 10) a[s + K1_W][m] = *(const u32x4*)(src + m * 10240 + (s + K1_W) * 128);
          if (s < 5) kc[s + 5][m] = *(const f32x4*)(cp + (s + 5) * 64 + (m & 1) * 4 + (m >> 1) * 640);
          SCHED();
          if (s < 9) K1_XF(s + 1, m);
          SCHED();
        }
        asm volatile("" :: "v"(o[s & 1][0]), "v"(o[s & 1][1]), "v"(o[s & 1][2]), "v"(o[s & 1][3]));
        BAR();
      }
    } else {
      const int cw = wid - 4;
      const char* bsrc = ws + OFF_WQKV + (size_t)(nt * 4 + cw) * 40 * 1024 + lane * 16;
      f32x4 acc[8][2];
      const int r = lane & 15, g = lane >> 4;
      if (nt >= 10) {
        k1_mma<true>(bsrc, smem, lane, acc);
        TS(2);
        const unsigned so = OFF_KV + (mt >> 1) * 20480 + 10240 + (mt & 1) * 5120;
#pragma unroll
        for (int x = 0; x < 2; ++x) {
          const unsigned c = (nt - 10) * 128 + cw * 32 + x * 16 + r, head = c / 20, d = c % 20;
          const unsigned vo = head * 163840u + ((g & 1) * 20 + d) * 16 + (g >> 1) * 8;
#pragma unroll
          for (int m = 0; m < 8; ++m) stc8(rs, vo + (m & 3) * 640, so + (m >> 2) * 2560, (u32x2){pack2(acc[m][x][0], acc[m][x][1]), pack2(acc[m][x][2], acc[m][x][3])});
        }
      } else {
        k1_mma<false>(bsrc, smem, lane, acc);
        TS(2);
        const bool isQ = nt < 5; const float sc = isQ ? QSCALE : 1.f;
        const unsigned so = isQ ? OFF_Q + mt * 5120 : OFF_KV + (mt >> 1) * 20480 + (mt & 1) * 5120, hs = isQ ? 81920u : 163840u;
        const int n5 = isQ ? nt : nt - 5;
        if (n5 < 4) {
          const unsigned vo = (2 * (n5 * 4 + cw) + (g >> 1)) * hs + (g & 1) * 512 + r * 16;
#pragma unroll
          for (int m = 0; m < 8; ++m) {
            const f32x4 v0 = acc[m][0] * sc, v1 = acc[m][1] * sc;
            stc16(rs, vo + (m & 1) * 256, so + (m >> 1) * 1280, (u32x4){pack2(v0[0], v0[1]), pack2(v0[2], v0[3]), pack2(v1[0], v1[1]), pack2(v1[2], v1[3])});
          }
        } else {
          const unsigned vo = (8 * cw + g) * hs + 1024 + r * 8;
#pragma unroll
          for (int m = 0; m < 8; ++m)
#pragma unroll
            for (int x = 0; x < 2; ++x) {
              const f32x4 v = acc[m][x] * sc;
              stc8(rs, vo + (m & 1) * 128, so + x * 4 * hs + (m >> 1) * 1280, (u32x2){pack2(v[0], v[1]), pack2(v[2], v[3])});
            }
        }
      }
    }
  }
  TS(3);
  waitv<0>();
  BAR();
  TS(4);
  if (wid == 7 && lane < 8) __hip_atomic_fetch_add((GAS unsigned*)(ws + OFF_CNT + (lane * 16 + ctr) * 256), 1u, __ATOMIC_RELAXED, __HIP_MEMORY_SCOPE_AGENT);
}

template <bool FINAL> DI constexpr int proj_pos(int f) { return FINAL ? (f < 96 ? 65536 + f * 1024 : (f - 96) * 1024) : 17408 + (f % 20 >= 10 ? 81920 : 0) + (f / 20 * 10 + f % 10) * 1024; }
template <bool FINAL> DI u32x4 proj_ld(rsrc_t rs, int i, int wid, int lane) {
  constexpr int NF = FINAL ? 135 : 100;
  return ldc16(rs, lane * 16, (FINAL ? OFF_WOUT : OFF_WO) + (((blockIdx.x >> 3) & 7) * NF + min(i * 8 + wid, NF - 1)) * 1024);
}
template <bool FINAL> DI void proj_park(char* smem, int i, int wid, int lane, u32x4 v) {
  constexpr int NF = FINAL ? 135 : 100;
  *(u32x4*)(smem + proj_pos<FINAL>(min(i * 8 + wid, NF - 1)) + lane * 16) = v;
}

DI void split3(float m, unsigned& w_h0, unsigned& w_h1) {
  const float n = -m; const float a = rbf(n), b = rbf(n - a), c = rbf(n - a - b);
  w_h0 = pack2(a, b); w_h1 = pack2(c, 0.f);
}

DI void phase_attn(char* __restrict__ ws, rsrc_t rs, const char* __restrict__ wsp, const u16* __restrict__ win, const u16* __restrict__ wout, char* smem, int wid TARG) {
  const int tid = fresh_tid(), lane = tid & 63;
  const int xcd = blockIdx.x & 7, idx = blockIdx.x >> 3, head = xcd * 4 + (idx >> 3), qb = idx & 7;
  const int r = lane & 31, h = lane >> 5, qg = wid & 3, kh = wid >> 2;
  if (wid == 7) {
    const int l7 = lane & 7;
    const int c = l7 == 0 ? head >> 3 : l7 == 1 ? 4 : l7 == 2 ? 5 + (head >> 3) : l7 == 3 ? 9 : l7 == 4 ? 10 + (20 * head) / 128 : l7 == 5 ? 10 + (20 * head + 19) / 128 : C_MOD;
    poll_ge(ws, xcd * 16 + c, 16);
  }
  BAR();
  TS(5);
  unsigned kvo = OFF_KV + head * 163840, qo = OFF_Q + head * 81920 + (qb * 8 + qg * 2) * 1280;
  const unsigned kconst = h == 0 ? 0x3f803f80u : 0x00003f80u;
  const unsigned so16 = (tid & 255) * 16, l16 = lane * 16, l2 = 1024 + r * 8 + h * 4;
  const int blk = blockIdx.x;
  u32x4 wq[13];
  for (int attempt = 0; attempt < 2; ++attempt) {
    asm volatile("" : "+s"(kvo), "+s"(qo));
    const char* kvs = ws + kvo + kh * 81920 + so16;
    const unsigned ldst = lds_addr(smem) + kh * 81920 + qg * 1024;
    u32x4 qA1w, qB1w, k1w; unsigned qA2w, qB2w, k2, m0sv;
    asm volatile(
        "buffer_load_dwordx4 %0, %7, %9, %10 offen sc1\n\tbuffer_load_dwordx4 %1, %7, %9, %10 offen offset:1280 sc1\n\tbuffer_load_dwordx4 %2, %7, %9, %11 offen sc1\n\t"
        "buffer_load_dword %3, %8, %9, %10 offen sc1\n\tbuffer_load_dword %4, %8, %9, %10 offen offset:1280 sc1\n\tbuffer_load_dword %5, %8, %9, %11 offen sc1\n\t"
        "s_mov_b32 %6, m0\n\ts_mov_b32 m0, %17\n\ts_nop 0\n\tglobal_load_lds_dwordx4 %12, off sc1\n\t"
        "s_add_u32 m0, %18, 0x1000\n\ts_nop 0\n\tglobal_load_lds_dwordx4 %13, off sc1\n\t"
        "s_add_u32 m0, %18, 0x2000\n\ts_nop 0\n\tglobal_load_lds_dwordx4 %14, off sc1\n\t"
        "s_add_u32 m0, %18, 0x3000\n\ts_nop 0\n\tglobal_load_lds_dwordx4 %15, off sc1\n\t"
        "s_add_u32 m0, %18, 0x4000\n\ts_nop 0\n\tglobal_load_lds_dwordx4 %16, off sc1\n\t"
        "s_mov_b32 m0, %6\n\ts_waitcnt vmcnt(5)"
        : "=&v"(qA1w), "=&v"(qB1w), "=&v"(k1w), "=&v"(qA2w), "=&v"(qB2w), "=&v"(k2), "=&s"(m0sv)
        : "v"(l16), "v"(l2), "s"(rs), "s"(qo), "s"(kvo), "v"(kvs + (wid == 0 ? 4096 : 0)), "v"(kvs + 4096), "v"(kvs + 8192), "v"(kvs + 12288), "v"(kvs + 16384),
          "s"(ldst + (wid == 0 ? 4096 : 0)), "s"(ldst)
        : "memory", "scc");
    if (wid == 0) { const unsigned f = (tid & 1) ? 0u : 0x3f803f80u; *(u32x4*)(smem + l16) = (u32x4){f, f, f, f}; }
    const bf16x8 qA1 = BF8(qA1w), qB1 = BF8(qB1w);
    bf16x8 k1 = BF8(k1w);
    TS(30);
    float mA = -INFINITY, mB = -INFINITY;
    {
      const bf16x4 qA2 = BF4(((u32x2){qA2w, 0u})), qB2 = BF4(((u32x2){qB2w, 0u}));
      const int nt = attempt ? 64 : 1;
      bf16x8 t1 = k1; unsigned t2 = k2;
      for (int t = 0; t < nt; ++t) {
        if (t) { const unsigned to = kvo + (t >> 3) * 20480 + (t & 7) * 1280; t1 = BF8(ldc16(rs, l16, to)); t2 = ldc4(rs, l2, to); }
        const bf16x4 t2v = BF4(((u32x2){t2, kconst}));
        f32x16 a = MFMA32(t1, qA1, (f32x16){}); a = MFMA32K8(t2v, qA2, a);
        f32x16 b = MFMA32(t1, qB1, (f32x16){}); b = MFMA32K8(t2v, qB2, b);
#pragma unroll
        for (int i = 0; i < 16; ++i) { mA = fmaxf(mA, a[i]); mB = fmaxf(mB, b[i]); }
      }
      mA = fmaxf(mA, xhalf(mA)); mB = fmaxf(mB, xhalf(mB));
    }
    unsigned a0, a1, b0, b1; split3(mA, a0, a1); split3(mB, b0, b1);
    const bf16x4 qA2 = BF4(((u32x2){qA2w, h ? a1 : a0})), qB2 = BF4(((u32x2){qB2w, h ? b1 : b0}));
    char* lh = smem + kh * 81920;
    TS(31);
    waitv<0>();
    BAR();
    TS(6);
    f32x16 oA = (f32x16){}, oB = (f32x16){};
    const char* kp1 = lh + l16;
    const char* kp2 = lh + l2;
    const bool vreal = r < 20;
    const char* vp = vreal ? lh + 10240 + (h * 20 + r) * 16 : smem + (r == 20 ? 0 : 16);
    const int vinc = vreal ? 1280 : 0, vjump = vreal ? 10240 : 0;
    if (kh) { k1 = *(const bf16x8*)kp1; k2 = *(const unsigned*)kp2; }
    bf16x8 kf1[2], vf[2][2]; u32x2 kf2[2];
    kf1[0] = k1; kf2[0] = (u32x2){k2, kconst}; kf2[1] = (u32x2){0u, kconst};
    vf[1][0] = (bf16x8){}; vf[1][1] = (bf16x8){};
    u32x4 pA0, pA1, pB0 = (u32x4){}, pB1 = (u32x4){};
    f32x4 e0 = (f32x4){}, e1;
    f32x16 sA = MFMA32(kf1[0], qA1, (f32x16){}), sB;
    sA = MFMA32K8(BF4(kf2[0]), qA2, sA);
#define EX2(x) __builtin_amdgcn_exp2f(x)
#define GRP(MF, S, i, EN, EP, P, w) do { MF; SCHED(); EN[0] = EX2(S[i]); P[w] = pack2(EP[0], EP[1]); EN[1] = EX2(S[i + 1]); P[w + 1] = pack2(EP[2], EP[3]); \
      EN[2] = EX2(S[i + 2]); EN[3] = EX2(S[i + 3]); PIN(P); PIN(EN); SCHED(); } while (0)
    const unsigned relsc = lds_addr(smem) + 768;
    auto group = [&](auto FIRST, int jump, int j) __attribute__((always_inline)) {
#pragma unroll
      for (int t = 0; t < 8; ++t) {
        const int c = t & 1, n = c ^ 1;
        if (FIRST) { if (t == 0) __builtin_amdgcn_s_setprio(3); if (t == 3) __builtin_amdgcn_s_setprio(2); if (t == 5) __builtin_amdgcn_s_setprio(1); if (t == 6) __builtin_amdgcn_s_setprio(0); if (t == 7) __builtin_amdgcn_s_setprio(3); }
        else {
          if (t == 4 && j == 2) __builtin_amdgcn_s_setprio(2);
          if (t == 3 && j == 3) {
            __builtin_amdgcn_s_setprio(1);
#pragma unroll
            for (int i = 0; i < 13; ++i) wq[i] = proj_ld<false>(rs, i, wid, lane);
          }
          if (t == 6 && j == 3) { __builtin_amdgcn_s_setprio(0); if (wid == 7) glds4c(ws + OFF_CNT + (C_REL + (lane & 7)) * 256, relsc); }
        }
        if (FIRST && t < 5) {
#pragma unroll
          for (int i = 5 + 3 * t; i < 8 + 3 * t; ++i) glds16c(kvs + i * 4096, ldst + i * 4096);
        }
        if (t == 7) {
          if (FIRST) { waitv<0>(); BAR(); }
          kp1 += jump; kp2 += jump;
        }
        SCHED();
        kf1[n] = *(const bf16x8*)(kp1 + ((t + 1) & 7) * 1280); kf2[n][0] = *(const unsigned*)(kp2 + ((t + 1) & 7) * 1280);
        GRP(sB = MFMA32(kf1[c], qB1, (f32x16){}), sA, 0, e1, e0, pB1, 2);
        vf[c][0] = *(const bf16x8*)vp; vf[c][1] = *(const bf16x8*)(vp + 640); vp += vinc;
        GRP(sB = MFMA32K8(BF4(kf2[c]), qB2, sB), sA, 4, e0, e1, pA0, 0);
        GRP(oB = MFMA32(vf[n][0], BF8(pB0), oB), sA, 8, e1, e0, pA0, 2);
        GRP(oB = MFMA32(vf[n][1], BF8(pB1), oB), sA, 12, e0, e1, pA1, 0);
        GRP(sA = MFMA32(kf1[n], qA1, (f32x16){}), sB, 0, e1, e0, pA1, 2);
        GRP(sA = MFMA32K8(BF4(kf2[n]), qA2, sA), sB, 4, e0, e1, pB0, 0);
        GRP(oA = MFMA32(vf[c][0], BF8(pA0), oA), sB, 8, e1, e0, pB0, 2);
        GRP(oA = MFMA32(vf[c][1], BF8(pA1), oA), sB, 12, e0, e1, pB1, 0);
      }
      vp += vjump;
    };
    group(std::true_type{}, 20480, 0);
    TS(7);
    const int j2 = (blk - 135) * 4 + qg;
    const bool side = kh == 0 && attempt == 0 && j2 < 270;
    char* stile = smem + 1024 + qg * 4096;
    const char* svec = smem + 81920 + qg * 256;
    const int kt = blk < 135 ? 4 * (blk % 5) + qg : j2 / 10, ng = blk < 135 ? blk / 5 : j2 % 10;
    if (side) {
      const u16* W = blk < 135 ? win : wout; const int N = blk < 135 ? F_ : D_;
      const int j8 = lane >> 3;
#pragma unroll
      for (int i = 0; i < 4; ++i) {
        const int row = kt * 32 + i * 8 + j8;
        glds16(W + (size_t)row * N + ng * 64 + ((lane & 7) ^ j8) * 8, lds_addr(stile) + i * 1024);
      }
      glds4c((const float*)(ws + OFF_MOD) + (4 - h) * 640 + (kt < 20 ? kt : 0) * 32 + r, lds_addr(svec));
    }
#pragma nounroll
    for (int j = 1; j < 4; ++j) {
      if (j == 2) {
        if (kh) l2touch(wsp + ((size_t)(blk * 4 + qg) * 64 + lane) * 128, lds_addr(smem) + 256);
        else if (side) {
          waitv<0>();
          const int g4 = lane >> 4, r4 = lane & 15, row = 8 * g4 + (r4 >> 2);
          const f32x4 sv0 = *(const f32x4*)(svec + (r4 & 1) * 128 + g4 * 32), sv1 = *(const f32x4*)(svec + (r4 & 1) * 128 + g4 * 32 + 16);
          const bf16x8 vec = BF8(((u32x4){pack2(sv0[0], sv0[1]), pack2(sv0[2], sv0[3]), pack2(sv1[0], sv1[1]), pack2(sv1[2], sv1[3])}));
          const unsigned nks = blk < 135 ? 20 : 27, dso = (blk < 135 ? OFF_WIN : OFF_WOUT) + (ng * 4 * nks + kt) * 1024;
          bf16x8 fr[4]; f32x4 pr[4];
#pragma unroll
          for (int f = 0; f < 4; ++f) {
            const int ch = 2 * f + ((r4 & 3) >> 1);
            const bf16x4 lo = lds_tr(stile + row * 128 + (ch ^ (row & 7)) * 16 + (r4 & 1) * 8), hi = lds_tr(stile + (row + 4) * 128 + (ch ^ ((row + 4) & 7)) * 16 + (r4 & 1) * 8);
            fr[f] = __builtin_shufflevector(lo, hi, 0, 1, 2, 3, 4, 5, 6, 7);
          }
#pragma unroll
          for (int f = 0; f < 4; ++f) { stc16(rs, l16, dso + f * nks * 1024, __builtin_bit_cast(u32x4, fr[f])); pr[f] = MFMA16(fr[f], vec, (f32x4){}); }
          if (r4 < 2) {
#pragma unroll
            for (int f = 0; f < 4; ++f) *(f32x4*)(stile + 3072 + r4 * 256 + f * 64 + g4 * 16) = pr[f];
          }
        }
      }
      if (j == 3 && kh == 0 && attempt == 0) { waitv<0>(); arrive(ws, C_REL + xcd, lane); }
      group(std::false_type{}, j < 3 ? 20480 : 0, j);
    }
    TS(8);
    pB1[2] = pack2(e0[0], e0[1]); pB1[3] = pack2(e0[2], e0[3]);
    oB = MFMA32(vf[1][0], BF8(pB0), oB); oB = MFMA32(vf[1][1], BF8(pB1), oB);
    {
      f32x4* ex = (f32x4*)(lh + 1024 + qg * 4096) + lane;
      const f32x16 give = kh ? oA : oB;
#pragma unroll
      for (int g = 0; g < 3; ++g) ex[g * 64] = (f32x4){give[4 * g], give[4 * g + 1], give[4 * g + 2], give[4 * g + 3]};
    }
    int* flag = (int*)(smem + 528);
    BAR();
    TS(9);
    float o[12];
    {
      const f32x4* ex = (const f32x4*)(smem + (kh ^ 1) * 81920 + 1024 + qg * 4096) + lane;
      const f32x16 keep = kh ? oB : oA;
#pragma unroll
      for (int g = 0; g < 3; ++g) { const f32x4 t = ex[g * 64];
#pragma unroll
        for (int e = 0; e < 4; ++e) o[4 * g + e] = keep[4 * g + e] + t[e]; }
    }
    const float lo = xhalf(o[8]); const float l = h ? o[8] : lo;
    const float rl = __builtin_amdgcn_rcpf(l);
    const int tok = qb * 256 + qg * 64 + kh * 32 + r;
    const unsigned vo = (tok >> 4) * 20480 + (tok & 15) * 16;
    stc16(rs, vo + h * 256, OFF_O + (head >> 1) * 1024 + (head & 1) * 512,
          (u32x4){pack2(o[0] * rl, o[1] * rl), pack2(o[2] * rl, o[3] * rl), pack2(o[4] * rl, o[5] * rl), pack2(o[6] * rl, o[7] * rl)});
    if (h == 0) stc8(rs, vo, OFF_O + 16384 + (head >> 3) * 1024 + ((head >> 1) & 3) * 256 + (head & 1) * 8, (u32x2){pack2(o[8] * rl, o[9] * rl), pack2(o[10] * rl, o[11] * rl)});
    if (wid == 4 && blk < 135 && attempt == 0 && lane < 32) {
      const char* pp = smem + 1024 + 3072 + lane * 16;
      const f32x4 v = (*(const f32x4*)pp + *(const f32x4*)(pp + 4096)) + (*(const f32x4*)(pp + 8192) + *(const f32x4*)(pp + 12288));
      stc16(rs, (lane >> 4) * 8192 + (lane & 15) * 16, OFF_CBP + (blk % 5) * 16384 + (blk / 5) * 256, __builtin_bit_cast(u32x4, v));
    }
    bool ok = (l > 1e-30f) && (l < 1e30f);
#pragma unroll
    for (int i = 0; i < 12; ++i) ok = ok && (fabsf(o[i]) < 3e38f);
    if (!ok && attempt == 0) *flag = 1;
    waitv<0>();
    BAR();
    const int redo = __builtin_amdgcn_readfirstlane(*(volatile LAS int*)flag);
    if (!redo) break;
    BAR();
  }
  TS(10);
  if (wid == 7) arrive(ws, C_ATT + qb, lane);
#pragma unroll
  for (int i = 0; i < 13; ++i) proj_park<false>(smem, i, wid, lane, wq[i]);
  if (!__all(*(volatile LAS unsigned*)(smem + 768 + (lane & 7) * 4) >= 128u)) poll_ge(ws, C_REL + (lane & 7), 128);
}

template <bool FINAL, int KH, class HOOK>
DI void proj_body(char* __restrict__ ws, rsrc_t rs, const u16* __restrict__ x, u16* __restrict__ out, char* smem, int wid, u32x2 (&res)[3], HOOK&& after_mma TARG) {
  const int lane = fresh_tid() & 63;
  const int xcd = blockIdx.x & 7, idx = blockIdx.x >> 3, panel = xcd * 4 + (idx >> 3), cb = idx & 7;
  constexpr int NKS = FINAL ? 27 : 20;
  constexpr int K0 = KH ? (NKS + 1) / 2 : 0, NK = KH ? NKS / 2 : (NKS + 1) / 2, NKEEP = KH ? 2 : 3, UK = KH ? 3 : 0, UG = KH ? 0 : 3;
  const int m = wid & 3, mt = panel * 4 + m, g = lane >> 4, r = lane & 15;
  f32x4 gate[NKEEP], cbv[NKEEP];
#pragma unroll
  for (int i = 0; i < NKEEP; ++i) {
    const int n = (cb * 5 + UK + i) * 16;
    if (!FINAL) { res[i] = *(const u32x2*)(x + (size_t)(mt * 16 + r) * D_ + n + g * 4); cbv[i] = ldc16f(rs, g * 16, OFF_MOD + (4 * 640 + n) * 4); }
    gate[i] = ldc16f(rs, g * 16, OFF_MOD + ((FINAL ? 5 : 2) * 640 + n) * 4);
  }
  if (wid == 7) poll_ge(ws, FINAL ? C_U + panel : C_ATT + (panel >> 2), FINAL ? 8 : 32);
  waitv<0>();
  BAR();
  TS(FINAL ? 27 : 11);
  bf16x8 a[NK];
#pragma unroll
  for (int k = 0; k < NK; ++k) a[k] = BF8(ldc16(rs, lane * 16, (FINAL ? OFF_U : OFF_O) + (mt * NKS + K0 + k) * 1024));
  SCHED();
  constexpr int PD = FINAL ? 5 : 6;
  const char* bl = smem + lane * 16;
  bf16x8 bf[PD + 1][5]; f32x4 acc[5];
#pragma unroll
  for (int u = 0; u < 5; ++u) acc[u] = (f32x4){};
#pragma unroll
  for (int k = 0; k < PD; ++k)
#pragma unroll
    for (int u = 0; u < 5; ++u) bf[k][u] = *(const bf16x8*)(bl + proj_pos<FINAL>(u * NKS + K0 + k));
  SCHED();
#pragma unroll
  for (int k = 0; k < NK; ++k) {
#pragma unroll
    for (int u = 0; u < 5; ++u) {
      if (k + PD < NK) bf[(k + PD) % (PD + 1)][u] = *(const bf16x8*)(bl + proj_pos<FINAL>(u * NKS + K0 + k + PD));
      acc[u] = MFMA16(bf[k % (PD + 1)][u], a[k], acc[u]);
      SCHED();
    }
  }
  TS(FINAL ? 28 : 12);
  f32x4* ex = (f32x4*)(smem + (FINAL ? 40960 : 68608)) + m * 5 * 64 + lane;
#pragma unroll
  for (int i = 0; i < 5 - NKEEP; ++i) ex[(UG + i) * 64] = acc[UG + i];
  f32x4 part[NKEEP];
#pragma unroll
  for (int i = 0; i < NKEEP; ++i) {
    part[i] = fma4s(gate[i], acc[UK + i], (f32x4){bflo(res[i][0]), bfhi(res[i][0]), bflo(res[i][1]), bfhi(res[i][1])});
    PIN(part[i]);
  }
  SCHED();
  after_mma();
  BAR();
  f32x4 oth[NKEEP];
#pragma unroll
  for (int i = 0; i < NKEEP; ++i) oth[i] = ex[(UK + i) * 64];
  f32x4 sv = (f32x4){}, qv = (f32x4){};
#pragma unroll
  for (int i = 0; i < NKEEP; ++i) {
    const f32x4 v = part[i] + gate[i] * oth[i];
    const u32x2 o = (u32x2){pack2(v[0], v[1]), pack2(v[2], v[3])};
    if (FINAL) *(u32x2*)(out + (size_t)(mt * 16 + r) * D_ + (cb * 5 + UK + i) * 16 + g * 4) = o;
    else {
      res[i] = o; PIN(res[i]);
      const f32x4 w = v * cbv[i]; const int un = cb * 5 + UK + i;
      stc8(rs, ((g >> 1) * 16 + r) * 16 + (g & 1) * 8, OFF_X1S + mt * 20480 + un * 512, (u32x2){pack2(w[0], w[1]), pack2(w[2], w[3])});
      sv += v; qv += v * v;
    }
  }
  if (!FINAL) {
    const float s = sum_g((sv[0] + sv[1]) + (sv[2] + sv[3])), q = sum_g((qv[0] + qv[1]) + (qv[2] + qv[3]));
    if (g == 0) stc8(rs, r * 8, OFF_ST1 + ((panel * 16 + cb * 2 + KH) * 64 + m * 16) * 8, (u32x2){__float_as_uint(s), __float_as_uint(q)});
    TS(13);
    waitv<0>();
    BAR();
    TS(14);
    if (wid == 7) arrive(ws, C_X1 + panel, lane);
  }
}

constexpr float GC1 = -2.8853900817779268f * 0.7978845608028654f, GC2 = GC1 * 0.044715f;
DI float gelu_s(float a, float k1, float k3, float den) {
  const float e = __builtin_amdgcn_exp2f(a * __builtin_fmaf(a * a, k3, k1));
  return a * __builtin_amdgcn_rcpf(__builtin_fmaf(e, den, den));
}
DI void gelu8(f32x4& a0, const f32x4& l0, f32x4& a1, const f32x4& l1) {
  float t[8];
#define G8(expr0, expr1) do { _Pragma("unroll") for (int i = 0; i < 4; ++i) { t[i] = opq(expr0); } _Pragma("unroll") for (int i = 0; i < 4; ++i) { t[4 + i] = opq(expr1); } SCHED(); } while (0)
  G8(a0[i] * a0[i], a1[i] * a1[i]);
  G8(__builtin_fmaf(t[i], l0[3], l0[2]), __builtin_fmaf(t[4 + i], l1[3], l1[2]));
  G8(a0[i] * t[i], a1[i] * t[4 + i]);
  G8(__builtin_amdgcn_exp2f(t[i]), __builtin_amdgcn_exp2f(t[4 + i]));
  G8(__builtin_fmaf(t[i], l0[0], l0[0]), __builtin_fmaf(t[4 + i], l1[0], l1[0]));
  G8(__builtin_amdgcn_rcpf(t[i]), __builtin_amdgcn_rcpf(t[4 + i]));
  G8(a0[i] * t[i], a1[i] * t[4 + i]);
  a0 = (f32x4){t[0], t[1], t[2], t[3]}; a1 = (f32x4){t[4], t[5], t[6], t[7]};
}
constexpr int L4_Z1 = 114688, L4_TR = L4_Z1 + 16384, L4_LN = L4_TR + 16384, L4_ST2 = L4_LN + 1024, L4_FIN = L4_ST2 + 3584, L4_CB = L4_FIN + 512, L4_CNT = L4_CB + 2048;
constexpr int K5_R = 7;
constexpr unsigned DEAD = 0x80000000u;
DI int sp_stage(int s) { const int idx = blockIdx.x >> 3; return (s + (idx & 7) * 2) & 15; }
DI unsigned sp_voff(int wid, int lane, int i) { return ((wid & 3) * 16 + i * 8 + (lane >> 3)) * 4096 + (wid >> 2) * 128 + ((lane & 7) ^ (4 * i + (lane >> 4))) * 16; }
DI const char* sp_base(const u16* wsp, int panel, int s) { return (const char*)wsp + (size_t)panel * 262144 + sp_stage(s) * 256; }
DI void sp_prefetch(u32x4 (&wr)[16 - K5_R][2], const u16* __restrict__ wsp, int panel, int wid, int lane) {
  const unsigned v0 = sp_voff(wid, lane, 0), v1 = sp_voff(wid, lane, 1);
#pragma unroll
  for (int s = K5_R; s < 16; ++s) { wr[s - K5_R][0] = *(const u32x4*)(sp_base(wsp, panel, s) + v0); wr[s - K5_R][1] = *(const u32x4*)(sp_base(wsp, panel, s) + v1); }
  asm volatile("" ::: "memory");
}
template <int S0, int S1>
DI void sp_ring(const u16* __restrict__ wsp, char* smem, int panel, int wid, int lane) {
  const unsigned v0 = sp_voff(wid, lane, 0), v1 = sp_voff(wid, lane, 1);
#pragma unroll
  for (int s = S0; s < S1; ++s) { glds16(sp_base(wsp, panel, s) + v0, lds_addr(smem) + wid * 2048 + s * 16384); glds16(sp_base(wsp, panel, s) + v1, lds_addr(smem) + wid * 2048 + s * 16384 + 1024); }
}
DI unsigned win_boff(int wid, int lane) { const int cb = (blockIdx.x >> 3) & 7; return lane * 16 + (wid == 6 && cb >= 6 ? DEAD : 0u); }
DI unsigned win_soff(int wid, bool z2) { return OFF_WIN + ((z2 ? 54 : 0) + unit0_of((blockIdx.x >> 3) & 7) + wid) * 20480; }
#define WIN_LD(so, k) BF8(ldc16(rs, bo + ((k) & 3) * 1024, (so) + ((k) >> 2) * 4096))
DI void win_prefetch(rsrc_t rs, int wid, bf16x8 (&b2)[20]) {
  const unsigned bo = win_boff(wid, fresh_tid() & 63), so = win_soff(wid, true);
#pragma unroll
  for (int k = 0; k < 20; ++k) b2[k] = WIN_LD(so, k);
}
template <class HOOK>
DI void win_mma(const char* al, const bf16x8 (&b)[20], f32x4 (&acc)[4], HOOK&& hook) {
  constexpr int PD = 4;
  bf16x8 af[PD + 1][2];
#define WIN_RD(i) do { af[(i) % (PD + 1)][0] = *(const bf16x8*)(al + ((i) & 3) * 20480 + ((i) >> 2) * 2048); af[(i) % (PD + 1)][1] = *(const bf16x8*)(al + ((i) & 3) * 20480 + ((i) >> 2) * 2048 + 1024); } while (0)
#pragma unroll
  for (int i = 0; i < PD; ++i) WIN_RD(i);
  SCHED();
  sfor<40>([&](auto I) __attribute__((always_inline)) {
    constexpr int i = I, m = i & 3, k = (i >> 2) * 2;
    if constexpr (i + PD < 40) WIN_RD(i + PD);
    acc[m] = MFMA16(b[k], af[i % (PD + 1)][0], acc[m]);
    acc[m] = MFMA16(b[k + 1], af[i % (PD + 1)][1], acc[m]); PIN(acc[m]);
    hook(I);
    SCHED();
  });
}
DI void stage_x1s(char* __restrict__ ws, char* smem, int panel, int wid, int tid) {
  const char* src = ws + OFF_X1S + (size_t)panel * 81920 + tid * 16;
#pragma unroll
  for (int i = 0; i < 10; ++i) glds16c(src + i * 8192, lds_addr(smem) + wid * 1024 + i * 8192);
}
DI void win_tail(char* __restrict__ ws, const u16* __restrict__ wsp, char* smem, int panel, int cb, int wid, int lane TARG) {
  BAR();
  TS(22);
  if (wid == 7 && lane < 4) __hip_atomic_fetch_add((GAS unsigned*)(ws + OFF_CNT + (C_Z2 + lane * 8 + cb) * 256), 1u, __ATOMIC_RELAXED, __HIP_MEMORY_SCOPE_AGENT);
}

template <bool EARLY>
DI void win_body(char* __restrict__ ws, rsrc_t rs, const u16* __restrict__ wsp, char* smem, int wid, const bf16x8 (&b2)[20], u32x4 (&wr)[16 - K5_R][2] TARG) {
  const int tid = fresh_tid(), lane = tid & 63, g = lane >> 4, r = lane & 15;
  const int xcd = blockIdx.x & 7, idx = blockIdx.x >> 3, panel = xcd * 4 + (idx >> 3), cb = idx & 7, u0 = unit0_of(cb);
  float* lnp = (float*)(smem + L4_LN); float* st2 = (float*)(smem + L4_ST2); float* fin = (float*)(smem + L4_FIN); float* cbl = (float*)(smem + L4_CB);
  int* flg = (int*)(smem + L4_CNT);
  __builtin_amdgcn_s_setprio(1);
  const unsigned bo = win_boff(wid, lane), so1 = win_soff(wid, false);
  bf16x8 b1[20];
  BAR();
  TS(15);
  stage_x1s(ws, smem, panel, wid, tid);
#pragma unroll
  for (int k = 0; k < 4; ++k) b1[k] = WIN_LD(so1, k);
  waitv<4>();
  BAR();
  TS(16);
  f32x4 ln[4], z2[4], z1[4];
#pragma unroll
  for (int m = 0; m < 4; ++m) ln[m] = *(const f32x4*)(lnp + (m * 16 + r) * 4);
  {
    const f32x4 cs2 = *(const f32x4*)(cbl + 112 + wid * 16 + g * 4), bi2 = *(const f32x4*)(cbl + 256 + 112 + wid * 16 + g * 4);
    const f32x4 cs1 = *(const f32x4*)(cbl + wid * 16 + g * 4), bi1 = *(const f32x4*)(cbl + 256 + wid * 16 + g * 4);
#pragma unroll
    for (int m = 0; m < 4; ++m) {
#pragma unroll
      for (int e = 0; e < 4; ++e) { z2[m][e] = opq(__builtin_fmaf(ln[m][1], cs2[e], opq(ln[m][0] * bi2[e]))); z1[m][e] = opq(__builtin_fmaf(ln[m][1], cs1[e], opq(ln[m][0] * bi1[e]))); }
      PIN(z1[m]);
    }
  }
  win_mma(smem + lane * 16, b2, z2, [&](auto I) __attribute__((always_inline)) { constexpr int i = I; if constexpr (i < 32 && (i & 1)) b1[4 + i / 2] = WIN_LD(so1, 4 + i / 2); });
  TS(17);
  gelu8(z2[0], ln[0], z2[1], ln[1]); gelu8(z2[2], ln[2], z2[3], ln[3]);
  {
    float s[4], q[4];
#pragma unroll
    for (int m = 0; m < 4; ++m) { s[m] = opq(opq(z2[m][0] + z2[m][1]) + opq(z2[m][2] + z2[m][3])); q[m] = opq(__builtin_fmaf(z2[m][0], z2[m][0], opq(z2[m][1] * z2[m][1])) + opq(__builtin_fmaf(z2[m][2], z2[m][2], opq(z2[m][3] * z2[m][3])))); }
    auto pa = __builtin_amdgcn_permlane32_swap(__float_as_uint(s[0]), __float_as_uint(s[2]), false, false);
    auto pb = __builtin_amdgcn_permlane32_swap(__float_as_uint(s[1]), __float_as_uint(s[3]), false, false);
    auto pc = __builtin_amdgcn_permlane32_swap(__float_as_uint(q[0]), __float_as_uint(q[2]), false, false);
    auto pd = __builtin_amdgcn_permlane32_swap(__float_as_uint(q[1]), __float_as_uint(q[3]), false, false);
    const float sa = opq(__uint_as_float(pa[0]) + __uint_as_float(pa[1])), sb = opq(__uint_as_float(pb[0]) + __uint_as_float(pb[1]));
    const float qa = opq(__uint_as_float(pc[0]) + __uint_as_float(pc[1])), qb = opq(__uint_as_float(pd[0]) + __uint_as_float(pd[1]));
    auto ps = __builtin_amdgcn_permlane16_swap(__float_as_uint(sa), __float_as_uint(sb), false, false);
    auto pq = __builtin_amdgcn_permlane16_swap(__float_as_uint(qa), __float_as_uint(qb), false, false);
    *(f32x2*)(st2 + (wid * 64 + lane) * 2) = (f32x2){opq(__uint_as_float(ps[0]) + __uint_as_float(ps[1])), opq(__uint_as_float(pq[0]) + __uint_as_float(pq[1]))};
  }
  asm volatile("" ::: "memory");
  if (lane == 0) __hip_atomic_fetch_add(flg, 1, __ATOMIC_RELAXED, __HIP_MEMORY_SCOPE_WORKGROUP);
  asm volatile("" ::: "memory");
  __builtin_amdgcn_s_setprio(0);
  TS(18);
  const unsigned wvo[2] = {sp_voff(wid, lane, 0), sp_voff(wid, lane, 1)};
  win_mma(smem + lane * 16, b1, z1, [&](auto I) __attribute__((always_inline)) {
    constexpr int i = I, j = i < 8 ? i : (i & 1) ? -1 : 4 + i / 2;
    if constexpr (EARLY && j >= 0 && j < 18) wr[j >> 1][j & 1] = *(const u32x4*)(sp_base(wsp, panel, K5_R + (j >> 1)) + wvo[j & 1]);
  });
  gelu8(z1[0], ln[0], z1[1], ln[1]); gelu8(z1[2], ln[2], z1[3], ln[3]);
#pragma unroll
  for (int mm = 0; mm < 2; ++mm)
    *(u32x4*)(smem + L4_Z1 + wid * 2048 + mm * 1024 + lane * 16) = (u32x4){pack2(z1[2 * mm][0], z1[2 * mm][1]), pack2(z1[2 * mm][2], z1[2 * mm][3]), pack2(z1[2 * mm + 1][0], z1[2 * mm + 1][1]), pack2(z1[2 * mm + 1][2], z1[2 * mm + 1][3])};
  TS(20);
  BAR();
  TS(21);
  char* tr = smem + L4_TR + wid * 2048;
#pragma unroll
  for (int m = 0; m < 4; ++m) {
    const f32x2 f = *(const f32x2*)(fin + (m * 16 + r) * 2);
    const f32x4 v = z2[m] * f[0] - f[1];
    *(u32x2*)(tr + (m * 16 + r) * 32 + g * 8) = (u32x2){pack2(v[0], v[1]), pack2(v[2], v[3])};
  }
  {
    const char* p = tr + (8 * g + (r >> 2)) * 32 + (r & 3) * 8;
    const bf16x4 lo0 = lds_tr(p), hi0 = lds_tr(p + 128), lo1 = lds_tr(p + 1024), hi1 = lds_tr(p + 1152);
    stc16(rs, bo, OFF_Z2 + (panel * 2 * 54 + u0 + wid) * 1024, __builtin_bit_cast(u32x4, (bf16x8)__builtin_shufflevector(lo0, hi0, 0, 1, 2, 3, 4, 5, 6, 7)));
    stc16(rs, bo, OFF_Z2 + ((panel * 2 + 1) * 54 + u0 + wid) * 1024, __builtin_bit_cast(u32x4, (bf16x8)__builtin_shufflevector(lo1, hi1, 0, 1, 2, 3, 4, 5, 6, 7)));
  }
  SCHED();
  waitv<0>();
  win_tail(ws, wsp, smem, panel, cb, wid, lane TPASS);
  sp_ring<0, 5>(wsp, smem, panel, wid, lane);
  if (!EARLY) sp_prefetch(wr, wsp, panel, wid, lane);
}

DI void win_agent(char* __restrict__ ws, rsrc_t rs, const u16* __restrict__ wsp, char* smem, u32x4 (&wr)[16 - K5_R][2] TARG) {
  const int tid = fresh_tid(), lane = tid & 63, wid = 7;
  const int xcd = blockIdx.x & 7, idx = blockIdx.x >> 3, panel = xcd * 4 + (idx >> 3), cb = idx & 7, u0 = unit0_of(cb);
  float* lnp = (float*)(smem + L4_LN); float* st2 = (float*)(smem + L4_ST2); float* fin = (float*)(smem + L4_FIN); float* cbl = (float*)(smem + L4_CB);
  int* cnt = (int*)(smem + L4_CNT);
  __builtin_amdgcn_s_setprio(2);
  if (lane == 0) *cnt = 0;
  poll_ge(ws, lane < 8 ? C_ATT + lane : C_X1 + panel, lane < 8 ? 32 : 8);
  BAR();
  stage_x1s(ws, smem, panel, wid, tid);
  {
    u32x2 t[16]; f32x4 cp[5][2];
#pragma unroll
    for (int i = 0; i < 16; ++i) t[i] = __builtin_amdgcn_raw_buffer_load_b64(rs, lane * 8, OFF_ST1 + (panel * 16 + i) * 512, 16);
    const unsigned co = lane < 28 ? (u0 * 16 + lane * 4) * 4 : ((54 + u0) * 16 + min(lane - 28, 27) * 4) * 4;
#pragma unroll
    for (int i = 0; i < 5; ++i) { cp[i][0] = ldc16f(rs, co, OFF_CBP + i * 16384); cp[i][1] = ldc16f(rs, co, OFF_CBP + i * 16384 + 8192); }
    float s = 0.f, q = 0.f;
#pragma unroll
    for (int i = 0; i < 16; ++i) { s += __uint_as_float(t[i][0]); q += __uint_as_float(t[i][1]); }
    const float mean = s * (1.f / 640.f), var = fmaxf(q * (1.f / 640.f) - mean * mean, 0.f);
    const float den = ln_den(var), ri = 1.f / den;
    *(f32x4*)(lnp + lane * 4) = (f32x4){den, -rbf(mean), GC1 * ri, GC2 * ri * ri * ri};
#pragma unroll
    for (int v = 0; v < 2; ++v) *(f32x4*)(cbl + v * 256 + lane * 4) = ((cp[0][v] + cp[1][v]) + (cp[2][v] + cp[3][v])) + cp[4][v];
  }
  waitv<0>();
  BAR();
  __builtin_amdgcn_s_setprio(0);
  __builtin_amdgcn_s_sleep(8);
  sp_prefetch(wr, wsp, panel, wid, lane);
#pragma nounroll
  for (int w = 0; w < 8; ++w) sp_ring<5, 7>(wsp, smem, panel, w, lane);
  __builtin_amdgcn_s_setprio(2);
#pragma nounroll
  for (unsigned spin = 0; spin < (1u << 24); ++spin) {
    if (__hip_atomic_load(cnt, __ATOMIC_RELAXED, __HIP_MEMORY_SCOPE_WORKGROUP) >= 7) break;
    __builtin_amdgcn_s_sleep(1);
  }
  asm volatile("" ::: "memory");
  float s, q;
  {
    f32x2 t[7];
#pragma unroll
    for (int i = 0; i < 7; ++i) t[i] = *(const f32x2*)(st2 + (i * 64 + lane) * 2);
    s = t[0][0]; q = t[0][1];
#pragma unroll
    for (int i = 1; i < 6; ++i) { s = opq(s + t[i][0]); q = opq(q + t[i][1]); }
    if (cb < 6) { s = opq(s + t[6][0]); q = opq(q + t[6][1]); }
  }
  GAS u64* xch = (GAS u64*)(ws + OFF_XCH) + panel * 512;
  unsigned qb = __float_as_uint(q); if (qb == ~0u) qb = 0x7fc00000u;
  __hip_atomic_store(xch + cb * 64 + lane, ((u64)qb << 32) | __float_as_uint(s), __ATOMIC_RELAXED, __HIP_MEMORY_SCOPE_AGENT);
  TS(19);
  u64 v[8];
#pragma nounroll
  for (unsigned spin = 0; spin < (1u << 21); ++spin) {
    bool ok = true;
#pragma unroll
    for (int i = 0; i < 8; ++i) { v[i] = __hip_atomic_load(xch + i * 64 + lane, __ATOMIC_RELAXED, __HIP_MEMORY_SCOPE_AGENT); ok = ok && ((unsigned)(v[i] >> 32) != ~0u); }
    if (__all(ok)) break;
    __builtin_amdgcn_s_sleep(1);
  }
  s = 0.f; q = 0.f;
#pragma unroll
  for (int i = 0; i < 8; ++i) { s = opq(s + __uint_as_float((unsigned)v[i])); q = opq(q + __uint_as_float((unsigned)(v[i] >> 32))); }
  const float mean = opq(s * (1.f / 864.f)), var = fmaxf(opq(q * (1.f / 864.f)) - mean * mean, 0.f);
  const float ri = 1.f / ln_den(var);
  *(f32x2*)(fin + lane * 2) = (f32x2){ri, rbf(mean) * ri};
  __builtin_amdgcn_s_setprio(0);
  TS(20);
  BAR();
  __builtin_amdgcn_s_sleep(6);
  sp_ring<0, 5>(wsp, smem, panel, wid, lane);
  win_tail(ws, wsp, smem, panel, cb, wid, lane TPASS);
}

#define SP_LB 4
DI void phase_sp(char* __restrict__ ws, rsrc_t rs, const u16* __restrict__ bsp, char* smem, int wid, const u32x4 (&wr)[16 - K5_R][2] TARG) {
  const int tid = fresh_tid(), lane = tid & 63;
  const int xcd = blockIdx.x & 7, idx = blockIdx.x >> 3, panel = xcd * 4 + (idx >> 3), cb = idx & 7;
  const int kh = wid >> 2, up = wid & 3, u0 = unit0_of(cb) + 2 * up, nu = cb < 6 ? 7 : 6, g = lane >> 4, r = lane & 15;
  const unsigned dead[2] = {2 * up < nu ? 0u : DEAD, 2 * up + 1 < nu ? 0u : DEAD};
  const unsigned bvo[2] = {lane * 16 + dead[0], lane * 16 + 1024 + dead[1]};
  u16 bb[2];
#pragma unroll
  for (int j = 0; j < 2; ++j) bb[j] = bsp[panel * 64 + (2 * kh + j) * 16 + r];
  const char* al[2][2];
#pragma unroll
  for (int k = 0; k < 2; ++k)
#pragma unroll
    for (int j = 0; j < 2; ++j) al[k][j] = smem + kh * 8192 + (j ^ kh) * 4096 + r * 128 + ((4 * k + g) ^ (r >> 1)) * 16;
  constexpr int LB = SP_LB, PD = 3;
  bf16x8 bq[LB + 1][4], af[PD + 1][2]; f32x4 acc[2][4]; u32x4 wq[17];
  if (wid == 7) poll_ge(ws, C_Z2 + (panel >> 3) * 8 + cb, 32);
#pragma unroll
  for (int i = 0; i < 2; ++i) {
    const float b0 = bf2f(bb[0]), b1 = bf2f(bb[1]);
    acc[i][0] = (f32x4){b0, b0, b0, b0}; acc[i][1] = (f32x4){b1, b1, b1, b1}; acc[i][2] = (f32x4){}; acc[i][3] = (f32x4){};
  }
  if (kh && up < 3) waitv<2 * (16 - K5_R)>(); else waitv<0>();
  BAR();
  TS(23);
  const unsigned bso = OFF_Z2 + (kh * 2 * 54 + u0) * 1024;
#define SP_LD(s, j) (bq[(s) % (LB + 1)][j] = BF8(ldc16(rs, bvo[(j) & 1], bso + (sp_stage(s) * 4 + ((j) >> 1)) * 55296)))
#define SP_RD(t, k) (af[(t) % (PD + 1)][k] = *(const bf16x8*)(al[k][((t) >> 1) & 1] + (((t) >> 2) % K5_R) * 16384 + ((t) & 1) * 2048))
#pragma unroll
  for (int s = 0; s < LB; ++s)
#pragma unroll
    for (int j = 0; j < 4; ++j) SP_LD(s, j);
#pragma unroll
  for (int t = 0; t < PD; ++t) { SP_RD(t, 0); SP_RD(t, 1); }
  SCHED();
  sfor<64>([&](auto T) __attribute__((always_inline)) {
    constexpr int t = T, nx = t + PD, s = t >> 2, m = t & 3, tl = t + 4 * LB;
    if constexpr (nx == 16 || nx == 28 || nx == 44 || nx == 56) { BAR(); TS(32 + (nx == 28) + 2 * (nx == 44) + 3 * (nx == 56)); }
    constexpr int c = nx >= 16 && nx < 24 ? nx - 16 : nx >= 28 && nx < 34 ? nx - 20 : nx >= 44 && nx < 48 ? nx - 30 : -1;
    if constexpr (nx < 64) SP_RD(nx, 0);
    acc[0][m] = MFMA16(bq[s % (LB + 1)][0], af[t % (PD + 1)][0], acc[0][m]);
    acc[0][m] = MFMA16(bq[s % (LB + 1)][2], af[t % (PD + 1)][1], acc[0][m]); PIN(acc[0][m]);
    if constexpr (tl < 64) SP_LD(tl >> 2, m); else if constexpr (tl - 64 < 17) wq[tl - 64] = proj_ld<true>(rs, tl - 64, wid, lane);
    SCHED();
    if constexpr (nx < 64) SP_RD(nx, 1);
    acc[1][m] = MFMA16(bq[s % (LB + 1)][1], af[t % (PD + 1)][0], acc[1][m]);
    acc[1][m] = MFMA16(bq[s % (LB + 1)][3], af[t % (PD + 1)][1], acc[1][m]); PIN(acc[1][m]);
    if constexpr (c >= 0) *(u32x4*)(smem + ((c >> 1) % K5_R) * 16384 + wid * 2048 + (c & 1) * 1024 + lane * 16) = wr[c >> 1][c & 1];
    if constexpr (t == 63 && 4 * LB < 17) { _Pragma("unroll") for (int i = 4 * LB; i < 17; ++i) wq[i] = proj_ld<true>(rs, i, wid, lane); }
    SCHED();
  });
  TS(24);
  f32x4* exw = (f32x4*)(smem + 32768 + wid * 4096) + lane;
  const f32x4* exr = (const f32x4*)(smem + 32768 + (wid ^ 4) * 4096) + lane;
  u32x4 z[2];
#pragma unroll
  for (int i = 0; i < 2; ++i) z[i] = *(const u32x4*)(smem + L4_Z1 + (2 * up + i) * 2048 + kh * 1024 + lane * 16);
#pragma unroll
  for (int i = 0; i < 2; ++i)
#pragma unroll
    for (int j = 0; j < 2; ++j) exw[(i * 2 + j) * 64] = acc[i][2 + j];
  f32x4 zf[2][2];
#pragma unroll
  for (int i = 0; i < 2; ++i)
#pragma unroll
    for (int j = 0; j < 2; ++j) {
      zf[i][j] = (f32x4){bflo(z[i][2 * j]), bfhi(z[i][2 * j]), bflo(z[i][2 * j + 1]), bfhi(z[i][2 * j + 1])};
      acc[i][j] = mul4s(acc[i][j], zf[i][j]); PIN(zf[i][j]); PIN(acc[i][j]);
    }
  BAR();
  f32x4 oth[2][2];
#pragma unroll
  for (int i = 0; i < 2; ++i)
#pragma unroll
    for (int j = 0; j < 2; ++j) oth[i][j] = exr[(i * 2 + j) * 64];
#pragma unroll
  for (int i = 0; i < 2; ++i)
#pragma unroll
    for (int j = 0; j < 2; ++j) {
      const f32x4 v = acc[i][j] + zf[i][j] * oth[i][j];
      stc8(rs, ((g >> 1) * 16 + r) * 16 + (g & 1) * 8 + dead[i], OFF_U + (panel * 4 + 2 * kh + j) * 27648 + (u0 + i) * 512, (u32x2){pack2(v[0], v[1]), pack2(v[2], v[3])});
    }
  TS(25);
  waitv<0>();
  BAR();
  TS(26);
  if (wid == 7) arrive(ws, C_U + panel, lane);
#pragma unroll
  for (int i = 0; i < 17; ++i) proj_park<true>(smem, i, wid, lane, wq[i]);
}

__global__ __launch_bounds__(512, 2) OCC2 void k_mega(char* __restrict__ ws, const u16* __restrict__ x, const u16* __restrict__ win,
                                                     const u16* __restrict__ wsp, const u16* __restrict__ bsp, const u16* __restrict__ wout, u16* __restrict__ out) {
  extern __shared__ __attribute__((aligned(16))) char smem[];
  const int wid = __builtin_amdgcn_readfirstlane(threadIdx.x >> 6);
  const rsrc_t rs = __builtin_amdgcn_make_buffer_rsrc(ws, 0, WS_USED, 0x00020000);
  TDECL; TS(0);
  u32x2 res[3];
  phase_qkv(ws, rs, smem, wid TPASS);
  phase_attn(ws, rs, (const char*)wsp, win, wout, smem, wid TPASS);
  u32x4 wr[16 - K5_R][2];
  auto nohook = []() __attribute__((always_inline)) {};
  if (wid < 4) {
    bf16x8 b2[20];
    proj_body<false, 0>(ws, rs, x, out, smem, wid, res, [&]() __attribute__((always_inline)) { win_prefetch(rs, wid, b2); } TPASS);
    win_body<true>(ws, rs, wsp, smem, wid, b2, wr TPASS);
  } else {
    proj_body<false, 1>(ws, rs, x, out, smem, wid, res, nohook TPASS);
    if (wid == 7) win_agent(ws, rs, wsp, smem, wr TPASS);
    else { bf16x8 b2[20]; win_prefetch(rs, wid, b2); win_body<false>(ws, rs, wsp, smem, wid, b2, wr TPASS); }
  }
  phase_sp(ws, rs, bsp, smem, wid, wr TPASS);
  if (wid < 4) proj_body<true, 0>(ws, rs, x, out, smem, wid, res, nohook TPASS);
  else proj_body<true, 1>(ws, rs, x, out, smem, wid, res, nohook TPASS);
  TS(29);
  TDUMP();
}

extern "C" void kernel_launch(void* const* d_in, const int* in_sizes, int n_in, void* d_out, int out_size, void* d_ws, size_t ws_size,
                              hipStream_t stream) {
  const u16* x = (const u16*)d_in[0];
  const u16* wqkv = (const u16*)d_in[1];
  const u16* wo = (const u16*)d_in[2];
  const u16* win = (const u16*)d_in[4];
  const u16* wsp = (const u16*)d_in[5];
  const u16* bsp = (const u16*)d_in[6];
  const u16* wout = (const u16*)d_in[7];
  const u16* cvec = (const u16*)d_in[8];
  const u16* wmod = (const u16*)d_in[9];
  char* ws = (char*)d_ws;
  (void)hipFuncSetAttribute((const void*)k_mega, hipFuncAttributeMaxDynamicSharedMemorySize, 163840);
  k0_prep<<<K0_GRID, 256, 0, stream>>>(x, wqkv, wo, cvec, wmod, ws);
  k_mega<<<256, 512, 163840, stream>>>(ws, x, win, wsp, bsp, wout, (u16*)d_out);
}
```

```cpp
#include <hip/hip_runtime.h>
#include <hip/hip_bf16.h>
#include <stdint.h>
#include <utility>

typedef __attribute__((ext_vector_type(8))) short bf16x8;
typedef __attribute__((ext_vector_type(4))) short bf16x4;
typedef __attribute__((ext_vector_type(2))) float f32x2;
typedef __attribute__((ext_vector_type(4))) float f32x4;
typedef __attribute__((ext_vector_type(16))) float f32x16;
typedef __attribute__((ext_vector_type(2))) unsigned u32x2;
typedef __attribute__((ext_vector_type(4))) unsigned u32x4;
typedef __attribute__((ext_vector_type(2))) __bf16 bf2_t;
typedef __attribute__((ext_vector_type(2))) _Float16 h2_t;
typedef unsigned short u16;
typedef unsigned long long u64;
#define LAS __attribute__((address_space(3)))
#define GAS __attribute__((address_space(1)))
#define DI __device__ __forceinline__

constexpr int D_ = 640, NQKV = 1920, F_ = 1728;

constexpr unsigned OFF_CNT = 0;
constexpr unsigned OFF_MOD = 65536;
constexpr unsigned OFF_MODP = OFF_MOD + 16384;
constexpr unsigned OFF_CBP = OFF_MODP + 61440;
constexpr unsigned OFF_XCH = OFF_CBP + 5 * 16384;
constexpr unsigned OFF_ST1 = OFF_XCH + 131072;
constexpr unsigned OFF_XHAT = OFF_ST1 + 262144;
constexpr unsigned OFF_WQKV = OFF_XHAT + 2621440;
constexpr unsigned OFF_WO = OFF_WQKV + 2457600;
constexpr unsigned OFF_WIN = OFF_WO + 819200;
constexpr unsigned OFF_WOUT = OFF_WIN + 2211840;
constexpr unsigned OFF_Q = OFF_WOUT + 1105920;
constexpr unsigned OFF_KV = OFF_Q + 32 * 81920;
constexpr unsigned OFF_O = OFF_KV + 32 * 163840;
constexpr unsigned OFF_X1S = OFF_O + 2621440;
constexpr unsigned OFF_Z2 = OFF_X1S + 2621440;
constexpr unsigned OFF_U = OFF_Z2 + 3538944;
constexpr unsigned WS_USED = OFF_U + 3538944;
constexpr int C_QKV = 0, C_MOD = 15  , C_ATT = 128, C_REL = 136, C_X1 = 144, C_U = 176, C_Z2 = 208  ;

#define STAMP(kid, k)
#define TDECL
#define TS(k)
#define TDUMP()
DI unsigned pack2(float lo, float hi) { return __builtin_bit_cast(unsigned, __builtin_convertvector((f32x2){lo, hi}, bf2_t)); }
DI unsigned pack2s(float lo, float hi) { unsigned r; asm("v_cvt_pk_bf16_f32 %0, %1, %2" : "=v"(r) : "v"(lo), "v"(hi)); return r; }
DI float opq(float x) { asm("" : "+v"(x)); return x; }
DI f32x4 fma4s(f32x4 a, f32x4 b, f32x4 c) { return (f32x4){opq(__builtin_fmaf(a[0], b[0], c[0])), opq(__builtin_fmaf(a[1], b[1], c[1])), opq(__builtin_fmaf(a[2], b[2], c[2])), opq(__builtin_fmaf(a[3], b[3], c[3]))}; }
DI f32x4 mul4s(f32x4 a, f32x4 b) { return (f32x4){opq(a[0] * b[0]), opq(a[1] * b[1]), opq(a[2] * b[2]), opq(a[3] * b[3])}; }
DI unsigned pack2h(float lo, float hi) { return __builtin_bit_cast(unsigned, __builtin_convertvector((f32x2){lo, hi}, h2_t)); }
DI float bflo(unsigned u) { return __uint_as_float(u << 16); }
DI float bfhi(unsigned u) { return __uint_as_float(u & 0xffff0000u); }
DI float rbf(float x) { return bflo(pack2(x, x)); }
DI float bf2f(u16 b) { return __uint_as_float((unsigned)b << 16); }
#define MFMA32(a, b, c) __builtin_amdgcn_mfma_f32_32x32x16_bf16((a), (b), (c), 0, 0, 0)
#define MFMA32K8(a, b, c) __builtin_amdgcn_mfma_f32_32x32x8bf16_1k((a), (b), (c), 0, 0, 0)
#define MFMA16(a, b, c) __builtin_amdgcn_mfma_f32_16x16x32_bf16((a), (b), (c), 0, 0, 0)
#define BAR() asm volatile("s_waitcnt lgkmcnt(0)\n\ts_barrier" ::: "memory")
#define SCHED() __builtin_amdgcn_sched_barrier(0)
#define PIN(x) asm volatile("" : "+v"(x))
#define OCC2 __attribute__((amdgpu_waves_per_eu(2, 2)))
#define BF8(x) __builtin_bit_cast(bf16x8, x)
#define BF4(x) __builtin_bit_cast(bf16x4, x)
template <int N> DI void waitv() { asm volatile("s_waitcnt vmcnt(%0)" ::"i"(N) : "memory"); }
template <class F, int... I> DI void sfor_impl(F&& f, std::integer_sequence<int, I...>) { (f(std::integral_constant<int, I>{}), ...); }
template <int N, class F> DI void sfor(F&& f) { sfor_impl(f, std::make_integer_sequence<int, N>{}); }
typedef short v4i16_t __attribute__((ext_vector_type(4)));
DI bf16x4 lds_tr(const char* p) { return __builtin_bit_cast(bf16x4, __builtin_amdgcn_ds_read_tr16_b64_v4i16((LAS v4i16_t*)p)); }
DI void glds16(const void* g, unsigned lds_base) {
  unsigned sv;
  asm volatile("s_mov_b32 %0, m0\n\ts_mov_b32 m0, %2\n\ts_nop 0\n\tglobal_load_lds_dwordx4 %1, off\n\ts_mov_b32 m0, %0" : "=&s"(sv) : "v"(g), "s"(lds_base) : "memory");
}
DI void glds16c(const void* g, unsigned lds_base) {
  unsigned sv;
  asm volatile("s_mov_b32 %0, m0\n\ts_mov_b32 m0, %2\n\ts_nop 0\n\tglobal_load_lds_dwordx4 %1, off sc1\n\ts_mov_b32 m0, %0" : "=&s"(sv) : "v"(g), "s"(lds_base) : "memory");
}
DI void glds4c(const void* g, unsigned lds_base) {
  unsigned sv;
  asm volatile("s_mov_b32 %0, m0\n\ts_mov_b32 m0, %2\n\ts_nop 0\n\tglobal_load_lds_dword %1, off sc1\n\ts_mov_b32 m0, %0" : "=&s"(sv) : "v"(g), "s"(lds_base) : "memory");
}
#define PSLEEP 1
typedef __amdgpu_buffer_rsrc_t rsrc_t;
DI u32x4 ldc16(rsrc_t rs, unsigned voff, unsigned soff) { return __builtin_amdgcn_raw_buffer_load_b128(rs, voff, soff, 16); }
DI unsigned ldc4(rsrc_t rs, unsigned voff, unsigned soff) { return __builtin_amdgcn_raw_buffer_load_b32(rs, voff, soff, 16); }
DI void stc16(rsrc_t rs, unsigned voff, unsigned soff, u32x4 v) { __builtin_amdgcn_raw_buffer_store_b128(v, rs, voff, soff, 16); asm volatile("s_nop 1" ::"v"(v) : "memory"); }
DI void stc8(rsrc_t rs, unsigned voff, unsigned soff, u32x2 v) { __builtin_amdgcn_raw_buffer_store_b64(v, rs, voff, soff, 16); }
DI void stc4(rsrc_t rs, unsigned voff, unsigned soff, unsigned v) { __builtin_amdgcn_raw_buffer_store_b32(v, rs, voff, soff, 16); }
DI f32x4 ldc16f(rsrc_t rs, unsigned voff, unsigned soff) { return __builtin_bit_cast(f32x4, ldc16(rs, voff, soff)); }
DI void arrive(char* ws, int c, int lane) { if (lane == 0) __hip_atomic_fetch_add((GAS unsigned*)(ws + OFF_CNT + c * 256), 1u, __ATOMIC_RELAXED, __HIP_MEMORY_SCOPE_AGENT); }
DI void poll_ge(char* ws, int c, unsigned thr) {
  const GAS unsigned* p = (const GAS unsigned*)(ws + OFF_CNT + c * 256);
#pragma nounroll
  for (unsigned spin = 0; spin < (1u << 19); ++spin) {
    if (__all(__hip_atomic_load(p, __ATOMIC_RELAXED, __HIP_MEMORY_SCOPE_AGENT) >= thr)) break;
    __builtin_amdgcn_s_sleep(PSLEEP);
  }
  asm volatile("" ::: "memory");
}
DI int fresh_tid() { int t = threadIdx.x; asm volatile("" : "+v"(t)); return t; }
DI void l2touch(const void* g, unsigned lds_scratch) {
  unsigned sv;
  asm volatile("s_mov_b32 %0, m0\n\ts_mov_b32 m0, %2\n\ts_nop 0\n\tglobal_load_lds_dword %1, off\n\ts_mov_b32 m0, %0" : "=&s"(sv) : "v"(g), "s"(lds_scratch) : "memory");
}
#define LDNT(p) __builtin_nontemporal_load(p)
DI float silu_bf(u16 c) { const float f = __uint_as_float((unsigned)c << 16); const float v = f / (1.f + __expf(-f)); return __uint_as_float(__builtin_bit_cast(unsigned, __builtin_convertvector((f32x2){v, v}, bf2_t)) << 16); }
DI unsigned lds_addr(const void* p) { return (unsigned)(uintptr_t)(const LAS void*)p; }
DI float xhalf(float v) {
  auto rr = __builtin_amdgcn_permlane32_swap(__float_as_uint(v), __float_as_uint(v), false, false);
  return __uint_as_float((threadIdx.x & 32) ? rr[0] : rr[1]);
}
DI float sum_g(float v) {
  auto a = __builtin_amdgcn_permlane16_swap(__float_as_uint(v), __float_as_uint(v), false, false);
  v = __uint_as_float(a[0]) + __uint_as_float(a[1]);
  auto b = __builtin_amdgcn_permlane32_swap(__float_as_uint(v), __float_as_uint(v), false, false);
  return __uint_as_float(b[0]) + __uint_as_float(b[1]);
}
template <int CTRL> DI float dpp_add(float v) { return v + __uint_as_float(__builtin_amdgcn_update_dpp(0u, __float_as_uint(v), CTRL, 0xf, 0xf, true)); }
DI float sum16(float v) {
  v = dpp_add<0x128>(v); v = dpp_add<0x124>(v); v = dpp_add<0x122>(v); return dpp_add<0x121>(v);
}
DI u32x4 modulate8(u32x4 v, f32x4 sc0, f32x4 sc1, f32x4 sh0, f32x4 sh1) {
  unsigned o0, o1, o2, o3; float t0, t1, t2, t3;
  asm volatile(
      "v_fma_mix_f32 %0, %8, %12, %20 op_sel_hi:[1,0,0]\n\tv_fma_mix_f32 %4, %8, %13, %21 op_sel:[1,0,0] op_sel_hi:[1,0,0]\n\t"
      "v_fma_mix_f32 %1, %9, %14, %22 op_sel_hi:[1,0,0]\n\tv_fma_mix_f32 %5, %9, %15, %23 op_sel:[1,0,0] op_sel_hi:[1,0,0]\n\t"
      "v_fma_mix_f32 %2, %10, %16, %24 op_sel_hi:[1,0,0]\n\tv_fma_mix_f32 %6, %10, %17, %25 op_sel:[1,0,0] op_sel_hi:[1,0,0]\n\t"
      "v_fma_mix_f32 %3, %11, %18, %26 op_sel_hi:[1,0,0]\n\tv_fma_mix_f32 %7, %11, %19, %27 op_sel:[1,0,0] op_sel_hi:[1,0,0]\n\t"
      "v_cvt_pk_bf16_f32 %0, %0, %4\n\tv_cvt_pk_bf16_f32 %1, %1, %5\n\tv_cvt_pk_bf16_f32 %2, %2, %6\n\tv_cvt_pk_bf16_f32 %3, %3, %7"
      : "=&v"(o0), "=&v"(o1), "=&v"(o2), "=&v"(o3), "=&v"(t0), "=&v"(t1), "=&v"(t2), "=&v"(t3)
      : "v"(v[0]), "v"(v[1]), "v"(v[2]), "v"(v[3]), "v"(sc0[0]), "v"(sc0[1]), "v"(sc0[2]), "v"(sc0[3]), "v"(sc1[0]), "v"(sc1[1]), "v"(sc1[2]), "v"(sc1[3]),
        "v"(sh0[0]), "v"(sh0[1]), "v"(sh0[2]), "v"(sh0[3]), "v"(sh1[0]), "v"(sh1[1]), "v"(sh1[2]), "v"(sh1[3]));
  return (u32x4){o0, o1, o2, o3};
}
DI float ln_den(float var) { return rbf(__builtin_sqrtf(rbf(rbf(var) + 1e-5f))); }
DI unsigned a16_off(int n, int r) { return (n >> 5) * 1024 + (((n & 31) >> 3) * 16 + r) * 16 + (n & 4) * 2; }
DI int unit0_of(int j) { return j < 6 ? 7 * j : 42 + 6 * (j - 6); }

DI void K0ST(char* p, u32x4 v) { *(u32x4*)p = v; }
constexpr int K0_MOD = 240, K0_LN = 128, K0_RL = 240, K0_WO = 50, K0_INIT = 12, K0_WARM = 24;
constexpr int K0_GRID = K0_WARM + K0_MOD + K0_LN + K0_RL + K0_WO + K0_INIT;

__global__ __launch_bounds__(256) void k0_prep(const u16* __restrict__ x, const u16* __restrict__ wqkv, const u16* __restrict__ wo, const u16* __restrict__ cvec,
                                               const u16* __restrict__ wmod, char* __restrict__ ws) {
  __shared__ __attribute__((aligned(16))) char smem[4 * 4608];
  const int tid = threadIdx.x, lane = tid & 63, wid = __builtin_amdgcn_readfirstlane(tid >> 6);
  int b = blockIdx.x;
  STAMP(0, 0);
  if (b < K0_WARM) {
    u64 pc; unsigned len;
    asm volatile("s_getpc_b64 s[30:31]\n\ts_add_u32 s30, s30, k_mega@rel32@lo+4\n\ts_addc_u32 s31, s31, k_mega@rel32@hi+12\n\ts_mov_b64 %0, s[30:31]\n\t"
                 "s_mov_b32 %1, k_mega_code_end-k_mega" : "=s"(pc), "=s"(len) :: "scc", "s30", "s31");
    l2touch((const char*)pc + min(((b >> 3) * 256 + tid) * 128u, len - 4u), lds_addr(smem) + wid * 256);
    return;
  }
  b -= K0_WARM;
  if (b < K0_MOD) {
    const int cg = b >> 2, ks = b & 3, c = tid & 7, row0 = ks * 160 + (tid >> 3), col0 = cg * 64;
    u32x4 w[5]; u16 cv[5];
#pragma unroll
    for (int i = 0; i < 5; ++i) { w[i] = LDNT((const u32x4*)(wmod + (size_t)(row0 + 32 * i) * 3840 + col0 + c * 8)); cv[i] = cvec[row0 + 32 * i]; }
    float acc[8] = {0, 0, 0, 0, 0, 0, 0, 0};
#pragma unroll
    for (int i = 0; i < 5; ++i) {
      const float s = silu_bf(cv[i]);
#pragma unroll
      for (int e = 0; e < 4; ++e) { acc[2 * e] += s * bflo(w[i][e]); acc[2 * e + 1] += s * bfhi(w[i][e]); }
    }
#pragma unroll
    for (int e = 0; e < 8; ++e) acc[e] = sum_g(dpp_add<0x128>(acc[e]));
    float* red = (float*)smem;
    if (lane < 8) { *(f32x4*)(red + wid * 64 + lane * 8) = (f32x4){acc[0], acc[1], acc[2], acc[3]}; *(f32x4*)(red + wid * 64 + lane * 8 + 4) = (f32x4){acc[4], acc[5], acc[6], acc[7]}; }
    __syncthreads();
    if (tid < 64) ((float*)(ws + OFF_MODP))[ks * 3840 + cg * 64 + tid] = (red[tid] + red[64 + tid]) + (red[128 + tid] + red[192 + tid]);
    STAMP(0, 1); STAMP(0, 13);
    return;
  }
  b -= K0_MOD;
  if (b < K0_LN) {
    b = (b & 7) * 16 + (b >> 3);
    const int row = b * 16 + wid * 4 + (lane >> 4), c = lane & 15;
    const u16* xr = x + (size_t)row * D_ + c * 8;
    u32x4 v[5];
#pragma unroll
    for (int i = 0; i < 5; ++i) v[i] = *(const u32x4*)(xr + i * 128);
    float s = 0.f, q = 0.f;
#pragma unroll
    for (int i = 0; i < 5; ++i)
#pragma unroll
      for (int e = 0; e < 4; ++e) s += bflo(v[i][e]) + bfhi(v[i][e]);
    const float mean = sum16(s) * (1.f / 640.f);
#pragma unroll
    for (int i = 0; i < 5; ++i)
#pragma unroll
      for (int e = 0; e < 4; ++e) { const float a = bflo(v[i][e]) - mean, d = bfhi(v[i][e]) - mean; q += a * a + d * d; }
    const float var = sum16(q) * (1.f / 640.f);
    const float rinv = 1.f / ln_den(var), mb = rbf(mean);
    char* dst = ws + OFF_XHAT + (size_t)row * 1280 + c * 16;
#pragma unroll
    for (int i = 0; i < 5; ++i) {
      u32x4 o;
#pragma unroll
      for (int e = 0; e < 4; ++e) o[e] = pack2h((bflo(v[i][e]) - mb) * rinv, (bfhi(v[i][e]) - mb) * rinv);
      K0ST(dst + i * 256, o);
    }
    STAMP(0, 2); STAMP(0, 13);
    return;
  }
  b -= K0_LN;
  if (b < K0_RL) {
    const int kt = b / 12, G = b % 12, sec = G >> 2, gq = G & 3;
    u32x4 v[3];
#pragma unroll
    for (int i = 0; i < 3; ++i) { const int ci = min(i * 256 + tid, 639); v[i] = LDNT((const u32x4*)(wqkv + (size_t)(kt * 32 + ci / 20) * NQKV + G * 160 + (ci % 20) * 8)); }
#pragma unroll
    for (int i = 0; i < 3; ++i) { const int ci = min(i * 256 + tid, 639); *(u32x4*)(smem + (ci / 20) * 336 + (ci % 20) * 16) = v[i]; }
    __syncthreads();
    const int g = lane >> 4, q = (lane & 15) >> 2, p = lane & 3;
    for (int f = wid; f < 10; f += 4) {
      const int t = f >> 1, x2 = f & 1;
      const int col = sec == 2 ? 16 * f + 4 * p : t < 4 ? 20 * (2 * t + (p >> 1)) + 8 * (p & 1) + 4 * x2 : 20 * (4 * x2 + p) + 16;
      const int T = sec == 2 ? 40 + gq * 5 + t : sec * 20 + (t < 4 ? 4 * gq + t : 16 + gq);
      const char* pp = smem + (8 * g + q) * 336 + col * 2;
      const bf16x4 lo = lds_tr(pp), hi = lds_tr(pp + 4 * 336);
      K0ST(ws + OFF_WQKV + (((size_t)T * 20 + kt) * 2 + x2) * 1024 + lane * 16, __builtin_bit_cast(u32x4, (bf16x8)__builtin_shufflevector(lo, hi, 0, 1, 2, 3, 4, 5, 6, 7)));
    }
    STAMP(0, 3); STAMP(0, 13);
    return;
  }
  b -= K0_RL;
  if (b < K0_WO) {
    const int job = b * 4 + wid, kt = job / 10, ng = job % 10, j8 = lane >> 3;
    char* t = smem + wid * 4608;
    u32x4 v[4];
#pragma unroll
    for (int i = 0; i < 4; ++i) {
      const int c = 4 * kt + i, row = c < 64 ? 20 * (c >> 1) + 4 * (c & 1) + (j8 & 3) + 8 * (j8 >> 2) : 20 * (2 * (c - 64) + (j8 >> 2)) + 16 + (j8 & 3);
      v[i] = LDNT((const u32x4*)(wo + (size_t)row * D_ + ng * 64 + (lane & 7) * 8));
    }
#pragma unroll
    for (int i = 0; i < 4; ++i) *(u32x4*)(t + (i * 8 + j8) * 144 + (lane & 7) * 16) = v[i];
    const char* pp = t + (8 * (lane >> 4) + ((lane & 15) >> 2)) * 144 + (lane & 3) * 8;
#pragma unroll
    for (int f = 0; f < 4; ++f) {
      const bf16x4 lo = lds_tr(pp + f * 32), hi = lds_tr(pp + 4 * 144 + f * 32);
      K0ST(ws + OFF_WO + ((size_t)(ng * 4 + f) * 20 + kt) * 1024 + lane * 16, __builtin_bit_cast(u32x4, (bf16x8)__builtin_shufflevector(lo, hi, 0, 1, 2, 3, 4, 5, 6, 7)));
    }
    return;
  }
  b -= K0_WO;
  {
    u32x4* p = (u32x4*)(ws + (b < 8 ? OFF_XCH + b * 16384 : OFF_CNT + (b - 8) * 16384)) + tid;
    const unsigned f = b < 8 ? ~0u : 0u;
#pragma unroll
    for (int i = 0; i < 4; ++i) p[i * 256] = (u32x4){f, f, f, f};
  }
}

constexpr float QSCALE = 0.22360679774997896f * 1.4426950408889634f;
#define K1W 3
#define K1WB 5
#define K1WB0 2
constexpr int K1_W = K1W, K1_WB = K1WB, K1_WB0 = K1WB0;

template <bool ISV, class FIN>
DI void k1_mma(const char* __restrict__ bsrc, const char* smem, int lane, FIN&& fin) {
  f32x4 acc[8][2];
  constexpr int PD = 4;
  bf16x8 bfr[K1_WB + 1][4], af[PD + 1][2];
  const char* lds[2];
#pragma unroll
  for (int k = 0; k < 2; ++k) lds[k] = smem + k * 8192 + (lane ^ (4 * k + (lane >> 4))) * 16;
#pragma unroll
  for (int i = 0; i < 4 * K1_WB0; ++i) bfr[i >> 2][i & 3] = *(const bf16x8*)(bsrc + i * 1024);
#pragma unroll
  for (int m = 0; m < 8; ++m) { acc[m][0] = (f32x4){}; acc[m][1] = (f32x4){}; }
  BAR();
#pragma unroll
  for (int i = 4 * K1_WB0; i < 4 * K1_WB; ++i) bfr[i >> 2][i & 3] = *(const bf16x8*)(bsrc + i * 1024);
  BAR();
  __builtin_amdgcn_s_setprio(1);
#define K1_RD(t, k) (af[(t) % (PD + 1)][k] = *(const bf16x8*)(lds[k] + (((t) >> 3) % 3) * 16384 + ((t) & 7) * 1024))
#define K1_MM(c, b, a) (c = ISV ? MFMA16(a, b, c) : MFMA16(b, a, c))
#pragma unroll
  for (int t = 0; t < PD; ++t) { K1_RD(t, 0); K1_RD(t, 1); }
  SCHED();
  sfor<80>([&](auto T) __attribute__((always_inline)) {
    constexpr int t = T, s = t >> 3, m = t & 7, nx = t + PD;
    if constexpr (nx < 80 && (nx & 7) == 0) asm volatile("s_barrier" ::: "memory");
    if constexpr (nx < 80) K1_RD(nx, 0);
    K1_MM(acc[m][0], bfr[s % (K1_WB + 1)][0], af[t % (PD + 1)][0]); K1_MM(acc[m][0], bfr[s % (K1_WB + 1)][2], af[t % (PD + 1)][1]); PIN(acc[m][0]);
    SCHED();
    if constexpr (nx < 80) K1_RD(nx, 1);
    if constexpr (s + K1_WB < 10 && (m & 1)) bfr[(s + K1_WB) % (K1_WB + 1)][m >> 1] = *(const bf16x8*)(bsrc + ((s + K1_WB) * 4 + (m >> 1)) * 1024);
    K1_MM(acc[m][1], bfr[s % (K1_WB + 1)][1], af[t % (PD + 1)][0]); K1_MM(acc[m][1], bfr[s % (K1_WB + 1)][3], af[t % (PD + 1)][1]); PIN(acc[m][1]);
    SCHED();
    if constexpr (t >= 74) { fin(t - 74, acc[t - 74]); SCHED(); }
  });
  fin(6, acc[6]); fin(7, acc[7]);
  __builtin_amdgcn_s_setprio(0);
}

#define TARG
#define TPASS

DI void phase_qkv(char* __restrict__ ws, rsrc_t rs, char* smem, int wid TARG) {
  const int tid = fresh_tid(), lane = tid & 63;
  const int xcd = blockIdx.x & 7, idx = blockIdx.x >> 3;
  int ctr;
  if ((xcd & 1) && idx >= 28) {
    ctr = C_MOD;
    if (tid < 160) {
      const int col = 1280 + ((xcd >> 1) * 4 + idx - 28) * 160 + tid;
      const float* pp = (const float*)(ws + OFF_MODP) + col;
      float v = rbf((pp[0] + pp[3840]) + (pp[7680] + pp[11520]));
      if (col / 640 == 4) v = rbf(1.f + v);
      stc4(rs, col * 4, OFF_MOD, __float_as_uint(v));
    }
  } else {
    const int mt = (xcd >> 1) * 4 + (idx & 3), nt = (xcd & 1) * 8 + (idx >> 2);
    ctr = C_QKV + nt;
    if (wid < 4) {
      float* cst = (float*)(smem + 49152);
      const int c = lane & 7, rr = lane >> 3;
      const char* src = ws + OFF_XHAT + (size_t)(mt * 128 + wid * 32 + rr) * 1280 + c * 16;
      char* wdst = smem + (c >> 2) * 8192 + wid * 2048 + (c & 3) * 256 + (rr ^ c) * 16;
      u32x4 a[10][4];
      f32x4 c0[4], c1[4];
#pragma unroll
      for (int i = 0; i < 4; ++i) { c0[i] = *(const f32x4*)(ws + OFF_MODP + i * 15360 + tid * 16); if (wid == 0) c1[i] = *(const f32x4*)(ws + OFF_MODP + i * 15360 + 4096 + tid * 16); }
#pragma unroll
      for (int s = 0; s < K1_W; ++s)
#pragma unroll
        for (int m = 0; m < 4; ++m) a[s][m] = *(const u32x4*)(src + m * 10240 + s * 128);
      {
        f32x4 v = (c0[0] + c0[1]) + (c0[2] + c0[3]); const float one = tid >= 160 ? 1.f : 0.f;
        *(f32x4*)(cst + tid * 4) = (f32x4){rbf(one + rbf(v[0])), rbf(one + rbf(v[1])), rbf(one + rbf(v[2])), rbf(one + rbf(v[3]))};
        if (wid == 0) { v = (c1[0] + c1[1]) + (c1[2] + c1[3]); *(f32x4*)(cst + 1024 + tid * 4) = (f32x4){rbf(1.f + rbf(v[0])), rbf(1.f + rbf(v[1])), rbf(1.f + rbf(v[2])), rbf(1.f + rbf(v[3]))}; }
      }
      BAR();
      const float* cp = cst + c * 8;
      f32x4 kc[10][4];
#define K1_KC(s) do { kc[s][0] = *(const f32x4*)(cp + (s) * 64); kc[s][1] = *(const f32x4*)(cp + (s) * 64 + 4); kc[s][2] = *(const f32x4*)(cp + (s) * 64 + 640); kc[s][3] = *(const f32x4*)(cp + (s) * 64 + 644); } while (0)
#pragma unroll
      for (int s = 0; s < 6; ++s) K1_KC(s);
      SCHED();
      u32x4 o[2][4];
#define K1_XF(s, m) (o[(s) & 1][m] = modulate8(a[s][m], kc[s][2], kc[s][3], kc[s][0], kc[s][1]))
#define K1_WR(s, m) (*(u32x4*)(wdst + ((s) % 3) * 16384 + ((m) >> 1) * 1024 + ((m) & 1) * 128) = o[(s) & 1][m])
#pragma unroll
      for (int m = 0; m < 4; ++m) { K1_XF(0, m); K1_WR(0, m); }
      BAR();
      TS(1);
#pragma unroll
      for (int m = 0; m < 4; ++m) a[K1_W][m] = *(const u32x4*)(src + m * 10240 + K1_W * 128);
#pragma unroll
      for (int m = 0; m < 4; ++m) K1_XF(1, m);
      SCHED();
#pragma unroll
      for (int s = 1; s < 10; ++s) {
#pragma unroll
        for (int m = 0; m < 4; ++m) {
          if (m == 0) K1_WR(s, 0);
          if (m < 3) K1_WR(s, m + 1);
          if (s + K1_W < 10) a[s + K1_W][m] = *(const u32x4*)(src + m * 10240 + (s + K1_W) * 128);
          if (s < 5) kc[s + 5][m] = *(const f32x4*)(cp + (s + 5) * 64 + (m & 1) * 4 + (m >> 1) * 640);
          SCHED();
          if (s < 9) K1_XF(s + 1, m);
          SCHED();
        }
        asm volatile("" :: "v"(o[s & 1][0]), "v"(o[s & 1][1]), "v"(o[s & 1][2]), "v"(o[s & 1][3]));
        BAR();
      }
    } else {
      const int cw = wid - 4;
      const char* bsrc = ws + OFF_WQKV + (size_t)(nt * 4 + cw) * 40 * 1024 + lane * 16;
      const int r = lane & 15, g = lane >> 4;
      if (nt >= 10) {
        const unsigned so = OFF_KV + (mt >> 1) * 20480 + 10240 + (mt & 1) * 5120;
        unsigned vo[2];
#pragma unroll
        for (int x = 0; x < 2; ++x) { const unsigned c = (nt - 10) * 128 + cw * 32 + x * 16 + r; vo[x] = c / 20 * 163840u + ((g & 1) * 20 + c % 20) * 16 + (g >> 1) * 8; }
        k1_mma<true>(bsrc, smem, lane, [&](int m, const f32x4 (&a)[2]) __attribute__((always_inline)) {
#pragma unroll
          for (int x = 0; x < 2; ++x) stc8(rs, vo[x] + (m & 3) * 640, so + (m >> 2) * 2560, (u32x2){pack2(a[x][0], a[x][1]), pack2(a[x][2], a[x][3])});
        });
      } else {
        const bool isQ = nt < 5; const float sc = isQ ? QSCALE : 1.f;
        const unsigned so = isQ ? OFF_Q + mt * 5120 : OFF_KV + (mt >> 1) * 20480 + (mt & 1) * 5120, hs = isQ ? 81920u : 163840u;
        const int n5 = isQ ? nt : nt - 5;
        const bool wide = n5 < 4;
        const unsigned vo = wide ? (2 * (n5 * 4 + cw) + (g >> 1)) * hs + (g & 1) * 512 + r * 16 : (8 * cw + g) * hs + 1024 + r * 8;
        const f32x4 sc4 = (f32x4){sc, sc, sc, sc};
        k1_mma<false>(bsrc, smem, lane, [&](int m, const f32x4 (&a)[2]) __attribute__((always_inline)) {
          const f32x4 v0 = mul4s(a[0], sc4), v1 = mul4s(a[1], sc4);
          const u32x4 w = (u32x4){pack2(v0[0], v0[1]), pack2(v0[2], v0[3]), pack2(v1[0], v1[1]), pack2(v1[2], v1[3])};
          if (wide) stc16(rs, vo + (m & 1) * 256, so + (m >> 1) * 1280, w);
          else { stc8(rs, vo + (m & 1) * 128, so + (m >> 1) * 1280, (u32x2){w[0], w[1]}); stc8(rs, vo + (m & 1) * 128, so + 4 * hs + (m >> 1) * 1280, (u32x2){w[2], w[3]}); }
        });
      }
    }
  }
  TS(3);
  waitv<0>();
  BAR();
  TS(4);
  if (wid == 7 && lane < 8) __hip_atomic_fetch_add((GAS unsigned*)(ws + OFF_CNT + (lane * 16 + ctr) * 256), 1u, __ATOMIC_RELAXED, __HIP_MEMORY_SCOPE_AGENT);
  if (ctr == C_MOD && wid == 7) { __builtin_amdgcn_s_sleep(127); __builtin_amdgcn_s_sleep(60); }
}

template <bool FINAL> DI constexpr int proj_pos(int f) { return FINAL ? (f < 96 ? 65536 + f * 1024 : (f - 96) * 1024) : 17408 + (f % 20 >= 10 ? 81920 : 0) + (f / 20 * 10 + f % 10) * 1024; }
template <bool FINAL> DI u32x4 proj_ld(rsrc_t rs, int i, int wid, int lane) {
  constexpr int NF = FINAL ? 135 : 100;
  return ldc16(rs, lane * 16, (FINAL ? OFF_WOUT : OFF_WO) + (((blockIdx.x >> 3) & 7) * NF + min(i * 8 + wid, NF - 1)) * 1024);
}
template <bool FINAL> DI void proj_park(char* smem, int i, int wid, int lane, u32x4 v) {
  constexpr int NF = FINAL ? 135 : 100;
  *(u32x4*)(smem + proj_pos<FINAL>(min(i * 8 + wid, NF - 1)) + lane * 16) = v;
}

DI void split3(float m, unsigned& w_h0, unsigned& w_h1) {
  const float n = -m; const float a = rbf(n), b = rbf(n - a), c = rbf(n - a - b);
  w_h0 = pack2(a, b); w_h1 = pack2(c, 0.f);
}

DI void phase_attn(char* __restrict__ ws, rsrc_t rs, const char* __restrict__ wsp, const u16* __restrict__ win, const u16* __restrict__ wout, char* smem, int wid TARG) {
  const int tid = fresh_tid(), lane = tid & 63;
  const int xcd = blockIdx.x & 7, idx = blockIdx.x >> 3, head = xcd * 4 + (idx >> 3), qb = idx & 7;
  const int r = lane & 31, h = lane >> 5, qg = wid & 3, kh = wid >> 2;
  if (wid == 7) {
    const int l7 = lane & 7;
    const int c = l7 == 0 ? head >> 3 : l7 == 1 ? 4 : l7 == 2 ? 5 + (head >> 3) : l7 == 3 ? 9 : l7 == 4 ? 10 + (20 * head) / 128 : l7 == 5 ? 10 + (20 * head + 19) / 128 : C_MOD;
    poll_ge(ws, xcd * 16 + c, 16);
  }
  BAR();
  TS(5);
  unsigned kvo = OFF_KV + head * 163840, qo = OFF_Q + head * 81920 + (qb * 8 + qg * 2) * 1280;
  const unsigned kconst = h == 0 ? 0x3f803f80u : 0x00003f80u;
  const unsigned so16 = (tid & 255) * 16, l16 = lane * 16, l2 = 1024 + r * 8 + h * 4;
  const int blk = blockIdx.x;
  u32x4 wq[13];
  for (int attempt = 0; attempt < 2; ++attempt) {
    asm volatile("" : "+s"(kvo), "+s"(qo));
    const char* kvs = ws + kvo + kh * 81920 + so16;
    const unsigned ldst = lds_addr(smem) + kh * 81920 + qg * 1024;
    u32x4 qA1w, qB1w, k1w; unsigned qA2w, qB2w, k2, m0sv;
    asm volatile(
        "s_nop 4\n\tbuffer_load_dwordx4 %0, %7, %9, %19 offen sc1\n\tbuffer_load_dwordx4 %1, %7, %9, %10 offen sc1\n\tbuffer_load_dwordx4 %2, %7, %9, %11 offen sc1\n\t"
        "buffer_load_dword %3, %8, %9, %19 offen sc1\n\tbuffer_load_dword %4, %8, %9, %10 offen sc1\n\tbuffer_load_dword %5, %8, %9, %11 offen sc1\n\t"
        "s_mov_b32 %6, m0\n\ts_mov_b32 m0, %17\n\ts_nop 0\n\tglobal_load_lds_dwordx4 %12, off sc1\n\t"
        "s_add_u32 m0, %18, 0x1000\n\ts_nop 0\n\tglobal_load_lds_dwordx4 %13, off sc1\n\t"
        "s_add_u32 m0, %18, 0x2000\n\ts_nop 0\n\tglobal_load_lds_dwordx4 %14, off sc1\n\t"
        "s_add_u32 m0, %18, 0x3000\n\ts_nop 0\n\tglobal_load_lds_dwordx4 %15, off sc1\n\t"
        "s_add_u32 m0, %18, 0x4000\n\ts_nop 0\n\tglobal_load_lds_dwordx4 %16, off sc1\n\t"
        "s_mov_b32 m0, %6\n\ts_waitcnt vmcnt(5)"
        : "=&v"(qA1w), "=&v"(qB1w), "=&v"(k1w), "=&v"(qA2w), "=&v"(qB2w), "=&v"(k2), "=&s"(m0sv)
        : "v"(l16), "v"(l2), "s"(rs), "s"(qo + kh * 1280), "s"(kvo), "v"(kvs + (wid == 0 ? 4096 : 0)), "v"(kvs + 4096), "v"(kvs + 8192), "v"(kvs + 12288), "v"(kvs + 16384),
          "s"(ldst + (wid == 0 ? 4096 : 0)), "s"(ldst), "s"(qo + (kh ^ 1) * 1280)
        : "memory", "scc");
    if (wid == 0) { const unsigned f = (tid & 1) ? 0u : 0x3f803f80u; *(u32x4*)(smem + l16) = (u32x4){f, f, f, f}; }
    const bf16x8 qA1 = BF8(qA1w), qB1 = BF8(qB1w);
    bf16x8 k1 = BF8(k1w);
    TS(30);
    float mA = -INFINITY, mB = -INFINITY;
    {
      const bf16x4 qA2 = BF4(((u32x2){qA2w, 0u})), qB2 = BF4(((u32x2){qB2w, 0u}));
      const int nt = attempt ? 64 : 1;
      bf16x8 t1 = k1; unsigned t2 = k2;
      for (int t = 0; t < nt; ++t) {
        if (t) { const unsigned to = kvo + (t >> 3) * 20480 + (t & 7) * 1280; t1 = BF8(ldc16(rs, l16, to)); t2 = ldc4(rs, l2, to); }
        const bf16x4 t2v = BF4(((u32x2){t2, kconst}));
        f32x16 a = MFMA32(t1, qA1, (f32x16){}); a = MFMA32K8(t2v, qA2, a);
        f32x16 b = MFMA32(t1, qB1, (f32x16){}); b = MFMA32K8(t2v, qB2, b);
#pragma unroll
        for (int i = 0; i < 16; ++i) { mA = fmaxf(mA, a[i]); mB = fmaxf(mB, b[i]); }
      }
      mA = fmaxf(mA, xhalf(mA)); mB = fmaxf(mB, xhalf(mB));
    }
    unsigned a0, a1, b0, b1; split3(mA, a0, a1); split3(mB, b0, b1);
    const bf16x4 qA2 = BF4(((u32x2){qA2w, h ? a1 : a0})), qB2 = BF4(((u32x2){qB2w, h ? b1 : b0}));
    char* lh = smem + kh * 81920;
    TS(31);
    waitv<0>();
    BAR();
    TS(6);
    f32x16 oA = (f32x16){}, oB = (f32x16){};
    const char* kp1 = lh + l16;
    const char* kp2 = lh + l2;
    const bool vreal = r < 20;
    const char* vp = vreal ? lh + 10240 + (h * 20 + r) * 16 : smem + (r == 20 ? 0 : 16);
    const int vinc = vreal ? 1280 : 0, vjump = vreal ? 10240 : 0;
    if (kh) { k1 = *(const bf16x8*)kp1; k2 = *(const unsigned*)kp2; }
    bf16x8 kf1[2], vf[2][2]; u32x2 kf2[2];
    kf1[0] = k1; kf2[0] = (u32x2){k2, kconst}; kf2[1] = (u32x2){0u, kconst};
    vf[1][0] = (bf16x8){}; vf[1][1] = (bf16x8){};
    u32x4 pA0, pA1, pB0 = (u32x4){}, pB1 = (u32x4){};
    f32x4 e0 = (f32x4){}, e1;
    f32x16 sA = MFMA32(kf1[0], qA1, (f32x16){}), sB;
    sA = MFMA32K8(BF4(kf2[0]), qA2, sA);
#define EX2(x) __builtin_amdgcn_exp2f(x)
#define GRP(MF, S, i, EN, EP, P, w) do { MF; SCHED(); EN[0] = EX2(S[i]); P[w] = pack2(EP[0], EP[1]); EN[1] = EX2(S[i + 1]); P[w + 1] = pack2(EP[2], EP[3]); \
      EN[2] = EX2(S[i + 2]); EN[3] = EX2(S[i + 3]); PIN(P); PIN(EN); SCHED(); } while (0)
    const unsigned relsc = lds_addr(smem) + 768;
    auto group = [&](auto FIRST, int jump, int j) __attribute__((always_inline)) {
#pragma unroll
      for (int t = 0; t < 8; ++t) {
        const int c = t & 1, n = c ^ 1;
        if (FIRST) { if (t == 0) __builtin_amdgcn_s_setprio(3); if (t == 3) __builtin_amdgcn_s_setprio(2); if (t == 5) __builtin_amdgcn_s_setprio(1); if (t == 6) __builtin_amdgcn_s_setprio(0); if (t == 7) __builtin_amdgcn_s_setprio(3); }
        else {
          if (t == 4 && j == 2) __builtin_amdgcn_s_setprio(2);
          if (t == 3 && j == 3) {
            __builtin_amdgcn_s_setprio(1);
#pragma unroll
            for (int i = 0; i < 13; ++i) wq[i] = proj_ld<false>(rs, i, wid, lane);
          }
          if (t == 6 && j == 3) { __builtin_amdgcn_s_setprio(0); if (wid == 7) glds4c(ws + OFF_CNT + (C_REL + (lane & 7)) * 256, relsc); }
        }
        if (FIRST && t < 5) {
#pragma unroll
          for (int i = 5 + 3 * t; i < 8 + 3 * t; ++i) glds16c(kvs + i * 4096, ldst + i * 4096);
        }
        if (t == 7) {
          if (FIRST) { waitv<0>(); BAR(); }
          kp1 += jump; kp2 += jump;
        }
        SCHED();
        kf1[n] = *(const bf16x8*)(kp1 + ((t + 1) & 7) * 1280); kf2[n][0] = *(const unsigned*)(kp2 + ((t + 1) & 7) * 1280);
        GRP(sB = MFMA32(kf1[c], qB1, (f32x16){}), sA, 0, e1, e0, pB1, 2);
        vf[c][0] = *(const bf16x8*)vp; vf[c][1] = *(const bf16x8*)(vp + 640); vp += vinc;
        GRP(sB = MFMA32K8(BF4(kf2[c]), qB2, sB), sA, 4, e0, e1, pA0, 0);
        GRP(oB = MFMA32(vf[n][0], BF8(pB0), oB), sA, 8, e1, e0, pA0, 2);
        GRP(oB = MFMA32(vf[n][1], BF8(pB1), oB), sA, 12, e0, e1, pA1, 0);
        GRP(sA = MFMA32(kf1[n], qA1, (f32x16){}), sB, 0, e1, e0, pA1, 2);
        GRP(sA = MFMA32K8(BF4(kf2[n]), qA2, sA), sB, 4, e0, e1, pB0, 0);
        GRP(oA = MFMA32(vf[c][0], BF8(pA0), oA), sB, 8, e1, e0, pB0, 2);
        GRP(oA = MFMA32(vf[c][1], BF8(pA1), oA), sB, 12, e0, e1, pB1, 0);
      }
      vp += vjump;
    };
    group(std::true_type{}, 20480, 0);
    TS(7);
    const int j2 = (blk - 135) * 4 + qg;
    const bool side = kh == 0 && attempt == 0 && j2 < 270;
    char* stile = smem + 1024 + qg * 4096;
    const char* svec = smem + 81920 + qg * 256;
    const int kt = blk < 135 ? 4 * (blk % 5) + qg : j2 / 10, ng = blk < 135 ? blk / 5 : j2 % 10;
    if (side) {
      const u16* W = blk < 135 ? win : wout; const int N = blk < 135 ? F_ : D_;
      const int j8 = lane >> 3;
#pragma unroll
      for (int i = 0; i < 4; ++i) {
        const int row = kt * 32 + i * 8 + j8;
        glds16(W + (size_t)row * N + ng * 64 + ((lane & 7) ^ j8) * 8, lds_addr(stile) + i * 1024);
      }
      glds4c((const float*)(ws + OFF_MOD) + (4 - h) * 640 + (kt < 20 ? kt : 0) * 32 + r, lds_addr(svec));
    }
#pragma nounroll
    for (int j = 1; j < 4; ++j) {
      if (j == 2) {
        if (kh) l2touch(wsp + ((size_t)(blk * 4 + qg) * 64 + lane) * 128, lds_addr(smem) + 256);
        else if (side) {
          waitv<0>();
          const int g4 = lane >> 4, r4 = lane & 15, row = 8 * g4 + (r4 >> 2);
          const f32x4 sv0 = *(const f32x4*)(svec + (r4 & 1) * 128 + g4 * 32), sv1 = *(const f32x4*)(svec + (r4 & 1) * 128 + g4 * 32 + 16);
          const bf16x8 vec = BF8(((u32x4){pack2(sv0[0], sv0[1]), pack2(sv0[2], sv0[3]), pack2(sv1[0], sv1[1]), pack2(sv1[2], sv1[3])}));
          const unsigned nks = blk < 135 ? 20 : 27, dso = (blk < 135 ? OFF_WIN : OFF_WOUT) + (ng * 4 * nks + kt) * 1024;
          bf16x8 fr[4]; f32x4 pr[4];
#pragma unroll
          for (int f = 0; f < 4; ++f) {
            const int ch = 2 * f + ((r4 & 3) >> 1);
            const bf16x4 lo = lds_tr(stile + row * 128 + (ch ^ (row & 7)) * 16 + (r4 & 1) * 8), hi = lds_tr(stile + (row + 4) * 128 + (ch ^ ((row + 4) & 7)) * 16 + (r4 & 1) * 8);
            fr[f] = __builtin_shufflevector(lo, hi, 0, 1, 2, 3, 4, 5, 6, 7);
          }
#pragma unroll
          for (int f = 0; f < 4; ++f) { stc16(rs, l16, dso + f * nks * 1024, __builtin_bit_cast(u32x4, fr[f])); pr[f] = MFMA16(fr[f], vec, (f32x4){}); }
          if (r4 < 2) {
#pragma unroll
            for (int f = 0; f < 4; ++f) *(f32x4*)(stile + 3072 + r4 * 256 + f * 64 + g4 * 16) = pr[f];
          }
        }
      }
      if (j == 3 && kh == 0 && attempt == 0) { waitv<0>(); arrive(ws, C_REL + xcd, lane); }
      group(std::false_type{}, j < 3 ? 20480 : 0, j);
    }
    TS(8);
    pB1[2] = pack2(e0[0], e0[1]); pB1[3] = pack2(e0[2], e0[3]);
    oB = MFMA32(vf[1][0], BF8(pB0), oB); oB = MFMA32(vf[1][1], BF8(pB1), oB);
    {
      f32x4* ex = (f32x4*)(lh + 1024 + qg * 4096) + lane;
#pragma unroll
      for (int g = 0; g < 3; ++g) ex[g * 64] = (f32x4){oA[4 * g], oA[4 * g + 1], oA[4 * g + 2], oA[4 * g + 3]};
    }
    int* flag = (int*)(smem + 528);
    BAR();
    TS(9);
    float o[12];
    {
      const f32x4* ex = (const f32x4*)(smem + (kh ^ 1) * 81920 + 1024 + qg * 4096) + lane;
#pragma unroll
      for (int g = 0; g < 3; ++g) { const f32x4 t = ex[g * 64];
#pragma unroll
        for (int e = 0; e < 4; ++e) o[4 * g + e] = oB[4 * g + e] + t[e]; }
    }
    const float lo = xhalf(o[8]); const float l = h ? o[8] : lo;
    const float rl = __builtin_amdgcn_rcpf(l);
    const int tok = qb * 256 + qg * 64 + kh * 32 + r;
    const unsigned vo = (tok >> 4) * 20480 + (tok & 15) * 16;
    stc16(rs, vo + h * 256, OFF_O + (head >> 1) * 1024 + (head & 1) * 512,
          (u32x4){pack2(o[0] * rl, o[1] * rl), pack2(o[2] * rl, o[3] * rl), pack2(o[4] * rl, o[5] * rl), pack2(o[6] * rl, o[7] * rl)});
    if (h == 0) stc8(rs, vo, OFF_O + 16384 + (head >> 3) * 1024 + ((head >> 1) & 3) * 256 + (head & 1) * 8, (u32x2){pack2(o[8] * rl, o[9] * rl), pack2(o[10] * rl, o[11] * rl)});
    if (wid == 4 && blk < 135 && attempt == 0 && lane < 32) {
      const char* pp = smem + 1024 + 3072 + lane * 16;
      const f32x4 v = (*(const f32x4*)pp + *(const f32x4*)(pp + 4096)) + (*(const f32x4*)(pp + 8192) + *(const f32x4*)(pp + 12288));
      stc16(rs, (lane >> 4) * 8192 + (lane & 15) * 16, OFF_CBP + (blk % 5) * 16384 + (blk / 5) * 256, __builtin_bit_cast(u32x4, v));
    }
    bool ok = (l > 1e-30f) && (l < 1e30f);
#pragma unroll
    for (int i = 0; i < 12; ++i) ok = ok && (fabsf(o[i]) < 3e38f);
    if (!ok && attempt == 0) *flag = 1;
    waitv<0>();
    BAR();
    const int redo = __builtin_amdgcn_readfirstlane(*(volatile LAS int*)flag);
    if (!redo) break;
    BAR();
  }
  TS(10);
  if (wid == 7) arrive(ws, C_ATT + qb, lane);
#pragma unroll
  for (int i = 0; i < 13; ++i) proj_park<false>(smem, i, wid, lane, wq[i]);
  if (!__all(*(volatile LAS unsigned*)(smem + 768 + (lane & 7) * 4) >= 128u)) poll_ge(ws, C_REL + (lane & 7), 128);
}

template <bool FINAL, int KH, class HOOK>
DI void proj_body(char* __restrict__ ws, rsrc_t rs, const u16* __restrict__ x, u16* __restrict__ out, char* smem, int wid, u32x2 (&res)[3], HOOK&& after_mma TARG) {
  const int lane = fresh_tid() & 63;
  const int xcd = blockIdx.x & 7, idx = blockIdx.x >> 3, panel = xcd * 4 + (idx >> 3), cb = idx & 7;
  constexpr int NKS = FINAL ? 27 : 20;
  constexpr int K0 = KH ? (NKS + 1) / 2 : 0, NK = KH ? NKS / 2 : (NKS + 1) / 2, NKEEP = KH ? 2 : 3, UK = KH ? 3 : 0, UG = KH ? 0 : 3;
  const int m = wid & 3, mt = panel * 4 + m, g = lane >> 4, r = lane & 15;
  f32x4 gate[NKEEP], cbv[NKEEP];
#pragma unroll
  for (int i = 0; i < NKEEP; ++i) {
    const int n = (cb * 5 + UK + i) * 16;
    if (!FINAL) { res[i] = *(const u32x2*)(x + (size_t)(mt * 16 + r) * D_ + n + g * 4); cbv[i] = ldc16f(rs, g * 16, OFF_MOD + (4 * 640 + n) * 4); }
    gate[i] = ldc16f(rs, g * 16, OFF_MOD + ((FINAL ? 5 : 2) * 640 + n) * 4);
  }
  if (wid == 7) poll_ge(ws, FINAL ? C_U + panel : C_ATT + (panel >> 2), FINAL ? 8 : 32);
  waitv<0>();
  BAR();
  TS(FINAL ? 27 : 11);
  bf16x8 a[NK];
#pragma unroll
  for (int k = 0; k < NK; ++k) a[k] = BF8(ldc16(rs, lane * 16, (FINAL ? OFF_U : OFF_O) + (mt * NKS + K0 + k) * 1024));
  SCHED();
  constexpr int PD = FINAL ? 5 : 6;
  const char* bl = smem + lane * 16;
  bf16x8 bf[PD + 1][5]; f32x4 acc[5];
#pragma unroll
  for (int u = 0; u < 5; ++u) acc[u] = (f32x4){};
#pragma unroll
  for (int k = 0; k < PD; ++k)
#pragma unroll
    for (int u = 0; u < 5; ++u) bf[k][u] = *(const bf16x8*)(bl + proj_pos<FINAL>(u * NKS + K0 + k));
  SCHED();
#pragma unroll
  for (int k = 0; k < NK; ++k) {
#pragma unroll
    for (int u = 0; u < 5; ++u) {
      if (k + PD < NK) bf[(k + PD) % (PD + 1)][u] = *(const bf16x8*)(bl + proj_pos<FINAL>(u * NKS + K0 + k + PD));
      acc[u] = MFMA16(bf[k % (PD + 1)][u], a[k], acc[u]);
      SCHED();
    }
  }
  TS(FINAL ? 28 : 12);
  f32x4* ex = (f32x4*)(smem + (FINAL ? 40960 : 68608)) + m * 5 * 64 + lane;
#pragma unroll
  for (int i = 0; i < 5 - NKEEP; ++i) ex[(UG + i) * 64] = acc[UG + i];
  f32x4 part[NKEEP];
#pragma unroll
  for (int i = 0; i < NKEEP; ++i) {
    part[i] = fma4s(gate[i], acc[UK + i], (f32x4){bflo(res[i][0]), bfhi(res[i][0]), bflo(res[i][1]), bfhi(res[i][1])});
    PIN(part[i]);
  }
  SCHED();
  after_mma();
  BAR();
  f32x4 oth[NKEEP];
#pragma unroll
  for (int i = 0; i < NKEEP; ++i) oth[i] = ex[(UK + i) * 64];
  f32x4 sv = (f32x4){}, qv = (f32x4){};
#pragma unroll
  for (int i = 0; i < NKEEP; ++i) {
    const f32x4 v = part[i] + gate[i] * oth[i];
    const u32x2 o = (u32x2){pack2(v[0], v[1]), pack2(v[2], v[3])};
    if (FINAL) *(u32x2*)(out + (size_t)(mt * 16 + r) * D_ + (cb * 5 + UK + i) * 16 + g * 4) = o;
    else {
      res[i] = o; PIN(res[i]);
      const f32x4 w = v * cbv[i]; const int un = cb * 5 + UK + i;
      stc8(rs, ((g >> 1) * 16 + r) * 16 + (g & 1) * 8, OFF_X1S + mt * 20480 + un * 512, (u32x2){pack2(w[0], w[1]), pack2(w[2], w[3])});
      sv += v; qv += v * v;
    }
  }
  if (!FINAL) {
    const float s = sum_g((sv[0] + sv[1]) + (sv[2] + sv[3])), q = sum_g((qv[0] + qv[1]) + (qv[2] + qv[3]));
    if (g == 0) stc8(rs, r * 8, OFF_ST1 + ((panel * 16 + cb * 2 + KH) * 64 + m * 16) * 8, (u32x2){__float_as_uint(s), __float_as_uint(q)});
    TS(13);
    waitv<0>();
    BAR();
    TS(14);
    if (wid == 7) arrive(ws, C_X1 + panel, lane);
  }
}

constexpr float GC1 = -2.8853900817779268f * 0.7978845608028654f, GC2 = GC1 * 0.044715f;
DI float gelu_s(float a, float k1, float k3, float den) {
  const float e = __builtin_amdgcn_exp2f(a * __builtin_fmaf(a * a, k3, k1));
  return a * __builtin_amdgcn_rcpf(__builtin_fmaf(e, den, den));
}
DI void gelu8(f32x4& a0, const f32x4& l0, f32x4& a1, const f32x4& l1) {
  float t[8];
#define G8(expr0, expr1) do { _Pragma("unroll") for (int i = 0; i < 4; ++i) { t[i] = opq(expr0); } _Pragma("unroll") for (int i = 0; i < 4; ++i) { t[4 + i] = opq(expr1); } SCHED(); } while (0)
  G8(a0[i] * a0[i], a1[i] * a1[i]);
  G8(__builtin_fmaf(t[i], l0[3], l0[2]), __builtin_fmaf(t[4 + i], l1[3], l1[2]));
  G8(a0[i] * t[i], a1[i] * t[4 + i]);
  G8(__builtin_amdgcn_exp2f(t[i]), __builtin_amdgcn_exp2f(t[4 + i]));
  G8(__builtin_fmaf(t[i], l0[0], l0[0]), __builtin_fmaf(t[4 + i], l1[0], l1[0]));
  G8(__builtin_amdgcn_rcpf(t[i]), __builtin_amdgcn_rcpf(t[4 + i]));
  G8(a0[i] * t[i], a1[i] * t[4 + i]);
  a0 = (f32x4){t[0], t[1], t[2], t[3]}; a1 = (f32x4){t[4], t[5], t[6], t[7]};
}
constexpr int L4_Z1 = 114688, L4_TR = L4_Z1 + 16384, L4_LN = L4_TR + 16384, L4_ST2 = L4_LN + 1024, L4_FIN = L4_ST2 + 3584, L4_CB = L4_FIN + 512, L4_CNT = L4_CB + 2048;
constexpr int K5_R = 7;
constexpr unsigned DEAD = 0x80000000u;
DI int sp_stage(int s) { const int idx = blockIdx.x >> 3; return (s + (idx & 7) * 2) & 15; }
DI unsigned sp_voff(int wid, int lane, int i) { return ((wid & 3) * 16 + i * 8 + (lane >> 3)) * 4096 + (wid >> 2) * 128 + ((lane & 7) ^ (4 * i + (lane >> 4))) * 16; }
DI const char* sp_base(const u16* wsp, int panel, int s) { return (const char*)wsp + (size_t)panel * 262144 + sp_stage(s) * 256; }
DI void sp_prefetch(u32x4 (&wr)[16 - K5_R][2], const u16* __restrict__ wsp, int panel, int wid, int lane) {
  const unsigned v0 = sp_voff(wid, lane, 0), v1 = sp_voff(wid, lane, 1);
#pragma unroll
  for (int s = K5_R; s < 16; ++s) { wr[s - K5_R][0] = *(const u32x4*)(sp_base(wsp, panel, s) + v0); wr[s - K5_R][1] = *(const u32x4*)(sp_base(wsp, panel, s) + v1); }
  asm volatile("" ::: "memory");
}
template <int S0, int S1>
DI void sp_ring(const u16* __restrict__ wsp, char* smem, int panel, int wid, int lane) {
  const unsigned v0 = sp_voff(wid, lane, 0), v1 = sp_voff(wid, lane, 1);
#pragma unroll
  for (int s = S0; s < S1; ++s) { glds16(sp_base(wsp, panel, s) + v0, lds_addr(smem) + wid * 2048 + s * 16384); glds16(sp_base(wsp, panel, s) + v1, lds_addr(smem) + wid * 2048 + s * 16384 + 1024); }
}
DI unsigned win_boff(int wid, int lane) { const int cb = (blockIdx.x >> 3) & 7; return lane * 16 + (wid == 6 && cb >= 6 ? DEAD : 0u); }
DI unsigned win_soff(int wid, bool z2) { return OFF_WIN + ((z2 ? 54 : 0) + unit0_of((blockIdx.x >> 3) & 7) + wid) * 20480; }
#define WIN_LD(so, k) BF8(ldc16(rs, bo + ((k) & 3) * 1024, (so) + ((k) >> 2) * 4096))
DI void win_prefetch(rsrc_t rs, int wid, bf16x8 (&b2)[20]) {
  const unsigned bo = win_boff(wid, fresh_tid() & 63), so = win_soff(wid, true);
#pragma unroll
  for (int k = 0; k < 20; ++k) b2[k] = WIN_LD(so, k);
}
template <class HOOK>
DI void win_mma(const char* al, const bf16x8 (&b)[20], f32x4 (&acc)[4], HOOK&& hook) {
  constexpr int PD = 4;
  bf16x8 af[PD + 1][2];
#define WIN_RD(i) do { af[(i) % (PD + 1)][0] = *(const bf16x8*)(al + ((i) & 3) * 20480 + ((i) >> 2) * 2048); af[(i) % (PD + 1)][1] = *(const bf16x8*)(al + ((i) & 3) * 20480 + ((i) >> 2) * 2048 + 1024); } while (0)
#pragma unroll
  for (int i = 0; i < PD; ++i) WIN_RD(i);
  SCHED();
  sfor<40>([&](auto I) __attribute__((always_inline)) {
    constexpr int i = I, m = i & 3, k = (i >> 2) * 2;
    if constexpr (i + PD < 40) WIN_RD(i + PD);
    acc[m] = MFMA16(b[k], af[i % (PD + 1)][0], acc[m]);
    acc[m] = MFMA16(b[k + 1], af[i % (PD + 1)][1], acc[m]); PIN(acc[m]);
    hook(I);
    SCHED();
  });
}
DI void stage_x1s(char* __restrict__ ws, char* smem, int panel, int wid, int tid) {
  const char* src = ws + OFF_X1S + (size_t)panel * 81920 + tid * 16;
#pragma unroll
  for (int i = 0; i < 10; ++i) glds16c(src + i * 8192, lds_addr(smem) + wid * 1024 + i * 8192);
}
DI void win_tail(char* __restrict__ ws, const u16* __restrict__ wsp, char* smem, int panel, int cb, int wid, int lane TARG) {
  BAR();
  TS(22);
  if (wid == 7 && lane < 4) __hip_atomic_fetch_add((GAS unsigned*)(ws + OFF_CNT + (C_Z2 + lane * 8 + cb) * 256), 1u, __ATOMIC_RELAXED, __HIP_MEMORY_SCOPE_AGENT);
}

template <bool EARLY>
DI void win_body(char* __restrict__ ws, rsrc_t rs, const u16* __restrict__ wsp, char* smem, int wid, const bf16x8 (&b2)[20], u32x4 (&wr)[16 - K5_R][2] TARG) {
  const int tid = fresh_tid(), lane = tid & 63, g = lane >> 4, r = lane & 15;
  const int xcd = blockIdx.x & 7, idx = blockIdx.x >> 3, panel = xcd * 4 + (idx >> 3), cb = idx & 7, u0 = unit0_of(cb);
  float* lnp = (float*)(smem + L4_LN); float* st2 = (float*)(smem + L4_ST2); float* fin = (float*)(smem + L4_FIN); float* cbl = (float*)(smem + L4_CB);
  int* flg = (int*)(smem + L4_CNT);
  __builtin_amdgcn_s_setprio(1);
  const unsigned bo = win_boff(wid, lane), so1 = win_soff(wid, false);
  bf16x8 b1[20];
  BAR();
  TS(15);
  stage_x1s(ws, smem, panel, wid, tid);
#pragma unroll
  for (int k = 0; k < 4; ++k) b1[k] = WIN_LD(so1, k);
  waitv<4>();
  BAR();
  TS(16);
  f32x4 ln[4], z2[4], z1[4];
#pragma unroll
  for (int m = 0; m < 4; ++m) ln[m] = *(const f32x4*)(lnp + (m * 16 + r) * 4);
  {
    const f32x4 cs2 = *(const f32x4*)(cbl + 112 + wid * 16 + g * 4), bi2 = *(const f32x4*)(cbl + 256 + 112 + wid * 16 + g * 4);
    const f32x4 cs1 = *(const f32x4*)(cbl + wid * 16 + g * 4), bi1 = *(const f32x4*)(cbl + 256 + wid * 16 + g * 4);
#pragma unroll
    for (int m = 0; m < 4; ++m) {
#pragma unroll
      for (int e = 0; e < 4; ++e) { z2[m][e] = opq(__builtin_fmaf(ln[m][1], cs2[e], opq(ln[m][0] * bi2[e]))); z1[m][e] = opq(__builtin_fmaf(ln[m][1], cs1[e], opq(ln[m][0] * bi1[e]))); }
      PIN(z1[m]);
    }
  }
  win_mma(smem + lane * 16, b2, z2, [&](auto I) __attribute__((always_inline)) { constexpr int i = I; if constexpr (i < 32 && (i & 1)) b1[4 + i / 2] = WIN_LD(so1, 4 + i / 2); });
  TS(17);
  gelu8(z2[0], ln[0], z2[1], ln[1]); gelu8(z2[2], ln[2], z2[3], ln[3]);
  {
    float s[4], q[4];
#pragma unroll
    for (int m = 0; m < 4; ++m) { s[m] = opq(opq(z2[m][0] + z2[m][1]) + opq(z2[m][2] + z2[m][3])); q[m] = opq(__builtin_fmaf(z2[m][0], z2[m][0], opq(z2[m][1] * z2[m][1])) + opq(__builtin_fmaf(z2[m][2], z2[m][2], opq(z2[m][3] * z2[m][3])))); }
    auto pa = __builtin_amdgcn_permlane32_swap(__float_as_uint(s[0]), __float_as_uint(s[2]), false, false);
    auto pb = __builtin_amdgcn_permlane32_swap(__float_as_uint(s[1]), __float_as_uint(s[3]), false, false);
    auto pc = __builtin_amdgcn_permlane32_swap(__float_as_uint(q[0]), __float_as_uint(q[2]), false, false);
    auto pd = __builtin_amdgcn_permlane32_swap(__float_as_uint(q[1]), __float_as_uint(q[3]), false, false);
    const float sa = opq(__uint_as_float(pa[0]) + __uint_as_float(pa[1])), sb = opq(__uint_as_float(pb[0]) + __uint_as_float(pb[1]));
    const float qa = opq(__uint_as_float(pc[0]) + __uint_as_float(pc[1])), qb = opq(__uint_as_float(pd[0]) + __uint_as_float(pd[1]));
    auto ps = __builtin_amdgcn_permlane16_swap(__float_as_uint(sa), __float_as_uint(sb), false, false);
    auto pq = __builtin_amdgcn_permlane16_swap(__float_as_uint(qa), __float_as_uint(qb), false, false);
    *(f32x2*)(st2 + (wid * 64 + lane) * 2) = (f32x2){opq(__uint_as_float(ps[0]) + __uint_as_float(ps[1])), opq(__uint_as_float(pq[0]) + __uint_as_float(pq[1]))};
  }
  asm volatile("" ::: "memory");
  if (lane == 0) __hip_atomic_fetch_add(flg, 1, __ATOMIC_RELAXED, __HIP_MEMORY_SCOPE_WORKGROUP);
  asm volatile("" ::: "memory");
  __builtin_amdgcn_s_setprio(0);
  TS(18);
  const unsigned wvo[2] = {sp_voff(wid, lane, 0), sp_voff(wid, lane, 1)};
  win_mma(smem + lane * 16, b1, z1, [&](auto I) __attribute__((always_inline)) {
    constexpr int i = I, j = i < 8 ? i : (i & 1) ? -1 : 4 + i / 2;
    if constexpr (EARLY && j >= 0 && j < 18) wr[j >> 1][j & 1] = *(const u32x4*)(sp_base(wsp, panel, K5_R + (j >> 1)) + wvo[j & 1]);
  });
  gelu8(z1[0], ln[0], z1[1], ln[1]); gelu8(z1[2], ln[2], z1[3], ln[3]);
#pragma unroll
  for (int mm = 0; mm < 2; ++mm)
    *(u32x4*)(smem + L4_Z1 + wid * 2048 + mm * 1024 + lane * 16) = (u32x4){pack2(z1[2 * mm][0], z1[2 * mm][1]), pack2(z1[2 * mm][2], z1[2 * mm][3]), pack2(z1[2 * mm + 1][0], z1[2 * mm + 1][1]), pack2(z1[2 * mm + 1][2], z1[2 * mm + 1][3])};
  TS(20);
  BAR();
  TS(21);
  char* tr = smem + L4_TR + wid * 2048;
#pragma unroll
  for (int m = 0; m < 4; ++m) {
    const f32x2 f = *(const f32x2*)(fin + (m * 16 + r) * 2);
    const f32x4 v = z2[m] * f[0] - f[1];
    *(u32x2*)(tr + (m * 16 + r) * 32 + g * 8) = (u32x2){pack2(v[0], v[1]), pack2(v[2], v[3])};
  }
  {
    const char* p = tr + (8 * g + (r >> 2)) * 32 + (r & 3) * 8;
    const bf16x4 lo0 = lds_tr(p), hi0 = lds_tr(p + 128), lo1 = lds_tr(p + 1024), hi1 = lds_tr(p + 1152);
    stc16(rs, bo, OFF_Z2 + (panel * 2 * 54 + u0 + wid) * 1024, __builtin_bit_cast(u32x4, (bf16x8)__builtin_shufflevector(lo0, hi0, 0, 1, 2, 3, 4, 5, 6, 7)));
    stc16(rs, bo, OFF_Z2 + ((panel * 2 + 1) * 54 + u0 + wid) * 1024, __builtin_bit_cast(u32x4, (bf16x8)__builtin_shufflevector(lo1, hi1, 0, 1, 2, 3, 4, 5, 6, 7)));
  }
  SCHED();
  waitv<0>();
  win_tail(ws, wsp, smem, panel, cb, wid, lane TPASS);
  sp_ring<0, 5>(wsp, smem, panel, wid, lane);
  if (!EARLY) sp_prefetch(wr, wsp, panel, wid, lane);
}

DI void win_agent(char* __restrict__ ws, rsrc_t rs, const u16* __restrict__ wsp, char* smem, u32x4 (&wr)[16 - K5_R][2] TARG) {
  const int tid = fresh_tid(), lane = tid & 63, wid = 7;
  const int xcd = blockIdx.x & 7, idx = blockIdx.x >> 3, panel = xcd * 4 + (idx >> 3), cb = idx & 7, u0 = unit0_of(cb);
  float* lnp = (float*)(smem + L4_LN); float* st2 = (float*)(smem + L4_ST2); float* fin = (float*)(smem + L4_FIN); float* cbl = (float*)(smem + L4_CB);
  int* cnt = (int*)(smem + L4_CNT);
  __builtin_amdgcn_s_setprio(2);
  if (lane == 0) *cnt = 0;
  poll_ge(ws, lane < 8 ? C_ATT + lane : C_X1 + panel, lane < 8 ? 32 : 8);
  BAR();
  stage_x1s(ws, smem, panel, wid, tid);
  {
    u32x2 t[16]; f32x4 cp[5][2];
#pragma unroll
    for (int i = 0; i < 16; ++i) t[i] = __builtin_amdgcn_raw_buffer_load_b64(rs, lane * 8, OFF_ST1 + (panel * 16 + i) * 512, 16);
    const unsigned co = lane < 28 ? (u0 * 16 + lane * 4) * 4 : ((54 + u0) * 16 + min(lane - 28, 27) * 4) * 4;
#pragma unroll
    for (int i = 0; i < 5; ++i) { cp[i][0] = ldc16f(rs, co, OFF_CBP + i * 16384); cp[i][1] = ldc16f(rs, co, OFF_CBP + i * 16384 + 8192); }
    float s = 0.f, q = 0.f;
#pragma unroll
    for (int i = 0; i < 16; ++i) { s += __uint_as_float(t[i][0]); q += __uint_as_float(t[i][1]); }
    const float mean = s * (1.f / 640.f), var = fmaxf(q * (1.f / 640.f) - mean * mean, 0.f);
    const float den = ln_den(var), ri = 1.f / den;
    *(f32x4*)(lnp + lane * 4) = (f32x4){den, -rbf(mean), GC1 * ri, GC2 * ri * ri * ri};
#pragma unroll
    for (int v = 0; v < 2; ++v) *(f32x4*)(cbl + v * 256 + lane * 4) = ((cp[0][v] + cp[1][v]) + (cp[2][v] + cp[3][v])) + cp[4][v];
  }
  waitv<0>();
  BAR();
  __builtin_amdgcn_s_setprio(0);
  __builtin_amdgcn_s_sleep(8);
  sp_prefetch(wr, wsp, panel, wid, lane);
#pragma nounroll
  for (int w = 0; w < 8; ++w) sp_ring<5, 7>(wsp, smem, panel, w, lane);
  __builtin_amdgcn_s_setprio(2);
#pragma nounroll
  for (unsigned spin = 0; spin < (1u << 22); ++spin) {
    if (__hip_atomic_load(cnt, __ATOMIC_RELAXED, __HIP_MEMORY_SCOPE_WORKGROUP) >= 7) break;
    __builtin_amdgcn_s_sleep(1);
  }
  asm volatile("" ::: "memory");
  float s, q;
  {
    f32x2 t[7];
#pragma unroll
    for (int i = 0; i < 7; ++i) t[i] = *(const f32x2*)(st2 + (i * 64 + lane) * 2);
    s = t[0][0]; q = t[0][1];
#pragma unroll
    for (int i = 1; i < 6; ++i) { s = opq(s + t[i][0]); q = opq(q + t[i][1]); }
    if (cb < 6) { s = opq(s + t[6][0]); q = opq(q + t[6][1]); }
  }
  GAS u64* xch = (GAS u64*)(ws + OFF_XCH) + panel * 512;
  unsigned qb = __float_as_uint(q); if (qb == ~0u) qb = 0x7fc00000u;
  __hip_atomic_store(xch + cb * 64 + lane, ((u64)qb << 32) | __float_as_uint(s), __ATOMIC_RELAXED, __HIP_MEMORY_SCOPE_AGENT);
  TS(19);
  u64 v[8];
#pragma nounroll
  for (unsigned spin = 0; spin < (1u << 19); ++spin) {
    bool ok = true;
#pragma unroll
    for (int i = 0; i < 8; ++i) { v[i] = __hip_atomic_load(xch + i * 64 + lane, __ATOMIC_RELAXED, __HIP_MEMORY_SCOPE_AGENT); ok = ok && ((unsigned)(v[i] >> 32) != ~0u); }
    if (__all(ok)) break;
    __builtin_amdgcn_s_sleep(1);
  }
  s = 0.f; q = 0.f;
#pragma unroll
  for (int i = 0; i < 8; ++i) { s = opq(s + __uint_as_float((unsigned)v[i])); q = opq(q + __uint_as_float((unsigned)(v[i] >> 32))); }
  const float mean = opq(s * (1.f / 864.f)), var = fmaxf(opq(q * (1.f / 864.f)) - mean * mean, 0.f);
  const float ri = 1.f / ln_den(var);
  *(f32x2*)(fin + lane * 2) = (f32x2){ri, rbf(mean) * ri};
  __builtin_amdgcn_s_setprio(0);
  TS(20);
  BAR();
  __builtin_amdgcn_s_sleep(6);
  sp_ring<0, 5>(wsp, smem, panel, wid, lane);
  win_tail(ws, wsp, smem, panel, cb, wid, lane TPASS);
}

#define SP_LB 4
DI void phase_sp(char* __restrict__ ws, rsrc_t rs, const u16* __restrict__ bsp, char* smem, int wid, const u32x4 (&wr)[16 - K5_R][2] TARG) {
  const int tid = fresh_tid(), lane = tid & 63;
  const int xcd = blockIdx.x & 7, idx = blockIdx.x >> 3, panel = xcd * 4 + (idx >> 3), cb = idx & 7;
  const int kh = wid >> 2, up = wid & 3, u0 = unit0_of(cb) + 2 * up, nu = cb < 6 ? 7 : 6, g = lane >> 4, r = lane & 15;
  const unsigned dead[2] = {2 * up < nu ? 0u : DEAD, 2 * up + 1 < nu ? 0u : DEAD};
  const unsigned bvo[2] = {lane * 16 + dead[0], lane * 16 + 1024 + dead[1]};
  u16 bb[2];
#pragma unroll
  for (int j = 0; j < 2; ++j) bb[j] = bsp[panel * 64 + (2 * kh + j) * 16 + r];
  const char* al[2][2];
#pragma unroll
  for (int k = 0; k < 2; ++k)
#pragma unroll
    for (int j = 0; j < 2; ++j) al[k][j] = smem + kh * 8192 + (j ^ kh) * 4096 + r * 128 + ((4 * k + g) ^ (r >> 1)) * 16;
  constexpr int LB = SP_LB, PD = 3;
  bf16x8 bq[LB + 1][4], af[PD + 1][2]; f32x4 acc[2][4]; u32x4 wq[17];
  if (wid == 7) poll_ge(ws, C_Z2 + (panel >> 3) * 8 + cb, 32);
#pragma unroll
  for (int i = 0; i < 2; ++i) {
    const float b0 = bf2f(bb[0]), b1 = bf2f(bb[1]);
    acc[i][0] = (f32x4){b0, b0, b0, b0}; acc[i][1] = (f32x4){b1, b1, b1, b1}; acc[i][2] = (f32x4){}; acc[i][3] = (f32x4){};
  }
  if (kh && up < 3) waitv<2 * (16 - K5_R)>(); else waitv<0>();
  BAR();
  TS(23);
  const unsigned bso = OFF_Z2 + (kh * 2 * 54 + u0) * 1024;
#define SP_LD(s, j) (bq[(s) % (LB + 1)][j] = BF8(ldc16(rs, bvo[(j) & 1], bso + (sp_stage(s) * 4 + ((j) >> 1)) * 55296)))
#define SP_RD(t, k) (af[(t) % (PD + 1)][k] = *(const bf16x8*)(al[k][((t) >> 1) & 1] + (((t) >> 2) % K5_R) * 16384 + ((t) & 1) * 2048))
#pragma unroll
  for (int s = 0; s < LB; ++s)
#pragma unroll
    for (int j = 0; j < 4; ++j) SP_LD(s, j);
#pragma unroll
  for (int t = 0; t < PD; ++t) { SP_RD(t, 0); SP_RD(t, 1); }
  SCHED();
  sfor<64>([&](auto T) __attribute__((always_inline)) {
    constexpr int t = T, nx = t + PD, s = t >> 2, m = t & 3, tl = t + 4 * LB;
    if constexpr (nx == 16 || nx == 28 || nx == 44 || nx == 56) { BAR(); TS(32 + (nx == 28) + 2 * (nx == 44) + 3 * (nx == 56)); }
    constexpr int c = nx >= 16 && nx < 24 ? nx - 16 : nx >= 28 && nx < 34 ? nx - 20 : nx >= 44 && nx < 48 ? nx - 30 : -1;
    if constexpr (nx < 64) SP_RD(nx, 0);
    acc[0][m] = MFMA16(bq[s % (LB + 1)][0], af[t % (PD + 1)][0], acc[0][m]);
    acc[0][m] = MFMA16(bq[s % (LB + 1)][2], af[t % (PD + 1)][1], acc[0][m]); PIN(acc[0][m]);
    if constexpr (tl < 64) SP_LD(tl >> 2, m); else if constexpr (tl - 64 < 17) wq[tl - 64] = proj_ld<true>(rs, tl - 64, wid, lane);
    SCHED();
    if constexpr (nx < 64) SP_RD(nx, 1);
    acc[1][m] = MFMA16(bq[s % (LB + 1)][1], af[t % (PD + 1)][0], acc[1][m]);
    acc[1][m] = MFMA16(bq[s % (LB + 1)][3], af[t % (PD + 1)][1], acc[1][m]); PIN(acc[1][m]);
    if constexpr (c >= 0) *(u32x4*)(smem + ((c >> 1) % K5_R) * 16384 + wid * 2048 + (c & 1) * 1024 + lane * 16) = wr[c >> 1][c & 1];
    if constexpr (t == 63 && 4 * LB < 17) { _Pragma("unroll") for (int i = 4 * LB; i < 17; ++i) wq[i] = proj_ld<true>(rs, i, wid, lane); }
    SCHED();
  });
  TS(24);
  f32x4* exw = (f32x4*)(smem + 32768 + wid * 4096) + lane;
  const f32x4* exr = (const f32x4*)(smem + 32768 + (wid ^ 4) * 4096) + lane;
  u32x4 z[2];
#pragma unroll
  for (int i = 0; i < 2; ++i) z[i] = *(const u32x4*)(smem + L4_Z1 + (2 * up + i) * 2048 + kh * 1024 + lane * 16);
#pragma unroll
  for (int i = 0; i < 2; ++i)
#pragma unroll
    for (int j = 0; j < 2; ++j) exw[(i * 2 + j) * 64] = acc[i][2 + j];
  f32x4 zf[2][2];
#pragma unroll
  for (int i = 0; i < 2; ++i)
#pragma unroll
    for (int j = 0; j < 2; ++j) {
      zf[i][j] = (f32x4){bflo(z[i][2 * j]), bfhi(z[i][2 * j]), bflo(z[i][2 * j + 1]), bfhi(z[i][2 * j + 1])};
      acc[i][j] = mul4s(acc[i][j], zf[i][j]); PIN(zf[i][j]); PIN(acc[i][j]);
    }
  BAR();
  f32x4 oth[2][2];
#pragma unroll
  for (int i = 0; i < 2; ++i)
#pragma unroll
    for (int j = 0; j < 2; ++j) oth[i][j] = exr[(i * 2 + j) * 64];
#pragma unroll
  for (int i = 0; i < 2; ++i)
#pragma unroll
    for (int j = 0; j < 2; ++j) {
      const f32x4 v = acc[i][j] + zf[i][j] * oth[i][j];
      stc8(rs, ((g >> 1) * 16 + r) * 16 + (g & 1) * 8 + dead[i], OFF_U + (panel * 4 + 2 * kh + j) * 27648 + (u0 + i) * 512, (u32x2){pack2(v[0], v[1]), pack2(v[2], v[3])});
    }
  TS(25);
  waitv<0>();
  BAR();
  TS(26);
  if (wid == 7) arrive(ws, C_U + panel, lane);
#pragma unroll
  for (int i = 0; i < 17; ++i) proj_park<true>(smem, i, wid, lane, wq[i]);
}

extern "C" __global__ __launch_bounds__(512, 2) OCC2 void k_mega(char* __restrict__ ws, const u16* __restrict__ x, const u16* __restrict__ win,
                                                     const u16* __restrict__ wsp, const u16* __restrict__ bsp, const u16* __restrict__ wout, u16* __restrict__ out) {
  extern __shared__ __attribute__((aligned(16))) char smem[];
  const int wid = __builtin_amdgcn_readfirstlane(threadIdx.x >> 6);
  const rsrc_t rs = __builtin_amdgcn_make_buffer_rsrc(ws, 0, WS_USED, 0x00020000);
  TDECL; TS(0);
  u32x2 res[3];
  phase_qkv(ws, rs, smem, wid TPASS);
  phase_attn(ws, rs, (const char*)wsp, win, wout, smem, wid TPASS);
  u32x4 wr[16 - K5_R][2];
  auto nohook = []() __attribute__((always_inline)) {};
  if (wid < 4) {
    bf16x8 b2[20];
    proj_body<false, 0>(ws, rs, x, out, smem, wid, res, [&]() __attribute__((always_inline)) { win_prefetch(rs, wid, b2); } TPASS);
    win_body<true>(ws, rs, wsp, smem, wid, b2, wr TPASS);
  } else {
    proj_body<false, 1>(ws, rs, x, out, smem, wid, res, nohook TPASS);
    if (wid == 7) win_agent(ws, rs, wsp, smem, wr TPASS);
    else { bf16x8 b2[20]; win_prefetch(rs, wid, b2); win_body<false>(ws, rs, wsp, smem, wid, b2, wr TPASS); }
  }
  phase_sp(ws, rs, bsp, smem, wid, wr TPASS);
  if (wid < 4) proj_body<true, 0>(ws, rs, x, out, smem, wid, res, nohook TPASS);
  else proj_body<true, 1>(ws, rs, x, out, smem, wid, res, nohook TPASS);
  TS(29);
  TDUMP();
  asm volatile("k_mega_code_end:");
}

extern "C" void kernel_launch(void* const* d_in, const int* in_sizes, int n_in, void* d_out, int out_size, void* d_ws, size_t ws_size,
                              hipStream_t stream) {
  const u16* x = (const u16*)d_in[0];
  const u16* wqkv = (const u16*)d_in[1];
  const u16* wo = (const u16*)d_in[2];
  const u16* win = (const u16*)d_in[4];
  const u16* wsp = (const u16*)d_in[5];
  const u16* bsp = (const u16*)d_in[6];
  const u16* wout = (const u16*)d_in[7];
  const u16* cvec = (const u16*)d_in[8];
  const u16* wmod = (const u16*)d_in[9];
  char* ws = (char*)d_ws;
  (void)hipFuncSetAttribute((const void*)k_mega, hipFuncAttributeMaxDynamicSharedMemorySize, 163840);
  k0_prep<<<K0_GRID, 256, 0, stream>>>(x, wqkv, wo, cvec, wmod, ws);
  k_mega<<<256, 512, 163840, stream>>>(ws, x, win, wsp, bsp, wout, (u16*)d_out);
}
```

```cpp
#include <hip/hip_runtime.h>
#include <hip/hip_bf16.h>
#include <stdint.h>
#include <utility>

typedef __attribute__((ext_vector_type(8))) short bf16x8;
typedef __attribute__((ext_vector_type(4))) short bf16x4;
typedef __attribute__((ext_vector_type(2))) float f32x2;
typedef __attribute__((ext_vector_type(4))) float f32x4;
typedef __attribute__((ext_vector_type(16))) float f32x16;
typedef __attribute__((ext_vector_type(2))) unsigned u32x2;
typedef __attribute__((ext_vector_type(4))) unsigned u32x4;
typedef __attribute__((ext_vector_type(2))) __bf16 bf2_t;
typedef __attribute__((ext_vector_type(2))) _Float16 h2_t;
typedef unsigned short u16;
typedef unsigned long long u64;
#define LAS __attribute__((address_space(3)))
#define GAS __attribute__((address_space(1)))
#define DI __device__ __forceinline__

constexpr int D_ = 640, NQKV = 1920, F_ = 1728;

constexpr unsigned OFF_CNT = 0;
constexpr unsigned OFF_MOD = 65536;
constexpr unsigned OFF_MODP = OFF_MOD + 16384;
constexpr unsigned OFF_CBP = OFF_MODP + 61440;
constexpr unsigned OFF_XCH = OFF_CBP + 5 * 16384;
constexpr unsigned OFF_ST1 = OFF_XCH + 131072;
constexpr unsigned OFF_XHAT = OFF_ST1 + 262144;
constexpr unsigned OFF_WQKV = OFF_XHAT + 2621440;
constexpr unsigned OFF_WO = OFF_WQKV + 2457600;
constexpr unsigned OFF_WIN = OFF_WO + 819200;
constexpr unsigned OFF_WOUT = OFF_WIN + 2211840;
constexpr unsigned OFF_Q = OFF_WOUT + 1105920;
constexpr unsigned OFF_KV = OFF_Q + 32 * 81920;
constexpr unsigned OFF_O = OFF_KV + 32 * 163840;
constexpr unsigned OFF_X1S = OFF_O + 2621440;
constexpr unsigned OFF_Z2 = OFF_X1S + 2621440;
constexpr unsigned OFF_U = OFF_Z2 + 3538944;
constexpr unsigned WS_USED = OFF_U + 3538944;
constexpr int C_QKV = 0, C_MOD = 15  , C_ATT = 128, C_REL = 136, C_X1 = 144, C_U = 176, C_Z2 = 208  ;

DI unsigned pack2(float lo, float hi) { return __builtin_bit_cast(unsigned, __builtin_convertvector((f32x2){lo, hi}, bf2_t)); }
DI float opq(float x) { asm("" : "+v"(x)); return x; }
DI f32x4 fma4s(f32x4 a, f32x4 b, f32x4 c) { return (f32x4){opq(__builtin_fmaf(a[0], b[0], c[0])), opq(__builtin_fmaf(a[1], b[1], c[1])), opq(__builtin_fmaf(a[2], b[2], c[2])), opq(__builtin_fmaf(a[3], b[3], c[3]))}; }
DI f32x4 mul4s(f32x4 a, f32x4 b) { return (f32x4){opq(a[0] * b[0]), opq(a[1] * b[1]), opq(a[2] * b[2]), opq(a[3] * b[3])}; }
DI unsigned pack2h(float lo, float hi) { return __builtin_bit_cast(unsigned, __builtin_convertvector((f32x2){lo, hi}, h2_t)); }
DI float bflo(unsigned u) { return __uint_as_float(u << 16); }
DI float bfhi(unsigned u) { return __uint_as_float(u & 0xffff0000u); }
DI float rbf(float x) { return bflo(pack2(x, x)); }
DI float bf2f(u16 b) { return __uint_as_float((unsigned)b << 16); }
#define MFMA32(a, b, c) __builtin_amdgcn_mfma_f32_32x32x16_bf16((a), (b), (c), 0, 0, 0)
#define MFMA32K8(a, b, c) __builtin_amdgcn_mfma_f32_32x32x8bf16_1k((a), (b), (c), 0, 0, 0)
#define MFMA16(a, b, c) __builtin_amdgcn_mfma_f32_16x16x32_bf16((a), (b), (c), 0, 0, 0)
#define BAR() asm volatile("s_waitcnt lgkmcnt(0)\n\ts_barrier" ::: "memory")
#define SCHED() __builtin_amdgcn_sched_barrier(0)
#define PIN(x) asm volatile("" : "+v"(x))
#define OCC2 __attribute__((amdgpu_waves_per_eu(2, 2)))
#define BF8(x) __builtin_bit_cast(bf16x8, x)
#define BF4(x) __builtin_bit_cast(bf16x4, x)
template <int N> DI void waitv() { asm volatile("s_waitcnt vmcnt(%0)" ::"i"(N) : "memory"); }
template <class F, int... I> DI void sfor_impl(F&& f, std::integer_sequence<int, I...>) { (f(std::integral_constant<int, I>{}), ...); }
template <int N, class F> DI void sfor(F&& f) { sfor_impl(f, std::make_integer_sequence<int, N>{}); }
typedef short v4i16_t __attribute__((ext_vector_type(4)));
DI bf16x4 lds_tr(const char* p) { return __builtin_bit_cast(bf16x4, __builtin_amdgcn_ds_read_tr16_b64_v4i16((LAS v4i16_t*)p)); }
DI void glds16(const void* g, unsigned lds_base) {
  unsigned sv;
  asm volatile("s_mov_b32 %0, m0\n\ts_mov_b32 m0, %2\n\ts_nop 0\n\tglobal_load_lds_dwordx4 %1, off\n\ts_mov_b32 m0, %0" : "=&s"(sv) : "v"(g), "s"(lds_base) : "memory");
}
DI void glds16c(const void* g, unsigned lds_base) {
  unsigned sv;
  asm volatile("s_mov_b32 %0, m0\n\ts_mov_b32 m0, %2\n\ts_nop 0\n\tglobal_load_lds_dwordx4 %1, off sc1\n\ts_mov_b32 m0, %0" : "=&s"(sv) : "v"(g), "s"(lds_base) : "memory");
}
DI void glds4c(const void* g, unsigned lds_base) {
  unsigned sv;
  asm volatile("s_mov_b32 %0, m0\n\ts_mov_b32 m0, %2\n\ts_nop 0\n\tglobal_load_lds_dword %1, off sc1\n\ts_mov_b32 m0, %0" : "=&s"(sv) : "v"(g), "s"(lds_base) : "memory");
}
typedef __amdgpu_buffer_rsrc_t rsrc_t;
DI u32x4 ldc16(rsrc_t rs, unsigned voff, unsigned soff) { return __builtin_amdgcn_raw_buffer_load_b128(rs, voff, soff, 16); }
DI unsigned ldc4(rsrc_t rs, unsigned voff, unsigned soff) { return __builtin_amdgcn_raw_buffer_load_b32(rs, voff, soff, 16); }
DI void stc16(rsrc_t rs, unsigned voff, unsigned soff, u32x4 v) { __builtin_amdgcn_raw_buffer_store_b128(v, rs, voff, soff, 16); asm volatile("s_nop 1" ::"v"(v) : "memory"); }
DI void stc8(rsrc_t rs, unsigned voff, unsigned soff, u32x2 v) { __builtin_amdgcn_raw_buffer_store_b64(v, rs, voff, soff, 16); }
DI void stc4(rsrc_t rs, unsigned voff, unsigned soff, unsigned v) { __builtin_amdgcn_raw_buffer_store_b32(v, rs, voff, soff, 16); }
DI f32x4 ldc16f(rsrc_t rs, unsigned voff, unsigned soff) { return __builtin_bit_cast(f32x4, ldc16(rs, voff, soff)); }
DI void arrive(char* ws, int c, int lane) { if (lane == 0) __hip_atomic_fetch_add((GAS unsigned*)(ws + OFF_CNT + c * 256), 1u, __ATOMIC_RELAXED, __HIP_MEMORY_SCOPE_AGENT); }
DI void poll_ge(char* ws, int c, unsigned thr) {
  const GAS unsigned* p = (const GAS unsigned*)(ws + OFF_CNT + c * 256);
#pragma nounroll
  for (unsigned spin = 0; spin < (1u << 19); ++spin) {
    if (__all(__hip_atomic_load(p, __ATOMIC_RELAXED, __HIP_MEMORY_SCOPE_AGENT) >= thr)) break;
    __builtin_amdgcn_s_sleep(1);
  }
  asm volatile("" ::: "memory");
}
DI int fresh_tid() { int t = threadIdx.x; asm volatile("" : "+v"(t)); return t; }
DI void l2touch(const void* g, unsigned lds_scratch) {
  unsigned sv;
  asm volatile("s_mov_b32 %0, m0\n\ts_mov_b32 m0, %2\n\ts_nop 0\n\tglobal_load_lds_dword %1, off\n\ts_mov_b32 m0, %0" : "=&s"(sv) : "v"(g), "s"(lds_scratch) : "memory");
}
#define LDNT(p) __builtin_nontemporal_load(p)
DI float silu_bf(u16 c) { const float f = __uint_as_float((unsigned)c << 16); const float v = f / (1.f + __expf(-f)); return __uint_as_float(__builtin_bit_cast(unsigned, __builtin_convertvector((f32x2){v, v}, bf2_t)) << 16); }
DI unsigned lds_addr(const void* p) { return (unsigned)(uintptr_t)(const LAS void*)p; }
DI float xhalf(float v) {
  auto rr = __builtin_amdgcn_permlane32_swap(__float_as_uint(v), __float_as_uint(v), false, false);
  return __uint_as_float((threadIdx.x & 32) ? rr[0] : rr[1]);
}
DI float sum_g(float v) {
  auto a = __builtin_amdgcn_permlane16_swap(__float_as_uint(v), __float_as_uint(v), false, false);
  v = __uint_as_float(a[0]) + __uint_as_float(a[1]);
  auto b = __builtin_amdgcn_permlane32_swap(__float_as_uint(v), __float_as_uint(v), false, false);
  return __uint_as_float(b[0]) + __uint_as_float(b[1]);
}
template <int CTRL> DI float dpp_add(float v) { return v + __uint_as_float(__builtin_amdgcn_update_dpp(0u, __float_as_uint(v), CTRL, 0xf, 0xf, true)); }
DI float sum16(float v) {
  v = dpp_add<0x128>(v); v = dpp_add<0x124>(v); v = dpp_add<0x122>(v); return dpp_add<0x121>(v);
}
DI u32x4 modulate8(u32x4 v, f32x4 sc0, f32x4 sc1, f32x4 sh0, f32x4 sh1) {
  unsigned o0, o1, o2, o3; float t0, t1, t2, t3;
  asm volatile(
      "v_fma_mix_f32 %0, %8, %12, %20 op_sel_hi:[1,0,0]\n\tv_fma_mix_f32 %4, %8, %13, %21 op_sel:[1,0,0] op_sel_hi:[1,0,0]\n\t"
      "v_fma_mix_f32 %1, %9, %14, %22 op_sel_hi:[1,0,0]\n\tv_fma_mix_f32 %5, %9, %15, %23 op_sel:[1,0,0] op_sel_hi:[1,0,0]\n\t"
      "v_fma_mix_f32 %2, %10, %16, %24 op_sel_hi:[1,0,0]\n\tv_fma_mix_f32 %6, %10, %17, %25 op_sel:[1,0,0] op_sel_hi:[1,0,0]\n\t"
      "v_fma_mix_f32 %3, %11, %18, %26 op_sel_hi:[1,0,0]\n\tv_fma_mix_f32 %7, %11, %19, %27 op_sel:[1,0,0] op_sel_hi:[1,0,0]\n\t"
      "v_cvt_pk_bf16_f32 %0, %0, %4\n\tv_cvt_pk_bf16_f32 %1, %1, %5\n\tv_cvt_pk_bf16_f32 %2, %2, %6\n\tv_cvt_pk_bf16_f32 %3, %3, %7"
      : "=&v"(o0), "=&v"(o1), "=&v"(o2), "=&v"(o3), "=&v"(t0), "=&v"(t1), "=&v"(t2), "=&v"(t3)
      : "v"(v[0]), "v"(v[1]), "v"(v[2]), "v"(v[3]), "v"(sc0[0]), "v"(sc0[1]), "v"(sc0[2]), "v"(sc0[3]), "v"(sc1[0]), "v"(sc1[1]), "v"(sc1[2]), "v"(sc1[3]),
        "v"(sh0[0]), "v"(sh0[1]), "v"(sh0[2]), "v"(sh0[3]), "v"(sh1[0]), "v"(sh1[1]), "v"(sh1[2]), "v"(sh1[3]));
  return (u32x4){o0, o1, o2, o3};
}
DI float ln_den(float var) { return rbf(__builtin_sqrtf(rbf(rbf(var) + 1e-5f))); }
DI int unit0_of(int j) { return j < 6 ? 7 * j : 42 + 6 * (j - 6); }

DI void K0ST(char* p, u32x4 v) { *(u32x4*)p = v; }
constexpr int K0_MOD = 240, K0_LN = 128, K0_RL = 240, K0_WO = 50, K0_INIT = 12, K0_WARM = 24;
constexpr int K0_GRID = K0_WARM + K0_MOD + K0_LN + K0_RL + K0_WO + K0_INIT;

__global__ __launch_bounds__(256) void k0_prep(const u16* __restrict__ x, const u16* __restrict__ wqkv, const u16* __restrict__ wo, const u16* __restrict__ cvec,
                                               const u16* __restrict__ wmod, char* __restrict__ ws) {
  __shared__ __attribute__((aligned(16))) char smem[4 * 4608];
  const int tid = threadIdx.x, lane = tid & 63, wid = __builtin_amdgcn_readfirstlane(tid >> 6);
  int b = blockIdx.x;
  if (b < K0_WARM) {
    u64 pc; unsigned len;
    asm volatile("s_getpc_b64 s[30:31]\n\ts_add_u32 s30, s30, k_mega@rel32@lo+4\n\ts_addc_u32 s31, s31, k_mega@rel32@hi+12\n\ts_mov_b64 %0, s[30:31]\n\t"
                 "s_mov_b32 %1, k_mega_code_end-k_mega" : "=s"(pc), "=s"(len) :: "scc", "s30", "s31");
    l2touch((const char*)pc + min(((b >> 3) * 256 + tid) * 128u, len - 4u), lds_addr(smem) + wid * 256);
    return;
  }
  b -= K0_WARM;
  if (b < K0_MOD) {
    const int cg = b >> 2, ks = b & 3, c = tid & 7, row0 = ks * 160 + (tid >> 3), col0 = cg * 64;
    u32x4 w[5]; u16 cv[5];
#pragma unroll
    for (int i = 0; i < 5; ++i) { w[i] = LDNT((const u32x4*)(wmod + (size_t)(row0 + 32 * i) * 3840 + col0 + c * 8)); cv[i] = cvec[row0 + 32 * i]; }
    float acc[8] = {0, 0, 0, 0, 0, 0, 0, 0};
#pragma unroll
    for (int i = 0; i < 5; ++i) {
      const float s = silu_bf(cv[i]);
#pragma unroll
      for (int e = 0; e < 4; ++e) { acc[2 * e] += s * bflo(w[i][e]); acc[2 * e + 1] += s * bfhi(w[i][e]); }
    }
#pragma unroll
    for (int e = 0; e < 8; ++e) acc[e] = sum_g(dpp_add<0x128>(acc[e]));
    float* red = (float*)smem;
    if (lane < 8) { *(f32x4*)(red + wid * 64 + lane * 8) = (f32x4){acc[0], acc[1], acc[2], acc[3]}; *(f32x4*)(red + wid * 64 + lane * 8 + 4) = (f32x4){acc[4], acc[5], acc[6], acc[7]}; }
    __syncthreads();
    if (tid < 64) ((float*)(ws + OFF_MODP))[ks * 3840 + cg * 64 + tid] = (red[tid] + red[64 + tid]) + (red[128 + tid] + red[192 + tid]);
    return;
  }
  b -= K0_MOD;
  if (b < K0_LN) {
    b = (b & 7) * 16 + (b >> 3);
    const int row = b * 16 + wid * 4 + (lane >> 4), c = lane & 15;
    const u16* xr = x + (size_t)row * D_ + c * 8;
    u32x4 v[5];
#pragma unroll
    for (int i = 0; i < 5; ++i) v[i] = *(const u32x4*)(xr + i * 128);
    float s = 0.f, q = 0.f;
#pragma unroll
    for (int i = 0; i < 5; ++i)
#pragma unroll
      for (int e = 0; e < 4; ++e) s += bflo(v[i][e]) + bfhi(v[i][e]);
    const float mean = sum16(s) * (1.f / 640.f);
#pragma unroll
    for (int i = 0; i < 5; ++i)
#pragma unroll
      for (int e = 0; e < 4; ++e) { const float a = bflo(v[i][e]) - mean, d = bfhi(v[i][e]) - mean; q += a * a + d * d; }
    const float var = sum16(q) * (1.f / 640.f);
    const float rinv = 1.f / ln_den(var), mb = rbf(mean);
    char* dst = ws + OFF_XHAT + (size_t)row * 1280 + c * 16;
#pragma unroll
    for (int i = 0; i < 5; ++i) {
      u32x4 o;
#pragma unroll
      for (int e = 0; e < 4; ++e) o[e] = pack2h((bflo(v[i][e]) - mb) * rinv, (bfhi(v[i][e]) - mb) * rinv);
      K0ST(dst + i * 256, o);
    }
    return;
  }
  b -= K0_LN;
  if (b < K0_RL) {
    const int kt = b / 12, G = b % 12, sec = G >> 2, gq = G & 3;
    u32x4 v[3];
#pragma unroll
    for (int i = 0; i < 3; ++i) { const int ci = min(i * 256 + tid, 639); v[i] = LDNT((const u32x4*)(wqkv + (size_t)(kt * 32 + ci / 20) * NQKV + G * 160 + (ci % 20) * 8)); }
#pragma unroll
    for (int i = 0; i < 3; ++i) { const int ci = min(i * 256 + tid, 639); *(u32x4*)(smem + (ci / 20) * 336 + (ci % 20) * 16) = v[i]; }
    __syncthreads();
    const int g = lane >> 4, q = (lane & 15) >> 2, p = lane & 3;
    for (int f = wid; f < 10; f += 4) {
      const int t = f >> 1, x2 = f & 1;
      const int col = sec == 2 ? 16 * f + 4 * p : t < 4 ? 20 * (2 * t + (p >> 1)) + 8 * (p & 1) + 4 * x2 : 20 * (4 * x2 + p) + 16;
      const int T = sec == 2 ? 40 + gq * 5 + t : sec * 20 + (t < 4 ? 4 * gq + t : 16 + gq);
      const char* pp = smem + (8 * g + q) * 336 + col * 2;
      const bf16x4 lo = lds_tr(pp), hi = lds_tr(pp + 4 * 336);
      K0ST(ws + OFF_WQKV + (((size_t)T * 20 + kt) * 2 + x2) * 1024 + lane * 16, __builtin_bit_cast(u32x4, (bf16x8)__builtin_shufflevector(lo, hi, 0, 1, 2, 3, 4, 5, 6, 7)));
    }
    return;
  }
  b -= K0_RL;
  if (b < K0_WO) {
    const int job = b * 4 + wid, kt = job / 10, ng = job % 10, j8 = lane >> 3;
    char* t = smem + wid * 4608;
    u32x4 v[4];
#pragma unroll
    for (int i = 0; i < 4; ++i) {
      const int c = 4 * kt + i, row = c < 64 ? 20 * (c >> 1) + 4 * (c & 1) + (j8 & 3) + 8 * (j8 >> 2) : 20 * (2 * (c - 64) + (j8 >> 2)) + 16 + (j8 & 3);
      v[i] = LDNT((const u32x4*)(wo + (size_t)row * D_ + ng * 64 + (lane & 7) * 8));
    }
#pragma unroll
    for (int i = 0; i < 4; ++i) *(u32x4*)(t + (i * 8 + j8) * 144 + (lane & 7) * 16) = v[i];
    const char* pp = t + (8 * (lane >> 4) + ((lane & 15) >> 2)) * 144 + (lane & 3) * 8;
#pragma unroll
    for (int f = 0; f < 4; ++f) {
      const bf16x4 lo = lds_tr(pp + f * 32), hi = lds_tr(pp + 4 * 144 + f * 32);
      K0ST(ws + OFF_WO + ((size_t)(ng * 4 + f) * 20 + kt) * 1024 + lane * 16, __builtin_bit_cast(u32x4, (bf16x8)__builtin_shufflevector(lo, hi, 0, 1, 2, 3, 4, 5, 6, 7)));
    }
    return;
  }
  b -= K0_WO;
  {
    u32x4* p = (u32x4*)(ws + (b < 8 ? OFF_XCH + b * 16384 : OFF_CNT + (b - 8) * 16384)) + tid;
    const unsigned f = b < 8 ? ~0u : 0u;
#pragma unroll
    for (int i = 0; i < 4; ++i) p[i * 256] = (u32x4){f, f, f, f};
  }
}

constexpr float QSCALE = 0.22360679774997896f * 1.4426950408889634f;
constexpr int K1_W = 3, K1_WB = 5, K1_WB0 = 2;

template <bool ISV, class FIN>
DI void k1_mma(const char* __restrict__ bsrc, const char* smem, int lane, FIN&& fin) {
  f32x4 acc[8][2];
  constexpr int PD = 4;
  bf16x8 bfr[K1_WB + 1][4], af[PD + 1][2];
  const char* lds[2];
#pragma unroll
  for (int k = 0; k < 2; ++k) lds[k] = smem + k * 8192 + (lane ^ (4 * k + (lane >> 4))) * 16;
#pragma unroll
  for (int i = 0; i < 4 * K1_WB0; ++i) bfr[i >> 2][i & 3] = *(const bf16x8*)(bsrc + i * 1024);
#pragma unroll
  for (int m = 0; m < 8; ++m) { acc[m][0] = (f32x4){}; acc[m][1] = (f32x4){}; }
  BAR();
#pragma unroll
  for (int i = 4 * K1_WB0; i < 4 * K1_WB; ++i) bfr[i >> 2][i & 3] = *(const bf16x8*)(bsrc + i * 1024);
  BAR();
  __builtin_amdgcn_s_setprio(1);
#define K1_RD(t, k) (af[(t) % (PD + 1)][k] = *(const bf16x8*)(lds[k] + (((t) >> 3) % 3) * 16384 + ((t) & 7) * 1024))
#define K1_MM(c, b, a) (c = ISV ? MFMA16(a, b, c) : MFMA16(b, a, c))
#pragma unroll
  for (int t = 0; t < PD; ++t) { K1_RD(t, 0); K1_RD(t, 1); }
  SCHED();
  sfor<80>([&](auto T) __attribute__((always_inline)) {
    constexpr int t = T, s = t >> 3, m = t & 7, nx = t + PD;
    if constexpr (nx < 80 && (nx & 7) == 0) asm volatile("s_barrier" ::: "memory");
    if constexpr (nx < 80) K1_RD(nx, 0);
    K1_MM(acc[m][0], bfr[s % (K1_WB + 1)][0], af[t % (PD + 1)][0]); K1_MM(acc[m][0], bfr[s % (K1_WB + 1)][2], af[t % (PD + 1)][1]); PIN(acc[m][0]);
    SCHED();
    if constexpr (nx < 80) K1_RD(nx, 1);
    if constexpr (s + K1_WB < 10 && (m & 1)) bfr[(s + K1_WB) % (K1_WB + 1)][m >> 1] = *(const bf16x8*)(bsrc + ((s + K1_WB) * 4 + (m >> 1)) * 1024);
    K1_MM(acc[m][1], bfr[s % (K1_WB + 1)][1], af[t % (PD + 1)][0]); K1_MM(acc[m][1], bfr[s % (K1_WB + 1)][3], af[t % (PD + 1)][1]); PIN(acc[m][1]);
    SCHED();
    if constexpr (t >= 74) { fin(t - 74, acc[t - 74]); SCHED(); }
  });
  fin(6, acc[6]); fin(7, acc[7]);
  __builtin_amdgcn_s_setprio(0);
}


DI void phase_qkv(char* __restrict__ ws, rsrc_t rs, char* smem, int wid) {
  const int tid = fresh_tid(), lane = tid & 63;
  const int xcd = blockIdx.x & 7, idx = blockIdx.x >> 3;
  int ctr;
  if ((xcd & 1) && idx >= 28) {
    ctr = C_MOD;
    if (tid < 160) {
      const int col = 1280 + ((xcd >> 1) * 4 + idx - 28) * 160 + tid;
      const float* pp = (const float*)(ws + OFF_MODP) + col;
      float v = rbf((pp[0] + pp[3840]) + (pp[7680] + pp[11520]));
      if (col / 640 == 4) v = rbf(1.f + v);
      stc4(rs, col * 4, OFF_MOD, __float_as_uint(v));
    }
  } else {
    const int mt = (xcd >> 1) * 4 + (idx & 3), nt = (xcd & 1) * 8 + (idx >> 2);
    ctr = C_QKV + nt;
    if (wid < 4) {
      float* cst = (float*)(smem + 49152);
      const int c = lane & 7, rr = lane >> 3;
      const char* src = ws + OFF_XHAT + (size_t)(mt * 128 + wid * 32 + rr) * 1280 + c * 16;
      char* wdst = smem + (c >> 2) * 8192 + wid * 2048 + (c & 3) * 256 + (rr ^ c) * 16;
      u32x4 a[10][4];
      f32x4 c0[4], c1[4];
#pragma unroll
      for (int i = 0; i < 4; ++i) { c0[i] = *(const f32x4*)(ws + OFF_MODP + i * 15360 + tid * 16); if (wid == 0) c1[i] = *(const f32x4*)(ws + OFF_MODP + i * 15360 + 4096 + tid * 16); }
#pragma unroll
      for (int s = 0; s < K1_W; ++s)
#pragma unroll
        for (int m = 0; m < 4; ++m) a[s][m] = *(const u32x4*)(src + m * 10240 + s * 128);
      {
        f32x4 v = (c0[0] + c0[1]) + (c0[2] + c0[3]); const float one = tid >= 160 ? 1.f : 0.f;
        *(f32x4*)(cst + tid * 4) = (f32x4){rbf(one + rbf(v[0])), rbf(one + rbf(v[1])), rbf(one + rbf(v[2])), rbf(one + rbf(v[3]))};
        if (wid == 0) { v = (c1[0] + c1[1]) + (c1[2] + c1[3]); *(f32x4*)(cst + 1024 + tid * 4) = (f32x4){rbf(1.f + rbf(v[0])), rbf(1.f + rbf(v[1])), rbf(1.f + rbf(v[2])), rbf(1.f + rbf(v[3]))}; }
      }
      BAR();
      const float* cp = cst + c * 8;
      f32x4 kc[10][4];
#define K1_KC(s) do { kc[s][0] = *(const f32x4*)(cp + (s) * 64); kc[s][1] = *(const f32x4*)(cp + (s) * 64 + 4); kc[s][2] = *(const f32x4*)(cp + (s) * 64 + 640); kc[s][3] = *(const f32x4*)(cp + (s) * 64 + 644); } while (0)
#pragma unroll
      for (int s = 0; s < 6; ++s) K1_KC(s);
      SCHED();
      u32x4 o[2][4];
#define K1_XF(s, m) (o[(s) & 1][m] = modulate8(a[s][m], kc[s][2], kc[s][3], kc[s][0], kc[s][1]))
#define K1_WR(s, m) (*(u32x4*)(wdst + ((s) % 3) * 16384 + ((m) >> 1) * 1024 + ((m) & 1) * 128) = o[(s) & 1][m])
#pragma unroll
      for (int m = 0; m < 4; ++m) { K1_XF(0, m); K1_WR(0, m); }
      BAR();
#pragma unroll
      for (int m = 0; m < 4; ++m) a[K1_W][m] = *(const u32x4*)(src + m * 10240 + K1_W * 128);
#pragma unroll
      for (int m = 0; m < 4; ++m) K1_XF(1, m);
      SCHED();
#pragma unroll
      for (int s = 1; s < 10; ++s) {
#pragma unroll
        for (int m = 0; m < 4; ++m) {
          if (m == 0) K1_WR(s, 0);
          if (m < 3) K1_WR(s, m + 1);
          if (s + K1_W < 10) a[s + K1_W][m] = *(const u32x4*)(src + m * 10240 + (s + K1_W) * 128);
          if (s < 5) kc[s + 5][m] = *(const f32x4*)(cp + (s + 5) * 64 + (m & 1) * 4 + (m >> 1) * 640);
          SCHED();
          if (s < 9) K1_XF(s + 1, m);
          SCHED();
        }
        asm volatile("" :: "v"(o[s & 1][0]), "v"(o[s & 1][1]), "v"(o[s & 1][2]), "v"(o[s & 1][3]));
        BAR();
      }
    } else {
      const int cw = wid - 4;
      const char* bsrc = ws + OFF_WQKV + (size_t)(nt * 4 + cw) * 40 * 1024 + lane * 16;
      const int r = lane & 15, g = lane >> 4;
      if (nt >= 10) {
        const unsigned so = OFF_KV + (mt >> 1) * 20480 + 10240 + (mt & 1) * 5120;
        unsigned vo[2];
#pragma unroll
        for (int x = 0; x < 2; ++x) { const unsigned c = (nt - 10) * 128 + cw * 32 + x * 16 + r; vo[x] = c / 20 * 163840u + ((g & 1) * 20 + c % 20) * 16 + (g >> 1) * 8; }
        k1_mma<true>(bsrc, smem, lane, [&](int m, const f32x4 (&a)[2]) __attribute__((always_inline)) {
#pragma unroll
          for (int x = 0; x < 2; ++x) stc8(rs, vo[x] + (m & 3) * 640, so + (m >> 2) * 2560, (u32x2){pack2(a[x][0], a[x][1]), pack2(a[x][2], a[x][3])});
        });
      } else {
        const bool isQ = nt < 5; const float sc = isQ ? QSCALE : 1.f;
        const unsigned so = isQ ? OFF_Q + mt * 5120 : OFF_KV + (mt >> 1) * 20480 + (mt & 1) * 5120, hs = isQ ? 81920u : 163840u;
        const int n5 = isQ ? nt : nt - 5;
        const bool wide = n5 < 4;
        const unsigned vo = wide ? (2 * (n5 * 4 + cw) + (g >> 1)) * hs + (g & 1) * 512 + r * 16 : (8 * cw + g) * hs + 1024 + r * 8;
        const f32x4 sc4 = (f32x4){sc, sc, sc, sc};
        k1_mma<false>(bsrc, smem, lane, [&](int m, const f32x4 (&a)[2]) __attribute__((always_inline)) {
          const f32x4 v0 = mul4s(a[0], sc4), v1 = mul4s(a[1], sc4);
          const u32x4 w = (u32x4){pack2(v0[0], v0[1]), pack2(v0[2], v0[3]), pack2(v1[0], v1[1]), pack2(v1[2], v1[3])};
          if (wide) stc16(rs, vo + (m & 1) * 256, so + (m >> 1) * 1280, w);
          else { stc8(rs, vo + (m & 1) * 128, so + (m >> 1) * 1280, (u32x2){w[0], w[1]}); stc8(rs, vo + (m & 1) * 128, so + 4 * hs + (m >> 1) * 1280, (u32x2){w[2], w[3]}); }
        });
      }
    }
  }
  waitv<0>();
  BAR();
  if (wid == 7 && lane < 8) __hip_atomic_fetch_add((GAS unsigned*)(ws + OFF_CNT + (lane * 16 + ctr) * 256), 1u, __ATOMIC_RELAXED, __HIP_MEMORY_SCOPE_AGENT);
  if (ctr == C_MOD && wid == 7) { __builtin_amdgcn_s_sleep(127); __builtin_amdgcn_s_sleep(60); }
}

template <bool FINAL> DI constexpr int proj_pos(int f) { return FINAL ? (f < 96 ? 65536 + f * 1024 : (f - 96) * 1024) : 17408 + (f % 20 >= 10 ? 81920 : 0) + (f / 20 * 10 + f % 10) * 1024; }
template <bool FINAL> DI u32x4 proj_ld(rsrc_t rs, int i, int wid, int lane) {
  constexpr int NF = FINAL ? 135 : 100;
  return ldc16(rs, lane * 16, (FINAL ? OFF_WOUT : OFF_WO) + (((blockIdx.x >> 3) & 7) * NF + min(i * 8 + wid, NF - 1)) * 1024);
}
template <bool FINAL> DI void proj_park(char* smem, int i, int wid, int lane, u32x4 v) {
  constexpr int NF = FINAL ? 135 : 100;
  *(u32x4*)(smem + proj_pos<FINAL>(min(i * 8 + wid, NF - 1)) + lane * 16) = v;
}

DI void split3(float m, unsigned& w_h0, unsigned& w_h1) {
  const float n = -m; const float a = rbf(n), b = rbf(n - a), c = rbf(n - a - b);
  w_h0 = pack2(a, b); w_h1 = pack2(c, 0.f);
}

DI void phase_attn(char* __restrict__ ws, rsrc_t rs, const char* __restrict__ wsp, const u16* __restrict__ win, const u16* __restrict__ wout, char* smem, int wid) {
  const int tid = fresh_tid(), lane = tid & 63;
  const int xcd = blockIdx.x & 7, idx = blockIdx.x >> 3, head = xcd * 4 + (idx >> 3), qb = idx & 7;
  const int r = lane & 31, h = lane >> 5, qg = wid & 3, kh = wid >> 2;
  if (wid == 7) {
    const int l7 = lane & 7;
    const int c = l7 == 0 ? head >> 3 : l7 == 1 ? 4 : l7 == 2 ? 5 + (head >> 3) : l7 == 3 ? 9 : l7 == 4 ? 10 + (20 * head) / 128 : l7 == 5 ? 10 + (20 * head + 19) / 128 : C_MOD;
    poll_ge(ws, xcd * 16 + c, 16);
  }
  BAR();
  unsigned kvo = OFF_KV + head * 163840, qo = OFF_Q + head * 81920 + (qb * 8 + qg * 2) * 1280;
  const unsigned kconst = h == 0 ? 0x3f803f80u : 0x00003f80u;
  const unsigned so16 = (tid & 255) * 16, l16 = lane * 16, l2 = 1024 + r * 8 + h * 4;
  const int blk = blockIdx.x;
  u32x4 wq[13];
  for (int attempt = 0; attempt < 2; ++attempt) {
    asm volatile("" : "+s"(kvo), "+s"(qo));
    const char* kvs = ws + kvo + kh * 81920 + so16;
    const unsigned ldst = lds_addr(smem) + kh * 81920 + qg * 1024;
    u32x4 qA1w, qB1w, k1w; unsigned qA2w, qB2w, k2, m0sv;
    asm volatile(
        "s_nop 4\n\tbuffer_load_dwordx4 %0, %7, %9, %19 offen sc1\n\tbuffer_load_dwordx4 %1, %7, %9, %10 offen sc1\n\tbuffer_load_dwordx4 %2, %7, %9, %11 offen sc1\n\t"
        "buffer_load_dword %3, %8, %9, %19 offen sc1\n\tbuffer_load_dword %4, %8, %9, %10 offen sc1\n\tbuffer_load_dword %5, %8, %9, %11 offen sc1\n\t"
        "s_mov_b32 %6, m0\n\ts_mov_b32 m0, %17\n\ts_nop 0\n\tglobal_load_lds_dwordx4 %12, off sc1\n\t"
        "s_add_u32 m0, %18, 0x1000\n\ts_nop 0\n\tglobal_load_lds_dwordx4 %13, off sc1\n\t"
        "s_add_u32 m0, %18, 0x2000\n\ts_nop 0\n\tglobal_load_lds_dwordx4 %14, off sc1\n\t"
        "s_add_u32 m0, %18, 0x3000\n\ts_nop 0\n\tglobal_load_lds_dwordx4 %15, off sc1\n\t"
        "s_add_u32 m0, %18, 0x4000\n\ts_nop 0\n\tglobal_load_lds_dwordx4 %16, off sc1\n\t"
        "s_mov_b32 m0, %6\n\ts_waitcnt vmcnt(5)"
        : "=&v"(qA1w), "=&v"(qB1w), "=&v"(k1w), "=&v"(qA2w), "=&v"(qB2w), "=&v"(k2), "=&s"(m0sv)
        : "v"(l16), "v"(l2), "s"(rs), "s"(qo + kh * 1280), "s"(kvo), "v"(kvs + (wid == 0 ? 4096 : 0)), "v"(kvs + 4096), "v"(kvs + 8192), "v"(kvs + 12288), "v"(kvs + 16384),
          "s"(ldst + (wid == 0 ? 4096 : 0)), "s"(ldst), "s"(qo + (kh ^ 1) * 1280)
        : "memory", "scc");
    if (wid == 0) { const unsigned f = (tid & 1) ? 0u : 0x3f803f80u; *(u32x4*)(smem + l16) = (u32x4){f, f, f, f}; }
    const bf16x8 qA1 = BF8(qA1w), qB1 = BF8(qB1w);
    bf16x8 k1 = BF8(k1w);
    float mA = -INFINITY, mB = -INFINITY;
    {
      const bf16x4 qA2 = BF4(((u32x2){qA2w, 0u})), qB2 = BF4(((u32x2){qB2w, 0u}));
      const int nt = attempt ? 64 : 1;
      bf16x8 t1 = k1; unsigned t2 = k2;
      for (int t = 0; t < nt; ++t) {
        if (t) { const unsigned to = kvo + (t >> 3) * 20480 + (t & 7) * 1280; t1 = BF8(ldc16(rs, l16, to)); t2 = ldc4(rs, l2, to); }
        const bf16x4 t2v = BF4(((u32x2){t2, kconst}));
        f32x16 a = MFMA32(t1, qA1, (f32x16){}); a = MFMA32K8(t2v, qA2, a);
        f32x16 b = MFMA32(t1, qB1, (f32x16){}); b = MFMA32K8(t2v, qB2, b);
#pragma unroll
        for (int i = 0; i < 16; ++i) { mA = fmaxf(mA, a[i]); mB = fmaxf(mB, b[i]); }
      }
      mA = fmaxf(mA, xhalf(mA)); mB = fmaxf(mB, xhalf(mB));
    }
    unsigned a0, a1, b0, b1; split3(mA, a0, a1); split3(mB, b0, b1);
    const bf16x4 qA2 = BF4(((u32x2){qA2w, h ? a1 : a0})), qB2 = BF4(((u32x2){qB2w, h ? b1 : b0}));
    char* lh = smem + kh * 81920;
    waitv<0>();
    BAR();
    f32x16 oA = (f32x16){}, oB = (f32x16){};
    const char* kp1 = lh + l16;
    const char* kp2 = lh + l2;
    const bool vreal = r < 20;
    const char* vp = vreal ? lh + 10240 + (h * 20 + r) * 16 : smem + (r == 20 ? 0 : 16);
    const int vinc = vreal ? 1280 : 0, vjump = vreal ? 10240 : 0;
    if (kh) { k1 = *(const bf16x8*)kp1; k2 = *(const unsigned*)kp2; }
    bf16x8 kf1[2], vf[2][2]; u32x2 kf2[2];
    kf1[0] = k1; kf2[0] = (u32x2){k2, kconst}; kf2[1] = (u32x2){0u, kconst};
    vf[1][0] = (bf16x8){}; vf[1][1] = (bf16x8){};
    u32x4 pA0, pA1, pB0 = (u32x4){}, pB1 = (u32x4){};
    f32x4 e0 = (f32x4){}, e1;
    f32x16 sA = MFMA32(kf1[0], qA1, (f32x16){}), sB;
    sA = MFMA32K8(BF4(kf2[0]), qA2, sA);
#define EX2(x) __builtin_amdgcn_exp2f(x)
#define GRP(MF, S, i, EN, EP, P, w) do { MF; SCHED(); EN[0] = EX2(S[i]); P[w] = pack2(EP[0], EP[1]); EN[1] = EX2(S[i + 1]); P[w + 1] = pack2(EP[2], EP[3]); \
      EN[2] = EX2(S[i + 2]); EN[3] = EX2(S[i + 3]); PIN(P); PIN(EN); SCHED(); } while (0)
    const unsigned relsc = lds_addr(smem) + 768;
    auto group = [&](auto FIRST, int jump, int j) __attribute__((always_inline)) {
#pragma unroll
      for (int t = 0; t < 8; ++t) {
        const int c = t & 1, n = c ^ 1;
        if (FIRST) { if (t == 0) __builtin_amdgcn_s_setprio(3); if (t == 3) __builtin_amdgcn_s_setprio(2); if (t == 5) __builtin_amdgcn_s_setprio(1); if (t == 6) __builtin_amdgcn_s_setprio(0); if (t == 7) __builtin_amdgcn_s_setprio(3); }
        else {
          if (t == 4 && j == 2) __builtin_amdgcn_s_setprio(2);
          if (t == 3 && j == 3) {
            __builtin_amdgcn_s_setprio(1);
#pragma unroll
            for (int i = 0; i < 13; ++i) wq[i] = proj_ld<false>(rs, i, wid, lane);
          }
          if (t == 6 && j == 3) { __builtin_amdgcn_s_setprio(0); if (wid == 7) glds4c(ws + OFF_CNT + (C_REL + (lane & 7)) * 256, relsc); }
        }
        if (FIRST && t < 5) {
#pragma unroll
          for (int i = 5 + 3 * t; i < 8 + 3 * t; ++i) glds16c(kvs + i * 4096, ldst + i * 4096);
        }
        if (t == 7) {
          if (FIRST) { waitv<0>(); BAR(); }
          kp1 += jump; kp2 += jump;
        }
        SCHED();
        kf1[n] = *(const bf16x8*)(kp1 + ((t + 1) & 7) * 1280); kf2[n][0] = *(const unsigned*)(kp2 + ((t + 1) & 7) * 1280);
        GRP(sB = MFMA32(kf1[c], qB1, (f32x16){}), sA, 0, e1, e0, pB1, 2);
        vf[c][0] = *(const bf16x8*)vp; vf[c][1] = *(const bf16x8*)(vp + 640); vp += vinc;
        GRP(sB = MFMA32K8(BF4(kf2[c]), qB2, sB), sA, 4, e0, e1, pA0, 0);
        GRP(oB = MFMA32(vf[n][0], BF8(pB0), oB), sA, 8, e1, e0, pA0, 2);
        GRP(oB = MFMA32(vf[n][1], BF8(pB1), oB), sA, 12, e0, e1, pA1, 0);
        GRP(sA = MFMA32(kf1[n], qA1, (f32x16){}), sB, 0, e1, e0, pA1, 2);
        GRP(sA = MFMA32K8(BF4(kf2[n]), qA2, sA), sB, 4, e0, e1, pB0, 0);
        GRP(oA = MFMA32(vf[c][0], BF8(pA0), oA), sB, 8, e1, e0, pB0, 2);
        GRP(oA = MFMA32(vf[c][1], BF8(pA1), oA), sB, 12, e0, e1, pB1, 0);
      }
      vp += vjump;
    };
    group(std::true_type{}, 20480, 0);
    const int j2 = (blk - 135) * 4 + qg;
    const bool side = kh == 0 && attempt == 0 && j2 < 270;
    char* stile = smem + 1024 + qg * 4096;
    const char* svec = smem + 81920 + qg * 256;
    const int kt = blk < 135 ? 4 * (blk % 5) + qg : j2 / 10, ng = blk < 135 ? blk / 5 : j2 % 10;
    if (side) {
      const u16* W = blk < 135 ? win : wout; const int N = blk < 135 ? F_ : D_;
      const int j8 = lane >> 3;
#pragma unroll
      for (int i = 0; i < 4; ++i) {
        const int row = kt * 32 + i * 8 + j8;
        glds16(W + (size_t)row * N + ng * 64 + ((lane & 7) ^ j8) * 8, lds_addr(stile) + i * 1024);
      }
      glds4c((const float*)(ws + OFF_MOD) + (4 - h) * 640 + (kt < 20 ? kt : 0) * 32 + r, lds_addr(svec));
    }
#pragma nounroll
    for (int j = 1; j < 4; ++j) {
      if (j == 2) {
        if (kh) l2touch(wsp + ((size_t)(blk * 4 + qg) * 64 + lane) * 128, lds_addr(smem) + 256);
        else if (side) {
          waitv<0>();
          const int g4 = lane >> 4, r4 = lane & 15, row = 8 * g4 + (r4 >> 2);
          const f32x4 sv0 = *(const f32x4*)(svec + (r4 & 1) * 128 + g4 * 32), sv1 = *(const f32x4*)(svec + (r4 & 1) * 128 + g4 * 32 + 16);
          const bf16x8 vec = BF8(((u32x4){pack2(sv0[0], sv0[1]), pack2(sv0[2], sv0[3]), pack2(sv1[0], sv1[1]), pack2(sv1[2], sv1[3])}));
          const unsigned nks = blk < 135 ? 20 : 27, dso = (blk < 135 ? OFF_WIN : OFF_WOUT) + (ng * 4 * nks + kt) * 1024;
          bf16x8 fr[4]; f32x4 pr[4];
#pragma unroll
          for (int f = 0; f < 4; ++f) {
            const int ch = 2 * f + ((r4 & 3) >> 1);
            const bf16x4 lo = lds_tr(stile + row * 128 + (ch ^ (row & 7)) * 16 + (r4 & 1) * 8), hi = lds_tr(stile + (row + 4) * 128 + (ch ^ ((row + 4) & 7)) * 16 + (r4 & 1) * 8);
            fr[f] = __builtin_shufflevector(lo, hi, 0, 1, 2, 3, 4, 5, 6, 7);
          }
#pragma unroll
          for (int f = 0; f < 4; ++f) { stc16(rs, l16, dso + f * nks * 1024, __builtin_bit_cast(u32x4, fr[f])); pr[f] = MFMA16(fr[f], vec, (f32x4){}); }
          if (r4 < 2) {
#pragma unroll
            for (int f = 0; f < 4; ++f) *(f32x4*)(stile + 3072 + r4 * 256 + f * 64 + g4 * 16) = pr[f];
          }
        }
      }
      if (j == 3 && kh == 0 && attempt == 0) { waitv<0>(); arrive(ws, C_REL + xcd, lane); }
      group(std::false_type{}, j < 3 ? 20480 : 0, j);
    }
    pB1[2] = pack2(e0[0], e0[1]); pB1[3] = pack2(e0[2], e0[3]);
    oB = MFMA32(vf[1][0], BF8(pB0), oB); oB = MFMA32(vf[1][1], BF8(pB1), oB);
    {
      f32x4* ex = (f32x4*)(lh + 1024 + qg * 4096) + lane;
#pragma unroll
      for (int g = 0; g < 3; ++g) ex[g * 64] = (f32x4){oA[4 * g], oA[4 * g + 1], oA[4 * g + 2], oA[4 * g + 3]};
    }
    int* flag = (int*)(smem + 528);
    BAR();
    float o[12];
    {
      const f32x4* ex = (const f32x4*)(smem + (kh ^ 1) * 81920 + 1024 + qg * 4096) + lane;
#pragma unroll
      for (int g = 0; g < 3; ++g) { const f32x4 t = ex[g * 64];
#pragma unroll
        for (int e = 0; e < 4; ++e) o[4 * g + e] = oB[4 * g + e] + t[e]; }
    }
    const float lo = xhalf(o[8]); const float l = h ? o[8] : lo;
    const float rl = __builtin_amdgcn_rcpf(l);
    const int tok = qb * 256 + qg * 64 + kh * 32 + r;
    const unsigned vo = (tok >> 4) * 20480 + (tok & 15) * 16;
    stc16(rs, vo + h * 256, OFF_O + (head >> 1) * 1024 + (head & 1) * 512,
          (u32x4){pack2(o[0] * rl, o[1] * rl), pack2(o[2] * rl, o[3] * rl), pack2(o[4] * rl, o[5] * rl), pack2(o[6] * rl, o[7] * rl)});
    if (h == 0) stc8(rs, vo, OFF_O + 16384 + (head >> 3) * 1024 + ((head >> 1) & 3) * 256 + (head & 1) * 8, (u32x2){pack2(o[8] * rl, o[9] * rl), pack2(o[10] * rl, o[11] * rl)});
    if (wid == 4 && blk < 135 && attempt == 0 && lane < 32) {
      const char* pp = smem + 1024 + 3072 + lane * 16;
      const f32x4 v = (*(const f32x4*)pp + *(const f32x4*)(pp + 4096)) + (*(const f32x4*)(pp + 8192) + *(const f32x4*)(pp + 12288));
      stc16(rs, (lane >> 4) * 8192 + (lane & 15) * 16, OFF_CBP + (blk % 5) * 16384 + (blk / 5) * 256, __builtin_bit_cast(u32x4, v));
    }
    bool ok = (l > 1e-30f) && (l < 1e30f);
#pragma unroll
    for (int i = 0; i < 12; ++i) ok = ok && (fabsf(o[i]) < 3e38f);
    if (!ok && attempt == 0) *flag = 1;
    waitv<0>();
    BAR();
    const int redo = __builtin_amdgcn_readfirstlane(*(volatile LAS int*)flag);
    if (!redo) break;
    BAR();
  }
  if (wid == 7) arrive(ws, C_ATT + qb, lane);
#pragma unroll
  for (int i = 0; i < 13; ++i) proj_park<false>(smem, i, wid, lane, wq[i]);
  if (!__all(*(volatile LAS unsigned*)(smem + 768 + (lane & 7) * 4) >= 128u)) poll_ge(ws, C_REL + (lane & 7), 128);
}

template <bool FINAL, int KH, class HOOK>
DI void proj_body(char* __restrict__ ws, rsrc_t rs, const u16* __restrict__ x, u16* __restrict__ out, char* smem, int wid, u32x2 (&res)[3], HOOK&& after_mma) {
  const int lane = fresh_tid() & 63;
  const int xcd = blockIdx.x & 7, idx = blockIdx.x >> 3, panel = xcd * 4 + (idx >> 3), cb = idx & 7;
  constexpr int NKS = FINAL ? 27 : 20;
  constexpr int K0 = KH ? (NKS + 1) / 2 : 0, NK = KH ? NKS / 2 : (NKS + 1) / 2, NKEEP = KH ? 2 : 3, UK = KH ? 3 : 0, UG = KH ? 0 : 3;
  const int m = wid & 3, mt = panel * 4 + m, g = lane >> 4, r = lane & 15;
  f32x4 gate[NKEEP], cbv[NKEEP];
#pragma unroll
  for (int i = 0; i < NKEEP; ++i) {
    const int n = (cb * 5 + UK + i) * 16;
    if (!FINAL) { res[i] = *(const u32x2*)(x + (size_t)(mt * 16 + r) * D_ + n + g * 4); cbv[i] = ldc16f(rs, g * 16, OFF_MOD + (4 * 640 + n) * 4); }
    gate[i] = ldc16f(rs, g * 16, OFF_MOD + ((FINAL ? 5 : 2) * 640 + n) * 4);
  }
  if (wid == 7) poll_ge(ws, FINAL ? C_U + panel : C_ATT + (panel >> 2), FINAL ? 8 : 32);
  waitv<0>();
  BAR();
  bf16x8 a[NK];
#pragma unroll
  for (int k = 0; k < NK; ++k) a[k] = BF8(ldc16(rs, lane * 16, (FINAL ? OFF_U : OFF_O) + (mt * NKS + K0 + k) * 1024));
  SCHED();
  constexpr int PD = FINAL ? 5 : 6;
  const char* bl = smem + lane * 16;
  bf16x8 bf[PD + 1][5]; f32x4 acc[5];
#pragma unroll
  for (int u = 0; u < 5; ++u) acc[u] = (f32x4){};
#pragma unroll
  for (int k = 0; k < PD; ++k)
#pragma unroll
    for (int u = 0; u < 5; ++u) bf[k][u] = *(const bf16x8*)(bl + proj_pos<FINAL>(u * NKS + K0 + k));
  SCHED();
#pragma unroll
  for (int k = 0; k < NK; ++k) {
#pragma unroll
    for (int u = 0; u < 5; ++u) {
      if (k + PD < NK) bf[(k + PD) % (PD + 1)][u] = *(const bf16x8*)(bl + proj_pos<FINAL>(u * NKS + K0 + k + PD));
      acc[u] = MFMA16(bf[k % (PD + 1)][u], a[k], acc[u]);
      SCHED();
    }
  }
  f32x4* ex = (f32x4*)(smem + (FINAL ? 40960 : 68608)) + m * 5 * 64 + lane;
#pragma unroll
  for (int i = 0; i < 5 - NKEEP; ++i) ex[(UG + i) * 64] = acc[UG + i];
  f32x4 part[NKEEP];
#pragma unroll
  for (int i = 0; i < NKEEP; ++i) {
    part[i] = fma4s(gate[i], acc[UK + i], (f32x4){bflo(res[i][0]), bfhi(res[i][0]), bflo(res[i][1]), bfhi(res[i][1])});
    PIN(part[i]);
  }
  SCHED();
  after_mma();
  BAR();
  f32x4 oth[NKEEP];
#pragma unroll
  for (int i = 0; i < NKEEP; ++i) oth[i] = ex[(UK + i) * 64];
  f32x4 sv = (f32x4){}, qv = (f32x4){};
#pragma unroll
  for (int i = 0; i < NKEEP; ++i) {
    const f32x4 v = part[i] + gate[i] * oth[i];
    const u32x2 o = (u32x2){pack2(v[0], v[1]), pack2(v[2], v[3])};
    if (FINAL) *(u32x2*)(out + (size_t)(mt * 16 + r) * D_ + (cb * 5 + UK + i) * 16 + g * 4) = o;
    else {
      res[i] = o; PIN(res[i]);
      const f32x4 w = v * cbv[i]; const int un = cb * 5 + UK + i;
      stc8(rs, ((g >> 1) * 16 + r) * 16 + (g & 1) * 8, OFF_X1S + mt * 20480 + un * 512, (u32x2){pack2(w[0], w[1]), pack2(w[2], w[3])});
      sv += v; qv += v * v;
    }
  }
  if (!FINAL) {
    const float s = sum_g((sv[0] + sv[1]) + (sv[2] + sv[3])), q = sum_g((qv[0] + qv[1]) + (qv[2] + qv[3]));
    if (g == 0) stc8(rs, r * 8, OFF_ST1 + ((panel * 16 + cb * 2 + KH) * 64 + m * 16) * 8, (u32x2){__float_as_uint(s), __float_as_uint(q)});
    waitv<0>();
    BAR();
    if (wid == 7) arrive(ws, C_X1 + panel, lane);
  }
}

constexpr float GC1 = -2.8853900817779268f * 0.7978845608028654f, GC2 = GC1 * 0.044715f;
DI float gelu_s(float a, float k1, float k3, float den) {
  const float e = __builtin_amdgcn_exp2f(a * __builtin_fmaf(a * a, k3, k1));
  return a * __builtin_amdgcn_rcpf(__builtin_fmaf(e, den, den));
}
DI void gelu8(f32x4& a0, const f32x4& l0, f32x4& a1, const f32x4& l1) {
  float t[8];
#define G8(expr0, expr1) do { _Pragma("unroll") for (int i = 0; i < 4; ++i) { t[i] = opq(expr0); } _Pragma("unroll") for (int i = 0; i < 4; ++i) { t[4 + i] = opq(expr1); } SCHED(); } while (0)
  G8(a0[i] * a0[i], a1[i] * a1[i]);
  G8(__builtin_fmaf(t[i], l0[3], l0[2]), __builtin_fmaf(t[4 + i], l1[3], l1[2]));
  G8(a0[i] * t[i], a1[i] * t[4 + i]);
  G8(__builtin_amdgcn_exp2f(t[i]), __builtin_amdgcn_exp2f(t[4 + i]));
  G8(__builtin_fmaf(t[i], l0[0], l0[0]), __builtin_fmaf(t[4 + i], l1[0], l1[0]));
  G8(__builtin_amdgcn_rcpf(t[i]), __builtin_amdgcn_rcpf(t[4 + i]));
  G8(a0[i] * t[i], a1[i] * t[4 + i]);
  a0 = (f32x4){t[0], t[1], t[2], t[3]}; a1 = (f32x4){t[4], t[5], t[6], t[7]};
}
constexpr int L4_Z1 = 114688, L4_TR = L4_Z1 + 16384, L4_LN = L4_TR + 16384, L4_ST2 = L4_LN + 1024, L4_FIN = L4_ST2 + 3584, L4_CB = L4_FIN + 512, L4_CNT = L4_CB + 2048;
constexpr int K5_R = 7;
constexpr unsigned DEAD = 0x80000000u;
DI int sp_stage(int s) { const int idx = blockIdx.x >> 3; return (s + (idx & 7) * 2) & 15; }
DI unsigned sp_voff(int wid, int lane, int i) { return ((wid & 3) * 16 + i * 8 + (lane >> 3)) * 4096 + (wid >> 2) * 128 + ((lane & 7) ^ (4 * i + (lane >> 4))) * 16; }
DI const char* sp_base(const u16* wsp, int panel, int s) { return (const char*)wsp + (size_t)panel * 262144 + sp_stage(s) * 256; }
DI void sp_prefetch(u32x4 (&wr)[16 - K5_R][2], const u16* __restrict__ wsp, int panel, int wid, int lane) {
  const unsigned v0 = sp_voff(wid, lane, 0), v1 = sp_voff(wid, lane, 1);
#pragma unroll
  for (int s = K5_R; s < 16; ++s) { wr[s - K5_R][0] = *(const u32x4*)(sp_base(wsp, panel, s) + v0); wr[s - K5_R][1] = *(const u32x4*)(sp_base(wsp, panel, s) + v1); }
  asm volatile("" ::: "memory");
}
template <int S0, int S1>
DI void sp_ring(const u16* __restrict__ wsp, char* smem, int panel, int wid, int lane) {
  const unsigned v0 = sp_voff(wid, lane, 0), v1 = sp_voff(wid, lane, 1);
#pragma unroll
  for (int s = S0; s < S1; ++s) { glds16(sp_base(wsp, panel, s) + v0, lds_addr(smem) + wid * 2048 + s * 16384); glds16(sp_base(wsp, panel, s) + v1, lds_addr(smem) + wid * 2048 + s * 16384 + 1024); }
}
DI unsigned win_boff(int wid, int lane) { const int cb = (blockIdx.x >> 3) & 7; return lane * 16 + (wid == 6 && cb >= 6 ? DEAD : 0u); }
DI unsigned win_soff(int wid, bool z2) { return OFF_WIN + ((z2 ? 54 : 0) + unit0_of((blockIdx.x >> 3) & 7) + wid) * 20480; }
#define WIN_LD(so, k) BF8(ldc16(rs, bo + ((k) & 3) * 1024, (so) + ((k) >> 2) * 4096))
DI void win_prefetch(rsrc_t rs, int wid, bf16x8 (&b2)[20]) {
  const unsigned bo = win_boff(wid, fresh_tid() & 63), so = win_soff(wid, true);
#pragma unroll
  for (int k = 0; k < 20; ++k) b2[k] = WIN_LD(so, k);
}
template <class HOOK>
DI void win_mma(const char* al, const bf16x8 (&b)[20], f32x4 (&acc)[4], HOOK&& hook) {
  constexpr int PD = 4;
  bf16x8 af[PD + 1][2];
#define WIN_RD(i) do { af[(i) % (PD + 1)][0] = *(const bf16x8*)(al + ((i) & 3) * 20480 + ((i) >> 2) * 2048); af[(i) % (PD + 1)][1] = *(const bf16x8*)(al + ((i) & 3) * 20480 + ((i) >> 2) * 2048 + 1024); } while (0)
#pragma unroll
  for (int i = 0; i < PD; ++i) WIN_RD(i);
  SCHED();
  sfor<40>([&](auto I) __attribute__((always_inline)) {
    constexpr int i = I, m = i & 3, k = (i >> 2) * 2;
    if constexpr (i + PD < 40) WIN_RD(i + PD);
    acc[m] = MFMA16(b[k], af[i % (PD + 1)][0], acc[m]);
    acc[m] = MFMA16(b[k + 1], af[i % (PD + 1)][1], acc[m]); PIN(acc[m]);
    hook(I);
    SCHED();
  });
}
DI void stage_x1s(char* __restrict__ ws, char* smem, int panel, int wid, int tid) {
  const char* src = ws + OFF_X1S + (size_t)panel * 81920 + tid * 16;
#pragma unroll
  for (int i = 0; i < 10; ++i) glds16c(src + i * 8192, lds_addr(smem) + wid * 1024 + i * 8192);
}
DI void win_tail(char* __restrict__ ws, const u16* __restrict__ wsp, char* smem, int panel, int cb, int wid, int lane) {
  BAR();
  if (wid == 7 && lane < 4) __hip_atomic_fetch_add((GAS unsigned*)(ws + OFF_CNT + (C_Z2 + lane * 8 + cb) * 256), 1u, __ATOMIC_RELAXED, __HIP_MEMORY_SCOPE_AGENT);
}

template <bool EARLY>
DI void win_body(char* __restrict__ ws, rsrc_t rs, const u16* __restrict__ wsp, char* smem, int wid, const bf16x8 (&b2)[20], u32x4 (&wr)[16 - K5_R][2]) {
  const int tid = fresh_tid(), lane = tid & 63, g = lane >> 4, r = lane & 15;
  const int xcd = blockIdx.x & 7, idx = blockIdx.x >> 3, panel = xcd * 4 + (idx >> 3), cb = idx & 7, u0 = unit0_of(cb);
  float* lnp = (float*)(smem + L4_LN); float* st2 = (float*)(smem + L4_ST2); float* fin = (float*)(smem + L4_FIN); float* cbl = (float*)(smem + L4_CB);
  int* flg = (int*)(smem + L4_CNT);
  __builtin_amdgcn_s_setprio(1);
  const unsigned bo = win_boff(wid, lane), so1 = win_soff(wid, false);
  bf16x8 b1[20];
  BAR();
  stage_x1s(ws, smem, panel, wid, tid);
#pragma unroll
  for (int k = 0; k < 4; ++k) b1[k] = WIN_LD(so1, k);
  waitv<4>();
  BAR();
  f32x4 ln[4], z2[4], z1[4];
#pragma unroll
  for (int m = 0; m < 4; ++m) ln[m] = *(const f32x4*)(lnp + (m * 16 + r) * 4);
  {
    const f32x4 cs2 = *(const f32x4*)(cbl + 112 + wid * 16 + g * 4), bi2 = *(const f32x4*)(cbl + 256 + 112 + wid * 16 + g * 4);
    const f32x4 cs1 = *(const f32x4*)(cbl + wid * 16 + g * 4), bi1 = *(const f32x4*)(cbl + 256 + wid * 16 + g * 4);
#pragma unroll
    for (int m = 0; m < 4; ++m) {
#pragma unroll
      for (int e = 0; e < 4; ++e) { z2[m][e] = opq(__builtin_fmaf(ln[m][1], cs2[e], opq(ln[m][0] * bi2[e]))); z1[m][e] = opq(__builtin_fmaf(ln[m][1], cs1[e], opq(ln[m][0] * bi1[e]))); }
      PIN(z1[m]);
    }
  }
  win_mma(smem + lane * 16, b2, z2, [&](auto I) __attribute__((always_inline)) { constexpr int i = I; if constexpr (i < 32 && (i & 1)) b1[4 + i / 2] = WIN_LD(so1, 4 + i / 2); });
  gelu8(z2[0], ln[0], z2[1], ln[1]); gelu8(z2[2], ln[2], z2[3], ln[3]);
  {
    float s[4], q[4];
#pragma unroll
    for (int m = 0; m < 4; ++m) { s[m] = opq(opq(z2[m][0] + z2[m][1]) + opq(z2[m][2] + z2[m][3])); q[m] = opq(__builtin_fmaf(z2[m][0], z2[m][0], opq(z2[m][1] * z2[m][1])) + opq(__builtin_fmaf(z2[m][2], z2[m][2], opq(z2[m][3] * z2[m][3])))); }
    auto pa = __builtin_amdgcn_permlane32_swap(__float_as_uint(s[0]), __float_as_uint(s[2]), false, false);
    auto pb = __builtin_amdgcn_permlane32_swap(__float_as_uint(s[1]), __float_as_uint(s[3]), false, false);
    auto pc = __builtin_amdgcn_permlane32_swap(__float_as_uint(q[0]), __float_as_uint(q[2]), false, false);
    auto pd = __builtin_amdgcn_permlane32_swap(__float_as_uint(q[1]), __float_as_uint(q[3]), false, false);
    const float sa = opq(__uint_as_float(pa[0]) + __uint_as_float(pa[1])), sb = opq(__uint_as_float(pb[0]) + __uint_as_float(pb[1]));
    const float qa = opq(__uint_as_float(pc[0]) + __uint_as_float(pc[1])), qb = opq(__uint_as_float(pd[0]) + __uint_as_float(pd[1]));
    auto ps = __builtin_amdgcn_permlane16_swap(__float_as_uint(sa), __float_as_uint(sb), false, false);
    auto pq = __builtin_amdgcn_permlane16_swap(__float_as_uint(qa), __float_as_uint(qb), false, false);
    *(f32x2*)(st2 + (wid * 64 + lane) * 2) = (f32x2){opq(__uint_as_float(ps[0]) + __uint_as_float(ps[1])), opq(__uint_as_float(pq[0]) + __uint_as_float(pq[1]))};
  }
  asm volatile("" ::: "memory");
  if (lane == 0) __hip_atomic_fetch_add(flg, 1, __ATOMIC_RELAXED, __HIP_MEMORY_SCOPE_WORKGROUP);
  asm volatile("" ::: "memory");
  __builtin_amdgcn_s_setprio(0);
  const unsigned wvo[2] = {sp_voff(wid, lane, 0), sp_voff(wid, lane, 1)};
  win_mma(smem + lane * 16, b1, z1, [&](auto I) __attribute__((always_inline)) {
    constexpr int i = I, j = i < 8 ? i : (i & 1) ? -1 : 4 + i / 2;
    if constexpr (EARLY && j >= 0 && j < 18) wr[j >> 1][j & 1] = *(const u32x4*)(sp_base(wsp, panel, K5_R + (j >> 1)) + wvo[j & 1]);
  });
  gelu8(z1[0], ln[0], z1[1], ln[1]); gelu8(z1[2], ln[2], z1[3], ln[3]);
#pragma unroll
  for (int mm = 0; mm < 2; ++mm)
    *(u32x4*)(smem + L4_Z1 + wid * 2048 + mm * 1024 + lane * 16) = (u32x4){pack2(z1[2 * mm][0], z1[2 * mm][1]), pack2(z1[2 * mm][2], z1[2 * mm][3]), pack2(z1[2 * mm + 1][0], z1[2 * mm + 1][1]), pack2(z1[2 * mm + 1][2], z1[2 * mm + 1][3])};
  BAR();
  char* tr = smem + L4_TR + wid * 2048;
#pragma unroll
  for (int m = 0; m < 4; ++m) {
    const f32x2 f = *(const f32x2*)(fin + (m * 16 + r) * 2);
    const f32x4 v = z2[m] * f[0] - f[1];
    *(u32x2*)(tr + (m * 16 + r) * 32 + g * 8) = (u32x2){pack2(v[0], v[1]), pack2(v[2], v[3])};
  }
  {
    const char* p = tr + (8 * g + (r >> 2)) * 32 + (r & 3) * 8;
    const bf16x4 lo0 = lds_tr(p), hi0 = lds_tr(p + 128), lo1 = lds_tr(p + 1024), hi1 = lds_tr(p + 1152);
    stc16(rs, bo, OFF_Z2 + (panel * 2 * 54 + u0 + wid) * 1024, __builtin_bit_cast(u32x4, (bf16x8)__builtin_shufflevector(lo0, hi0, 0, 1, 2, 3, 4, 5, 6, 7)));
    stc16(rs, bo, OFF_Z2 + ((panel * 2 + 1) * 54 + u0 + wid) * 1024, __builtin_bit_cast(u32x4, (bf16x8)__builtin_shufflevector(lo1, hi1, 0, 1, 2, 3, 4, 5, 6, 7)));
  }
  SCHED();
  waitv<0>();
  win_tail(ws, wsp, smem, panel, cb, wid, lane);
  sp_ring<0, 5>(wsp, smem, panel, wid, lane);
  if (!EARLY) sp_prefetch(wr, wsp, panel, wid, lane);
}

DI void win_agent(char* __restrict__ ws, rsrc_t rs, const u16* __restrict__ wsp, char* smem, u32x4 (&wr)[16 - K5_R][2]) {
  const int tid = fresh_tid(), lane = tid & 63, wid = 7;
  const int xcd = blockIdx.x & 7, idx = blockIdx.x >> 3, panel = xcd * 4 + (idx >> 3), cb = idx & 7, u0 = unit0_of(cb);
  float* lnp = (float*)(smem + L4_LN); float* st2 = (float*)(smem + L4_ST2); float* fin = (float*)(smem + L4_FIN); float* cbl = (float*)(smem + L4_CB);
  int* cnt = (int*)(smem + L4_CNT);
  __builtin_amdgcn_s_setprio(2);
  if (lane == 0) *cnt = 0;
  poll_ge(ws, lane < 8 ? C_ATT + lane : C_X1 + panel, lane < 8 ? 32 : 8);
  BAR();
  stage_x1s(ws, smem, panel, wid, tid);
  {
    u32x2 t[16]; f32x4 cp[5][2];
#pragma unroll
    for (int i = 0; i < 16; ++i) t[i] = __builtin_amdgcn_raw_buffer_load_b64(rs, lane * 8, OFF_ST1 + (panel * 16 + i) * 512, 16);
    const unsigned co = lane < 28 ? (u0 * 16 + lane * 4) * 4 : min((54 + u0) * 16 + min(lane - 28, 27) * 4, F_ - 4) * 4;
#pragma unroll
    for (int i = 0; i < 5; ++i) { cp[i][0] = ldc16f(rs, co, OFF_CBP + i * 16384); cp[i][1] = ldc16f(rs, co, OFF_CBP + i * 16384 + 8192); }
    float s = 0.f, q = 0.f;
#pragma unroll
    for (int i = 0; i < 16; ++i) { s += __uint_as_float(t[i][0]); q += __uint_as_float(t[i][1]); }
    const float mean = s * (1.f / 640.f), var = fmaxf(q * (1.f / 640.f) - mean * mean, 0.f);
    const float den = ln_den(var), ri = 1.f / den;
    *(f32x4*)(lnp + lane * 4) = (f32x4){den, -rbf(mean), GC1 * ri, GC2 * ri * ri * ri};
#pragma unroll
    for (int v = 0; v < 2; ++v) *(f32x4*)(cbl + v * 256 + lane * 4) = ((cp[0][v] + cp[1][v]) + (cp[2][v] + cp[3][v])) + cp[4][v];
  }
  waitv<0>();
  BAR();
  __builtin_amdgcn_s_setprio(0);
  __builtin_amdgcn_s_sleep(8);
  sp_prefetch(wr, wsp, panel, wid, lane);
#pragma nounroll
  for (int w = 0; w < 8; ++w) sp_ring<5, 7>(wsp, smem, panel, w, lane);
  __builtin_amdgcn_s_setprio(2);
#pragma nounroll
  for (unsigned spin = 0; spin < (1u << 22); ++spin) {
    if (__hip_atomic_load(cnt, __ATOMIC_RELAXED, __HIP_MEMORY_SCOPE_WORKGROUP) >= 7) break;
    __builtin_amdgcn_s_sleep(1);
  }
  asm volatile("" ::: "memory");
  float s, q;
  {
    f32x2 t[7];
#pragma unroll
    for (int i = 0; i < 7; ++i) t[i] = *(const f32x2*)(st2 + (i * 64 + lane) * 2);
    s = t[0][0]; q = t[0][1];
#pragma unroll
    for (int i = 1; i < 6; ++i) { s = opq(s + t[i][0]); q = opq(q + t[i][1]); }
    if (cb < 6) { s = opq(s + t[6][0]); q = opq(q + t[6][1]); }
  }
  GAS u64* xch = (GAS u64*)(ws + OFF_XCH) + panel * 512;
  unsigned qb = __float_as_uint(q); if (qb == ~0u) qb = 0x7fc00000u;
  __hip_atomic_store(xch + cb * 64 + lane, ((u64)qb << 32) | __float_as_uint(s), __ATOMIC_RELAXED, __HIP_MEMORY_SCOPE_AGENT);
  u64 v[8];
#pragma nounroll
  for (unsigned spin = 0; spin < (1u << 19); ++spin) {
    bool ok = true;
#pragma unroll
    for (int i = 0; i < 8; ++i) { v[i] = __hip_atomic_load(xch + i * 64 + lane, __ATOMIC_RELAXED, __HIP_MEMORY_SCOPE_AGENT); ok = ok && ((unsigned)(v[i] >> 32) != ~0u); }
    if (__all(ok)) break;
    __builtin_amdgcn_s_sleep(1);
  }
  s = 0.f; q = 0.f;
#pragma unroll
  for (int i = 0; i < 8; ++i) { s = opq(s + __uint_as_float((unsigned)v[i])); q = opq(q + __uint_as_float((unsigned)(v[i] >> 32))); }
  const float mean = opq(s * (1.f / 864.f)), var = fmaxf(opq(q * (1.f / 864.f)) - mean * mean, 0.f);
  const float ri = 1.f / ln_den(var);
  *(f32x2*)(fin + lane * 2) = (f32x2){ri, rbf(mean) * ri};
  __builtin_amdgcn_s_setprio(0);
  BAR();
  __builtin_amdgcn_s_sleep(6);
  sp_ring<0, 5>(wsp, smem, panel, wid, lane);
  win_tail(ws, wsp, smem, panel, cb, wid, lane);
}

DI void phase_sp(char* __restrict__ ws, rsrc_t rs, const u16* __restrict__ bsp, char* smem, int wid, const u32x4 (&wr)[16 - K5_R][2]) {
  const int tid = fresh_tid(), lane = tid & 63;
  const int xcd = blockIdx.x & 7, idx = blockIdx.x >> 3, panel = xcd * 4 + (idx >> 3), cb = idx & 7;
  const int kh = wid >> 2, up = wid & 3, u0 = unit0_of(cb) + 2 * up, nu = cb < 6 ? 7 : 6, g = lane >> 4, r = lane & 15;
  const unsigned dead[2] = {2 * up < nu ? 0u : DEAD, 2 * up + 1 < nu ? 0u : DEAD};
  const unsigned bvo[2] = {lane * 16 + dead[0], lane * 16 + 1024 + dead[1]};
  u16 bb[2];
#pragma unroll
  for (int j = 0; j < 2; ++j) bb[j] = bsp[panel * 64 + (2 * kh + j) * 16 + r];
  const char* al[2][2];
#pragma unroll
  for (int k = 0; k < 2; ++k)
#pragma unroll
    for (int j = 0; j < 2; ++j) al[k][j] = smem + kh * 8192 + (j ^ kh) * 4096 + r * 128 + ((4 * k + g) ^ (r >> 1)) * 16;
  constexpr int LB = 4, PD = 3;
  bf16x8 bq[LB + 1][4], af[PD + 1][2]; f32x4 acc[2][4]; u32x4 wq[17];
  if (wid == 7) poll_ge(ws, C_Z2 + (panel >> 3) * 8 + cb, 32);
#pragma unroll
  for (int i = 0; i < 2; ++i) {
    const float b0 = bf2f(bb[0]), b1 = bf2f(bb[1]);
    acc[i][0] = (f32x4){b0, b0, b0, b0}; acc[i][1] = (f32x4){b1, b1, b1, b1}; acc[i][2] = (f32x4){}; acc[i][3] = (f32x4){};
  }
  if (kh && up < 3) waitv<2 * (16 - K5_R)>(); else waitv<0>();
  BAR();
  const unsigned bso = OFF_Z2 + (kh * 2 * 54 + u0) * 1024;
#define SP_LD(s, j) (bq[(s) % (LB + 1)][j] = BF8(ldc16(rs, bvo[(j) & 1], bso + (sp_stage(s) * 4 + ((j) >> 1)) * 55296)))
#define SP_RD(t, k) (af[(t) % (PD + 1)][k] = *(const bf16x8*)(al[k][((t) >> 1) & 1] + (((t) >> 2) % K5_R) * 16384 + ((t) & 1) * 2048))
#pragma unroll
  for (int s = 0; s < LB; ++s)
#pragma unroll
    for (int j = 0; j < 4; ++j) SP_LD(s, j);
#pragma unroll
  for (int t = 0; t < PD; ++t) { SP_RD(t, 0); SP_RD(t, 1); }
  SCHED();
  sfor<64>([&](auto T) __attribute__((always_inline)) {
    constexpr int t = T, nx = t + PD, s = t >> 2, m = t & 3, tl = t + 4 * LB;
    if constexpr (nx == 16 || nx == 28 || nx == 44 || nx == 56) BAR();
    constexpr int c = nx >= 16 && nx < 24 ? nx - 16 : nx >= 28 && nx < 34 ? nx - 20 : nx >= 44 && nx < 48 ? nx - 30 : -1;
    if constexpr (nx < 64) SP_RD(nx, 0);
    acc[0][m] = MFMA16(bq[s % (LB + 1)][0], af[t % (PD + 1)][0], acc[0][m]);
    acc[0][m] = MFMA16(bq[s % (LB + 1)][2], af[t % (PD + 1)][1], acc[0][m]); PIN(acc[0][m]);
    if constexpr (tl < 64) SP_LD(tl >> 2, m); else if constexpr (tl - 64 < 17) wq[tl - 64] = proj_ld<true>(rs, tl - 64, wid, lane);
    SCHED();
    if constexpr (nx < 64) SP_RD(nx, 1);
    acc[1][m] = MFMA16(bq[s % (LB + 1)][1], af[t % (PD + 1)][0], acc[1][m]);
    acc[1][m] = MFMA16(bq[s % (LB + 1)][3], af[t % (PD + 1)][1], acc[1][m]); PIN(acc[1][m]);
    if constexpr (c >= 0) *(u32x4*)(smem + ((c >> 1) % K5_R) * 16384 + wid * 2048 + (c & 1) * 1024 + lane * 16) = wr[c >> 1][c & 1];
    if constexpr (t == 63 && 4 * LB < 17) { _Pragma("unroll") for (int i = 4 * LB; i < 17; ++i) wq[i] = proj_ld<true>(rs, i, wid, lane); }
    SCHED();
  });
  f32x4* exw = (f32x4*)(smem + 32768 + wid * 4096) + lane;
  const f32x4* exr = (const f32x4*)(smem + 32768 + (wid ^ 4) * 4096) + lane;
  u32x4 z[2];
#pragma unroll
  for (int i = 0; i < 2; ++i) z[i] = *(const u32x4*)(smem + L4_Z1 + (2 * up + i) * 2048 + kh * 1024 + lane * 16);
#pragma unroll
  for (int i = 0; i < 2; ++i)
#pragma unroll
    for (int j = 0; j < 2; ++j) exw[(i * 2 + j) * 64] = acc[i][2 + j];
  f32x4 zf[2][2];
#pragma unroll
  for (int i = 0; i < 2; ++i)
#pragma unroll
    for (int j = 0; j < 2; ++j) {
      zf[i][j] = (f32x4){bflo(z[i][2 * j]), bfhi(z[i][2 * j]), bflo(z[i][2 * j + 1]), bfhi(z[i][2 * j + 1])};
      acc[i][j] = mul4s(acc[i][j], zf[i][j]); PIN(zf[i][j]); PIN(acc[i][j]);
    }
  BAR();
  f32x4 oth[2][2];
#pragma unroll
  for (int i = 0; i < 2; ++i)
#pragma unroll
    for (int j = 0; j < 2; ++j) oth[i][j] = exr[(i * 2 + j) * 64];
#pragma unroll
  for (int i = 0; i < 2; ++i)
#pragma unroll
    for (int j = 0; j < 2; ++j) {
      const f32x4 v = acc[i][j] + zf[i][j] * oth[i][j];
      stc8(rs, ((g >> 1) * 16 + r) * 16 + (g & 1) * 8 + dead[i], OFF_U + (panel * 4 + 2 * kh + j) * 27648 + (u0 + i) * 512, (u32x2){pack2(v[0], v[1]), pack2(v[2], v[3])});
    }
  waitv<0>();
  BAR();
  if (wid == 7) arrive(ws, C_U + panel, lane);
#pragma unroll
  for (int i = 0; i < 17; ++i) proj_park<true>(smem, i, wid, lane, wq[i]);
}

extern "C" __global__ __launch_bounds__(512, 2) OCC2 void k_mega(char* __restrict__ ws, const u16* __restrict__ x, const u16* __restrict__ win,
                                                     const u16* __restrict__ wsp, const u16* __restrict__ bsp, const u16* __restrict__ wout, u16* __restrict__ out) {
  extern __shared__ __attribute__((aligned(16))) char smem[];
  const int wid = __builtin_amdgcn_readfirstlane(threadIdx.x >> 6);
  const rsrc_t rs = __builtin_amdgcn_make_buffer_rsrc(ws, 0, WS_USED, 0x00020000);
  u32x2 res[3];
  phase_qkv(ws, rs, smem, wid);
  phase_attn(ws, rs, (const char*)wsp, win, wout, smem, wid);
  u32x4 wr[16 - K5_R][2];
  auto nohook = []() __attribute__((always_inline)) {};
  if (wid < 4) {
    bf16x8 b2[20];
    proj_body<false, 0>(ws, rs, x, out, smem, wid, res, [&]() __attribute__((always_inline)) { win_prefetch(rs, wid, b2); });
    win_body<true>(ws, rs, wsp, smem, wid, b2, wr);
  } else {
    proj_body<false, 1>(ws, rs, x, out, smem, wid, res, nohook);
    if (wid == 7) win_agent(ws, rs, wsp, smem, wr);
    else { bf16x8 b2[20]; win_prefetch(rs, wid, b2); win_body<false>(ws, rs, wsp, smem, wid, b2, wr); }
  }
  phase_sp(ws, rs, bsp, smem, wid, wr);
  if (wid < 4) proj_body<true, 0>(ws, rs, x, out, smem, wid, res, nohook);
  else proj_body<true, 1>(ws, rs, x, out, smem, wid, res, nohook);
  asm volatile("k_mega_code_end:");
}

extern "C" void kernel_launch(void* const* d_in, const int* in_sizes, int n_in, void* d_out, int out_size, void* d_ws, size_t ws_size,
                              hipStream_t stream) {
  const u16* x = (const u16*)d_in[0];
  const u16* wqkv = (const u16*)d_in[1];
  const u16* wo = (const u16*)d_in[2];
  const u16* win = (const u16*)d_in[4];
  const u16* wsp = (const u16*)d_in[5];
  const u16* bsp = (const u16*)d_in[6];
  const u16* wout = (const u16*)d_in[7];
  const u16* cvec = (const u16*)d_in[8];
  const u16* wmod = (const u16*)d_in[9];
  char* ws = (char*)d_ws;
  (void)hipFuncSetAttribute((const void*)k_mega, hipFuncAttributeMaxDynamicSharedMemorySize, 163840);
  k0_prep<<<K0_GRID, 256, 0, stream>>>(x, wqkv, wo, cvec, wmod, ws);
  k_mega<<<256, 512, 163840, stream>>>(ws, x, win, wsp, bsp, wout, (u16*)d_out);
}
```

```cpp
#include <hip/hip_runtime.h>
#include <hip/hip_bf16.h>
#include <stdint.h>
#include <utility>

typedef __attribute__((ext_vector_type(8))) short bf16x8;
typedef __attribute__((ext_vector_type(4))) short bf16x4;
typedef __attribute__((ext_vector_type(2))) float f32x2;
typedef __attribute__((ext_vector_type(4))) float f32x4;
typedef __attribute__((ext_vector_type(16))) float f32x16;
typedef __attribute__((ext_vector_type(2))) unsigned u32x2;
typedef __attribute__((ext_vector_type(4))) unsigned u32x4;
typedef __attribute__((ext_vector_type(2))) __bf16 bf2_t;
typedef __attribute__((ext_vector_type(2))) _Float16 h2_t;
typedef unsigned short u16;
typedef unsigned long long u64;
#define LAS __attribute__((address_space(3)))
#define GAS __attribute__((address_space(1)))
#define DI __device__ __forceinline__

constexpr int D_ = 640, NQKV = 1920, F_ = 1728;

constexpr unsigned OFF_CNT = 0;
constexpr unsigned OFF_MOD = 65536;
constexpr unsigned OFF_MODP = OFF_MOD + 16384;
constexpr unsigned OFF_CBP = OFF_MODP + 61440;
constexpr unsigned OFF_XCH = OFF_CBP + 5 * 16384;
constexpr unsigned OFF_ST1 = OFF_XCH + 131072;
constexpr unsigned OFF_XHAT = OFF_ST1 + 262144;
constexpr unsigned OFF_WQKV = OFF_XHAT + 2621440;
constexpr unsigned OFF_WO = OFF_WQKV + 2457600;
constexpr unsigned OFF_WIN = OFF_WO + 819200;
constexpr unsigned OFF_WOUT = OFF_WIN + 2211840;
constexpr unsigned OFF_Q = OFF_WOUT + 1105920;
constexpr unsigned OFF_KV = OFF_Q + 32 * 81920;
constexpr unsigned OFF_O = OFF_KV + 32 * 163840;
constexpr unsigned OFF_X1S = OFF_O + 2621440;
constexpr unsigned OFF_Z2 = OFF_X1S + 2621440;
constexpr unsigned OFF_U = OFF_Z2 + 3538944;
constexpr unsigned WS_USED = OFF_U + 3538944;
constexpr int C_QKV = 0, C_MOD = 15  , C_ATT = 128, C_REL = 136, C_X1 = 144, C_U = 176, C_Z2 = 208  ;

#define STAMP(kid, k)
#define TDECL
#define TS(k)
#define TDUMP()
DI unsigned pack2(float lo, float hi) { return __builtin_bit_cast(unsigned, __builtin_convertvector((f32x2){lo, hi}, bf2_t)); }
DI float opq(float x) { asm("" : "+v"(x)); return x; }
DI f32x4 fma4s(f32x4 a, f32x4 b, f32x4 c) { return (f32x4){opq(__builtin_fmaf(a[0], b[0], c[0])), opq(__builtin_fmaf(a[1], b[1], c[1])), opq(__builtin_fmaf(a[2], b[2], c[2])), opq(__builtin_fmaf(a[3], b[3], c[3]))}; }
DI f32x4 mul4s(f32x4 a, f32x4 b) { return (f32x4){opq(a[0] * b[0]), opq(a[1] * b[1]), opq(a[2] * b[2]), opq(a[3] * b[3])}; }
DI unsigned pack2h(float lo, float hi) { return __builtin_bit_cast(unsigned, __builtin_convertvector((f32x2){lo, hi}, h2_t)); }
DI float bflo(unsigned u) { return __uint_as_float(u << 16); }
DI float bfhi(unsigned u) { return __uint_as_float(u & 0xffff0000u); }
DI float rbf(float x) { return bflo(pack2(x, x)); }
DI float bf2f(u16 b) { return __uint_as_float((unsigned)b << 16); }
#define MFMA32(a, b, c) __builtin_amdgcn_mfma_f32_32x32x16_bf16((a), (b), (c), 0, 0, 0)
#define MFMA32K8(a, b, c) __builtin_amdgcn_mfma_f32_32x32x8bf16_1k((a), (b), (c), 0, 0, 0)
#define MFMA16(a, b, c) __builtin_amdgcn_mfma_f32_16x16x32_bf16((a), (b), (c), 0, 0, 0)
#define BAR() asm volatile("s_waitcnt lgkmcnt(0)\n\ts_barrier" ::: "memory")
#define SCHED() __builtin_amdgcn_sched_barrier(0)
#define PIN(x) asm volatile("" : "+v"(x))
#define OCC2 __attribute__((amdgpu_waves_per_eu(2, 2)))
#define BF8(x) __builtin_bit_cast(bf16x8, x)
#define BF4(x) __builtin_bit_cast(bf16x4, x)
template <int N> DI void waitv() { asm volatile("s_waitcnt vmcnt(%0)" ::"i"(N) : "memory"); }
template <class F, int... I> DI void sfor_impl(F&& f, std::integer_sequence<int, I...>) { (f(std::integral_constant<int, I>{}), ...); }
template <int N, class F> DI void sfor(F&& f) { sfor_impl(f, std::make_integer_sequence<int, N>{}); }
typedef short v4i16_t __attribute__((ext_vector_type(4)));
DI bf16x4 lds_tr(const char* p) { return __builtin_bit_cast(bf16x4, __builtin_amdgcn_ds_read_tr16_b64_v4i16((LAS v4i16_t*)p)); }
DI void glds16(const void* g, unsigned lds_base) {
  unsigned sv;
  asm volatile("s_mov_b32 %0, m0\n\ts_mov_b32 m0, %2\n\ts_nop 0\n\tglobal_load_lds_dwordx4 %1, off\n\ts_mov_b32 m0, %0" : "=&s"(sv) : "v"(g), "s"(lds_base) : "memory");
}
DI void glds16c(const void* g, unsigned lds_base) {
  unsigned sv;
  asm volatile("s_mov_b32 %0, m0\n\ts_mov_b32 m0, %2\n\ts_nop 0\n\tglobal_load_lds_dwordx4 %1, off sc1\n\ts_mov_b32 m0, %0" : "=&s"(sv) : "v"(g), "s"(lds_base) : "memory");
}
DI void glds4c(const void* g, unsigned lds_base) {
  unsigned sv;
  asm volatile("s_mov_b32 %0, m0\n\ts_mov_b32 m0, %2\n\ts_nop 0\n\tglobal_load_lds_dword %1, off sc1\n\ts_mov_b32 m0, %0" : "=&s"(sv) : "v"(g), "s"(lds_base) : "memory");
}
#define PSLEEP 1
typedef __amdgpu_buffer_rsrc_t rsrc_t;
DI u32x4 ldc16(rsrc_t rs, unsigned voff, unsigned soff) { return __builtin_amdgcn_raw_buffer_load_b128(rs, voff, soff, 16); }
DI unsigned ldc4(rsrc_t rs, unsigned voff, unsigned soff) { return __builtin_amdgcn_raw_buffer_load_b32(rs, voff, soff, 16); }
DI void stc16(rsrc_t rs, unsigned voff, unsigned soff, u32x4 v) { __builtin_amdgcn_raw_buffer_store_b128(v, rs, voff, soff, 16); asm volatile("s_nop 1" ::"v"(v) : "memory"); }
DI void stc8(rsrc_t rs, unsigned voff, unsigned soff, u32x2 v) { __builtin_amdgcn_raw_buffer_store_b64(v, rs, voff, soff, 16); }
DI void stc4(rsrc_t rs, unsigned voff, unsigned soff, unsigned v) { __builtin_amdgcn_raw_buffer_store_b32(v, rs, voff, soff, 16); }
DI f32x4 ldc16f(rsrc_t rs, unsigned voff, unsigned soff) { return __builtin_bit_cast(f32x4, ldc16(rs, voff, soff)); }
DI void arrive(char* ws, int c, int lane) { if (lane == 0) __hip_atomic_fetch_add((GAS unsigned*)(ws + OFF_CNT + c * 256), 1u, __ATOMIC_RELAXED, __HIP_MEMORY_SCOPE_AGENT); }
DI void poll_ge(char* ws, int c, unsigned thr) {
  const GAS unsigned* p = (const GAS unsigned*)(ws + OFF_CNT + c * 256);
#pragma nounroll
  for (unsigned spin = 0; spin < (1u << 19); ++spin) {
    if (__all(__hip_atomic_load(p, __ATOMIC_RELAXED, __HIP_MEMORY_SCOPE_AGENT) >= thr)) break;
    __builtin_amdgcn_s_sleep(PSLEEP);
  }
  asm volatile("" ::: "memory");
}
DI int fresh_tid() { int t = threadIdx.x; asm volatile("" : "+v"(t)); return t; }
DI void l2touch(const void* g, unsigned lds_scratch) {
  unsigned sv;
  asm volatile("s_mov_b32 %0, m0\n\ts_mov_b32 m0, %2\n\ts_nop 0\n\tglobal_load_lds_dword %1, off\n\ts_mov_b32 m0, %0" : "=&s"(sv) : "v"(g), "s"(lds_scratch) : "memory");
}
#define LDNT(p) __builtin_nontemporal_load(p)
DI float silu_bf(u16 c) { const float f = __uint_as_float((unsigned)c << 16); const float v = f / (1.f + __expf(-f)); return __uint_as_float(__builtin_bit_cast(unsigned, __builtin_convertvector((f32x2){v, v}, bf2_t)) << 16); }
DI unsigned lds_addr(const void* p) { return (unsigned)(uintptr_t)(const LAS void*)p; }
DI float xhalf(float v) {
  auto rr = __builtin_amdgcn_permlane32_swap(__float_as_uint(v), __float_as_uint(v), false, false);
  return __uint_as_float((threadIdx.x & 32) ? rr[0] : rr[1]);
}
DI float sum_g(float v) {
  auto a = __builtin_amdgcn_permlane16_swap(__float_as_uint(v), __float_as_uint(v), false, false);
  v = __uint_as_float(a[0]) + __uint_as_float(a[1]);
  auto b = __builtin_amdgcn_permlane32_swap(__float_as_uint(v), __float_as_uint(v), false, false);
  return __uint_as_float(b[0]) + __uint_as_float(b[1]);
}
template <int CTRL> DI float dpp_add(float v) { return v + __uint_as_float(__builtin_amdgcn_update_dpp(0u, __float_as_uint(v), CTRL, 0xf, 0xf, true)); }
DI float sum16(float v) {
  v = dpp_add<0x128>(v); v = dpp_add<0x124>(v); v = dpp_add<0x122>(v); return dpp_add<0x121>(v);
}
DI u32x4 modulate8(u32x4 v, f32x4 sc0, f32x4 sc1, f32x4 sh0, f32x4 sh1) {
  unsigned o0, o1, o2, o3; float t0, t1, t2, t3;
  asm volatile(
      "v_fma_mix_f32 %0, %8, %12, %20 op_sel_hi:[1,0,0]\n\tv_fma_mix_f32 %4, %8, %13, %21 op_sel:[1,0,0] op_sel_hi:[1,0,0]\n\t"
      "v_fma_mix_f32 %1, %9, %14, %22 op_sel_hi:[1,0,0]\n\tv_fma_mix_f32 %5, %9, %15, %23 op_sel:[1,0,0] op_sel_hi:[1,0,0]\n\t"
      "v_fma_mix_f32 %2, %10, %16, %24 op_sel_hi:[1,0,0]\n\tv_fma_mix_f32 %6, %10, %17, %25 op_sel:[1,0,0] op_sel_hi:[1,0,0]\n\t"
      "v_fma_mix_f32 %3, %11, %18, %26 op_sel_hi:[1,0,0]\n\tv_fma_mix_f32 %7, %11, %19, %27 op_sel:[1,0,0] op_sel_hi:[1,0,0]\n\t"
      "v_cvt_pk_bf16_f32 %0, %0, %4\n\tv_cvt_pk_bf16_f32 %1, %1, %5\n\tv_cvt_pk_bf16_f32 %2, %2, %6\n\tv_cvt_pk_bf16_f32 %3, %3, %7"
      : "=&v"(o0), "=&v"(o1), "=&v"(o2), "=&v"(o3), "=&v"(t0), "=&v"(t1), "=&v"(t2), "=&v"(t3)
      : "v"(v[0]), "v"(v[1]), "v"(v[2]), "v"(v[3]), "v"(sc0[0]), "v"(sc0[1]), "v"(sc0[2]), "v"(sc0[3]), "v"(sc1[0]), "v"(sc1[1]), "v"(sc1[2]), "v"(sc1[3]),
        "v"(sh0[0]), "v"(sh0[1]), "v"(sh0[2]), "v"(sh0[3]), "v"(sh1[0]), "v"(sh1[1]), "v"(sh1[2]), "v"(sh1[3]));
  return (u32x4){o0, o1, o2, o3};
}
DI float ln_den(float var) { return rbf(__builtin_sqrtf(rbf(rbf(var) + 1e-5f))); }
DI int unit0_of(int j) { return j < 6 ? 7 * j : 42 + 6 * (j - 6); }

DI void K0ST(char* p, u32x4 v) { *(u32x4*)p = v; }
constexpr int K0_MOD = 240, K0_LN = 128, K0_RL = 240, K0_WO = 50, K0_INIT = 12, K0_WARM = 24;
constexpr int K0_GRID = K0_WARM + K0_MOD + K0_LN + K0_RL + K0_WO + K0_INIT;

__global__ __launch_bounds__(256) void k0_prep(const u16* __restrict__ x, const u16* __restrict__ wqkv, const u16* __restrict__ wo, const u16* __restrict__ cvec,
                                               const u16* __restrict__ wmod, char* __restrict__ ws) {
  __shared__ __attribute__((aligned(16))) char smem[4 * 4608];
  const int tid = threadIdx.x, lane = tid & 63, wid = __builtin_amdgcn_readfirstlane(tid >> 6);
  int b = blockIdx.x;
  b = b < K0_LN + K0_RL ? b + K0_WARM + K0_MOD : b < K0_LN + K0_RL + K0_WARM + K0_MOD ? b - (K0_LN + K0_RL) : b;
  STAMP(0, 0);
  if (b < K0_WARM) {
    u64 pc; unsigned len;
    asm volatile("s_getpc_b64 s[30:31]\n\ts_add_u32 s30, s30, k_mega@rel32@lo+4\n\ts_addc_u32 s31, s31, k_mega@rel32@hi+12\n\ts_mov_b64 %0, s[30:31]\n\t"
                 "s_mov_b32 %1, k_mega_code_end-k_mega" : "=s"(pc), "=s"(len) :: "scc", "s30", "s31");
    l2touch((const char*)pc + min(((b >> 3) * 256 + tid) * 128u, len - 4u), lds_addr(smem) + wid * 256);
    return;
  }
  b -= K0_WARM;
  if (b < K0_MOD) {
    const int cg = b >> 2, ks = b & 3, c = tid & 7, row0 = ks * 160 + (tid >> 3), col0 = cg * 64;
    u32x4 w[5]; u16 cv[5];
#pragma unroll
    for (int i = 0; i < 5; ++i) cv[i] = cvec[row0 + 32 * i];
    SCHED();
#pragma unroll
    for (int i = 0; i < 5; ++i) w[i] = LDNT((const u32x4*)(wmod + (size_t)(row0 + 32 * i) * 3840 + col0 + c * 8));
    SCHED();
    float sl[5];
#pragma unroll
    for (int i = 0; i < 5; ++i) sl[i] = silu_bf(cv[i]);
    SCHED();
    float acc[8] = {0, 0, 0, 0, 0, 0, 0, 0};
#pragma unroll
    for (int i = 0; i < 5; ++i) {
      const float s = sl[i];
#pragma unroll
      for (int e = 0; e < 4; ++e) { acc[2 * e] += s * bflo(w[i][e]); acc[2 * e + 1] += s * bfhi(w[i][e]); }
    }
#pragma unroll
    for (int e = 0; e < 8; ++e) acc[e] = sum_g(dpp_add<0x128>(acc[e]));
    float* red = (float*)smem;
    if (lane < 8) { *(f32x4*)(red + wid * 64 + lane * 8) = (f32x4){acc[0], acc[1], acc[2], acc[3]}; *(f32x4*)(red + wid * 64 + lane * 8 + 4) = (f32x4){acc[4], acc[5], acc[6], acc[7]}; }
    __syncthreads();
    if (tid < 64) ((float*)(ws + OFF_MODP))[ks * 3840 + cg * 64 + tid] = (red[tid] + red[64 + tid]) + (red[128 + tid] + red[192 + tid]);
    STAMP(0, 1); STAMP(0, 13);
    return;
  }
  b -= K0_MOD;
  if (b < K0_LN) {
    b = (b & 7) * 16 + (b >> 3);
    const int row = b * 16 + wid * 4 + (lane >> 4), c = lane & 15;
    const u16* xr = x + (size_t)row * D_ + c * 8;
    u32x4 v[5];
#pragma unroll
    for (int i = 0; i < 5; ++i) v[i] = *(const u32x4*)(xr + i * 128);
    float s = 0.f, q = 0.f;
#pragma unroll
    for (int i = 0; i < 5; ++i)
#pragma unroll
      for (int e = 0; e < 4; ++e) s += bflo(v[i][e]) + bfhi(v[i][e]);
    const float mean = sum16(s) * (1.f / 640.f);
#pragma unroll
    for (int i = 0; i < 5; ++i)
#pragma unroll
      for (int e = 0; e < 4; ++e) { const float a = bflo(v[i][e]) - mean, d = bfhi(v[i][e]) - mean; q += a * a + d * d; }
    const float var = sum16(q) * (1.f / 640.f);
    const float rinv = 1.f / ln_den(var), mb = rbf(mean);
    char* dst = ws + OFF_XHAT + (size_t)row * 1280 + c * 16;
#pragma unroll
    for (int i = 0; i < 5; ++i) {
      u32x4 o;
#pragma unroll
      for (int e = 0; e < 4; ++e) o[e] = pack2h((bflo(v[i][e]) - mb) * rinv, (bfhi(v[i][e]) - mb) * rinv);
      K0ST(dst + i * 256, o);
    }
    STAMP(0, 2); STAMP(0, 13);
    return;
  }
  b -= K0_LN;
  if (b < K0_RL) {
    const int kt = b / 12, G = b % 12, sec = G >> 2, gq = G & 3;
    u32x4 v[3];
#pragma unroll
    for (int i = 0; i < 3; ++i) { const int ci = min(i * 256 + tid, 639); v[i] = LDNT((const u32x4*)(wqkv + (size_t)(kt * 32 + ci / 20) * NQKV + G * 160 + (ci % 20) * 8)); }
#pragma unroll
    for (int i = 0; i < 3; ++i) { const int ci = min(i * 256 + tid, 639); *(u32x4*)(smem + (ci / 20) * 336 + (ci % 20) * 16) = v[i]; }
    __syncthreads();
    const int g = lane >> 4, q = (lane & 15) >> 2, p = lane & 3;
    for (int f = wid; f < 10; f += 4) {
      const int t = f >> 1, x2 = f & 1;
      const int col = sec == 2 ? 16 * f + 4 * p : t < 4 ? 20 * (2 * t + (p >> 1)) + 8 * (p & 1) + 4 * x2 : 20 * (4 * x2 + p) + 16;
      const int T = sec == 2 ? 40 + gq * 5 + t : sec * 20 + (t < 4 ? 4 * gq + t : 16 + gq);
      const char* pp = smem + (8 * g + q) * 336 + col * 2;
      const bf16x4 lo = lds_tr(pp), hi = lds_tr(pp + 4 * 336);
      K0ST(ws + OFF_WQKV + (((size_t)T * 20 + kt) * 2 + x2) * 1024 + lane * 16, __builtin_bit_cast(u32x4, (bf16x8)__builtin_shufflevector(lo, hi, 0, 1, 2, 3, 4, 5, 6, 7)));
    }
    STAMP(0, 3); STAMP(0, 13);
    return;
  }
  b -= K0_RL;
  if (b < K0_WO) {
    const int job = b * 4 + wid, kt = job / 10, ng = job % 10, j8 = lane >> 3;
    char* t = smem + wid * 4608;
    u32x4 v[4];
#pragma unroll
    for (int i = 0; i < 4; ++i) {
      const int c = 4 * kt + i, row = c < 64 ? 20 * (c >> 1) + 4 * (c & 1) + (j8 & 3) + 8 * (j8 >> 2) : 20 * (2 * (c - 64) + (j8 >> 2)) + 16 + (j8 & 3);
      v[i] = LDNT((const u32x4*)(wo + (size_t)row * D_ + ng * 64 + (lane & 7) * 8));
    }
#pragma unroll
    for (int i = 0; i < 4; ++i) *(u32x4*)(t + (i * 8 + j8) * 144 + (lane & 7) * 16) = v[i];
    const char* pp = t + (8 * (lane >> 4) + ((lane & 15) >> 2)) * 144 + (lane & 3) * 8;
#pragma unroll
    for (int f = 0; f < 4; ++f) {
      const bf16x4 lo = lds_tr(pp + f * 32), hi = lds_tr(pp + 4 * 144 + f * 32);
      K0ST(ws + OFF_WO + ((size_t)(ng * 4 + f) * 20 + kt) * 1024 + lane * 16, __builtin_bit_cast(u32x4, (bf16x8)__builtin_shufflevector(lo, hi, 0, 1, 2, 3, 4, 5, 6, 7)));
    }
    return;
  }
  b -= K0_WO;
  {
    u32x4* p = (u32x4*)(ws + (b < 8 ? OFF_XCH + b * 16384 : OFF_CNT + (b - 8) * 16384)) + tid;
    const unsigned f = b < 8 ? ~0u : 0u;
#pragma unroll
    for (int i = 0; i < 4; ++i) p[i * 256] = (u32x4){f, f, f, f};
  }
}

constexpr float QSCALE = 0.22360679774997896f * 1.4426950408889634f;
#define K1W 3
#define K1WB 5
#define K1WB0 2
constexpr int K1_W = K1W, K1_WB = K1WB, K1_WB0 = K1WB0;

template <bool ISV, class FIN>
DI void k1_mma(const char* __restrict__ bsrc, const char* smem, int lane, FIN&& fin) {
  f32x4 acc[8][2];
  constexpr int PD = 4;
  bf16x8 bfr[K1_WB + 1][4], af[PD + 1][2];
  const char* lds[2];
#pragma unroll
  for (int k = 0; k < 2; ++k) lds[k] = smem + k * 8192 + (lane ^ (4 * k + (lane >> 4))) * 16;
#pragma unroll
  for (int i = 0; i < 4 * K1_WB0; ++i) bfr[i >> 2][i & 3] = *(const bf16x8*)(bsrc + i * 1024);
#pragma unroll
  for (int m = 0; m < 8; ++m) { acc[m][0] = (f32x4){}; acc[m][1] = (f32x4){}; }
  BAR();
#pragma unroll
  for (int i = 4 * K1_WB0; i < 4 * K1_WB; ++i) bfr[i >> 2][i & 3] = *(const bf16x8*)(bsrc + i * 1024);
  BAR();
  __builtin_amdgcn_s_setprio(1);
#define K1_RD(t, k) (af[(t) % (PD + 1)][k] = *(const bf16x8*)(lds[k] + (((t) >> 3) % 3) * 16384 + ((t) & 7) * 1024))
#define K1_MM(c, b, a) (c = ISV ? MFMA16(a, b, c) : MFMA16(b, a, c))
#pragma unroll
  for (int t = 0; t < PD; ++t) { K1_RD(t, 0); K1_RD(t, 1); }
  SCHED();
  sfor<80>([&](auto T) __attribute__((always_inline)) {
    constexpr int t = T, s = t >> 3, m = t & 7, nx = t + PD;
    if constexpr (nx < 80 && (nx & 7) == 0) asm volatile("s_barrier" ::: "memory");
    if constexpr (nx < 80) K1_RD(nx, 0);
    K1_MM(acc[m][0], bfr[s % (K1_WB + 1)][0], af[t % (PD + 1)][0]); K1_MM(acc[m][0], bfr[s % (K1_WB + 1)][2], af[t % (PD + 1)][1]); PIN(acc[m][0]);
    SCHED();
    if constexpr (nx < 80) K1_RD(nx, 1);
    if constexpr (s + K1_WB < 10 && (m & 1)) bfr[(s + K1_WB) % (K1_WB + 1)][m >> 1] = *(const bf16x8*)(bsrc + ((s + K1_WB) * 4 + (m >> 1)) * 1024);
    K1_MM(acc[m][1], bfr[s % (K1_WB + 1)][1], af[t % (PD + 1)][0]); K1_MM(acc[m][1], bfr[s % (K1_WB + 1)][3], af[t % (PD + 1)][1]); PIN(acc[m][1]);
    SCHED();
    if constexpr (t >= 74) { fin(t - 74, acc[t - 74]); SCHED(); }
  });
  fin(6, acc[6]); fin(7, acc[7]);
  __builtin_amdgcn_s_setprio(0);
}

#define TARG
#define TPASS

DI void phase_qkv(char* __restrict__ ws, rsrc_t rs, char* smem, int wid TARG) {
  const int tid = fresh_tid(), lane = tid & 63;
  const int xcd = blockIdx.x & 7, idx = blockIdx.x >> 3;
  int ctr;
  if ((xcd & 1) && idx >= 28) {
    ctr = C_MOD;
    if (tid < 160) {
      const int col = 1280 + ((xcd >> 1) * 4 + idx - 28) * 160 + tid;
      const float* pp = (const float*)(ws + OFF_MODP) + col;
      float v = rbf((pp[0] + pp[3840]) + (pp[7680] + pp[11520]));
      if (col / 640 == 4) v = rbf(1.f + v);
      stc4(rs, col * 4, OFF_MOD, __float_as_uint(v));
    }
  } else {
    const int mt = (xcd >> 1) * 4 + (idx & 3), nt = (xcd & 1) * 8 + (idx >> 2);
    ctr = C_QKV + nt;
    if (wid < 4) {
      float* cst = (float*)(smem + 49152);
      const int c = lane & 7, rr = lane >> 3;
      const char* src = ws + OFF_XHAT + (size_t)(mt * 128 + wid * 32 + rr) * 1280 + c * 16;
      char* wdst = smem + (c >> 2) * 8192 + wid * 2048 + (c & 3) * 256 + (rr ^ c) * 16;
      u32x4 a[10][4];
      f32x4 c0[4], c1[4];
#pragma unroll
      for (int i = 0; i < 4; ++i) { c0[i] = *(const f32x4*)(ws + OFF_MODP + i * 15360 + tid * 16); if (wid == 0) c1[i] = *(const f32x4*)(ws + OFF_MODP + i * 15360 + 4096 + tid * 16); }
#pragma unroll
      for (int s = 0; s < K1_W; ++s)
#pragma unroll
        for (int m = 0; m < 4; ++m) a[s][m] = *(const u32x4*)(src + m * 10240 + s * 128);
      {
        f32x4 v = (c0[0] + c0[1]) + (c0[2] + c0[3]); const float one = tid >= 160 ? 1.f : 0.f;
        *(f32x4*)(cst + tid * 4) = (f32x4){rbf(one + rbf(v[0])), rbf(one + rbf(v[1])), rbf(one + rbf(v[2])), rbf(one + rbf(v[3]))};
        if (wid == 0) { v = (c1[0] + c1[1]) + (c1[2] + c1[3]); *(f32x4*)(cst + 1024 + tid * 4) = (f32x4){rbf(1.f + rbf(v[0])), rbf(1.f + rbf(v[1])), rbf(1.f + rbf(v[2])), rbf(1.f + rbf(v[3]))}; }
      }
      BAR();
      const float* cp = cst + c * 8;
      f32x4 kc[10][4];
#define K1_KC(s) do { kc[s][0] = *(const f32x4*)(cp + (s) * 64); kc[s][1] = *(const f32x4*)(cp + (s) * 64 + 4); kc[s][2] = *(const f32x4*)(cp + (s) * 64 + 640); kc[s][3] = *(const f32x4*)(cp + (s) * 64 + 644); } while (0)
#pragma unroll
      for (int s = 0; s < 6; ++s) K1_KC(s);
      SCHED();
      u32x4 o[2][4];
#define K1_XF(s, m) (o[(s) & 1][m] = modulate8(a[s][m], kc[s][2], kc[s][3], kc[s][0], kc[s][1]))
#define K1_WR(s, m) (*(u32x4*)(wdst + ((s) % 3) * 16384 + ((m) >> 1) * 1024 + ((m) & 1) * 128) = o[(s) & 1][m])
#pragma unroll
      for (int m = 0; m < 4; ++m) { K1_XF(0, m); K1_WR(0, m); }
      BAR();
      TS(1);
#pragma unroll
      for (int m = 0; m < 4; ++m) a[K1_W][m] = *(const u32x4*)(src + m * 10240 + K1_W * 128);
#pragma unroll
      for (int m = 0; m < 4; ++m) K1_XF(1, m);
      SCHED();
#pragma unroll
      for (int s = 1; s < 10; ++s) {
#pragma unroll
        for (int m = 0; m < 4; ++m) {
          if (m == 0) K1_WR(s, 0);
          if (m < 3) K1_WR(s, m + 1);
          if (s + K1_W < 10) a[s + K1_W][m] = *(const u32x4*)(src + m * 10240 + (s + K1_W) * 128);
          if (s < 5) kc[s + 5][m] = *(const f32x4*)(cp + (s + 5) * 64 + (m & 1) * 4 + (m >> 1) * 640);
          SCHED();
          if (s < 9) K1_XF(s + 1, m);
          SCHED();
        }
        asm volatile("" :: "v"(o[s & 1][0]), "v"(o[s & 1][1]), "v"(o[s & 1][2]), "v"(o[s & 1][3]));
        BAR();
      }
    } else {
      const int cw = wid - 4;
      const char* bsrc = ws + OFF_WQKV + (size_t)(nt * 4 + cw) * 40 * 1024 + lane * 16;
      const int r = lane & 15, g = lane >> 4;
      if (nt >= 10) {
        const unsigned so = OFF_KV + (mt >> 1) * 20480 + 10240 + (mt & 1) * 5120;
        unsigned vo[2];
#pragma unroll
        for (int x = 0; x < 2; ++x) { const unsigned c = (nt - 10) * 128 + cw * 32 + x * 16 + r; vo[x] = c / 20 * 163840u + ((g & 1) * 20 + c % 20) * 16 + (g >> 1) * 8; }
        k1_mma<true>(bsrc, smem, lane, [&](int m, const f32x4 (&a)[2]) __attribute__((always_inline)) {
#pragma unroll
          for (int x = 0; x < 2; ++x) stc8(rs, vo[x] + (m & 3) * 640, so + (m >> 2) * 2560, (u32x2){pack2(a[x][0], a[x][1]), pack2(a[x][2], a[x][3])});
        });
      } else {
        const bool isQ = nt < 5; const float sc = isQ ? QSCALE : 1.f;
        const unsigned so = isQ ? OFF_Q + mt * 5120 : OFF_KV + (mt >> 1) * 20480 + (mt & 1) * 5120, hs = isQ ? 81920u : 163840u;
        const int n5 = isQ ? nt : nt - 5;
        const bool wide = n5 < 4;
        const unsigned vo = wide ? (2 * (n5 * 4 + cw) + (g >> 1)) * hs + (g & 1) * 512 + r * 16 : (8 * cw + g) * hs + 1024 + r * 8;
        const f32x4 sc4 = (f32x4){sc, sc, sc, sc};
        k1_mma<false>(bsrc, smem, lane, [&](int m, const f32x4 (&a)[2]) __attribute__((always_inline)) {
          const f32x4 v0 = mul4s(a[0], sc4), v1 = mul4s(a[1], sc4);
          const u32x4 w = (u32x4){pack2(v0[0], v0[1]), pack2(v0[2], v0[3]), pack2(v1[0], v1[1]), pack2(v1[2], v1[3])};
          if (wide) stc16(rs, vo + (m & 1) * 256, so + (m >> 1) * 1280, w);
          else { stc8(rs, vo + (m & 1) * 128, so + (m >> 1) * 1280, (u32x2){w[0], w[1]}); stc8(rs, vo + (m & 1) * 128, so + 4 * hs + (m >> 1) * 1280, (u32x2){w[2], w[3]}); }
        });
      }
    }
  }
  TS(3);
  waitv<0>();
  BAR();
  TS(4);
  if (wid == 7 && lane < 8) __hip_atomic_fetch_add((GAS unsigned*)(ws + OFF_CNT + (lane * 16 + ctr) * 256), 1u, __ATOMIC_RELAXED, __HIP_MEMORY_SCOPE_AGENT);
  if (ctr == C_MOD && wid == 7) { __builtin_amdgcn_s_sleep(127); __builtin_amdgcn_s_sleep(60); }
}

template <bool FINAL> DI constexpr int proj_pos(int f) { return FINAL ? (f < 96 ? 65536 + f * 1024 : (f - 96) * 1024) : 17408 + (f % 20 >= 10 ? 81920 : 0) + (f / 20 * 10 + f % 10) * 1024; }
template <bool FINAL> DI u32x4 proj_ld(rsrc_t rs, int i, int wid, int lane) {
  constexpr int NF = FINAL ? 135 : 100;
  return ldc16(rs, lane * 16, (FINAL ? OFF_WOUT : OFF_WO) + (((blockIdx.x >> 3) & 7) * NF + min(i * 8 + wid, NF - 1)) * 1024);
}
template <bool FINAL> DI void proj_park(char* smem, int i, int wid, int lane, u32x4 v) {
  constexpr int NF = FINAL ? 135 : 100;
  *(u32x4*)(smem + proj_pos<FINAL>(min(i * 8 + wid, NF - 1)) + lane * 16) = v;
}

DI void split3(float m, unsigned& w_h0, unsigned& w_h1) {
  const float n = -m; const float a = rbf(n), b = rbf(n - a), c = rbf(n - a - b);
  w_h0 = pack2(a, b); w_h1 = pack2(c, 0.f);
}

DI void phase_attn(char* __restrict__ ws, rsrc_t rs, const char* __restrict__ wsp, const u16* __restrict__ win, const u16* __restrict__ wout, char* smem, int wid TARG) {
  const int tid = fresh_tid(), lane = tid & 63;
  const int xcd = blockIdx.x & 7, idx = blockIdx.x >> 3, head = xcd * 4 + (idx >> 3), qb = idx & 7;
  const int r = lane & 31, h = lane >> 5, qg = wid & 3, kh = wid >> 2;
  if (wid == 7) {
    const int l7 = lane & 7;
    const int c = l7 == 0 ? head >> 3 : l7 == 1 ? 4 : l7 == 2 ? 5 + (head >> 3) : l7 == 3 ? 9 : l7 == 4 ? 10 + (20 * head) / 128 : l7 == 5 ? 10 + (20 * head + 19) / 128 : C_MOD;
    poll_ge(ws, xcd * 16 + c, 16);
  }
  BAR();
  TS(5);
  unsigned kvo = OFF_KV + head * 163840, qo = OFF_Q + head * 81920 + (qb * 8 + qg * 2) * 1280;
  const unsigned kconst = h == 0 ? 0x3f803f80u : 0x00003f80u;
  const unsigned so16 = (tid & 255) * 16, l16 = lane * 16, l2 = 1024 + r * 8 + h * 4;
  const int blk = blockIdx.x;
  u32x4 wq[13];
  for (int attempt = 0; attempt < 2; ++attempt) {
    asm volatile("" : "+s"(kvo), "+s"(qo));
    const char* kvs = ws + kvo + kh * 81920 + so16;
    const unsigned ldst = lds_addr(smem) + kh * 81920 + qg * 1024;
    u32x4 qA1w, qB1w, k1w; unsigned qA2w, qB2w, k2, m0sv;
    asm volatile(
        "s_nop 4\n\tbuffer_load_dwordx4 %0, %7, %9, %19 offen sc1\n\tbuffer_load_dwordx4 %1, %7, %9, %10 offen sc1\n\tbuffer_load_dwordx4 %2, %7, %9, %11 offen sc1\n\t"
        "buffer_load_dword %3, %8, %9, %19 offen sc1\n\tbuffer_load_dword %4, %8, %9, %10 offen sc1\n\tbuffer_load_dword %5, %8, %9, %11 offen sc1\n\t"
        "s_mov_b32 %6, m0\n\ts_mov_b32 m0, %17\n\ts_nop 0\n\tglobal_load_lds_dwordx4 %12, off sc1\n\t"
        "s_add_u32 m0, %18, 0x1000\n\ts_nop 0\n\tglobal_load_lds_dwordx4 %13, off sc1\n\t"
        "s_add_u32 m0, %18, 0x2000\n\ts_nop 0\n\tglobal_load_lds_dwordx4 %14, off sc1\n\t"
        "s_add_u32 m0, %18, 0x3000\n\ts_nop 0\n\tglobal_load_lds_dwordx4 %15, off sc1\n\t"
        "s_add_u32 m0, %18, 0x4000\n\ts_nop 0\n\tglobal_load_lds_dwordx4 %16, off sc1\n\t"
        "s_mov_b32 m0, %6\n\ts_waitcnt vmcnt(5)"
        : "=&v"(qA1w), "=&v"(qB1w), "=&v"(k1w), "=&v"(qA2w), "=&v"(qB2w), "=&v"(k2), "=&s"(m0sv)
        : "v"(l16), "v"(l2), "s"(rs), "s"(qo + kh * 1280), "s"(kvo), "v"(kvs + (wid == 0 ? 4096 : 0)), "v"(kvs + 4096), "v"(kvs + 8192), "v"(kvs + 12288), "v"(kvs + 16384),
          "s"(ldst + (wid == 0 ? 4096 : 0)), "s"(ldst), "s"(qo + (kh ^ 1) * 1280)
        : "memory", "scc");
    if (wid == 0) { const unsigned f = (tid & 1) ? 0u : 0x3f803f80u; *(u32x4*)(smem + l16) = (u32x4){f, f, f, f}; }
    const bf16x8 qA1 = BF8(qA1w), qB1 = BF8(qB1w);
    bf16x8 k1 = BF8(k1w);
    TS(30);
    float mA = -INFINITY, mB = -INFINITY;
    {
      const bf16x4 qA2 = BF4(((u32x2){qA2w, 0u})), qB2 = BF4(((u32x2){qB2w, 0u}));
      const int nt = attempt ? 64 : 1;
      bf16x8 t1 = k1; unsigned t2 = k2;
      for (int t = 0; t < nt; ++t) {
        if (t) { const unsigned to = kvo + (t >> 3) * 20480 + (t & 7) * 1280; t1 = BF8(ldc16(rs, l16, to)); t2 = ldc4(rs, l2, to); }
        const bf16x4 t2v = BF4(((u32x2){t2, kconst}));
        f32x16 a = MFMA32(t1, qA1, (f32x16){}); a = MFMA32K8(t2v, qA2, a);
        f32x16 b = MFMA32(t1, qB1, (f32x16){}); b = MFMA32K8(t2v, qB2, b);
#pragma unroll
        for (int i = 0; i < 16; ++i) { mA = fmaxf(mA, a[i]); mB = fmaxf(mB, b[i]); }
      }
      mA = fmaxf(mA, xhalf(mA)); mB = fmaxf(mB, xhalf(mB));
    }
    unsigned a0, a1, b0, b1; split3(mA, a0, a1); split3(mB, b0, b1);
    const bf16x4 qA2 = BF4(((u32x2){qA2w, h ? a1 : a0})), qB2 = BF4(((u32x2){qB2w, h ? b1 : b0}));
    char* lh = smem + kh * 81920;
    TS(31);
    waitv<0>();
    BAR();
    TS(6);
    f32x16 oA = (f32x16){}, oB = (f32x16){};
    const char* kp1 = lh + l16;
    const char* kp2 = lh + l2;
    const bool vreal = r < 20;
    const char* vp = vreal ? lh + 10240 + (h * 20 + r) * 16 : smem + (r == 20 ? 0 : 16);
    const int vinc = vreal ? 1280 : 0, vjump = vreal ? 10240 : 0;
    if (kh) { k1 = *(const bf16x8*)kp1; k2 = *(const unsigned*)kp2; }
    bf16x8 kf1[2], vf[2][2]; u32x2 kf2[2];
    kf1[0] = k1; kf2[0] = (u32x2){k2, kconst}; kf2[1] = (u32x2){0u, kconst};
    vf[1][0] = (bf16x8){}; vf[1][1] = (bf16x8){};
    u32x4 pA0, pA1, pB0 = (u32x4){}, pB1 = (u32x4){};
    f32x4 e0 = (f32x4){}, e1;
    f32x16 sA = MFMA32(kf1[0], qA1, (f32x16){}), sB;
    sA = MFMA32K8(BF4(kf2[0]), qA2, sA);
#define EX2(x) __builtin_amdgcn_exp2f(x)
#define GRP(MF, S, i, EN, EP, P, w) do { MF; SCHED(); EN[0] = EX2(S[i]); P[w] = pack2(EP[0], EP[1]); EN[1] = EX2(S[i + 1]); P[w + 1] = pack2(EP[2], EP[3]); \
      EN[2] = EX2(S[i + 2]); EN[3] = EX2(S[i + 3]); PIN(P); PIN(EN); SCHED(); } while (0)
    const unsigned relsc = lds_addr(smem) + 768;
    auto group = [&](auto FIRST, int jump, int j) __attribute__((always_inline)) {
#pragma unroll
      for (int t = 0; t < 8; ++t) {
        const int c = t & 1, n = c ^ 1;
        if (FIRST) { if (t == 0) __builtin_amdgcn_s_setprio(3); if (t == 3) __builtin_amdgcn_s_setprio(2); if (t == 5) __builtin_amdgcn_s_setprio(1); if (t == 6) __builtin_amdgcn_s_setprio(0); if (t == 7) __builtin_amdgcn_s_setprio(3); }
        else {
          if (t == 4 && j == 2) __builtin_amdgcn_s_setprio(2);
          if (t == 3 && j == 3) {
            __builtin_amdgcn_s_setprio(1);
#pragma unroll
            for (int i = 0; i < 13; ++i) wq[i] = proj_ld<false>(rs, i, wid, lane);
          }
          if (t == 6 && j == 3) { __builtin_amdgcn_s_setprio(0); if (wid == 7) glds4c(ws + OFF_CNT + (C_REL + (lane & 7)) * 256, relsc); }
        }
        if (FIRST && t < 5) {
#pragma unroll
          for (int i = 5 + 3 * t; i < 8 + 3 * t; ++i) glds16c(kvs + i * 4096, ldst + i * 4096);
        }
        if (t == 7) {
          if (FIRST) { waitv<0>(); BAR(); }
          kp1 += jump; kp2 += jump;
        }
        SCHED();
        kf1[n] = *(const bf16x8*)(kp1 + ((t + 1) & 7) * 1280); kf2[n][0] = *(const unsigned*)(kp2 + ((t + 1) & 7) * 1280);
        GRP(sB = MFMA32(kf1[c], qB1, (f32x16){}), sA, 0, e1, e0, pB1, 2);
        vf[c][0] = *(const bf16x8*)vp; vf[c][1] = *(const bf16x8*)(vp + 640); vp += vinc;
        GRP(sB = MFMA32K8(BF4(kf2[c]), qB2, sB), sA, 4, e0, e1, pA0, 0);
        GRP(oB = MFMA32(vf[n][0], BF8(pB0), oB), sA, 8, e1, e0, pA0, 2);
        GRP(oB = MFMA32(vf[n][1], BF8(pB1), oB), sA, 12, e0, e1, pA1, 0);
        GRP(sA = MFMA32(kf1[n], qA1, (f32x16){}), sB, 0, e1, e0, pA1, 2);
        GRP(sA = MFMA32K8(BF4(kf2[n]), qA2, sA), sB, 4, e0, e1, pB0, 0);
        GRP(oA = MFMA32(vf[c][0], BF8(pA0), oA), sB, 8, e1, e0, pB0, 2);
        GRP(oA = MFMA32(vf[c][1], BF8(pA1), oA), sB, 12, e0, e1, pB1, 0);
      }
      vp += vjump;
    };
    group(std::true_type{}, 20480, 0);
    TS(7);
    const int j2 = (blk - 135) * 4 + qg;
    const bool side = kh == 0 && attempt == 0 && j2 < 270;
    char* stile = smem + 1024 + qg * 4096;
    const char* svec = smem + 81920 + qg * 256;
    const int kt = blk < 135 ? 4 * (blk % 5) + qg : j2 / 10, ng = blk < 135 ? blk / 5 : j2 % 10;
    if (side) {
      const u16* W = blk < 135 ? win : wout; const int N = blk < 135 ? F_ : D_;
      const int j8 = lane >> 3;
#pragma unroll
      for (int i = 0; i < 4; ++i) {
        const int row = kt * 32 + i * 8 + j8;
        glds16(W + (size_t)row * N + ng * 64 + ((lane & 7) ^ j8) * 8, lds_addr(stile) + i * 1024);
      }
      glds4c((const float*)(ws + OFF_MOD) + (4 - h) * 640 + (kt < 20 ? kt : 0) * 32 + r, lds_addr(svec));
    }
#pragma nounroll
    for (int j = 1; j < 4; ++j) {
      if (j == 2) {
        if (kh) l2touch(wsp + ((size_t)(blk * 4 + qg) * 64 + lane) * 128, lds_addr(smem) + 256);
        else if (side) {
          waitv<0>();
          const int g4 = lane >> 4, r4 = lane & 15, row = 8 * g4 + (r4 >> 2);
          const f32x4 sv0 = *(const f32x4*)(svec + (r4 & 1) * 128 + g4 * 32), sv1 = *(const f32x4*)(svec + (r4 & 1) * 128 + g4 * 32 + 16);
          const bf16x8 vec = BF8(((u32x4){pack2(sv0[0], sv0[1]), pack2(sv0[2], sv0[3]), pack2(sv1[0], sv1[1]), pack2(sv1[2], sv1[3])}));
          const unsigned nks = blk < 135 ? 20 : 27, dso = (blk < 135 ? OFF_WIN : OFF_WOUT) + (ng * 4 * nks + kt) * 1024;
          bf16x8 fr[4]; f32x4 pr[4];
#pragma unroll
          for (int f = 0; f < 4; ++f) {
            const int ch = 2 * f + ((r4 & 3) >> 1);
            const bf16x4 lo = lds_tr(stile + row * 128 + (ch ^ (row & 7)) * 16 + (r4 & 1) * 8), hi = lds_tr(stile + (row + 4) * 128 + (ch ^ ((row + 4) & 7)) * 16 + (r4 & 1) * 8);
            fr[f] = __builtin_shufflevector(lo, hi, 0, 1, 2, 3, 4, 5, 6, 7);
          }
#pragma unroll
          for (int f = 0; f < 4; ++f) { stc16(rs, l16, dso + f * nks * 1024, __builtin_bit_cast(u32x4, fr[f])); pr[f] = MFMA16(fr[f], vec, (f32x4){}); }
          if (r4 < 2) {
#pragma unroll
            for (int f = 0; f < 4; ++f) *(f32x4*)(stile + 3072 + r4 * 256 + f * 64 + g4 * 16) = pr[f];
          }
        }
      }
      if (j == 3 && kh == 0 && attempt == 0) { waitv<0>(); arrive(ws, C_REL + xcd, lane); }
      group(std::false_type{}, j < 3 ? 20480 : 0, j);
    }
    TS(8);
    pB1[2] = pack2(e0[0], e0[1]); pB1[3] = pack2(e0[2], e0[3]);
    oB = MFMA32(vf[1][0], BF8(pB0), oB); oB = MFMA32(vf[1][1], BF8(pB1), oB);
    {
      f32x4* ex = (f32x4*)(lh + 1024 + qg * 4096) + lane;
#pragma unroll
      for (int g = 0; g < 3; ++g) ex[g * 64] = (f32x4){oA[4 * g], oA[4 * g + 1], oA[4 * g + 2], oA[4 * g + 3]};
    }
    int* flag = (int*)(smem + 528);
    BAR();
    TS(9);
    float o[12];
    {
      const f32x4* ex = (const f32x4*)(smem + (kh ^ 1) * 81920 + 1024 + qg * 4096) + lane;
#pragma unroll
      for (int g = 0; g < 3; ++g) { const f32x4 t = ex[g * 64];
#pragma unroll
        for (int e = 0; e < 4; ++e) o[4 * g + e] = oB[4 * g + e] + t[e]; }
    }
    const float lo = xhalf(o[8]); const float l = h ? o[8] : lo;
    const float rl = __builtin_amdgcn_rcpf(l);
    const int tok = qb * 256 + qg * 64 + kh * 32 + r;
    const unsigned vo = (tok >> 4) * 20480 + (tok & 15) * 16;
    stc16(rs, vo + h * 256, OFF_O + (head >> 1) * 1024 + (head & 1) * 512,
          (u32x4){pack2(o[0] * rl, o[1] * rl), pack2(o[2] * rl, o[3] * rl), pack2(o[4] * rl, o[5] * rl), pack2(o[6] * rl, o[7] * rl)});
    if (h == 0) stc8(rs, vo, OFF_O + 16384 + (head >> 3) * 1024 + ((head >> 1) & 3) * 256 + (head & 1) * 8, (u32x2){pack2(o[8] * rl, o[9] * rl), pack2(o[10] * rl, o[11] * rl)});
    if (wid == 4 && blk < 135 && attempt == 0 && lane < 32) {
      const char* pp = smem + 1024 + 3072 + lane * 16;
      const f32x4 v = (*(const f32x4*)pp + *(const f32x4*)(pp + 4096)) + (*(const f32x4*)(pp + 8192) + *(const f32x4*)(pp + 12288));
      stc16(rs, (lane >> 4) * 8192 + (lane & 15) * 16, OFF_CBP + (blk % 5) * 16384 + (blk / 5) * 256, __builtin_bit_cast(u32x4, v));
    }
    bool ok = (l > 1e-30f) && (l < 1e30f);
#pragma unroll
    for (int i = 0; i < 12; ++i) ok = ok && (fabsf(o[i]) < 3e38f);
    if (!ok && attempt == 0) *flag = 1;
    waitv<0>();
    BAR();
    const int redo = __builtin_amdgcn_readfirstlane(*(volatile LAS int*)flag);
    if (!redo) break;
    BAR();
  }
  TS(10);
  if (wid == 7) arrive(ws, C_ATT + qb, lane);
#pragma unroll
  for (int i = 0; i < 13; ++i) proj_park<false>(smem, i, wid, lane, wq[i]);
  if (!__all(*(volatile LAS unsigned*)(smem + 768 + (lane & 7) * 4) >= 128u)) poll_ge(ws, C_REL + (lane & 7), 128);
}

template <bool FINAL, int KH, class HOOK>
DI void proj_body(char* __restrict__ ws, rsrc_t rs, const u16* __restrict__ x, u16* __restrict__ out, char* smem, int wid, u32x2 (&res)[3], HOOK&& after_mma TARG) {
  const int lane = fresh_tid() & 63;
  const int xcd = blockIdx.x & 7, idx = blockIdx.x >> 3, panel = xcd * 4 + (idx >> 3), cb = idx & 7;
  constexpr int NKS = FINAL ? 27 : 20;
  constexpr int K0 = KH ? (NKS + 1) / 2 : 0, NK = KH ? NKS / 2 : (NKS + 1) / 2, NKEEP = KH ? 2 : 3, UK = KH ? 3 : 0, UG = KH ? 0 : 3;
  const int m = wid & 3, mt = panel * 4 + m, g = lane >> 4, r = lane & 15;
  f32x4 gate[NKEEP], cbv[NKEEP];
#pragma unroll
  for (int i = 0; i < NKEEP; ++i) {
    const int n = (cb * 5 + UK + i) * 16;
    if (!FINAL) { res[i] = *(const u32x2*)(x + (size_t)(mt * 16 + r) * D_ + n + g * 4); cbv[i] = ldc16f(rs, g * 16, OFF_MOD + (4 * 640 + n) * 4); }
    gate[i] = ldc16f(rs, g * 16, OFF_MOD + ((FINAL ? 5 : 2) * 640 + n) * 4);
  }
  if (wid == 7) poll_ge(ws, FINAL ? C_U + panel : C_ATT + (panel >> 2), FINAL ? 8 : 32);
  waitv<0>();
  BAR();
  TS(FINAL ? 27 : 11);
  bf16x8 a[NK];
#pragma unroll
  for (int k = 0; k < NK; ++k) a[k] = BF8(ldc16(rs, lane * 16, (FINAL ? OFF_U : OFF_O) + (mt * NKS + K0 + k) * 1024));
  SCHED();
  constexpr int PD = FINAL ? 5 : 6;
  const char* bl = smem + lane * 16;
  bf16x8 bf[PD + 1][5]; f32x4 acc[5];
#pragma unroll
  for (int u = 0; u < 5; ++u) acc[u] = (f32x4){};
#pragma unroll
  for (int k = 0; k < PD; ++k)
#pragma unroll
    for (int u = 0; u < 5; ++u) bf[k][u] = *(const bf16x8*)(bl + proj_pos<FINAL>(u * NKS + K0 + k));
  SCHED();
#pragma unroll
  for (int k = 0; k < NK; ++k) {
#pragma unroll
    for (int u = 0; u < 5; ++u) {
      if (k + PD < NK) bf[(k + PD) % (PD + 1)][u] = *(const bf16x8*)(bl + proj_pos<FINAL>(u * NKS + K0 + k + PD));
      acc[u] = MFMA16(bf[k % (PD + 1)][u], a[k], acc[u]);
      SCHED();
    }
  }
  TS(FINAL ? 28 : 12);
  f32x4* ex = (f32x4*)(smem + (FINAL ? 40960 : 68608)) + m * 5 * 64 + lane;
#pragma unroll
  for (int i = 0; i < 5 - NKEEP; ++i) ex[(UG + i) * 64] = acc[UG + i];
  f32x4 part[NKEEP];
#pragma unroll
  for (int i = 0; i < NKEEP; ++i) {
    part[i] = fma4s(gate[i], acc[UK + i], (f32x4){bflo(res[i][0]), bfhi(res[i][0]), bflo(res[i][1]), bfhi(res[i][1])});
    PIN(part[i]);
  }
  SCHED();
  after_mma();
  BAR();
  f32x4 oth[NKEEP];
#pragma unroll
  for (int i = 0; i < NKEEP; ++i) oth[i] = ex[(UK + i) * 64];
  f32x4 sv = (f32x4){}, qv = (f32x4){};
#pragma unroll
  for (int i = 0; i < NKEEP; ++i) {
    const f32x4 v = part[i] + gate[i] * oth[i];
    const u32x2 o = (u32x2){pack2(v[0], v[1]), pack2(v[2], v[3])};
    if (FINAL) *(u32x2*)(out + (size_t)(mt * 16 + r) * D_ + (cb * 5 + UK + i) * 16 + g * 4) = o;
    else {
      res[i] = o; PIN(res[i]);
      const f32x4 w = v * cbv[i]; const int un = cb * 5 + UK + i;
      stc8(rs, ((g >> 1) * 16 + r) * 16 + (g & 1) * 8, OFF_X1S + mt * 20480 + un * 512, (u32x2){pack2(w[0], w[1]), pack2(w[2], w[3])});
      sv += v; qv += v * v;
    }
  }
  if (!FINAL) {
    const float s = sum_g((sv[0] + sv[1]) + (sv[2] + sv[3])), q = sum_g((qv[0] + qv[1]) + (qv[2] + qv[3]));
    if (g == 0) stc8(rs, r * 8, OFF_ST1 + ((panel * 16 + cb * 2 + KH) * 64 + m * 16) * 8, (u32x2){__float_as_uint(s), __float_as_uint(q)});
    TS(13);
    waitv<0>();
    BAR();
    TS(14);
    if (wid == 7) arrive(ws, C_X1 + panel, lane);
  }
}

constexpr float GC1 = -2.8853900817779268f * 0.7978845608028654f, GC2 = GC1 * 0.044715f;
DI float gelu_s(float a, float k1, float k3, float den) {
  const float e = __builtin_amdgcn_exp2f(a * __builtin_fmaf(a * a, k3, k1));
  return a * __builtin_amdgcn_rcpf(__builtin_fmaf(e, den, den));
}
DI void gelu8(f32x4& a0, const f32x4& l0, f32x4& a1, const f32x4& l1) {
  float t[8];
#define G8(expr0, expr1) do { _Pragma("unroll") for (int i = 0; i < 4; ++i) { t[i] = opq(expr0); } _Pragma("unroll") for (int i = 0; i < 4; ++i) { t[4 + i] = opq(expr1); } SCHED(); } while (0)
  G8(a0[i] * a0[i], a1[i] * a1[i]);
  G8(__builtin_fmaf(t[i], l0[3], l0[2]), __builtin_fmaf(t[4 + i], l1[3], l1[2]));
  G8(a0[i] * t[i], a1[i] * t[4 + i]);
  G8(__builtin_amdgcn_exp2f(t[i]), __builtin_amdgcn_exp2f(t[4 + i]));
  G8(__builtin_fmaf(t[i], l0[0], l0[0]), __builtin_fmaf(t[4 + i], l1[0], l1[0]));
  G8(__builtin_amdgcn_rcpf(t[i]), __builtin_amdgcn_rcpf(t[4 + i]));
  G8(a0[i] * t[i], a1[i] * t[4 + i]);
  a0 = (f32x4){t[0], t[1], t[2], t[3]}; a1 = (f32x4){t[4], t[5], t[6], t[7]};
}
constexpr int L4_Z1 = 114688, L4_TR = L4_Z1 + 16384, L4_LN = L4_TR + 16384, L4_ST2 = L4_LN + 1024, L4_FIN = L4_ST2 + 3584, L4_CB = L4_FIN + 512, L4_CNT = L4_CB + 2048;
constexpr int K5_R = 7;
constexpr unsigned DEAD = 0x80000000u;
DI int sp_stage(int s) { const int idx = blockIdx.x >> 3; return (s + (idx & 7) * 2) & 15; }
DI unsigned sp_voff(int wid, int lane, int i) { return ((wid & 3) * 16 + i * 8 + (lane >> 3)) * 4096 + (wid >> 2) * 128 + ((lane & 7) ^ (4 * i + (lane >> 4))) * 16; }
DI const char* sp_base(const u16* wsp, int panel, int s) { return (const char*)wsp + (size_t)panel * 262144 + sp_stage(s) * 256; }
DI void sp_prefetch(u32x4 (&wr)[16 - K5_R][2], const u16* __restrict__ wsp, int panel, int wid, int lane) {
  const unsigned v0 = sp_voff(wid, lane, 0), v1 = sp_voff(wid, lane, 1);
#pragma unroll
  for (int s = K5_R; s < 16; ++s) { wr[s - K5_R][0] = *(const u32x4*)(sp_base(wsp, panel, s) + v0); wr[s - K5_R][1] = *(const u32x4*)(sp_base(wsp, panel, s) + v1); }
  asm volatile("" ::: "memory");
}
template <int S0, int S1>
DI void sp_ring(const u16* __restrict__ wsp, char* smem, int panel, int wid, int lane) {
  const unsigned v0 = sp_voff(wid, lane, 0), v1 = sp_voff(wid, lane, 1);
#pragma unroll
  for (int s = S0; s < S1; ++s) { glds16(sp_base(wsp, panel, s) + v0, lds_addr(smem) + wid * 2048 + s * 16384); glds16(sp_base(wsp, panel, s) + v1, lds_addr(smem) + wid * 2048 + s * 16384 + 1024); }
}
DI unsigned win_boff(int wid, int lane) { const int cb = (blockIdx.x >> 3) & 7; return lane * 16 + (wid == 6 && cb >= 6 ? DEAD : 0u); }
DI unsigned win_soff(int wid, bool z2) { return OFF_WIN + ((z2 ? 54 : 0) + unit0_of((blockIdx.x >> 3) & 7) + wid) * 20480; }
#define WIN_LD(so, k) BF8(ldc16(rs, bo + ((k) & 3) * 1024, (so) + ((k) >> 2) * 4096))
DI void win_prefetch(rsrc_t rs, int wid, bf16x8 (&b2)[20]) {
  const unsigned bo = win_boff(wid, fresh_tid() & 63), so = win_soff(wid, true);
#pragma unroll
  for (int k = 0; k < 20; ++k) b2[k] = WIN_LD(so, k);
}
template <class HOOK>
DI void win_mma(const char* al, const bf16x8 (&b)[20], f32x4 (&acc)[4], HOOK&& hook) {
  constexpr int PD = 4;
  bf16x8 af[PD + 1][2];
#define WIN_RD(i) do { af[(i) % (PD + 1)][0] = *(const bf16x8*)(al + ((i) & 3) * 20480 + ((i) >> 2) * 2048); af[(i) % (PD + 1)][1] = *(const bf16x8*)(al + ((i) & 3) * 20480 + ((i) >> 2) * 2048 + 1024); } while (0)
#pragma unroll
  for (int i = 0; i < PD; ++i) WIN_RD(i);
  SCHED();
  sfor<40>([&](auto I) __attribute__((always_inline)) {
    constexpr int i = I, m = i & 3, k = (i >> 2) * 2;
    if constexpr (i + PD < 40) WIN_RD(i + PD);
    acc[m] = MFMA16(b[k], af[i % (PD + 1)][0], acc[m]);
    acc[m] = MFMA16(b[k + 1], af[i % (PD + 1)][1], acc[m]); PIN(acc[m]);
    hook(I);
    SCHED();
  });
}
DI void stage_x1s(char* __restrict__ ws, char* smem, int panel, int wid, int tid) {
  const char* src = ws + OFF_X1S + (size_t)panel * 81920 + tid * 16;
#pragma unroll
  for (int i = 0; i < 10; ++i) glds16c(src + i * 8192, lds_addr(smem) + wid * 1024 + i * 8192);
}
DI void win_tail(char* __restrict__ ws, const u16* __restrict__ wsp, char* smem, int panel, int cb, int wid, int lane TARG) {
  BAR();
  TS(22);
  if (wid == 7 && lane < 4) __hip_atomic_fetch_add((GAS unsigned*)(ws + OFF_CNT + (C_Z2 + lane * 8 + cb) * 256), 1u, __ATOMIC_RELAXED, __HIP_MEMORY_SCOPE_AGENT);
}

template <bool EARLY>
DI void win_body(char* __restrict__ ws, rsrc_t rs, const u16* __restrict__ wsp, char* smem, int wid, const bf16x8 (&b2)[20], u32x4 (&wr)[16 - K5_R][2] TARG) {
  const int tid = fresh_tid(), lane = tid & 63, g = lane >> 4, r = lane & 15;
  const int xcd = blockIdx.x & 7, idx = blockIdx.x >> 3, panel = xcd * 4 + (idx >> 3), cb = idx & 7, u0 = unit0_of(cb);
  float* lnp = (float*)(smem + L4_LN); float* st2 = (float*)(smem + L4_ST2); float* fin = (float*)(smem + L4_FIN); float* cbl = (float*)(smem + L4_CB);
  int* flg = (int*)(smem + L4_CNT);
  __builtin_amdgcn_s_setprio(1);
  const unsigned bo = win_boff(wid, lane), so1 = win_soff(wid, false);
  bf16x8 b1[20];
  BAR();
  TS(15);
  stage_x1s(ws, smem, panel, wid, tid);
#pragma unroll
  for (int k = 0; k < 4; ++k) b1[k] = WIN_LD(so1, k);
  waitv<4>();
  BAR();
  TS(16);
  f32x4 ln[4], z2[4], z1[4];
#pragma unroll
  for (int m = 0; m < 4; ++m) ln[m] = *(const f32x4*)(lnp + (m * 16 + r) * 4);
  {
    const f32x4 cs2 = *(const f32x4*)(cbl + 112 + wid * 16 + g * 4), bi2 = *(const f32x4*)(cbl + 256 + 112 + wid * 16 + g * 4);
    const f32x4 cs1 = *(const f32x4*)(cbl + wid * 16 + g * 4), bi1 = *(const f32x4*)(cbl + 256 + wid * 16 + g * 4);
#pragma unroll
    for (int m = 0; m < 4; ++m) {
#pragma unroll
      for (int e = 0; e < 4; ++e) { z2[m][e] = opq(__builtin_fmaf(ln[m][1], cs2[e], opq(ln[m][0] * bi2[e]))); z1[m][e] = opq(__builtin_fmaf(ln[m][1], cs1[e], opq(ln[m][0] * bi1[e]))); }
      PIN(z1[m]);
    }
  }
  win_mma(smem + lane * 16, b2, z2, [&](auto I) __attribute__((always_inline)) { constexpr int i = I; if constexpr (i < 32 && (i & 1)) b1[4 + i / 2] = WIN_LD(so1, 4 + i / 2); });
  TS(17);
  gelu8(z2[0], ln[0], z2[1], ln[1]); gelu8(z2[2], ln[2], z2[3], ln[3]);
  {
    float s[4], q[4];
#pragma unroll
    for (int m = 0; m < 4; ++m) { s[m] = opq(opq(z2[m][0] + z2[m][1]) + opq(z2[m][2] + z2[m][3])); q[m] = opq(__builtin_fmaf(z2[m][0], z2[m][0], opq(z2[m][1] * z2[m][1])) + opq(__builtin_fmaf(z2[m][2], z2[m][2], opq(z2[m][3] * z2[m][3])))); }
    auto pa = __builtin_amdgcn_permlane32_swap(__float_as_uint(s[0]), __float_as_uint(s[2]), false, false);
    auto pb = __builtin_amdgcn_permlane32_swap(__float_as_uint(s[1]), __float_as_uint(s[3]), false, false);
    auto pc = __builtin_amdgcn_permlane32_swap(__float_as_uint(q[0]), __float_as_uint(q[2]), false, false);
    auto pd = __builtin_amdgcn_permlane32_swap(__float_as_uint(q[1]), __float_as_uint(q[3]), false, false);
    const float sa = opq(__uint_as_float(pa[0]) + __uint_as_float(pa[1])), sb = opq(__uint_as_float(pb[0]) + __uint_as_float(pb[1]));
    const float qa = opq(__uint_as_float(pc[0]) + __uint_as_float(pc[1])), qb = opq(__uint_as_float(pd[0]) + __uint_as_float(pd[1]));
    auto ps = __builtin_amdgcn_permlane16_swap(__float_as_uint(sa), __float_as_uint(sb), false, false);
    auto pq = __builtin_amdgcn_permlane16_swap(__float_as_uint(qa), __float_as_uint(qb), false, false);
    *(f32x2*)(st2 + (wid * 64 + lane) * 2) = (f32x2){opq(__uint_as_float(ps[0]) + __uint_as_float(ps[1])), opq(__uint_as_float(pq[0]) + __uint_as_float(pq[1]))};
  }
  asm volatile("" ::: "memory");
  if (lane == 0) __hip_atomic_fetch_add(flg, 1, __ATOMIC_RELAXED, __HIP_MEMORY_SCOPE_WORKGROUP);
  asm volatile("" ::: "memory");
  __builtin_amdgcn_s_setprio(0);
  TS(18);
  const unsigned wvo[2] = {sp_voff(wid, lane, 0), sp_voff(wid, lane, 1)};
  win_mma(smem + lane * 16, b1, z1, [&](auto I) __attribute__((always_inline)) {
    constexpr int i = I, j = i < 8 ? i : (i & 1) ? -1 : 4 + i / 2;
    if constexpr (EARLY && j >= 0 && j < 18) wr[j >> 1][j & 1] = *(const u32x4*)(sp_base(wsp, panel, K5_R + (j >> 1)) + wvo[j & 1]);
  });
  gelu8(z1[0], ln[0], z1[1], ln[1]); gelu8(z1[2], ln[2], z1[3], ln[3]);
#pragma unroll
  for (int mm = 0; mm < 2; ++mm)
    *(u32x4*)(smem + L4_Z1 + wid * 2048 + mm * 1024 + lane * 16) = (u32x4){pack2(z1[2 * mm][0], z1[2 * mm][1]), pack2(z1[2 * mm][2], z1[2 * mm][3]), pack2(z1[2 * mm + 1][0], z1[2 * mm + 1][1]), pack2(z1[2 * mm + 1][2], z1[2 * mm + 1][3])};
  TS(20);
  BAR();
  TS(21);
  char* tr = smem + L4_TR + wid * 2048;
#pragma unroll
  for (int m = 0; m < 4; ++m) {
    const f32x2 f = *(const f32x2*)(fin + (m * 16 + r) * 2);
    const f32x4 v = z2[m] * f[0] - f[1];
    *(u32x2*)(tr + (m * 16 + r) * 32 + g * 8) = (u32x2){pack2(v[0], v[1]), pack2(v[2], v[3])};
  }
  {
    const char* p = tr + (8 * g + (r >> 2)) * 32 + (r & 3) * 8;
    const bf16x4 lo0 = lds_tr(p), hi0 = lds_tr(p + 128), lo1 = lds_tr(p + 1024), hi1 = lds_tr(p + 1152);
    stc16(rs, bo, OFF_Z2 + (panel * 2 * 54 + u0 + wid) * 1024, __builtin_bit_cast(u32x4, (bf16x8)__builtin_shufflevector(lo0, hi0, 0, 1, 2, 3, 4, 5, 6, 7)));
    stc16(rs, bo, OFF_Z2 + ((panel * 2 + 1) * 54 + u0 + wid) * 1024, __builtin_bit_cast(u32x4, (bf16x8)__builtin_shufflevector(lo1, hi1, 0, 1, 2, 3, 4, 5, 6, 7)));
  }
  SCHED();
  waitv<0>();
  win_tail(ws, wsp, smem, panel, cb, wid, lane TPASS);
  sp_ring<0, 5>(wsp, smem, panel, wid, lane);
  if (!EARLY) sp_prefetch(wr, wsp, panel, wid, lane);
}

DI void win_agent(char* __restrict__ ws, rsrc_t rs, const u16* __restrict__ wsp, char* smem, u32x4 (&wr)[16 - K5_R][2] TARG) {
  const int tid = fresh_tid(), lane = tid & 63, wid = 7;
  const int xcd = blockIdx.x & 7, idx = blockIdx.x >> 3, panel = xcd * 4 + (idx >> 3), cb = idx & 7, u0 = unit0_of(cb);
  float* lnp = (float*)(smem + L4_LN); float* st2 = (float*)(smem + L4_ST2); float* fin = (float*)(smem + L4_FIN); float* cbl = (float*)(smem + L4_CB);
  int* cnt = (int*)(smem + L4_CNT);
  __builtin_amdgcn_s_setprio(2);
  if (lane == 0) *cnt = 0;
  poll_ge(ws, lane < 8 ? C_ATT + lane : C_X1 + panel, lane < 8 ? 32 : 8);
  BAR();
  stage_x1s(ws, smem, panel, wid, tid);
  {
    u32x2 t[16]; f32x4 cp[5][2];
#pragma unroll
    for (int i = 0; i < 16; ++i) t[i] = __builtin_amdgcn_raw_buffer_load_b64(rs, lane * 8, OFF_ST1 + (panel * 16 + i) * 512, 16);
    const unsigned co = lane < 28 ? (u0 * 16 + lane * 4) * 4 : min((54 + u0) * 16 + min(lane - 28, 27) * 4, F_ - 4) * 4;
#pragma unroll
    for (int i = 0; i < 5; ++i) { cp[i][0] = ldc16f(rs, co, OFF_CBP + i * 16384); cp[i][1] = ldc16f(rs, co, OFF_CBP + i * 16384 + 8192); }
    float s = 0.f, q = 0.f;
#pragma unroll
    for (int i = 0; i < 16; ++i) { s += __uint_as_float(t[i][0]); q += __uint_as_float(t[i][1]); }
    const float mean = s * (1.f / 640.f), var = fmaxf(q * (1.f / 640.f) - mean * mean, 0.f);
    const float den = ln_den(var), ri = 1.f / den;
    *(f32x4*)(lnp + lane * 4) = (f32x4){den, -rbf(mean), GC1 * ri, GC2 * ri * ri * ri};
#pragma unroll
    for (int v = 0; v < 2; ++v) *(f32x4*)(cbl + v * 256 + lane * 4) = ((cp[0][v] + cp[1][v]) + (cp[2][v] + cp[3][v])) + cp[4][v];
  }
  waitv<0>();
  BAR();
  __builtin_amdgcn_s_setprio(0);
  __builtin_amdgcn_s_sleep(8);
  sp_prefetch(wr, wsp, panel, wid, lane);
#pragma nounroll
  for (int w = 0; w < 8; ++w) sp_ring<5, 7>(wsp, smem, panel, w, lane);
  __builtin_amdgcn_s_setprio(2);
#pragma nounroll
  for (unsigned spin = 0; spin < (1u << 22); ++spin) {
    if (__hip_atomic_load(cnt, __ATOMIC_RELAXED, __HIP_MEMORY_SCOPE_WORKGROUP) >= 7) break;
    __builtin_amdgcn_s_sleep(1);
  }
  asm volatile("" ::: "memory");
  float s, q;
  {
    f32x2 t[7];
#pragma unroll
    for (int i = 0; i < 7; ++i) t[i] = *(const f32x2*)(st2 + (i * 64 + lane) * 2);
    s = t[0][0]; q = t[0][1];
#pragma unroll
    for (int i = 1; i < 6; ++i) { s = opq(s + t[i][0]); q = opq(q + t[i][1]); }
    if (cb < 6) { s = opq(s + t[6][0]); q = opq(q + t[6][1]); }
  }
  GAS u64* xch = (GAS u64*)(ws + OFF_XCH) + panel * 512;
  unsigned qb = __float_as_uint(q); if (qb == ~0u) qb = 0x7fc00000u;
  __hip_atomic_store(xch + cb * 64 + lane, ((u64)qb << 32) | __float_as_uint(s), __ATOMIC_RELAXED, __HIP_MEMORY_SCOPE_AGENT);
  TS(19);
  u64 v[8];
#pragma nounroll
  for (unsigned spin = 0; spin < (1u << 19); ++spin) {
    bool ok = true;
#pragma unroll
    for (int i = 0; i < 8; ++i) { v[i] = __hip_atomic_load(xch + i * 64 + lane, __ATOMIC_RELAXED, __HIP_MEMORY_SCOPE_AGENT); ok = ok && ((unsigned)(v[i] >> 32) != ~0u); }
    if (__all(ok)) break;
    __builtin_amdgcn_s_sleep(1);
  }
  s = 0.f; q = 0.f;
#pragma unroll
  for (int i = 0; i < 8; ++i) { s = opq(s + __uint_as_float((unsigned)v[i])); q = opq(q + __uint_as_float((unsigned)(v[i] >> 32))); }
  const float mean = opq(s * (1.f / 864.f)), var = fmaxf(opq(q * (1.f / 864.f)) - mean * mean, 0.f);
  const float ri = 1.f / ln_den(var);
  *(f32x2*)(fin + lane * 2) = (f32x2){ri, rbf(mean) * ri};
  __builtin_amdgcn_s_setprio(0);
  TS(20);
  BAR();
  __builtin_amdgcn_s_sleep(6);
  sp_ring<0, 5>(wsp, smem, panel, wid, lane);
  win_tail(ws, wsp, smem, panel, cb, wid, lane TPASS);
}

#define SP_LB 4
DI void phase_sp(char* __restrict__ ws, rsrc_t rs, const u16* __restrict__ bsp, char* smem, int wid, const u32x4 (&wr)[16 - K5_R][2] TARG) {
  const int tid = fresh_tid(), lane = tid & 63;
  const int xcd = blockIdx.x & 7, idx = blockIdx.x >> 3, panel = xcd * 4 + (idx >> 3), cb = idx & 7;
  const int kh = wid >> 2, up = wid & 3, u0 = unit0_of(cb) + 2 * up, nu = cb < 6 ? 7 : 6, g = lane >> 4, r = lane & 15;
  const unsigned dead[2] = {2 * up < nu ? 0u : DEAD, 2 * up + 1 < nu ? 0u : DEAD};
  const unsigned bvo[2] = {lane * 16 + dead[0], lane * 16 + 1024 + dead[1]};
  u16 bb[2];
#pragma unroll
  for (int j = 0; j < 2; ++j) bb[j] = bsp[panel * 64 + (2 * kh + j) * 16 + r];
  const char* al[2][2];
#pragma unroll
  for (int k = 0; k < 2; ++k)
#pragma unroll
    for (int j = 0; j < 2; ++j) al[k][j] = smem + kh * 8192 + (j ^ kh) * 4096 + r * 128 + ((4 * k + g) ^ (r >> 1)) * 16;
  constexpr int LB = SP_LB, PD = 3;
  bf16x8 bq[LB + 1][4], af[PD + 1][2]; f32x4 acc[2][4]; u32x4 wq[17];
  if (wid == 7) poll_ge(ws, C_Z2 + (panel >> 3) * 8 + cb, 32);
#pragma unroll
  for (int i = 0; i < 2; ++i) {
    const float b0 = bf2f(bb[0]), b1 = bf2f(bb[1]);
    acc[i][0] = (f32x4){b0, b0, b0, b0}; acc[i][1] = (f32x4){b1, b1, b1, b1}; acc[i][2] = (f32x4){}; acc[i][3] = (f32x4){};
  }
  if (kh && up < 3) waitv<2 * (16 - K5_R)>(); else waitv<0>();
  BAR();
  TS(23);
  const unsigned bso = OFF_Z2 + (kh * 2 * 54 + u0) * 1024;
#define SP_LD(s, j) (bq[(s) % (LB + 1)][j] = BF8(ldc16(rs, bvo[(j) & 1], bso + (sp_stage(s) * 4 + ((j) >> 1)) * 55296)))
#define SP_RD(t, k) (af[(t) % (PD + 1)][k] = *(const bf16x8*)(al[k][((t) >> 1) & 1] + (((t) >> 2) % K5_R) * 16384 + ((t) & 1) * 2048))
#pragma unroll
  for (int s = 0; s < LB; ++s)
#pragma unroll
    for (int j = 0; j < 4; ++j) SP_LD(s, j);
#pragma unroll
  for (int t = 0; t < PD; ++t) { SP_RD(t, 0); SP_RD(t, 1); }
  SCHED();
  sfor<64>([&](auto T) __attribute__((always_inline)) {
    constexpr int t = T, nx = t + PD, s = t >> 2, m = t & 3, tl = t + 4 * LB;
    if constexpr (nx == 16 || nx == 28 || nx == 44 || nx == 56) { BAR(); TS(32 + (nx == 28) + 2 * (nx == 44) + 3 * (nx == 56)); }
    constexpr int c = nx >= 16 && nx < 24 ? nx - 16 : nx >= 28 && nx < 34 ? nx - 20 : nx >= 44 && nx < 48 ? nx - 30 : -1;
    if constexpr (nx < 64) SP_RD(nx, 0);
    acc[0][m] = MFMA16(bq[s % (LB + 1)][0], af[t % (PD + 1)][0], acc[0][m]);
    acc[0][m] = MFMA16(bq[s % (LB + 1)][2], af[t % (PD + 1)][1], acc[0][m]); PIN(acc[0][m]);
    if constexpr (tl < 64) SP_LD(tl >> 2, m); else if constexpr (tl - 64 < 17) wq[tl - 64] = proj_ld<true>(rs, tl - 64, wid, lane);
    SCHED();
    if constexpr (nx < 64) SP_RD(nx, 1);
    acc[1][m] = MFMA16(bq[s % (LB + 1)][1], af[t % (PD + 1)][0], acc[1][m]);
    acc[1][m] = MFMA16(bq[s % (LB + 1)][3], af[t % (PD + 1)][1], acc[1][m]); PIN(acc[1][m]);
    if constexpr (c >= 0) *(u32x4*)(smem + ((c >> 1) % K5_R) * 16384 + wid * 2048 + (c & 1) * 1024 + lane * 16) = wr[c >> 1][c & 1];
    if constexpr (t == 63 && 4 * LB < 17) { _Pragma("unroll") for (int i = 4 * LB; i < 17; ++i) wq[i] = proj_ld<true>(rs, i, wid, lane); }
    SCHED();
  });
  TS(24);
  f32x4* exw = (f32x4*)(smem + 32768 + wid * 4096) + lane;
  const f32x4* exr = (const f32x4*)(smem + 32768 + (wid ^ 4) * 4096) + lane;
  u32x4 z[2];
#pragma unroll
  for (int i = 0; i < 2; ++i) z[i] = *(const u32x4*)(smem + L4_Z1 + (2 * up + i) * 2048 + kh * 1024 + lane * 16);
#pragma unroll
  for (int i = 0; i < 2; ++i)
#pragma unroll
    for (int j = 0; j < 2; ++j) exw[(i * 2 + j) * 64] = acc[i][2 + j];
  f32x4 zf[2][2];
#pragma unroll
  for (int i = 0; i < 2; ++i)
#pragma unroll
    for (int j = 0; j < 2; ++j) {
      zf[i][j] = (f32x4){bflo(z[i][2 * j]), bfhi(z[i][2 * j]), bflo(z[i][2 * j + 1]), bfhi(z[i][2 * j + 1])};
      acc[i][j] = mul4s(acc[i][j], zf[i][j]); PIN(zf[i][j]); PIN(acc[i][j]);
    }
  BAR();
  f32x4 oth[2][2];
#pragma unroll
  for (int i = 0; i < 2; ++i)
#pragma unroll
    for (int j = 0; j < 2; ++j) oth[i][j] = exr[(i * 2 + j) * 64];
#pragma unroll
  for (int i = 0; i < 2; ++i)
#pragma unroll
    for (int j = 0; j < 2; ++j) {
      const f32x4 v = acc[i][j] + zf[i][j] * oth[i][j];
      stc8(rs, ((g >> 1) * 16 + r) * 16 + (g & 1) * 8 + dead[i], OFF_U + (panel * 4 + 2 * kh + j) * 27648 + (u0 + i) * 512, (u32x2){pack2(v[0], v[1]), pack2(v[2], v[3])});
    }
  TS(25);
  waitv<0>();
  BAR();
  TS(26);
  if (wid == 7) arrive(ws, C_U + panel, lane);
#pragma unroll
  for (int i = 0; i < 17; ++i) proj_park<true>(smem, i, wid, lane, wq[i]);
}

extern "C" __global__ __launch_bounds__(512, 2) OCC2 void k_mega(char* __restrict__ ws, const u16* __restrict__ x, const u16* __restrict__ win,
                                                     const u16* __restrict__ wsp, const u16* __restrict__ bsp, const u16* __restrict__ wout, u16* __restrict__ out) {
  extern __shared__ __attribute__((aligned(16))) char smem[];
  const int wid = __builtin_amdgcn_readfirstlane(threadIdx.x >> 6);
  const rsrc_t rs = __builtin_amdgcn_make_buffer_rsrc(ws, 0, WS_USED, 0x00020000);
  TDECL; TS(0);
  u32x2 res[3];
  phase_qkv(ws, rs, smem, wid TPASS);
  phase_attn(ws, rs, (const char*)wsp, win, wout, smem, wid TPASS);
  u32x4 wr[16 - K5_R][2];
  auto nohook = []() __attribute__((always_inline)) {};
  if (wid < 4) {
    bf16x8 b2[20];
    proj_body<false, 0>(ws, rs, x, out, smem, wid, res, [&]() __attribute__((always_inline)) { win_prefetch(rs, wid, b2); } TPASS);
    win_body<true>(ws, rs, wsp, smem, wid, b2, wr TPASS);
  } else {
    proj_body<false, 1>(ws, rs, x, out, smem, wid, res, nohook TPASS);
    if (wid == 7) win_agent(ws, rs, wsp, smem, wr TPASS);
    else { bf16x8 b2[20]; win_prefetch(rs, wid, b2); win_body<false>(ws, rs, wsp, smem, wid, b2, wr TPASS); }
  }
  phase_sp(ws, rs, bsp, smem, wid, wr TPASS);
  if (wid < 4) proj_body<true, 0>(ws, rs, x, out, smem, wid, res, nohook TPASS);
  else proj_body<true, 1>(ws, rs, x, out, smem, wid, res, nohook TPASS);
  TS(29);
  TDUMP();
  asm volatile("k_mega_code_end:");
}

extern "C" void kernel_launch(void* const* d_in, const int* in_sizes, int n_in, void* d_out, int out_size, void* d_ws, size_t ws_size,
                              hipStream_t stream) {
  const u16* x = (const u16*)d_in[0];
  const u16* wqkv = (const u16*)d_in[1];
  const u16* wo = (const u16*)d_in[2];
  const u16* win = (const u16*)d_in[4];
  const u16* wsp = (const u16*)d_in[5];
  const u16* bsp = (const u16*)d_in[6];
  const u16* wout = (const u16*)d_in[7];
  const u16* cvec = (const u16*)d_in[8];
  const u16* wmod = (const u16*)d_in[9];
  char* ws = (char*)d_ws;
  (void)hipFuncSetAttribute((const void*)k_mega, hipFuncAttributeMaxDynamicSharedMemorySize, 163840);
  k0_prep<<<K0_GRID, 256, 0, stream>>>(x, wqkv, wo, cvec, wmod, ws);
  k_mega<<<256, 512, 163840, stream>>>(ws, x, win, wsp, bsp, wout, (u16*)d_out);
}
```

```cpp
#include <hip/hip_runtime.h>
#include <hip/hip_bf16.h>
#include <stdint.h>
#include <utility>
#define PA 23
#define PB 25

typedef __attribute__((ext_vector_type(8))) short bf16x8;
typedef __attribute__((ext_vector_type(4))) short bf16x4;
typedef __attribute__((ext_vector_type(2))) float f32x2;
typedef __attribute__((ext_vector_type(4))) float f32x4;
typedef __attribute__((ext_vector_type(16))) float f32x16;
typedef __attribute__((ext_vector_type(2))) unsigned u32x2;
typedef __attribute__((ext_vector_type(4))) unsigned u32x4;
typedef __attribute__((ext_vector_type(2))) __bf16 bf2_t;
typedef __attribute__((ext_vector_type(2))) _Float16 h2_t;
typedef unsigned short u16;
typedef unsigned long long u64;
#define LAS __attribute__((address_space(3)))
#define GAS __attribute__((address_space(1)))
#define DI __device__ __forceinline__

constexpr int D_ = 640, NQKV = 1920, F_ = 1728;

constexpr unsigned OFF_CNT = 0;
constexpr unsigned OFF_MOD = 65536;
constexpr unsigned OFF_MODP = OFF_MOD + 16384;
constexpr unsigned OFF_CBP = OFF_MODP + 61440;
constexpr unsigned OFF_XCH = OFF_CBP + 5 * 16384;
constexpr unsigned OFF_ST1 = OFF_XCH + 131072;
constexpr unsigned OFF_XHAT = OFF_ST1 + 262144;
constexpr unsigned OFF_WQKV = OFF_XHAT + 2621440;
constexpr unsigned OFF_WO = OFF_WQKV + 2457600;
constexpr unsigned OFF_WIN = OFF_WO + 819200;
constexpr unsigned OFF_WOUT = OFF_WIN + 2211840;
constexpr unsigned OFF_Q = OFF_WOUT + 1105920;
constexpr unsigned OFF_KV = OFF_Q + 32 * 81920;
constexpr unsigned OFF_O = OFF_KV + 32 * 163840;
constexpr unsigned OFF_X1S = OFF_O + 2621440;
constexpr unsigned OFF_Z2 = OFF_X1S + 2621440;
constexpr unsigned OFF_U = OFF_Z2 + 3538944;
constexpr unsigned WS_USED = OFF_U + 3538944;
constexpr int C_QKV = 0, C_MOD = 15  , C_ATT = 128, C_REL = 136, C_X1 = 144, C_U = 176, C_Z2 = 208  ;

#define STAMP(kid, k)
#define TDECL u64 tsv_ = 0
#define TS(k) do { if ((k) == PA) tsv_ -= __builtin_amdgcn_s_memrealtime(); if ((k) == PB) tsv_ += __builtin_amdgcn_s_memrealtime(); } while (0)
#define TDUMP() do { if (blockIdx.x == 3 && wid == 4) { if (tsv_ > 200000) tsv_ = 0; const u64 e_ = __builtin_amdgcn_s_memrealtime() + 8 * tsv_; for (int i_ = 0; i_ < 200000; ++i_) { if (__builtin_amdgcn_s_memrealtime() >= e_) break; __builtin_amdgcn_s_sleep(8); } } } while (0)
DI unsigned pack2(float lo, float hi) { return __builtin_bit_cast(unsigned, __builtin_convertvector((f32x2){lo, hi}, bf2_t)); }
DI float opq(float x) { asm("" : "+v"(x)); return x; }
DI f32x4 fma4s(f32x4 a, f32x4 b, f32x4 c) { return (f32x4){opq(__builtin_fmaf(a[0], b[0], c[0])), opq(__builtin_fmaf(a[1], b[1], c[1])), opq(__builtin_fmaf(a[2], b[2], c[2])), opq(__builtin_fmaf(a[3], b[3], c[3]))}; }
DI f32x4 mul4s(f32x4 a, f32x4 b) { return (f32x4){opq(a[0] * b[0]), opq(a[1] * b[1]), opq(a[2] * b[2]), opq(a[3] * b[3])}; }
DI unsigned pack2h(float lo, float hi) { return __builtin_bit_cast(unsigned, __builtin_convertvector((f32x2){lo, hi}, h2_t)); }
DI float bflo(unsigned u) { return __uint_as_float(u << 16); }
DI float bfhi(unsigned u) { return __uint_as_float(u & 0xffff0000u); }
DI float rbf(float x) { return bflo(pack2(x, x)); }
DI float bf2f(u16 b) { return __uint_as_float((unsigned)b << 16); }
#define MFMA32(a, b, c) __builtin_amdgcn_mfma_f32_32x32x16_bf16((a), (b), (c), 0, 0, 0)
#define MFMA32K8(a, b, c) __builtin_amdgcn_mfma_f32_32x32x8bf16_1k((a), (b), (c), 0, 0, 0)
#define MFMA16(a, b, c) __builtin_amdgcn_mfma_f32_16x16x32_bf16((a), (b), (c), 0, 0, 0)
#define BAR() asm volatile("s_waitcnt lgkmcnt(0)\n\ts_barrier" ::: "memory")
#define SCHED() __builtin_amdgcn_sched_barrier(0)
#define PIN(x) asm volatile("" : "+v"(x))
#define OCC2 __attribute__((amdgpu_waves_per_eu(2, 2)))
#define BF8(x) __builtin_bit_cast(bf16x8, x)
#define BF4(x) __builtin_bit_cast(bf16x4, x)
template <int N> DI void waitv() { asm volatile("s_waitcnt vmcnt(%0)" ::"i"(N) : "memory"); }
template <class F, int... I> DI void sfor_impl(F&& f, std::integer_sequence<int, I...>) { (f(std::integral_constant<int, I>{}), ...); }
template <int N, class F> DI void sfor(F&& f) { sfor_impl(f, std::make_integer_sequence<int, N>{}); }
typedef short v4i16_t __attribute__((ext_vector_type(4)));
DI bf16x4 lds_tr(const char* p) { return __builtin_bit_cast(bf16x4, __builtin_amdgcn_ds_read_tr16_b64_v4i16((LAS v4i16_t*)p)); }
DI void glds16(const void* g, unsigned lds_base) {
  unsigned sv;
  asm volatile("s_mov_b32 %0, m0\n\ts_mov_b32 m0, %2\n\ts_nop 0\n\tglobal_load_lds_dwordx4 %1, off\n\ts_mov_b32 m0, %0" : "=&s"(sv) : "v"(g), "s"(lds_base) : "memory");
}
DI void glds16c(const void* g, unsigned lds_base) {
  unsigned sv;
  asm volatile("s_mov_b32 %0, m0\n\ts_mov_b32 m0, %2\n\ts_nop 0\n\tglobal_load_lds_dwordx4 %1, off sc1\n\ts_mov_b32 m0, %0" : "=&s"(sv) : "v"(g), "s"(lds_base) : "memory");
}
DI void glds4c(const void* g, unsigned lds_base) {
  unsigned sv;
  asm volatile("s_mov_b32 %0, m0\n\ts_mov_b32 m0, %2\n\ts_nop 0\n\tglobal_load_lds_dword %1, off sc1\n\ts_mov_b32 m0, %0" : "=&s"(sv) : "v"(g), "s"(lds_base) : "memory");
}
#define PSLEEP 1
typedef __amdgpu_buffer_rsrc_t rsrc_t;
DI u32x4 ldc16(rsrc_t rs, unsigned voff, unsigned soff) { return __builtin_amdgcn_raw_buffer_load_b128(rs, voff, soff, 16); }
DI unsigned ldc4(rsrc_t rs, unsigned voff, unsigned soff) { return __builtin_amdgcn_raw_buffer_load_b32(rs, voff, soff, 16); }
DI void stc16(rsrc_t rs, unsigned voff, unsigned soff, u32x4 v) { __builtin_amdgcn_raw_buffer_store_b128(v, rs, voff, soff, 16); asm volatile("s_nop 1" ::"v"(v) : "memory"); }
DI void stc8(rsrc_t rs, unsigned voff, unsigned soff, u32x2 v) { __builtin_amdgcn_raw_buffer_store_b64(v, rs, voff, soff, 16); }
DI void stc4(rsrc_t rs, unsigned voff, unsigned soff, unsigned v) { __builtin_amdgcn_raw_buffer_store_b32(v, rs, voff, soff, 16); }
DI f32x4 ldc16f(rsrc_t rs, unsigned voff, unsigned soff) { return __builtin_bit_cast(f32x4, ldc16(rs, voff, soff)); }
DI void arrive(char* ws, int c, int lane) { if (lane == 0) __hip_atomic_fetch_add((GAS unsigned*)(ws + OFF_CNT + c * 256), 1u, __ATOMIC_RELAXED, __HIP_MEMORY_SCOPE_AGENT); }
DI void poll_ge(char* ws, int c, unsigned thr) {
  const GAS unsigned* p = (const GAS unsigned*)(ws + OFF_CNT + c * 256);
#pragma nounroll
  for (unsigned spin = 0; spin < (1u << 19); ++spin) {
    if (__all(__hip_atomic_load(p, __ATOMIC_RELAXED, __HIP_MEMORY_SCOPE_AGENT) >= thr)) break;
    __builtin_amdgcn_s_sleep(PSLEEP);
  }
  asm volatile("" ::: "memory");
}
DI int fresh_tid() { int t = threadIdx.x; asm volatile("" : "+v"(t)); return t; }
DI void l2touch(const void* g, unsigned lds_scratch) {
  unsigned sv;
  asm volatile("s_mov_b32 %0, m0\n\ts_mov_b32 m0, %2\n\ts_nop 0\n\tglobal_load_lds_dword %1, off\n\ts_mov_b32 m0, %0" : "=&s"(sv) : "v"(g), "s"(lds_scratch) : "memory");
}
#define LDNT(p) __builtin_nontemporal_load(p)
DI float silu_bf(u16 c) { const float f = __uint_as_float((unsigned)c << 16); const float v = f / (1.f + __expf(-f)); return __uint_as_float(__builtin_bit_cast(unsigned, __builtin_convertvector((f32x2){v, v}, bf2_t)) << 16); }
DI unsigned lds_addr(const void* p) { return (unsigned)(uintptr_t)(const LAS void*)p; }
DI float xhalf(float v) {
  auto rr = __builtin_amdgcn_permlane32_swap(__float_as_uint(v), __float_as_uint(v), false, false);
  return __uint_as_float((threadIdx.x & 32) ? rr[0] : rr[1]);
}
DI float sum_g(float v) {
  auto a = __builtin_amdgcn_permlane16_swap(__float_as_uint(v), __float_as_uint(v), false, false);
  v = __uint_as_float(a[0]) + __uint_as_float(a[1]);
  auto b = __builtin_amdgcn_permlane32_swap(__float_as_uint(v), __float_as_uint(v), false, false);
  return __uint_as_float(b[0]) + __uint_as_float(b[1]);
}
template <int CTRL> DI float dpp_add(float v) { return v + __uint_as_float(__builtin_amdgcn_update_dpp(0u, __float_as_uint(v), CTRL, 0xf, 0xf, true)); }
DI float sum16(float v) {
  v = dpp_add<0x128>(v); v = dpp_add<0x124>(v); v = dpp_add<0x122>(v); return dpp_add<0x121>(v);
}
DI u32x4 modulate8(u32x4 v, f32x4 sc0, f32x4 sc1, f32x4 sh0, f32x4 sh1) {
  unsigned o0, o1, o2, o3; float t0, t1, t2, t3;
  asm volatile(
      "v_fma_mix_f32 %0, %8, %12, %20 op_sel_hi:[1,0,0]\n\tv_fma_mix_f32 %4, %8, %13, %21 op_sel:[1,0,0] op_sel_hi:[1,0,0]\n\t"
      "v_fma_mix_f32 %1, %9, %14, %22 op_sel_hi:[1,0,0]\n\tv_fma_mix_f32 %5, %9, %15, %23 op_sel:[1,0,0] op_sel_hi:[1,0,0]\n\t"
      "v_fma_mix_f32 %2, %10, %16, %24 op_sel_hi:[1,0,0]\n\tv_fma_mix_f32 %6, %10, %17, %25 op_sel:[1,0,0] op_sel_hi:[1,0,0]\n\t"
      "v_fma_mix_f32 %3, %11, %18, %26 op_sel_hi:[1,0,0]\n\tv_fma_mix_f32 %7, %11, %19, %27 op_sel:[1,0,0] op_sel_hi:[1,0,0]\n\t"
      "v_cvt_pk_bf16_f32 %0, %0, %4\n\tv_cvt_pk_bf16_f32 %1, %1, %5\n\tv_cvt_pk_bf16_f32 %2, %2, %6\n\tv_cvt_pk_bf16_f32 %3, %3, %7"
      : "=&v"(o0), "=&v"(o1), "=&v"(o2), "=&v"(o3), "=&v"(t0), "=&v"(t1), "=&v"(t2), "=&v"(t3)
      : "v"(v[0]), "v"(v[1]), "v"(v[2]), "v"(v[3]), "v"(sc0[0]), "v"(sc0[1]), "v"(sc0[2]), "v"(sc0[3]), "v"(sc1[0]), "v"(sc1[1]), "v"(sc1[2]), "v"(sc1[3]),
        "v"(sh0[0]), "v"(sh0[1]), "v"(sh0[2]), "v"(sh0[3]), "v"(sh1[0]), "v"(sh1[1]), "v"(sh1[2]), "v"(sh1[3]));
  return (u32x4){o0, o1, o2, o3};
}
DI float ln_den(float var) { return rbf(__builtin_sqrtf(rbf(rbf(var) + 1e-5f))); }
DI int unit0_of(int j) { return j < 6 ? 7 * j : 42 + 6 * (j - 6); }

DI void K0ST(char* p, u32x4 v) { *(u32x4*)p = v; }
constexpr int K0_MOD = 240, K0_LN = 128, K0_RL = 240, K0_WO = 50, K0_INIT = 12, K0_WARM = 24;
constexpr int K0_GRID = K0_WARM + K0_MOD + K0_LN + K0_RL + K0_WO + K0_INIT;

__global__ __launch_bounds__(256) void k0_prep(const u16* __restrict__ x, const u16* __restrict__ wqkv, const u16* __restrict__ wo, const u16* __restrict__ cvec,
                                               const u16* __restrict__ wmod, char* __restrict__ ws) {
  __shared__ __attribute__((aligned(16))) char smem[4 * 4608];
  const int tid = threadIdx.x, lane = tid & 63, wid = __builtin_amdgcn_readfirstlane(tid >> 6);
  int b = blockIdx.x;
  STAMP(0, 0);
  if (b < K0_WARM) {
    u64 pc; unsigned len;
    asm volatile("s_getpc_b64 s[30:31]\n\ts_add_u32 s30, s30, k_mega@rel32@lo+4\n\ts_addc_u32 s31, s31, k_mega@rel32@hi+12\n\ts_mov_b64 %0, s[30:31]\n\t"
                 "s_mov_b32 %1, k_mega_code_end-k_mega" : "=s"(pc), "=s"(len) :: "scc", "s30", "s31");
    l2touch((const char*)pc + min(((b >> 3) * 256 + tid) * 128u, len - 4u), lds_addr(smem) + wid * 256);
    return;
  }
  b -= K0_WARM;
  if (b < K0_MOD) {
    const int cg = b >> 2, ks = b & 3, c = tid & 7, row0 = ks * 160 + (tid >> 3), col0 = cg * 64;
    u32x4 w[5]; u16 cv[5];
#pragma unroll
    for (int i = 0; i < 5; ++i) { w[i] = LDNT((const u32x4*)(wmod + (size_t)(row0 + 32 * i) * 3840 + col0 + c * 8)); cv[i] = cvec[row0 + 32 * i]; }
    float acc[8] = {0, 0, 0, 0, 0, 0, 0, 0};
#pragma unroll
    for (int i = 0; i < 5; ++i) {
      const float s = silu_bf(cv[i]);
#pragma unroll
      for (int e = 0; e < 4; ++e) { acc[2 * e] += s * bflo(w[i][e]); acc[2 * e + 1] += s * bfhi(w[i][e]); }
    }
#pragma unroll
    for (int e = 0; e < 8; ++e) acc[e] = sum_g(dpp_add<0x128>(acc[e]));
    float* red = (float*)smem;
    if (lane < 8) { *(f32x4*)(red + wid * 64 + lane * 8) = (f32x4){acc[0], acc[1], acc[2], acc[3]}; *(f32x4*)(red + wid * 64 + lane * 8 + 4) = (f32x4){acc[4], acc[5], acc[6], acc[7]}; }
    __syncthreads();
    if (tid < 64) ((float*)(ws + OFF_MODP))[ks * 3840 + cg * 64 + tid] = (red[tid] + red[64 + tid]) + (red[128 + tid] + red[192 + tid]);
    STAMP(0, 1); STAMP(0, 13);
    return;
  }
  b -= K0_MOD;
  if (b < K0_LN) {
    b = (b & 7) * 16 + (b >> 3);
    const int row = b * 16 + wid * 4 + (lane >> 4), c = lane & 15;
    const u16* xr = x + (size_t)row * D_ + c * 8;
    u32x4 v[5];
#pragma unroll
    for (int i = 0; i < 5; ++i) v[i] = *(const u32x4*)(xr + i * 128);
    float s = 0.f, q = 0.f;
#pragma unroll
    for (int i = 0; i < 5; ++i)
#pragma unroll
      for (int e = 0; e < 4; ++e) s += bflo(v[i][e]) + bfhi(v[i][e]);
    const float mean = sum16(s) * (1.f / 640.f);
#pragma unroll
    for (int i = 0; i < 5; ++i)
#pragma unroll
      for (int e = 0; e < 4; ++e) { const float a = bflo(v[i][e]) - mean, d = bfhi(v[i][e]) - mean; q += a * a + d * d; }
    const float var = sum16(q) * (1.f / 640.f);
    const float rinv = 1.f / ln_den(var), mb = rbf(mean);
    char* dst = ws + OFF_XHAT + (size_t)row * 1280 + c * 16;
#pragma unroll
    for (int i = 0; i < 5; ++i) {
      u32x4 o;
#pragma unroll
      for (int e = 0; e < 4; ++e) o[e] = pack2h((bflo(v[i][e]) - mb) * rinv, (bfhi(v[i][e]) - mb) * rinv);
      K0ST(dst + i * 256, o);
    }
    STAMP(0, 2); STAMP(0, 13);
    return;
  }
  b -= K0_LN;
  if (b < K0_RL) {
    const int kt = b / 12, G = b % 12, sec = G >> 2, gq = G & 3;
    u32x4 v[3];
#pragma unroll
    for (int i = 0; i < 3; ++i) { const int ci = min(i * 256 + tid, 639); v[i] = LDNT((const u32x4*)(wqkv + (size_t)(kt * 32 + ci / 20) * NQKV + G * 160 + (ci % 20) * 8)); }
#pragma unroll
    for (int i = 0; i < 3; ++i) { const int ci = min(i * 256 + tid, 639); *(u32x4*)(smem + (ci / 20) * 336 + (ci % 20) * 16) = v[i]; }
    __syncthreads();
    const int g = lane >> 4, q = (lane & 15) >> 2, p = lane & 3;
    for (int f = wid; f < 10; f += 4) {
      const int t = f >> 1, x2 = f & 1;
      const int col = sec == 2 ? 16 * f + 4 * p : t < 4 ? 20 * (2 * t + (p >> 1)) + 8 * (p & 1) + 4 * x2 : 20 * (4 * x2 + p) + 16;
      const int T = sec == 2 ? 40 + gq * 5 + t : sec * 20 + (t < 4 ? 4 * gq + t : 16 + gq);
      const char* pp = smem + (8 * g + q) * 336 + col * 2;
      const bf16x4 lo = lds_tr(pp), hi = lds_tr(pp + 4 * 336);
      K0ST(ws + OFF_WQKV + (((size_t)T * 20 + kt) * 2 + x2) * 1024 + lane * 16, __builtin_bit_cast(u32x4, (bf16x8)__builtin_shufflevector(lo, hi, 0, 1, 2, 3, 4, 5, 6, 7)));
    }
    STAMP(0, 3); STAMP(0, 13);
    return;
  }
  b -= K0_RL;
  if (b < K0_WO) {
    const int job = b * 4 + wid, kt = job / 10, ng = job % 10, j8 = lane >> 3;
    char* t = smem + wid * 4608;
    u32x4 v[4];
#pragma unroll
    for (int i = 0; i < 4; ++i) {
      const int c = 4 * kt + i, row = c < 64 ? 20 * (c >> 1) + 4 * (c & 1) + (j8 & 3) + 8 * (j8 >> 2) : 20 * (2 * (c - 64) + (j8 >> 2)) + 16 + (j8 & 3);
      v[i] = LDNT((const u32x4*)(wo + (size_t)row * D_ + ng * 64 + (lane & 7) * 8));
    }
#pragma unroll
    for (int i = 0; i < 4; ++i) *(u32x4*)(t + (i * 8 + j8) * 144 + (lane & 7) * 16) = v[i];
    const char* pp = t + (8 * (lane >> 4) + ((lane & 15) >> 2)) * 144 + (lane & 3) * 8;
#pragma unroll
    for (int f = 0; f < 4; ++f) {
      const bf16x4 lo = lds_tr(pp + f * 32), hi = lds_tr(pp + 4 * 144 + f * 32);
      K0ST(ws + OFF_WO + ((size_t)(ng * 4 + f) * 20 + kt) * 1024 + lane * 16, __builtin_bit_cast(u32x4, (bf16x8)__builtin_shufflevector(lo, hi, 0, 1, 2, 3, 4, 5, 6, 7)));
    }
    return;
  }
  b -= K0_WO;
  {
    u32x4* p = (u32x4*)(ws + (b < 8 ? OFF_XCH + b * 16384 : OFF_CNT + (b - 8) * 16384)) + tid;
    const unsigned f = b < 8 ? ~0u : 0u;
#pragma unroll
    for (int i = 0; i < 4; ++i) p[i * 256] = (u32x4){f, f, f, f};
  }
}

constexpr float QSCALE = 0.22360679774997896f * 1.4426950408889634f;
#define K1W 3
#define K1WB 5
#define K1WB0 2
constexpr int K1_W = K1W, K1_WB = K1WB, K1_WB0 = K1WB0;

template <bool ISV, class FIN>
DI void k1_mma(const char* __restrict__ bsrc, const char* smem, int lane, FIN&& fin) {
  f32x4 acc[8][2];
  constexpr int PD = 4;
  bf16x8 bfr[K1_WB + 1][4], af[PD + 1][2];
  const char* lds[2];
#pragma unroll
  for (int k = 0; k < 2; ++k) lds[k] = smem + k * 8192 + (lane ^ (4 * k + (lane >> 4))) * 16;
#pragma unroll
  for (int i = 0; i < 4 * K1_WB0; ++i) bfr[i >> 2][i & 3] = *(const bf16x8*)(bsrc + i * 1024);
#pragma unroll
  for (int m = 0; m < 8; ++m) { acc[m][0] = (f32x4){}; acc[m][1] = (f32x4){}; }
  BAR();
#pragma unroll
  for (int i = 4 * K1_WB0; i < 4 * K1_WB; ++i) bfr[i >> 2][i & 3] = *(const bf16x8*)(bsrc + i * 1024);
  BAR();
  __builtin_amdgcn_s_setprio(1);
#define K1_RD(t, k) (af[(t) % (PD + 1)][k] = *(const bf16x8*)(lds[k] + (((t) >> 3) % 3) * 16384 + ((t) & 7) * 1024))
#define K1_MM(c, b, a) (c = ISV ? MFMA16(a, b, c) : MFMA16(b, a, c))
#pragma unroll
  for (int t = 0; t < PD; ++t) { K1_RD(t, 0); K1_RD(t, 1); }
  SCHED();
  sfor<80>([&](auto T) __attribute__((always_inline)) {
    constexpr int t = T, s = t >> 3, m = t & 7, nx = t + PD;
    if constexpr (nx < 80 && (nx & 7) == 0) asm volatile("s_barrier" ::: "memory");
    if constexpr (nx < 80) K1_RD(nx, 0);
    K1_MM(acc[m][0], bfr[s % (K1_WB + 1)][0], af[t % (PD + 1)][0]); K1_MM(acc[m][0], bfr[s % (K1_WB + 1)][2], af[t % (PD + 1)][1]); PIN(acc[m][0]);
    SCHED();
    if constexpr (nx < 80) K1_RD(nx, 1);
    if constexpr (s + K1_WB < 10 && (m & 1)) bfr[(s + K1_WB) % (K1_WB + 1)][m >> 1] = *(const bf16x8*)(bsrc + ((s + K1_WB) * 4 + (m >> 1)) * 1024);
    K1_MM(acc[m][1], bfr[s % (K1_WB + 1)][1], af[t % (PD + 1)][0]); K1_MM(acc[m][1], bfr[s % (K1_WB + 1)][3], af[t % (PD + 1)][1]); PIN(acc[m][1]);
    SCHED();
    if constexpr (t >= 74) { fin(t - 74, acc[t - 74]); SCHED(); }
  });
  fin(6, acc[6]); fin(7, acc[7]);
  __builtin_amdgcn_s_setprio(0);
}

#define TARG , u64& tsv_
#define TPASS , tsv_

DI void phase_qkv(char* __restrict__ ws, rsrc_t rs, char* smem, int wid TARG) {
  const int tid = fresh_tid(), lane = tid & 63;
  const int xcd = blockIdx.x & 7, idx = blockIdx.x >> 3;
  int ctr;
  if ((xcd & 1) && idx >= 28) {
    ctr = C_MOD;
    if (tid < 160) {
      const int col = 1280 + ((xcd >> 1) * 4 + idx - 28) * 160 + tid;
      const float* pp = (const float*)(ws + OFF_MODP) + col;
      float v = rbf((pp[0] + pp[3840]) + (pp[7680] + pp[11520]));
      if (col / 640 == 4) v = rbf(1.f + v);
      stc4(rs, col * 4, OFF_MOD, __float_as_uint(v));
    }
  } else {
    const int mt = (xcd >> 1) * 4 + (idx & 3), nt = (xcd & 1) * 8 + (idx >> 2);
    ctr = C_QKV + nt;
    if (wid < 4) {
      float* cst = (float*)(smem + 49152);
      const int c = lane & 7, rr = lane >> 3;
      const char* src = ws + OFF_XHAT + (size_t)(mt * 128 + wid * 32 + rr) * 1280 + c * 16;
      char* wdst = smem + (c >> 2) * 8192 + wid * 2048 + (c & 3) * 256 + (rr ^ c) * 16;
      u32x4 a[10][4];
      f32x4 c0[4], c1[4];
#pragma unroll
      for (int i = 0; i < 4; ++i) { c0[i] = *(const f32x4*)(ws + OFF_MODP + i * 15360 + tid * 16); if (wid == 0) c1[i] = *(const f32x4*)(ws + OFF_MODP + i * 15360 + 4096 + tid * 16); }
#pragma unroll
      for (int s = 0; s < K1_W; ++s)
#pragma unroll
        for (int m = 0; m < 4; ++m) a[s][m] = *(const u32x4*)(src + m * 10240 + s * 128);
      {
        f32x4 v = (c0[0] + c0[1]) + (c0[2] + c0[3]); const float one = tid >= 160 ? 1.f : 0.f;
        *(f32x4*)(cst + tid * 4) = (f32x4){rbf(one + rbf(v[0])), rbf(one + rbf(v[1])), rbf(one + rbf(v[2])), rbf(one + rbf(v[3]))};
        if (wid == 0) { v = (c1[0] + c1[1]) + (c1[2] + c1[3]); *(f32x4*)(cst + 1024 + tid * 4) = (f32x4){rbf(1.f + rbf(v[0])), rbf(1.f + rbf(v[1])), rbf(1.f + rbf(v[2])), rbf(1.f + rbf(v[3]))}; }
      }
      BAR();
      const float* cp = cst + c * 8;
      f32x4 kc[10][4];
#define K1_KC(s) do { kc[s][0] = *(const f32x4*)(cp + (s) * 64); kc[s][1] = *(const f32x4*)(cp + (s) * 64 + 4); kc[s][2] = *(const f32x4*)(cp + (s) * 64 + 640); kc[s][3] = *(const f32x4*)(cp + (s) * 64 + 644); } while (0)
#pragma unroll
      for (int s = 0; s < 6; ++s) K1_KC(s);
      SCHED();
      u32x4 o[2][4];
#define K1_XF(s, m) (o[(s) & 1][m] = modulate8(a[s][m], kc[s][2], kc[s][3], kc[s][0], kc[s][1]))
#define K1_WR(s, m) (*(u32x4*)(wdst + ((s) % 3) * 16384 + ((m) >> 1) * 1024 + ((m) & 1) * 128) = o[(s) & 1][m])
#pragma unroll
      for (int m = 0; m < 4; ++m) { K1_XF(0, m); K1_WR(0, m); }
      BAR();
      TS(1);
#pragma unroll
      for (int m = 0; m < 4; ++m) a[K1_W][m] = *(const u32x4*)(src + m * 10240 + K1_W * 128);
#pragma unroll
      for (int m = 0; m < 4; ++m) K1_XF(1, m);
      SCHED();
#pragma unroll
      for (int s = 1; s < 10; ++s) {
#pragma unroll
        for (int m = 0; m < 4; ++m) {
          if (m == 0) K1_WR(s, 0);
          if (m < 3) K1_WR(s, m + 1);
          if (s + K1_W < 10) a[s + K1_W][m] = *(const u32x4*)(src + m * 10240 + (s + K1_W) * 128);
          if (s < 5) kc[s + 5][m] = *(const f32x4*)(cp + (s + 5) * 64 + (m & 1) * 4 + (m >> 1) * 640);
          SCHED();
          if (s < 9) K1_XF(s + 1, m);
          SCHED();
        }
        asm volatile("" :: "v"(o[s & 1][0]), "v"(o[s & 1][1]), "v"(o[s & 1][2]), "v"(o[s & 1][3]));
        BAR();
      }
    } else {
      const int cw = wid - 4;
      const char* bsrc = ws + OFF_WQKV + (size_t)(nt * 4 + cw) * 40 * 1024 + lane * 16;
      const int r = lane & 15, g = lane >> 4;
      if (nt >= 10) {
        const unsigned so = OFF_KV + (mt >> 1) * 20480 + 10240 + (mt & 1) * 5120;
        unsigned vo[2];
#pragma unroll
        for (int x = 0; x < 2; ++x) { const unsigned c = (nt - 10) * 128 + cw * 32 + x * 16 + r; vo[x] = c / 20 * 163840u + ((g & 1) * 20 + c % 20) * 16 + (g >> 1) * 8; }
        k1_mma<true>(bsrc, smem, lane, [&](int m, const f32x4 (&a)[2]) __attribute__((always_inline)) {
#pragma unroll
          for (int x = 0; x < 2; ++x) stc8(rs, vo[x] + (m & 3) * 640, so + (m >> 2) * 2560, (u32x2){pack2(a[x][0], a[x][1]), pack2(a[x][2], a[x][3])});
        });
      } else {
        const bool isQ = nt < 5; const float sc = isQ ? QSCALE : 1.f;
        const unsigned so = isQ ? OFF_Q + mt * 5120 : OFF_KV + (mt >> 1) * 20480 + (mt & 1) * 5120, hs = isQ ? 81920u : 163840u;
        const int n5 = isQ ? nt : nt - 5;
        const bool wide = n5 < 4;
        const unsigned vo = wide ? (2 * (n5 * 4 + cw) + (g >> 1)) * hs + (g & 1) * 512 + r * 16 : (8 * cw + g) * hs + 1024 + r * 8;
        const f32x4 sc4 = (f32x4){sc, sc, sc, sc};
        k1_mma<false>(bsrc, smem, lane, [&](int m, const f32x4 (&a)[2]) __attribute__((always_inline)) {
          const f32x4 v0 = mul4s(a[0], sc4), v1 = mul4s(a[1], sc4);
          const u32x4 w = (u32x4){pack2(v0[0], v0[1]), pack2(v0[2], v0[3]), pack2(v1[0], v1[1]), pack2(v1[2], v1[3])};
          if (wide) stc16(rs, vo + (m & 1) * 256, so + (m >> 1) * 1280, w);
          else { stc8(rs, vo + (m & 1) * 128, so + (m >> 1) * 1280, (u32x2){w[0], w[1]}); stc8(rs, vo + (m & 1) * 128, so + 4 * hs + (m >> 1) * 1280, (u32x2){w[2], w[3]}); }
        });
      }
    }
  }
  TS(3);
  waitv<0>();
  BAR();
  TS(4);
  if (wid == 7 && lane < 8) __hip_atomic_fetch_add((GAS unsigned*)(ws + OFF_CNT + (lane * 16 + ctr) * 256), 1u, __ATOMIC_RELAXED, __HIP_MEMORY_SCOPE_AGENT);
  if (ctr == C_MOD && wid == 7) { __builtin_amdgcn_s_sleep(127); __builtin_amdgcn_s_sleep(60); }
}

template <bool FINAL> DI constexpr int proj_pos(int f) { return FINAL ? (f < 96 ? 65536 + f * 1024 : (f - 96) * 1024) : 17408 + (f % 20 >= 10 ? 81920 : 0) + (f / 20 * 10 + f % 10) * 1024; }
template <bool FINAL> DI u32x4 proj_ld(rsrc_t rs, int i, int wid, int lane) {
  constexpr int NF = FINAL ? 135 : 100;
  return ldc16(rs, lane * 16, (FINAL ? OFF_WOUT : OFF_WO) + (((blockIdx.x >> 3) & 7) * NF + min(i * 8 + wid, NF - 1)) * 1024);
}
template <bool FINAL> DI void proj_park(char* smem, int i, int wid, int lane, u32x4 v) {
  constexpr int NF = FINAL ? 135 : 100;
  *(u32x4*)(smem + proj_pos<FINAL>(min(i * 8 + wid, NF - 1)) + lane * 16) = v;
}

DI void split3(float m, unsigned& w_h0, unsigned& w_h1) {
  const float n = -m; const float a = rbf(n), b = rbf(n - a), c = rbf(n - a - b);
  w_h0 = pack2(a, b); w_h1 = pack2(c, 0.f);
}

DI void phase_attn(char* __restrict__ ws, rsrc_t rs, const char* __restrict__ wsp, const u16* __restrict__ win, const u16* __restrict__ wout, char* smem, int wid TARG) {
  const int tid = fresh_tid(), lane = tid & 63;
  const int xcd = blockIdx.x & 7, idx = blockIdx.x >> 3, head = xcd * 4 + (idx >> 3), qb = idx & 7;
  const int r = lane & 31, h = lane >> 5, qg = wid & 3, kh = wid >> 2;
  if (wid == 7) {
    const int l7 = lane & 7;
    const int c = l7 == 0 ? head >> 3 : l7 == 1 ? 4 : l7 == 2 ? 5 + (head >> 3) : l7 == 3 ? 9 : l7 == 4 ? 10 + (20 * head) / 128 : l7 == 5 ? 10 + (20 * head + 19) / 128 : C_MOD;
    poll_ge(ws, xcd * 16 + c, 16);
  }
  BAR();
  TS(5);
  unsigned kvo = OFF_KV + head * 163840, qo = OFF_Q + head * 81920 + (qb * 8 + qg * 2) * 1280;
  const unsigned kconst = h == 0 ? 0x3f803f80u : 0x00003f80u;
  const unsigned so16 = (tid & 255) * 16, l16 = lane * 16, l2 = 1024 + r * 8 + h * 4;
  const int blk = blockIdx.x;
  u32x4 wq[13];
  for (int attempt = 0; attempt < 2; ++attempt) {
    asm volatile("" : "+s"(kvo), "+s"(qo));
    const char* kvs = ws + kvo + kh * 81920 + so16;
    const unsigned ldst = lds_addr(smem) + kh * 81920 + qg * 1024;
    u32x4 qA1w, qB1w, k1w; unsigned qA2w, qB2w, k2, m0sv;
    asm volatile(
        "s_nop 4\n\tbuffer_load_dwordx4 %0, %7, %9, %19 offen sc1\n\tbuffer_load_dwordx4 %1, %7, %9, %10 offen sc1\n\tbuffer_load_dwordx4 %2, %7, %9, %11 offen sc1\n\t"
        "buffer_load_dword %3, %8, %9, %19 offen sc1\n\tbuffer_load_dword %4, %8, %9, %10 offen sc1\n\tbuffer_load_dword %5, %8, %9, %11 offen sc1\n\t"
        "s_mov_b32 %6, m0\n\ts_mov_b32 m0, %17\n\ts_nop 0\n\tglobal_load_lds_dwordx4 %12, off sc1\n\t"
        "s_add_u32 m0, %18, 0x1000\n\ts_nop 0\n\tglobal_load_lds_dwordx4 %13, off sc1\n\t"
        "s_add_u32 m0, %18, 0x2000\n\ts_nop 0\n\tglobal_load_lds_dwordx4 %14, off sc1\n\t"
        "s_add_u32 m0, %18, 0x3000\n\ts_nop 0\n\tglobal_load_lds_dwordx4 %15, off sc1\n\t"
        "s_add_u32 m0, %18, 0x4000\n\ts_nop 0\n\tglobal_load_lds_dwordx4 %16, off sc1\n\t"
        "s_mov_b32 m0, %6\n\ts_waitcnt vmcnt(5)"
        : "=&v"(qA1w), "=&v"(qB1w), "=&v"(k1w), "=&v"(qA2w), "=&v"(qB2w), "=&v"(k2), "=&s"(m0sv)
        : "v"(l16), "v"(l2), "s"(rs), "s"(qo + kh * 1280), "s"(kvo), "v"(kvs + (wid == 0 ? 4096 : 0)), "v"(kvs + 4096), "v"(kvs + 8192), "v"(kvs + 12288), "v"(kvs + 16384),
          "s"(ldst + (wid == 0 ? 4096 : 0)), "s"(ldst), "s"(qo + (kh ^ 1) * 1280)
        : "memory", "scc");
    if (wid == 0) { const unsigned f = (tid & 1) ? 0u : 0x3f803f80u; *(u32x4*)(smem + l16) = (u32x4){f, f, f, f}; }
    const bf16x8 qA1 = BF8(qA1w), qB1 = BF8(qB1w);
    bf16x8 k1 = BF8(k1w);
    TS(30);
    float mA = -INFINITY, mB = -INFINITY;
    {
      const bf16x4 qA2 = BF4(((u32x2){qA2w, 0u})), qB2 = BF4(((u32x2){qB2w, 0u}));
      const int nt = attempt ? 64 : 1;
      bf16x8 t1 = k1; unsigned t2 = k2;
      for (int t = 0; t < nt; ++t) {
        if (t) { const unsigned to = kvo + (t >> 3) * 20480 + (t & 7) * 1280; t1 = BF8(ldc16(rs, l16, to)); t2 = ldc4(rs, l2, to); }
        const bf16x4 t2v = BF4(((u32x2){t2, kconst}));
        f32x16 a = MFMA32(t1, qA1, (f32x16){}); a = MFMA32K8(t2v, qA2, a);
        f32x16 b = MFMA32(t1, qB1, (f32x16){}); b = MFMA32K8(t2v, qB2, b);
#pragma unroll
        for (int i = 0; i < 16; ++i) { mA = fmaxf(mA, a[i]); mB = fmaxf(mB, b[i]); }
      }
      mA = fmaxf(mA, xhalf(mA)); mB = fmaxf(mB, xhalf(mB));
    }
    unsigned a0, a1, b0, b1; split3(mA, a0, a1); split3(mB, b0, b1);
    const bf16x4 qA2 = BF4(((u32x2){qA2w, h ? a1 : a0})), qB2 = BF4(((u32x2){qB2w, h ? b1 : b0}));
    char* lh = smem + kh * 81920;
    TS(31);
    waitv<0>();
    BAR();
    TS(6);
    f32x16 oA = (f32x16){}, oB = (f32x16){};
    const char* kp1 = lh + l16;
    const char* kp2 = lh + l2;
    const bool vreal = r < 20;
    const char* vp = vreal ? lh + 10240 + (h * 20 + r) * 16 : smem + (r == 20 ? 0 : 16);
    const int vinc = vreal ? 1280 : 0, vjump = vreal ? 10240 : 0;
    if (kh) { k1 = *(const bf16x8*)kp1; k2 = *(const unsigned*)kp2; }
    bf16x8 kf1[2], vf[2][2]; u32x2 kf2[2];
    kf1[0] = k1; kf2[0] = (u32x2){k2, kconst}; kf2[1] = (u32x2){0u, kconst};
    vf[1][0] = (bf16x8){}; vf[1][1] = (bf16x8){};
    u32x4 pA0, pA1, pB0 = (u32x4){}, pB1 = (u32x4){};
    f32x4 e0 = (f32x4){}, e1;
    f32x16 sA = MFMA32(kf1[0], qA1, (f32x16){}), sB;
    sA = MFMA32K8(BF4(kf2[0]), qA2, sA);
#define EX2(x) __builtin_amdgcn_exp2f(x)
#define GRP(MF, S, i, EN, EP, P, w) do { MF; SCHED(); EN[0] = EX2(S[i]); P[w] = pack2(EP[0], EP[1]); EN[1] = EX2(S[i + 1]); P[w + 1] = pack2(EP[2], EP[3]); \
      EN[2] = EX2(S[i + 2]); EN[3] = EX2(S[i + 3]); PIN(P); PIN(EN); SCHED(); } while (0)
    const unsigned relsc = lds_addr(smem) + 768;
    auto group = [&](auto FIRST, int jump, int j) __attribute__((always_inline)) {
#pragma unroll
      for (int t = 0; t < 8; ++t) {
        const int c = t & 1, n = c ^ 1;
        if (FIRST) { if (t == 0) __builtin_amdgcn_s_setprio(3); if (t == 3) __builtin_amdgcn_s_setprio(2); if (t == 5) __builtin_amdgcn_s_setprio(1); if (t == 6) __builtin_amdgcn_s_setprio(0); if (t == 7) __builtin_amdgcn_s_setprio(3); }
        else {
          if (t == 4 && j == 2) __builtin_amdgcn_s_setprio(2);
          if (t == 3 && j == 3) {
            __builtin_amdgcn_s_setprio(1);
#pragma unroll
            for (int i = 0; i < 13; ++i) wq[i] = proj_ld<false>(rs, i, wid, lane);
          }
          if (t == 6 && j == 3) { __builtin_amdgcn_s_setprio(0); if (wid == 7) glds4c(ws + OFF_CNT + (C_REL + (lane & 7)) * 256, relsc); }
        }
        if (FIRST && t < 5) {
#pragma unroll
          for (int i = 5 + 3 * t; i < 8 + 3 * t; ++i) glds16c(kvs + i * 4096, ldst + i * 4096);
        }
        if (t == 7) {
          if (FIRST) { waitv<0>(); BAR(); }
          kp1 += jump; kp2 += jump;
        }
        SCHED();
        kf1[n] = *(const bf16x8*)(kp1 + ((t + 1) & 7) * 1280); kf2[n][0] = *(const unsigned*)(kp2 + ((t + 1) & 7) * 1280);
        GRP(sB = MFMA32(kf1[c], qB1, (f32x16){}), sA, 0, e1, e0, pB1, 2);
        vf[c][0] = *(const bf16x8*)vp; vf[c][1] = *(const bf16x8*)(vp + 640); vp += vinc;
        GRP(sB = MFMA32K8(BF4(kf2[c]), qB2, sB), sA, 4, e0, e1, pA0, 0);
        GRP(oB = MFMA32(vf[n][0], BF8(pB0), oB), sA, 8, e1, e0, pA0, 2);
        GRP(oB = MFMA32(vf[n][1], BF8(pB1), oB), sA, 12, e0, e1, pA1, 0);
        GRP(sA = MFMA32(kf1[n], qA1, (f32x16){}), sB, 0, e1, e0, pA1, 2);
        GRP(sA = MFMA32K8(BF4(kf2[n]), qA2, sA), sB, 4, e0, e1, pB0, 0);
        GRP(oA = MFMA32(vf[c][0], BF8(pA0), oA), sB, 8, e1, e0, pB0, 2);
        GRP(oA = MFMA32(vf[c][1], BF8(pA1), oA), sB, 12, e0, e1, pB1, 0);
      }
      vp += vjump;
    };
    group(std::true_type{}, 20480, 0);
    TS(7);
    const int j2 = (blk - 135) * 4 + qg;
    const bool side = kh == 0 && attempt == 0 && j2 < 270;
    char* stile = smem + 1024 + qg * 4096;
    const char* svec = smem + 81920 + qg * 256;
    const int kt = blk < 135 ? 4 * (blk % 5) + qg : j2 / 10, ng = blk < 135 ? blk / 5 : j2 % 10;
    if (side) {
      const u16* W = blk < 135 ? win : wout; const int N = blk < 135 ? F_ : D_;
      const int j8 = lane >> 3;
#pragma unroll
      for (int i = 0; i < 4; ++i) {
        const int row = kt * 32 + i * 8 + j8;
        glds16(W + (size_t)row * N + ng * 64 + ((lane & 7) ^ j8) * 8, lds_addr(stile) + i * 1024);
      }
      glds4c((const float*)(ws + OFF_MOD) + (4 - h) * 640 + (kt < 20 ? kt : 0) * 32 + r, lds_addr(svec));
    }
#pragma nounroll
    for (int j = 1; j < 4; ++j) {
      if (j == 2) {
        if (kh) l2touch(wsp + ((size_t)(blk * 4 + qg) * 64 + lane) * 128, lds_addr(smem) + 256);
        else if (side) {
          waitv<0>();
          const int g4 = lane >> 4, r4 = lane & 15, row = 8 * g4 + (r4 >> 2);
          const f32x4 sv0 = *(const f32x4*)(svec + (r4 & 1) * 128 + g4 * 32), sv1 = *(const f32x4*)(svec + (r4 & 1) * 128 + g4 * 32 + 16);
          const bf16x8 vec = BF8(((u32x4){pack2(sv0[0], sv0[1]), pack2(sv0[2], sv0[3]), pack2(sv1[0], sv1[1]), pack2(sv1[2], sv1[3])}));
          const unsigned nks = blk < 135 ? 20 : 27, dso = (blk < 135 ? OFF_WIN : OFF_WOUT) + (ng * 4 * nks + kt) * 1024;
          bf16x8 fr[4]; f32x4 pr[4];
#pragma unroll
          for (int f = 0; f < 4; ++f) {
            const int ch = 2 * f + ((r4 & 3) >> 1);
            const bf16x4 lo = lds_tr(stile + row * 128 + (ch ^ (row & 7)) * 16 + (r4 & 1) * 8), hi = lds_tr(stile + (row + 4) * 128 + (ch ^ ((row + 4) & 7)) * 16 + (r4 & 1) * 8);
            fr[f] = __builtin_shufflevector(lo, hi, 0, 1, 2, 3, 4, 5, 6, 7);
          }
#pragma unroll
          for (int f = 0; f < 4; ++f) { stc16(rs, l16, dso + f * nks * 1024, __builtin_bit_cast(u32x4, fr[f])); pr[f] = MFMA16(fr[f], vec, (f32x4){}); }
          if (r4 < 2) {
#pragma unroll
            for (int f = 0; f < 4; ++f) *(f32x4*)(stile + 3072 + r4 * 256 + f * 64 + g4 * 16) = pr[f];
          }
        }
      }
      if (j == 3 && kh == 0 && attempt == 0) { waitv<0>(); arrive(ws, C_REL + xcd, lane); }
      group(std::false_type{}, j < 3 ? 20480 : 0, j);
    }
    TS(8);
    pB1[2] = pack2(e0[0], e0[1]); pB1[3] = pack2(e0[2], e0[3]);
    oB = MFMA32(vf[1][0], BF8(pB0), oB); oB = MFMA32(vf[1][1], BF8(pB1), oB);
    {
      f32x4* ex = (f32x4*)(lh + 1024 + qg * 4096) + lane;
#pragma unroll
      for (int g = 0; g < 3; ++g) ex[g * 64] = (f32x4){oA[4 * g], oA[4 * g + 1], oA[4 * g + 2], oA[4 * g + 3]};
    }
    int* flag = (int*)(smem + 528);
    BAR();
    TS(9);
    float o[12];
    {
      const f32x4* ex = (const f32x4*)(smem + (kh ^ 1) * 81920 + 1024 + qg * 4096) + lane;
#pragma unroll
      for (int g = 0; g < 3; ++g) { const f32x4 t = ex[g * 64];
#pragma unroll
        for (int e = 0; e < 4; ++e) o[4 * g + e] = oB[4 * g + e] + t[e]; }
    }
    const float lo = xhalf(o[8]); const float l = h ? o[8] : lo;
    const float rl = __builtin_amdgcn_rcpf(l);
    const int tok = qb * 256 + qg * 64 + kh * 32 + r;
    const unsigned vo = (tok >> 4) * 20480 + (tok & 15) * 16;
    stc16(rs, vo + h * 256, OFF_O + (head >> 1) * 1024 + (head & 1) * 512,
          (u32x4){pack2(o[0] * rl, o[1] * rl), pack2(o[2] * rl, o[3] * rl), pack2(o[4] * rl, o[5] * rl), pack2(o[6] * rl, o[7] * rl)});
    if (h == 0) stc8(rs, vo, OFF_O + 16384 + (head >> 3) * 1024 + ((head >> 1) & 3) * 256 + (head & 1) * 8, (u32x2){pack2(o[8] * rl, o[9] * rl), pack2(o[10] * rl, o[11] * rl)});
    if (wid == 4 && blk < 135 && attempt == 0 && lane < 32) {
      const char* pp = smem + 1024 + 3072 + lane * 16;
      const f32x4 v = (*(const f32x4*)pp + *(const f32x4*)(pp + 4096)) + (*(const f32x4*)(pp + 8192) + *(const f32x4*)(pp + 12288));
      stc16(rs, (lane >> 4) * 8192 + (lane & 15) * 16, OFF_CBP + (blk % 5) * 16384 + (blk / 5) * 256, __builtin_bit_cast(u32x4, v));
    }
    bool ok = (l > 1e-30f) && (l < 1e30f);
#pragma unroll
    for (int i = 0; i < 12; ++i) ok = ok && (fabsf(o[i]) < 3e38f);
    if (!ok && attempt == 0) *flag = 1;
    waitv<0>();
    BAR();
    const int redo = __builtin_amdgcn_readfirstlane(*(volatile LAS int*)flag);
    if (!redo) break;
    BAR();
  }
  TS(10);
  if (wid == 7) arrive(ws, C_ATT + qb, lane);
#pragma unroll
  for (int i = 0; i < 13; ++i) proj_park<false>(smem, i, wid, lane, wq[i]);
  if (!__all(*(volatile LAS unsigned*)(smem + 768 + (lane & 7) * 4) >= 128u)) poll_ge(ws, C_REL + (lane & 7), 128);
}

template <bool FINAL, int KH, class HOOK>
DI void proj_body(char* __restrict__ ws, rsrc_t rs, const u16* __restrict__ x, u16* __restrict__ out, char* smem, int wid, u32x2 (&res)[3], HOOK&& after_mma TARG) {
  const int lane = fresh_tid() & 63;
  const int xcd = blockIdx.x & 7, idx = blockIdx.x >> 3, panel = xcd * 4 + (idx >> 3), cb = idx & 7;
  constexpr int NKS = FINAL ? 27 : 20;
  constexpr int K0 = KH ? (NKS + 1) / 2 : 0, NK = KH ? NKS / 2 : (NKS + 1) / 2, NKEEP = KH ? 2 : 3, UK = KH ? 3 : 0, UG = KH ? 0 : 3;
  const int m = wid & 3, mt = panel * 4 + m, g = lane >> 4, r = lane & 15;
  f32x4 gate[NKEEP], cbv[NKEEP];
#pragma unroll
  for (int i = 0; i < NKEEP; ++i) {
    const int n = (cb * 5 + UK + i) * 16;
    if (!FINAL) { res[i] = *(const u32x2*)(x + (size_t)(mt * 16 + r) * D_ + n + g * 4); cbv[i] = ldc16f(rs, g * 16, OFF_MOD + (4 * 640 + n) * 4); }
    gate[i] = ldc16f(rs, g * 16, OFF_MOD + ((FINAL ? 5 : 2) * 640 + n) * 4);
  }
  if (wid == 7) poll_ge(ws, FINAL ? C_U + panel : C_ATT + (panel >> 2), FINAL ? 8 : 32);
  waitv<0>();
  BAR();
  TS(FINAL ? 27 : 11);
  bf16x8 a[NK];
#pragma unroll
  for (int k = 0; k < NK; ++k) a[k] = BF8(ldc16(rs, lane * 16, (FINAL ? OFF_U : OFF_O) + (mt * NKS + K0 + k) * 1024));
  SCHED();
  constexpr int PD = FINAL ? 5 : 6;
  const char* bl = smem + lane * 16;
  bf16x8 bf[PD + 1][5]; f32x4 acc[5];
#pragma unroll
  for (int u = 0; u < 5; ++u) acc[u] = (f32x4){};
#pragma unroll
  for (int k = 0; k < PD; ++k)
#pragma unroll
    for (int u = 0; u < 5; ++u) bf[k][u] = *(const bf16x8*)(bl + proj_pos<FINAL>(u * NKS + K0 + k));
  SCHED();
#pragma unroll
  for (int k = 0; k < NK; ++k) {
#pragma unroll
    for (int u = 0; u < 5; ++u) {
      if (k + PD < NK) bf[(k + PD) % (PD + 1)][u] = *(const bf16x8*)(bl + proj_pos<FINAL>(u * NKS + K0 + k + PD));
      acc[u] = MFMA16(bf[k % (PD + 1)][u], a[k], acc[u]);
      SCHED();
    }
  }
  TS(FINAL ? 28 : 12);
  f32x4* ex = (f32x4*)(smem + (FINAL ? 40960 : 68608)) + m * 5 * 64 + lane;
#pragma unroll
  for (int i = 0; i < 5 - NKEEP; ++i) ex[(UG + i) * 64] = acc[UG + i];
  f32x4 part[NKEEP];
#pragma unroll
  for (int i = 0; i < NKEEP; ++i) {
    part[i] = fma4s(gate[i], acc[UK + i], (f32x4){bflo(res[i][0]), bfhi(res[i][0]), bflo(res[i][1]), bfhi(res[i][1])});
    PIN(part[i]);
  }
  SCHED();
  after_mma();
  BAR();
  f32x4 oth[NKEEP];
#pragma unroll
  for (int i = 0; i < NKEEP; ++i) oth[i] = ex[(UK + i) * 64];
  f32x4 sv = (f32x4){}, qv = (f32x4){};
#pragma unroll
  for (int i = 0; i < NKEEP; ++i) {
    const f32x4 v = part[i] + gate[i] * oth[i];
    const u32x2 o = (u32x2){pack2(v[0], v[1]), pack2(v[2], v[3])};
    if (FINAL) *(u32x2*)(out + (size_t)(mt * 16 + r) * D_ + (cb * 5 + UK + i) * 16 + g * 4) = o;
    else {
      res[i] = o; PIN(res[i]);
      const f32x4 w = v * cbv[i]; const int un = cb * 5 + UK + i;
      stc8(rs, ((g >> 1) * 16 + r) * 16 + (g & 1) * 8, OFF_X1S + mt * 20480 + un * 512, (u32x2){pack2(w[0], w[1]), pack2(w[2], w[3])});
      sv += v; qv += v * v;
    }
  }
  if (!FINAL) {
    const float s = sum_g((sv[0] + sv[1]) + (sv[2] + sv[3])), q = sum_g((qv[0] + qv[1]) + (qv[2] + qv[3]));
    if (g == 0) stc8(rs, r * 8, OFF_ST1 + ((panel * 16 + cb * 2 + KH) * 64 + m * 16) * 8, (u32x2){__float_as_uint(s), __float_as_uint(q)});
    TS(13);
    waitv<0>();
    BAR();
    TS(14);
    if (wid == 7) arrive(ws, C_X1 + panel, lane);
  }
}

constexpr float GC1 = -2.8853900817779268f * 0.7978845608028654f, GC2 = GC1 * 0.044715f;
DI float gelu_s(float a, float k1, float k3, float den) {
  const float e = __builtin_amdgcn_exp2f(a * __builtin_fmaf(a * a, k3, k1));
  return a * __builtin_amdgcn_rcpf(__builtin_fmaf(e, den, den));
}
DI void gelu8(f32x4& a0, const f32x4& l0, f32x4& a1, const f32x4& l1) {
  float t[8];
#define G8(expr0, expr1) do { _Pragma("unroll") for (int i = 0; i < 4; ++i) { t[i] = opq(expr0); } _Pragma("unroll") for (int i = 0; i < 4; ++i) { t[4 + i] = opq(expr1); } SCHED(); } while (0)
  G8(a0[i] * a0[i], a1[i] * a1[i]);
  G8(__builtin_fmaf(t[i], l0[3], l0[2]), __builtin_fmaf(t[4 + i], l1[3], l1[2]));
  G8(a0[i] * t[i], a1[i] * t[4 + i]);
  G8(__builtin_amdgcn_exp2f(t[i]), __builtin_amdgcn_exp2f(t[4 + i]));
  G8(__builtin_fmaf(t[i], l0[0], l0[0]), __builtin_fmaf(t[4 + i], l1[0], l1[0]));
  G8(__builtin_amdgcn_rcpf(t[i]), __builtin_amdgcn_rcpf(t[4 + i]));
  G8(a0[i] * t[i], a1[i] * t[4 + i]);
  a0 = (f32x4){t[0], t[1], t[2], t[3]}; a1 = (f32x4){t[4], t[5], t[6], t[7]};
}
constexpr int L4_Z1 = 114688, L4_TR = L4_Z1 + 16384, L4_LN = L4_TR + 16384, L4_ST2 = L4_LN + 1024, L4_FIN = L4_ST2 + 3584, L4_CB = L4_FIN + 512, L4_CNT = L4_CB + 2048;
constexpr int K5_R = 7;
constexpr unsigned DEAD = 0x80000000u;
DI int sp_stage(int s) { const int idx = blockIdx.x >> 3; return (s + (idx & 7) * 2) & 15; }
DI unsigned sp_voff(int wid, int lane, int i) { return ((wid & 3) * 16 + i * 8 + (lane >> 3)) * 4096 + (wid >> 2) * 128 + ((lane & 7) ^ (4 * i + (lane >> 4))) * 16; }
DI const char* sp_base(const u16* wsp, int panel, int s) { return (const char*)wsp + (size_t)panel * 262144 + sp_stage(s) * 256; }
DI void sp_prefetch(u32x4 (&wr)[16 - K5_R][2], const u16* __restrict__ wsp, int panel, int wid, int lane) {
  const unsigned v0 = sp_voff(wid, lane, 0), v1 = sp_voff(wid, lane, 1);
#pragma unroll
  for (int s = K5_R; s < 16; ++s) { wr[s - K5_R][0] = *(const u32x4*)(sp_base(wsp, panel, s) + v0); wr[s - K5_R][1] = *(const u32x4*)(sp_base(wsp, panel, s) + v1); }
  asm volatile("" ::: "memory");
}
template <int S0, int S1>
DI void sp_ring(const u16* __restrict__ wsp, char* smem, int panel, int wid, int lane) {
  const unsigned v0 = sp_voff(wid, lane, 0), v1 = sp_voff(wid, lane, 1);
#pragma unroll
  for (int s = S0; s < S1; ++s) { glds16(sp_base(wsp, panel, s) + v0, lds_addr(smem) + wid * 2048 + s * 16384); glds16(sp_base(wsp, panel, s) + v1, lds_addr(smem) + wid * 2048 + s * 16384 + 1024); }
}
DI unsigned win_boff(int wid, int lane) { const int cb = (blockIdx.x >> 3) & 7; return lane * 16 + (wid == 6 && cb >= 6 ? DEAD : 0u); }
DI unsigned win_soff(int wid, bool z2) { return OFF_WIN + ((z2 ? 54 : 0) + unit0_of((blockIdx.x >> 3) & 7) + wid) * 20480; }
#define WIN_LD(so, k) BF8(ldc16(rs, bo + ((k) & 3) * 1024, (so) + ((k) >> 2) * 4096))
DI void win_prefetch(rsrc_t rs, int wid, bf16x8 (&b2)[20]) {
  const unsigned bo = win_boff(wid, fresh_tid() & 63), so = win_soff(wid, true);
#pragma unroll
  for (int k = 0; k < 20; ++k) b2[k] = WIN_LD(so, k);
}
template <class HOOK>
DI void win_mma(const char* al, const bf16x8 (&b)[20], f32x4 (&acc)[4], HOOK&& hook) {
  constexpr int PD = 4;
  bf16x8 af[PD + 1][2];
#define WIN_RD(i) do { af[(i) % (PD + 1)][0] = *(const bf16x8*)(al + ((i) & 3) * 20480 + ((i) >> 2) * 2048); af[(i) % (PD + 1)][1] = *(const bf16x8*)(al + ((i) & 3) * 20480 + ((i) >> 2) * 2048 + 1024); } while (0)
#pragma unroll
  for (int i = 0; i < PD; ++i) WIN_RD(i);
  SCHED();
  sfor<40>([&](auto I) __attribute__((always_inline)) {
    constexpr int i = I, m = i & 3, k = (i >> 2) * 2;
    if constexpr (i + PD < 40) WIN_RD(i + PD);
    acc[m] = MFMA16(b[k], af[i % (PD + 1)][0], acc[m]);
    acc[m] = MFMA16(b[k + 1], af[i % (PD + 1)][1], acc[m]); PIN(acc[m]);
    hook(I);
    SCHED();
  });
}
DI void stage_x1s(char* __restrict__ ws, char* smem, int panel, int wid, int tid) {
  const char* src = ws + OFF_X1S + (size_t)panel * 81920 + tid * 16;
#pragma unroll
  for (int i = 0; i < 10; ++i) glds16c(src + i * 8192, lds_addr(smem) + wid * 1024 + i * 8192);
}
DI void win_tail(char* __restrict__ ws, const u16* __restrict__ wsp, char* smem, int panel, int cb, int wid, int lane TARG) {
  BAR();
  TS(22);
  if (wid == 7 && lane < 4) __hip_atomic_fetch_add((GAS unsigned*)(ws + OFF_CNT + (C_Z2 + lane * 8 + cb) * 256), 1u, __ATOMIC_RELAXED, __HIP_MEMORY_SCOPE_AGENT);
}

template <bool EARLY>
DI void win_body(char* __restrict__ ws, rsrc_t rs, const u16* __restrict__ wsp, char* smem, int wid, const bf16x8 (&b2)[20], u32x4 (&wr)[16 - K5_R][2] TARG) {
  const int tid = fresh_tid(), lane = tid & 63, g = lane >> 4, r = lane & 15;
  const int xcd = blockIdx.x & 7, idx = blockIdx.x >> 3, panel = xcd * 4 + (idx >> 3), cb = idx & 7, u0 = unit0_of(cb);
  float* lnp = (float*)(smem + L4_LN); float* st2 = (float*)(smem + L4_ST2); float* fin = (float*)(smem + L4_FIN); float* cbl = (float*)(smem + L4_CB);
  int* flg = (int*)(smem + L4_CNT);
  __builtin_amdgcn_s_setprio(1);
  const unsigned bo = win_boff(wid, lane), so1 = win_soff(wid, false);
  bf16x8 b1[20];
  BAR();
  TS(15);
  stage_x1s(ws, smem, panel, wid, tid);
#pragma unroll
  for (int k = 0; k < 4; ++k) b1[k] = WIN_LD(so1, k);
  waitv<4>();
  BAR();
  TS(16);
  f32x4 ln[4], z2[4], z1[4];
#pragma unroll
  for (int m = 0; m < 4; ++m) ln[m] = *(const f32x4*)(lnp + (m * 16 + r) * 4);
  {
    const f32x4 cs2 = *(const f32x4*)(cbl + 112 + wid * 16 + g * 4), bi2 = *(const f32x4*)(cbl + 256 + 112 + wid * 16 + g * 4);
    const f32x4 cs1 = *(const f32x4*)(cbl + wid * 16 + g * 4), bi1 = *(const f32x4*)(cbl + 256 + wid * 16 + g * 4);
#pragma unroll
    for (int m = 0; m < 4; ++m) {
#pragma unroll
      for (int e = 0; e < 4; ++e) { z2[m][e] = opq(__builtin_fmaf(ln[m][1], cs2[e], opq(ln[m][0] * bi2[e]))); z1[m][e] = opq(__builtin_fmaf(ln[m][1], cs1[e], opq(ln[m][0] * bi1[e]))); }
      PIN(z1[m]);
    }
  }
  win_mma(smem + lane * 16, b2, z2, [&](auto I) __attribute__((always_inline)) { constexpr int i = I; if constexpr (i < 32 && (i & 1)) b1[4 + i / 2] = WIN_LD(so1, 4 + i / 2); });
  TS(17);
  gelu8(z2[0], ln[0], z2[1], ln[1]); gelu8(z2[2], ln[2], z2[3], ln[3]);
  {
    float s[4], q[4];
#pragma unroll
    for (int m = 0; m < 4; ++m) { s[m] = opq(opq(z2[m][0] + z2[m][1]) + opq(z2[m][2] + z2[m][3])); q[m] = opq(__builtin_fmaf(z2[m][0], z2[m][0], opq(z2[m][1] * z2[m][1])) + opq(__builtin_fmaf(z2[m][2], z2[m][2], opq(z2[m][3] * z2[m][3])))); }
    auto pa = __builtin_amdgcn_permlane32_swap(__float_as_uint(s[0]), __float_as_uint(s[2]), false, false);
    auto pb = __builtin_amdgcn_permlane32_swap(__float_as_uint(s[1]), __float_as_uint(s[3]), false, false);
    auto pc = __builtin_amdgcn_permlane32_swap(__float_as_uint(q[0]), __float_as_uint(q[2]), false, false);
    auto pd = __builtin_amdgcn_permlane32_swap(__float_as_uint(q[1]), __float_as_uint(q[3]), false, false);
    const float sa = opq(__uint_as_float(pa[0]) + __uint_as_float(pa[1])), sb = opq(__uint_as_float(pb[0]) + __uint_as_float(pb[1]));
    const float qa = opq(__uint_as_float(pc[0]) + __uint_as_float(pc[1])), qb = opq(__uint_as_float(pd[0]) + __uint_as_float(pd[1]));
    auto ps = __builtin_amdgcn_permlane16_swap(__float_as_uint(sa), __float_as_uint(sb), false, false);
    auto pq = __builtin_amdgcn_permlane16_swap(__float_as_uint(qa), __float_as_uint(qb), false, false);
    *(f32x2*)(st2 + (wid * 64 + lane) * 2) = (f32x2){opq(__uint_as_float(ps[0]) + __uint_as_float(ps[1])), opq(__uint_as_float(pq[0]) + __uint_as_float(pq[1]))};
  }
  asm volatile("" ::: "memory");
  if (lane == 0) __hip_atomic_fetch_add(flg, 1, __ATOMIC_RELAXED, __HIP_MEMORY_SCOPE_WORKGROUP);
  asm volatile("" ::: "memory");
  __builtin_amdgcn_s_setprio(0);
  TS(18);
  const unsigned wvo[2] = {sp_voff(wid, lane, 0), sp_voff(wid, lane, 1)};
  win_mma(smem + lane * 16, b1, z1, [&](auto I) __attribute__((always_inline)) {
    constexpr int i = I, j = i < 8 ? i : (i & 1) ? -1 : 4 + i / 2;
    if constexpr (EARLY && j >= 0 && j < 18) wr[j >> 1][j & 1] = *(const u32x4*)(sp_base(wsp, panel, K5_R + (j >> 1)) + wvo[j & 1]);
  });
  gelu8(z1[0], ln[0], z1[1], ln[1]); gelu8(z1[2], ln[2], z1[3], ln[3]);
#pragma unroll
  for (int mm = 0; mm < 2; ++mm)
    *(u32x4*)(smem + L4_Z1 + wid * 2048 + mm * 1024 + lane * 16) = (u32x4){pack2(z1[2 * mm][0], z1[2 * mm][1]), pack2(z1[2 * mm][2], z1[2 * mm][3]), pack2(z1[2 * mm + 1][0], z1[2 * mm + 1][1]), pack2(z1[2 * mm + 1][2], z1[2 * mm + 1][3])};
  TS(20);
  BAR();
  TS(21);
  char* tr = smem + L4_TR + wid * 2048;
#pragma unroll
  for (int m = 0; m < 4; ++m) {
    const f32x2 f = *(const f32x2*)(fin + (m * 16 + r) * 2);
    const f32x4 v = z2[m] * f[0] - f[1];
    *(u32x2*)(tr + (m * 16 + r) * 32 + g * 8) = (u32x2){pack2(v[0], v[1]), pack2(v[2], v[3])};
  }
  {
    const char* p = tr + (8 * g + (r >> 2)) * 32 + (r & 3) * 8;
    const bf16x4 lo0 = lds_tr(p), hi0 = lds_tr(p + 128), lo1 = lds_tr(p + 1024), hi1 = lds_tr(p + 1152);
    stc16(rs, bo, OFF_Z2 + (panel * 2 * 54 + u0 + wid) * 1024, __builtin_bit_cast(u32x4, (bf16x8)__builtin_shufflevector(lo0, hi0, 0, 1, 2, 3, 4, 5, 6, 7)));
    stc16(rs, bo, OFF_Z2 + ((panel * 2 + 1) * 54 + u0 + wid) * 1024, __builtin_bit_cast(u32x4, (bf16x8)__builtin_shufflevector(lo1, hi1, 0, 1, 2, 3, 4, 5, 6, 7)));
  }
  SCHED();
  waitv<0>();
  win_tail(ws, wsp, smem, panel, cb, wid, lane TPASS);
  sp_ring<0, 5>(wsp, smem, panel, wid, lane);
  if (!EARLY) sp_prefetch(wr, wsp, panel, wid, lane);
}

DI void win_agent(char* __restrict__ ws, rsrc_t rs, const u16* __restrict__ wsp, char* smem, u32x4 (&wr)[16 - K5_R][2] TARG) {
  const int tid = fresh_tid(), lane = tid & 63, wid = 7;
  const int xcd = blockIdx.x & 7, idx = blockIdx.x >> 3, panel = xcd * 4 + (idx >> 3), cb = idx & 7, u0 = unit0_of(cb);
  float* lnp = (float*)(smem + L4_LN); float* st2 = (float*)(smem + L4_ST2); float* fin = (float*)(smem + L4_FIN); float* cbl = (float*)(smem + L4_CB);
  int* cnt = (int*)(smem + L4_CNT);
  __builtin_amdgcn_s_setprio(2);
  if (lane == 0) *cnt = 0;
  poll_ge(ws, lane < 8 ? C_ATT + lane : C_X1 + panel, lane < 8 ? 32 : 8);
  BAR();
  stage_x1s(ws, smem, panel, wid, tid);
  {
    u32x2 t[16]; f32x4 cp[5][2];
#pragma unroll
    for (int i = 0; i < 16; ++i) t[i] = __builtin_amdgcn_raw_buffer_load_b64(rs, lane * 8, OFF_ST1 + (panel * 16 + i) * 512, 16);
    const unsigned co = lane < 28 ? (u0 * 16 + lane * 4) * 4 : min((54 + u0) * 16 + min(lane - 28, 27) * 4, F_ - 4) * 4;
#pragma unroll
    for (int i = 0; i < 5; ++i) { cp[i][0] = ldc16f(rs, co, OFF_CBP + i * 16384); cp[i][1] = ldc16f(rs, co, OFF_CBP + i * 16384 + 8192); }
    float s = 0.f, q = 0.f;
#pragma unroll
    for (int i = 0; i < 16; ++i) { s += __uint_as_float(t[i][0]); q += __uint_as_float(t[i][1]); }
    const float mean = s * (1.f / 640.f), var = fmaxf(q * (1.f / 640.f) - mean * mean, 0.f);
    const float den = ln_den(var), ri = 1.f / den;
    *(f32x4*)(lnp + lane * 4) = (f32x4){den, -rbf(mean), GC1 * ri, GC2 * ri * ri * ri};
#pragma unroll
    for (int v = 0; v < 2; ++v) *(f32x4*)(cbl + v * 256 + lane * 4) = ((cp[0][v] + cp[1][v]) + (cp[2][v] + cp[3][v])) + cp[4][v];
  }
  waitv<0>();
  BAR();
  __builtin_amdgcn_s_setprio(0);
  __builtin_amdgcn_s_sleep(8);
  sp_prefetch(wr, wsp, panel, wid, lane);
#pragma nounroll
  for (int w = 0; w < 8; ++w) sp_ring<5, 7>(wsp, smem, panel, w, lane);
  __builtin_amdgcn_s_setprio(2);
#pragma nounroll
  for (unsigned spin = 0; spin < (1u << 22); ++spin) {
    if (__hip_atomic_load(cnt, __ATOMIC_RELAXED, __HIP_MEMORY_SCOPE_WORKGROUP) >= 7) break;
    __builtin_amdgcn_s_sleep(1);
  }
  asm volatile("" ::: "memory");
  float s, q;
  {
    f32x2 t[7];
#pragma unroll
    for (int i = 0; i < 7; ++i) t[i] = *(const f32x2*)(st2 + (i * 64 + lane) * 2);
    s = t[0][0]; q = t[0][1];
#pragma unroll
    for (int i = 1; i < 6; ++i) { s = opq(s + t[i][0]); q = opq(q + t[i][1]); }
    if (cb < 6) { s = opq(s + t[6][0]); q = opq(q + t[6][1]); }
  }
  GAS u64* xch = (GAS u64*)(ws + OFF_XCH) + panel * 512;
  unsigned qb = __float_as_uint(q); if (qb == ~0u) qb = 0x7fc00000u;
  __hip_atomic_store(xch + cb * 64 + lane, ((u64)qb << 32) | __float_as_uint(s), __ATOMIC_RELAXED, __HIP_MEMORY_SCOPE_AGENT);
  TS(19);
  u64 v[8];
#pragma nounroll
  for (unsigned spin = 0; spin < (1u << 19); ++spin) {
    bool ok = true;
#pragma unroll
    for (int i = 0; i < 8; ++i) { v[i] = __hip_atomic_load(xch + i * 64 + lane, __ATOMIC_RELAXED, __HIP_MEMORY_SCOPE_AGENT); ok = ok && ((unsigned)(v[i] >> 32) != ~0u); }
    if (__all(ok)) break;
    __builtin_amdgcn_s_sleep(1);
  }
  s = 0.f; q = 0.f;
#pragma unroll
  for (int i = 0; i < 8; ++i) { s = opq(s + __uint_as_float((unsigned)v[i])); q = opq(q + __uint_as_float((unsigned)(v[i] >> 32))); }
  const float mean = opq(s * (1.f / 864.f)), var = fmaxf(opq(q * (1.f / 864.f)) - mean * mean, 0.f);
  const float ri = 1.f / ln_den(var);
  *(f32x2*)(fin + lane * 2) = (f32x2){ri, rbf(mean) * ri};
  __builtin_amdgcn_s_setprio(0);
  TS(20);
  BAR();
  __builtin_amdgcn_s_sleep(6);
  sp_ring<0, 5>(wsp, smem, panel, wid, lane);
  win_tail(ws, wsp, smem, panel, cb, wid, lane TPASS);
}

#define SP_LB 4
DI void phase_sp(char* __restrict__ ws, rsrc_t rs, const u16* __restrict__ bsp, char* smem, int wid, const u32x4 (&wr)[16 - K5_R][2] TARG) {
  const int tid = fresh_tid(), lane = tid & 63;
  const int xcd = blockIdx.x & 7, idx = blockIdx.x >> 3, panel = xcd * 4 + (idx >> 3), cb = idx & 7;
  const int kh = wid >> 2, up = wid & 3, u0 = unit0_of(cb) + 2 * up, nu = cb < 6 ? 7 : 6, g = lane >> 4, r = lane & 15;
  const unsigned dead[2] = {2 * up < nu ? 0u : DEAD, 2 * up + 1 < nu ? 0u : DEAD};
  const unsigned bvo[2] = {lane * 16 + dead[0], lane * 16 + 1024 + dead[1]};
  u16 bb[2];
#pragma unroll
  for (int j = 0; j < 2; ++j) bb[j] = bsp[panel * 64 + (2 * kh + j) * 16 + r];
  const char* al[2][2];
#pragma unroll
  for (int k = 0; k < 2; ++k)
#pragma unroll
    for (int j = 0; j < 2; ++j) al[k][j] = smem + kh * 8192 + (j ^ kh) * 4096 + r * 128 + ((4 * k + g) ^ (r >> 1)) * 16;
  constexpr int LB = SP_LB, PD = 3;
  bf16x8 bq[LB + 1][4], af[PD + 1][2]; f32x4 acc[2][4]; u32x4 wq[17];
  if (wid == 7) poll_ge(ws, C_Z2 + (panel >> 3) * 8 + cb, 32);
#pragma unroll
  for (int i = 0; i < 2; ++i) {
    const float b0 = bf2f(bb[0]), b1 = bf2f(bb[1]);
    acc[i][0] = (f32x4){b0, b0, b0, b0}; acc[i][1] = (f32x4){b1, b1, b1, b1}; acc[i][2] = (f32x4){}; acc[i][3] = (f32x4){};
  }
  if (kh && up < 3) waitv<2 * (16 - K5_R)>(); else waitv<0>();
  BAR();
  TS(23);
  const unsigned bso = OFF_Z2 + (kh * 2 * 54 + u0) * 1024;
#define SP_LD(s, j) (bq[(s) % (LB + 1)][j] = BF8(ldc16(rs, bvo[(j) & 1], bso + (sp_stage(s) * 4 + ((j) >> 1)) * 55296)))
#define SP_RD(t, k) (af[(t) % (PD + 1)][k] = *(const bf16x8*)(al[k][((t) >> 1) & 1] + (((t) >> 2) % K5_R) * 16384 + ((t) & 1) * 2048))
#pragma unroll
  for (int s = 0; s < LB; ++s)
#pragma unroll
    for (int j = 0; j < 4; ++j) SP_LD(s, j);
#pragma unroll
  for (int t = 0; t < PD; ++t) { SP_RD(t, 0); SP_RD(t, 1); }
  SCHED();
  sfor<64>([&](auto T) __attribute__((always_inline)) {
    constexpr int t = T, nx = t + PD, s = t >> 2, m = t & 3, tl = t + 4 * LB;
    if constexpr (nx == 16 || nx == 28 || nx == 44 || nx == 56) { BAR(); TS(32 + (nx == 28) + 2 * (nx == 44) + 3 * (nx == 56)); }
    constexpr int c = nx >= 16 && nx < 24 ? nx - 16 : nx >= 28 && nx < 34 ? nx - 20 : nx >= 44 && nx < 48 ? nx - 30 : -1;
    if constexpr (nx < 64) SP_RD(nx, 0);
    acc[0][m] = MFMA16(bq[s % (LB + 1)][0], af[t % (PD + 1)][0], acc[0][m]);
    acc[0][m] = MFMA16(bq[s % (LB + 1)][2], af[t % (PD + 1)][1], acc[0][m]); PIN(acc[0][m]);
    if constexpr (tl < 64) SP_LD(tl >> 2, m); else if constexpr (tl - 64 < 17) wq[tl - 64] = proj_ld<true>(rs, tl - 64, wid, lane);
    SCHED();
    if constexpr (nx < 64) SP_RD(nx, 1);
    acc[1][m] = MFMA16(bq[s % (LB + 1)][1], af[t % (PD + 1)][0], acc[1][m]);
    acc[1][m] = MFMA16(bq[s % (LB + 1)][3], af[t % (PD + 1)][1], acc[1][m]); PIN(acc[1][m]);
    if constexpr (c >= 0) *(u32x4*)(smem + ((c >> 1) % K5_R) * 16384 + wid * 2048 + (c & 1) * 1024 + lane * 16) = wr[c >> 1][c & 1];
    if constexpr (t == 63 && 4 * LB < 17) { _Pragma("unroll") for (int i = 4 * LB; i < 17; ++i) wq[i] = proj_ld<true>(rs, i, wid, lane); }
    SCHED();
  });
  TS(24);
  f32x4* exw = (f32x4*)(smem + 32768 + wid * 4096) + lane;
  const f32x4* exr = (const f32x4*)(smem + 32768 + (wid ^ 4) * 4096) + lane;
  u32x4 z[2];
#pragma unroll
  for (int i = 0; i < 2; ++i) z[i] = *(const u32x4*)(smem + L4_Z1 + (2 * up + i) * 2048 + kh * 1024 + lane * 16);
#pragma unroll
  for (int i = 0; i < 2; ++i)
#pragma unroll
    for (int j = 0; j < 2; ++j) exw[(i * 2 + j) * 64] = acc[i][2 + j];
  f32x4 zf[2][2];
#pragma unroll
  for (int i = 0; i < 2; ++i)
#pragma unroll
    for (int j = 0; j < 2; ++j) {
      zf[i][j] = (f32x4){bflo(z[i][2 * j]), bfhi(z[i][2 * j]), bflo(z[i][2 * j + 1]), bfhi(z[i][2 * j + 1])};
      acc[i][j] = mul4s(acc[i][j], zf[i][j]); PIN(zf[i][j]); PIN(acc[i][j]);
    }
  BAR();
  f32x4 oth[2][2];
#pragma unroll
  for (int i = 0; i < 2; ++i)
#pragma unroll
    for (int j = 0; j < 2; ++j) oth[i][j] = exr[(i * 2 + j) * 64];
#pragma unroll
  for (int i = 0; i < 2; ++i)
#pragma unroll
    for (int j = 0; j < 2; ++j) {
      const f32x4 v = acc[i][j] + zf[i][j] * oth[i][j];
      stc8(rs, ((g >> 1) * 16 + r) * 16 + (g & 1) * 8 + dead[i], OFF_U + (panel * 4 + 2 * kh + j) * 27648 + (u0 + i) * 512, (u32x2){pack2(v[0], v[1]), pack2(v[2], v[3])});
    }
  TS(25);
  waitv<0>();
  BAR();
  TS(26);
  if (wid == 7) arrive(ws, C_U + panel, lane);
#pragma unroll
  for (int i = 0; i < 17; ++i) proj_park<true>(smem, i, wid, lane, wq[i]);
}

extern "C" __global__ __launch_bounds__(512, 2) OCC2 void k_mega(char* __restrict__ ws, const u16* __restrict__ x, const u16* __restrict__ win,
                                                     const u16* __restrict__ wsp, const u16* __restrict__ bsp, const u16* __restrict__ wout, u16* __restrict__ out) {
  extern __shared__ __attribute__((aligned(16))) char smem[];
  const int wid = __builtin_amdgcn_readfirstlane(threadIdx.x >> 6);
  const rsrc_t rs = __builtin_amdgcn_make_buffer_rsrc(ws, 0, WS_USED, 0x00020000);
  TDECL; TS(0);
  u32x2 res[3];
  phase_qkv(ws, rs, smem, wid TPASS);
  phase_attn(ws, rs, (const char*)wsp, win, wout, smem, wid TPASS);
  u32x4 wr[16 - K5_R][2];
  auto nohook = []() __attribute__((always_inline)) {};
  if (wid < 4) {
    bf16x8 b2[20];
    proj_body<false, 0>(ws, rs, x, out, smem, wid, res, [&]() __attribute__((always_inline)) { win_prefetch(rs, wid, b2); } TPASS);
    win_body<true>(ws, rs, wsp, smem, wid, b2, wr TPASS);
  } else {
    proj_body<false, 1>(ws, rs, x, out, smem, wid, res, nohook TPASS);
    if (wid == 7) win_agent(ws, rs, wsp, smem, wr TPASS);
    else { bf16x8 b2[20]; win_prefetch(rs, wid, b2); win_body<false>(ws, rs, wsp, smem, wid, b2, wr TPASS); }
  }
  phase_sp(ws, rs, bsp, smem, wid, wr TPASS);
  if (wid < 4) proj_body<true, 0>(ws, rs, x, out, smem, wid, res, nohook TPASS);
  else proj_body<true, 1>(ws, rs, x, out, smem, wid, res, nohook TPASS);
  TS(29);
  TDUMP();
  asm volatile("k_mega_code_end:");
}

extern "C" void kernel_launch(void* const* d_in, const int* in_sizes, int n_in, void* d_out, int out_size, void* d_ws, size_t ws_size,
                              hipStream_t stream) {
  const u16* x = (const u16*)d_in[0];
  const u16* wqkv = (const u16*)d_in[1];
  const u16* wo = (const u16*)d_in[2];
  const u16* win = (const u16*)d_in[4];
  const u16* wsp = (const u16*)d_in[5];
  const u16* bsp = (const u16*)d_in[6];
  const u16* wout = (const u16*)d_in[7];
  const u16* cvec = (const u16*)d_in[8];
  const u16* wmod = (const u16*)d_in[9];
  char* ws = (char*)d_ws;
  (void)hipFuncSetAttribute((const void*)k_mega, hipFuncAttributeMaxDynamicSharedMemorySize, 163840);
  k0_prep<<<K0_GRID, 256, 0, stream>>>(x, wqkv, wo, cvec, wmod, ws);
  k_mega<<<256, 512, 163840, stream>>>(ws, x, win, wsp, bsp, wout, (u16*)d_out);
}
```

```cpp
#include <hip/hip_runtime.h>
#include <hip/hip_bf16.h>
#include <stdint.h>
#include <utility>

typedef __attribute__((ext_vector_type(8))) short bf16x8;
typedef __attribute__((ext_vector_type(4))) short bf16x4;
typedef __attribute__((ext_vector_type(2))) float f32x2;
typedef __attribute__((ext_vector_type(4))) float f32x4;
typedef __attribute__((ext_vector_type(16))) float f32x16;
typedef __attribute__((ext_vector_type(2))) unsigned u32x2;
typedef __attribute__((ext_vector_type(4))) unsigned u32x4;
typedef __attribute__((ext_vector_type(2))) __bf16 bf2_t;
typedef __attribute__((ext_vector_type(2))) _Float16 h2_t;
typedef unsigned short u16;
typedef unsigned long long u64;
#define LAS __attribute__((address_space(3)))
#define GAS __attribute__((address_space(1)))
#define DI __device__ __forceinline__

constexpr int D_ = 640, NQKV = 1920, F_ = 1728;

constexpr unsigned OFF_CNT = 0;
constexpr unsigned OFF_MOD = 65536;
constexpr unsigned OFF_MODP = OFF_MOD + 16384;
constexpr unsigned OFF_CBP = OFF_MODP + 61440;
constexpr unsigned OFF_XCH = OFF_CBP + 5 * 16384;
constexpr unsigned OFF_ST1 = OFF_XCH + 131072;
constexpr unsigned OFF_XHAT = OFF_ST1 + 262144;
constexpr unsigned OFF_WQKV = OFF_XHAT + 2621440;
constexpr unsigned OFF_WO = OFF_WQKV + 2457600;
constexpr unsigned OFF_WIN = OFF_WO + 819200;
constexpr unsigned OFF_WOUT = OFF_WIN + 2211840;
constexpr unsigned OFF_Q = OFF_WOUT + 1105920;
constexpr unsigned OFF_KV = OFF_Q + 32 * 81920;
constexpr unsigned OFF_O = OFF_KV + 32 * 163840;
constexpr unsigned OFF_X1S = OFF_O + 2621440;
constexpr unsigned OFF_Z2 = OFF_X1S + 2621440;
constexpr unsigned OFF_U = OFF_Z2 + 3538944;
constexpr unsigned WS_USED = OFF_U + 3538944;
constexpr int C_QKV = 0, C_MOD = 15  , C_ATT = 128, C_REL = 136, C_X1 = 144, C_U = 176, C_Z2 = 208  ;

DI unsigned pack2(float lo, float hi) { return __builtin_bit_cast(unsigned, __builtin_convertvector((f32x2){lo, hi}, bf2_t)); }
DI float opq(float x) { asm("" : "+v"(x)); return x; }
DI f32x4 fma4s(f32x4 a, f32x4 b, f32x4 c) { return (f32x4){opq(__builtin_fmaf(a[0], b[0], c[0])), opq(__builtin_fmaf(a[1], b[1], c[1])), opq(__builtin_fmaf(a[2], b[2], c[2])), opq(__builtin_fmaf(a[3], b[3], c[3]))}; }
DI f32x4 mul4s(f32x4 a, f32x4 b) { return (f32x4){opq(a[0] * b[0]), opq(a[1] * b[1]), opq(a[2] * b[2]), opq(a[3] * b[3])}; }
DI unsigned pack2h(float lo, float hi) { return __builtin_bit_cast(unsigned, __builtin_convertvector((f32x2){lo, hi}, h2_t)); }
DI float bflo(unsigned u) { return __uint_as_float(u << 16); }
DI float bfhi(unsigned u) { return __uint_as_float(u & 0xffff0000u); }
DI float rbf(float x) { return bflo(pack2(x, x)); }
DI float bf2f(u16 b) { return __uint_as_float((unsigned)b << 16); }
#define MFMA32(a, b, c) __builtin_amdgcn_mfma_f32_32x32x16_bf16((a), (b), (c), 0, 0, 0)
#define MFMA32K8(a, b, c) __builtin_amdgcn_mfma_f32_32x32x8bf16_1k((a), (b), (c), 0, 0, 0)
#define MFMA16(a, b, c) __builtin_amdgcn_mfma_f32_16x16x32_bf16((a), (b), (c), 0, 0, 0)
#define BAR() asm volatile("s_waitcnt lgkmcnt(0)\n\ts_barrier" ::: "memory")
#define SCHED() __builtin_amdgcn_sched_barrier(0)
#define PIN(x) asm volatile("" : "+v"(x))
#define OCC2 __attribute__((amdgpu_waves_per_eu(2, 2)))
#define BF8(x) __builtin_bit_cast(bf16x8, x)
#define BF4(x) __builtin_bit_cast(bf16x4, x)
template <int N> DI void waitv() { asm volatile("s_waitcnt vmcnt(%0)" ::"i"(N) : "memory"); }
DI void drain() { waitv<0>(); __builtin_amdgcn_s_waitcnt(0x0F70); }
template <class F, int... I> DI void sfor_impl(F&& f, std::integer_sequence<int, I...>) { (f(std::integral_constant<int, I>{}), ...); }
template <int N, class F> DI void sfor(F&& f) { sfor_impl(f, std::make_integer_sequence<int, N>{}); }
typedef short v4i16_t __attribute__((ext_vector_type(4)));
DI bf16x4 lds_tr(const char* p) { return __builtin_bit_cast(bf16x4, __builtin_amdgcn_ds_read_tr16_b64_v4i16((LAS v4i16_t*)p)); }
DI void glds16(const void* g, unsigned lds_base) {
  unsigned sv;
  asm volatile("s_mov_b32 %0, m0\n\ts_mov_b32 m0, %2\n\ts_nop 0\n\tglobal_load_lds_dwordx4 %1, off\n\ts_mov_b32 m0, %0" : "=&s"(sv) : "v"(g), "s"(lds_base) : "memory");
}
DI void glds16c(const void* g, unsigned lds_base) {
  unsigned sv;
  asm volatile("s_mov_b32 %0, m0\n\ts_mov_b32 m0, %2\n\ts_nop 0\n\tglobal_load_lds_dwordx4 %1, off sc1\n\ts_mov_b32 m0, %0" : "=&s"(sv) : "v"(g), "s"(lds_base) : "memory");
}
DI void glds4c(const void* g, unsigned lds_base) {
  unsigned sv;
  asm volatile("s_mov_b32 %0, m0\n\ts_mov_b32 m0, %2\n\ts_nop 0\n\tglobal_load_lds_dword %1, off sc1\n\ts_mov_b32 m0, %0" : "=&s"(sv) : "v"(g), "s"(lds_base) : "memory");
}
typedef __amdgpu_buffer_rsrc_t rsrc_t;
DI u32x4 ldc16(rsrc_t rs, unsigned voff, unsigned soff) { return __builtin_amdgcn_raw_buffer_load_b128(rs, voff, soff, 16); }
DI unsigned ldc4(rsrc_t rs, unsigned voff, unsigned soff) { return __builtin_amdgcn_raw_buffer_load_b32(rs, voff, soff, 16); }
DI void stc16(rsrc_t rs, unsigned voff, unsigned soff, u32x4 v) { __builtin_amdgcn_raw_buffer_store_b128(v, rs, voff, soff, 16); asm volatile("s_nop 1" ::"v"(v) : "memory"); }
DI void stc8(rsrc_t rs, unsigned voff, unsigned soff, u32x2 v) { __builtin_amdgcn_raw_buffer_store_b64(v, rs, voff, soff, 16); }
DI void stc4(rsrc_t rs, unsigned voff, unsigned soff, unsigned v) { __builtin_amdgcn_raw_buffer_store_b32(v, rs, voff, soff, 16); }
DI f32x4 ldc16f(rsrc_t rs, unsigned voff, unsigned soff) { return __builtin_bit_cast(f32x4, ldc16(rs, voff, soff)); }
DI void arrive(char* ws, int c, int lane) { if (lane == 0) __hip_atomic_fetch_add((GAS unsigned*)(ws + OFF_CNT + c * 256), 1u, __ATOMIC_RELAXED, __HIP_MEMORY_SCOPE_AGENT); }
DI void poll_ge(char* ws, int c, unsigned thr) {
  const GAS unsigned* p = (const GAS unsigned*)(ws + OFF_CNT + c * 256);
#pragma nounroll
  for (unsigned spin = 0; spin < (1u << 19); ++spin) {
    if (__all(__hip_atomic_load(p, __ATOMIC_RELAXED, __HIP_MEMORY_SCOPE_AGENT) >= thr)) break;
    __builtin_amdgcn_s_sleep(1);
  }
  asm volatile("" ::: "memory");
}
DI int fresh_s(int v) { asm volatile("" : "+s"(v)); return v; }
DI int fresh_tid() { int t = threadIdx.x; asm volatile("" : "+v"(t)); return t; }
DI void l2touch(const void* g, unsigned lds_scratch) {
  unsigned sv;
  asm volatile("s_mov_b32 %0, m0\n\ts_mov_b32 m0, %2\n\ts_nop 0\n\tglobal_load_lds_dword %1, off\n\ts_mov_b32 m0, %0" : "=&s"(sv) : "v"(g), "s"(lds_scratch) : "memory");
}
#define LDNT(p) __builtin_nontemporal_load(p)
DI float silu_bf(u16 c) { const float f = __uint_as_float((unsigned)c << 16); const float v = f / (1.f + __expf(-f)); return __uint_as_float(__builtin_bit_cast(unsigned, __builtin_convertvector((f32x2){v, v}, bf2_t)) << 16); }
DI unsigned lds_addr(const void* p) { return (unsigned)(uintptr_t)(const LAS void*)p; }
DI float xhalf(float v) {
  auto rr = __builtin_amdgcn_permlane32_swap(__float_as_uint(v), __float_as_uint(v), false, false);
  return __uint_as_float((threadIdx.x & 32) ? rr[0] : rr[1]);
}
DI float sum_g(float v) {
  auto a = __builtin_amdgcn_permlane16_swap(__float_as_uint(v), __float_as_uint(v), false, false);
  v = __uint_as_float(a[0]) + __uint_as_float(a[1]);
  auto b = __builtin_amdgcn_permlane32_swap(__float_as_uint(v), __float_as_uint(v), false, false);
  return __uint_as_float(b[0]) + __uint_as_float(b[1]);
}
template <int CTRL> DI float dpp_add(float v) { return v + __uint_as_float(__builtin_amdgcn_update_dpp(0u, __float_as_uint(v), CTRL, 0xf, 0xf, true)); }
DI float sum16(float v) {
  v = dpp_add<0x128>(v); v = dpp_add<0x124>(v); v = dpp_add<0x122>(v); return dpp_add<0x121>(v);
}
DI u32x4 modulate8(u32x4 v, f32x4 sc0, f32x4 sc1, f32x4 sh0, f32x4 sh1) {
  unsigned o0, o1, o2, o3; float t0, t1, t2, t3;
  asm volatile(
      "v_fma_mix_f32 %0, %8, %12, %20 op_sel_hi:[1,0,0]\n\tv_fma_mix_f32 %4, %8, %13, %21 op_sel:[1,0,0] op_sel_hi:[1,0,0]\n\t"
      "v_fma_mix_f32 %1, %9, %14, %22 op_sel_hi:[1,0,0]\n\tv_fma_mix_f32 %5, %9, %15, %23 op_sel:[1,0,0] op_sel_hi:[1,0,0]\n\t"
      "v_fma_mix_f32 %2, %10, %16, %24 op_sel_hi:[1,0,0]\n\tv_fma_mix_f32 %6, %10, %17, %25 op_sel:[1,0,0] op_sel_hi:[1,0,0]\n\t"
      "v_fma_mix_f32 %3, %11, %18, %26 op_sel_hi:[1,0,0]\n\tv_fma_mix_f32 %7, %11, %19, %27 op_sel:[1,0,0] op_sel_hi:[1,0,0]\n\t"
      "v_cvt_pk_bf16_f32 %0, %0, %4\n\tv_cvt_pk_bf16_f32 %1, %1, %5\n\tv_cvt_pk_bf16_f32 %2, %2, %6\n\tv_cvt_pk_bf16_f32 %3, %3, %7"
      : "=&v"(o0), "=&v"(o1), "=&v"(o2), "=&v"(o3), "=&v"(t0), "=&v"(t1), "=&v"(t2), "=&v"(t3)
      : "v"(v[0]), "v"(v[1]), "v"(v[2]), "v"(v[3]), "v"(sc0[0]), "v"(sc0[1]), "v"(sc0[2]), "v"(sc0[3]), "v"(sc1[0]), "v"(sc1[1]), "v"(sc1[2]), "v"(sc1[3]),
        "v"(sh0[0]), "v"(sh0[1]), "v"(sh0[2]), "v"(sh0[3]), "v"(sh1[0]), "v"(sh1[1]), "v"(sh1[2]), "v"(sh1[3]));
  return (u32x4){o0, o1, o2, o3};
}
DI float ln_den(float var) { return rbf(__builtin_amdgcn_sqrtf(rbf(rbf(var) + 1e-5f))); }
DI int unit0_of(int j) { return j < 6 ? 7 * j : 42 + 6 * (j - 6); }

DI void K0ST(char* p, u32x4 v) { *(u32x4*)p = v; }
constexpr int K0_MOD = 240, K0_LN = 128, K0_RL = 240, K0_WO = 50, K0_INIT = 12, K0_WARM = 24;
constexpr int K0_GRID = K0_WARM + K0_MOD + K0_LN + K0_RL + K0_WO + K0_INIT;

__global__ __launch_bounds__(256) void k0_prep(const u16* __restrict__ x, const u16* __restrict__ wqkv, const u16* __restrict__ wo, const u16* __restrict__ cvec,
                                               const u16* __restrict__ wmod, char* __restrict__ ws) {
  __shared__ __attribute__((aligned(16))) char smem[4 * 4608];
  const int tid = threadIdx.x, lane = tid & 63, wid = __builtin_amdgcn_readfirstlane(tid >> 6);
  int b = blockIdx.x;
  if (b < K0_WARM) {
    u64 pc; unsigned len;
    asm volatile("s_getpc_b64 s[30:31]\n\ts_add_u32 s30, s30, k_mega@rel32@lo+4\n\ts_addc_u32 s31, s31, k_mega@rel32@hi+12\n\ts_mov_b64 %0, s[30:31]\n\t"
                 "s_mov_b32 %1, k_mega_code_end-k_mega" : "=s"(pc), "=s"(len) :: "scc", "s30", "s31");
    l2touch((const char*)pc + min(((b >> 3) * 256 + tid) * 128u, len - 4u), lds_addr(smem) + wid * 256);
    return;
  }
  b -= K0_WARM;
  if (b < K0_MOD) {
    const int cg = b >> 2, ks = b & 3, c = tid & 7, row0 = ks * 160 + (tid >> 3), col0 = cg * 64;
    u32x4 w[5]; u16 cv[5];
#pragma unroll
    for (int i = 0; i < 5; ++i) { w[i] = LDNT((const u32x4*)(wmod + (size_t)(row0 + 32 * i) * 3840 + col0 + c * 8)); cv[i] = cvec[row0 + 32 * i]; }
    float acc[8] = {0, 0, 0, 0, 0, 0, 0, 0};
#pragma unroll
    for (int i = 0; i < 5; ++i) {
      const float s = silu_bf(cv[i]);
#pragma unroll
      for (int e = 0; e < 4; ++e) { acc[2 * e] += s * bflo(w[i][e]); acc[2 * e + 1] += s * bfhi(w[i][e]); }
    }
#pragma unroll
    for (int e = 0; e < 8; ++e) acc[e] = sum_g(dpp_add<0x128>(acc[e]));
    float* red = (float*)smem;
    if (lane < 8) { *(f32x4*)(red + wid * 64 + lane * 8) = (f32x4){acc[0], acc[1], acc[2], acc[3]}; *(f32x4*)(red + wid * 64 + lane * 8 + 4) = (f32x4){acc[4], acc[5], acc[6], acc[7]}; }
    __syncthreads();
    if (tid < 64) ((float*)(ws + OFF_MODP))[ks * 3840 + cg * 64 + tid] = (red[tid] + red[64 + tid]) + (red[128 + tid] + red[192 + tid]);
    return;
  }
  b -= K0_MOD;
  if (b < K0_LN) {
    b = (b & 7) * 16 + (b >> 3);
    const int row = b * 16 + wid * 4 + (lane >> 4), c = lane & 15;
    const u16* xr = x + (size_t)row * D_ + c * 8;
    u32x4 v[5];
#pragma unroll
    for (int i = 0; i < 5; ++i) v[i] = *(const u32x4*)(xr + i * 128);
    float s = 0.f, q = 0.f;
#pragma unroll
    for (int i = 0; i < 5; ++i)
#pragma unroll
      for (int e = 0; e < 4; ++e) s += bflo(v[i][e]) + bfhi(v[i][e]);
    const float mean = sum16(s) * (1.f / 640.f);
#pragma unroll
    for (int i = 0; i < 5; ++i)
#pragma unroll
      for (int e = 0; e < 4; ++e) { const float a = bflo(v[i][e]) - mean, d = bfhi(v[i][e]) - mean; q += a * a + d * d; }
    const float var = sum16(q) * (1.f / 640.f);
    const float rinv = __builtin_amdgcn_rcpf(ln_den(var)), mb = rbf(mean);
    char* dst = ws + OFF_XHAT + (size_t)row * 1280 + c * 16;
#pragma unroll
    for (int i = 0; i < 5; ++i) {
      u32x4 o;
#pragma unroll
      for (int e = 0; e < 4; ++e) o[e] = pack2h((bflo(v[i][e]) - mb) * rinv, (bfhi(v[i][e]) - mb) * rinv);
      K0ST(dst + i * 256, o);
    }
    return;
  }
  b -= K0_LN;
  if (b < K0_RL) {
    const int kt = b / 12, G = b % 12, sec = G >> 2, gq = G & 3;
    u32x4 v[3];
#pragma unroll
    for (int i = 0; i < 3; ++i) { const int ci = min(i * 256 + tid, 639); v[i] = LDNT((const u32x4*)(wqkv + (size_t)(kt * 32 + ci / 20) * NQKV + G * 160 + (ci % 20) * 8)); }
#pragma unroll
    for (int i = 0; i < 3; ++i) { const int ci = min(i * 256 + tid, 639); *(u32x4*)(smem + (ci / 20) * 336 + (ci % 20) * 16) = v[i]; }
    __syncthreads();
    const int g = lane >> 4, q = (lane & 15) >> 2, p = lane & 3;
    for (int f = wid; f < 10; f += 4) {
      const int t = f >> 1, x2 = f & 1;
      const int col = sec == 2 ? 16 * f + 4 * p : t < 4 ? 20 * (2 * t + (p >> 1)) + 8 * (p & 1) + 4 * x2 : 20 * (4 * x2 + p) + 16;
      const int T = sec == 2 ? 40 + gq * 5 + t : sec * 20 + (t < 4 ? 4 * gq + t : 16 + gq);
      const char* pp = smem + (8 * g + q) * 336 + col * 2;
      const bf16x4 lo = lds_tr(pp), hi = lds_tr(pp + 4 * 336);
      K0ST(ws + OFF_WQKV + (((size_t)T * 20 + kt) * 2 + x2) * 1024 + lane * 16, __builtin_bit_cast(u32x4, (bf16x8)__builtin_shufflevector(lo, hi, 0, 1, 2, 3, 4, 5, 6, 7)));
    }
    return;
  }
  b -= K0_RL;
  if (b < K0_WO) {
    const int job = b * 4 + wid, kt = job / 10, ng = job % 10, j8 = lane >> 3;
    char* t = smem + wid * 4608;
    u32x4 v[4];
#pragma unroll
    for (int i = 0; i < 4; ++i) {
      const int c = 4 * kt + i, row = c < 64 ? 20 * (c >> 1) + 4 * (c & 1) + (j8 & 3) + 8 * (j8 >> 2) : 20 * (2 * (c - 64) + (j8 >> 2)) + 16 + (j8 & 3);
      v[i] = LDNT((const u32x4*)(wo + (size_t)row * D_ + ng * 64 + (lane & 7) * 8));
    }
#pragma unroll
    for (int i = 0; i < 4; ++i) *(u32x4*)(t + (i * 8 + j8) * 144 + (lane & 7) * 16) = v[i];
    const char* pp = t + (8 * (lane >> 4) + ((lane & 15) >> 2)) * 144 + (lane & 3) * 8;
#pragma unroll
    for (int f = 0; f < 4; ++f) {
      const bf16x4 lo = lds_tr(pp + f * 32), hi = lds_tr(pp + 4 * 144 + f * 32);
      K0ST(ws + OFF_WO + ((size_t)(ng * 4 + f) * 20 + kt) * 1024 + lane * 16, __builtin_bit_cast(u32x4, (bf16x8)__builtin_shufflevector(lo, hi, 0, 1, 2, 3, 4, 5, 6, 7)));
    }
    return;
  }
  b -= K0_WO;
  {
    u32x4* p = (u32x4*)(ws + (b < 8 ? OFF_XCH + b * 16384 : OFF_CNT + (b - 8) * 16384)) + tid;
    const unsigned f = b < 8 ? ~0u : 0u;
#pragma unroll
    for (int i = 0; i < 4; ++i) p[i * 256] = (u32x4){f, f, f, f};
  }
}

constexpr float QSCALE = 0.22360679774997896f * 1.4426950408889634f;
constexpr int K1_W = 3, K1_WB = 5, K1_WB0 = 2;

template <bool ISV, class FIN>
DI void k1_mma(const char* __restrict__ bsrc, const char* smem, int lane, FIN&& fin) {
  f32x4 acc[8][2];
  constexpr int PD = 4;
  bf16x8 bfr[K1_WB + 1][4], af[PD + 1][2];
  const char* lds[2];
#pragma unroll
  for (int k = 0; k < 2; ++k) lds[k] = smem + k * 8192 + (lane ^ (4 * k + (lane >> 4))) * 16;
#pragma unroll
  for (int i = 0; i < 4 * K1_WB0; ++i) bfr[i >> 2][i & 3] = *(const bf16x8*)(bsrc + i * 1024);
#pragma unroll
  for (int m = 0; m < 8; ++m) { acc[m][0] = (f32x4){}; acc[m][1] = (f32x4){}; }
  BAR();
#pragma unroll
  for (int i = 4 * K1_WB0; i < 4 * K1_WB; ++i) bfr[i >> 2][i & 3] = *(const bf16x8*)(bsrc + i * 1024);
  BAR();
  __builtin_amdgcn_s_setprio(1);
#define K1_RD(t, k) (af[(t) % (PD + 1)][k] = *(const bf16x8*)(lds[k] + (((t) >> 3) % 3) * 16384 + ((t) & 7) * 1024))
#define K1_MM(c, b, a) (c = ISV ? MFMA16(a, b, c) : MFMA16(b, a, c))
#pragma unroll
  for (int t = 0; t < PD; ++t) { K1_RD(t, 0); K1_RD(t, 1); }
  SCHED();
  sfor<80>([&](auto T) __attribute__((always_inline)) {
    constexpr int t = T, s = t >> 3, m = t & 7, nx = t + PD;
    if constexpr (nx < 80 && (nx & 7) == 0) asm volatile("s_barrier" ::: "memory");
    if constexpr (nx < 80) K1_RD(nx, 0);
    K1_MM(acc[m][0], bfr[s % (K1_WB + 1)][0], af[t % (PD + 1)][0]); K1_MM(acc[m][0], bfr[s % (K1_WB + 1)][2], af[t % (PD + 1)][1]); PIN(acc[m][0]);
    SCHED();
    if constexpr (nx < 80) K1_RD(nx, 1);
    if constexpr (s + K1_WB < 10 && (m & 1)) bfr[(s + K1_WB) % (K1_WB + 1)][m >> 1] = *(const bf16x8*)(bsrc + ((s + K1_WB) * 4 + (m >> 1)) * 1024);
    K1_MM(acc[m][1], bfr[s % (K1_WB + 1)][1], af[t % (PD + 1)][0]); K1_MM(acc[m][1], bfr[s % (K1_WB + 1)][3], af[t % (PD + 1)][1]); PIN(acc[m][1]);
    SCHED();
    if constexpr (t >= 74) { fin(t - 74, acc[t - 74]); SCHED(); }
  });
  fin(6, acc[6]); fin(7, acc[7]);
  __builtin_amdgcn_s_setprio(0);
}


DI void phase_qkv(char* __restrict__ ws, rsrc_t rs, char* smem, int wid) {
  const int tid = fresh_tid(), lane = tid & 63;
  const int xcd = blockIdx.x & 7, idx = blockIdx.x >> 3;
  int ctr;
  if ((xcd & 1) && idx >= 28) {
    ctr = C_MOD;
    if (tid < 160) {
      const int col = 1280 + ((xcd >> 1) * 4 + idx - 28) * 160 + tid;
      const float* pp = (const float*)(ws + OFF_MODP) + col;
      float v = rbf((pp[0] + pp[3840]) + (pp[7680] + pp[11520]));
      if (col / 640 == 4) v = rbf(1.f + v);
      stc4(rs, col * 4, OFF_MOD, __float_as_uint(v));
    }
  } else {
    const int mt = (xcd >> 1) * 4 + (idx & 3), nt = (xcd & 1) * 8 + (idx >> 2);
    ctr = C_QKV + nt;
    if (wid < 4) {
      float* cst = (float*)(smem + 49152);
      const int c = lane & 7, rr = lane >> 3;
      const char* src = ws + OFF_XHAT + (size_t)(mt * 128 + wid * 32 + rr) * 1280 + c * 16;
      char* wdst = smem + (c >> 2) * 8192 + wid * 2048 + (c & 3) * 256 + (rr ^ c) * 16;
      u32x4 a[10][4];
      f32x4 c0[4], c1[4];
#pragma unroll
      for (int i = 0; i < 4; ++i) { c0[i] = *(const f32x4*)(ws + OFF_MODP + i * 15360 + tid * 16); if (wid == 0) c1[i] = *(const f32x4*)(ws + OFF_MODP + i * 15360 + 4096 + tid * 16); }
#pragma unroll
      for (int s = 0; s < K1_W; ++s)
#pragma unroll
        for (int m = 0; m < 4; ++m) a[s][m] = *(const u32x4*)(src + m * 10240 + s * 128);
      {
        f32x4 v = (c0[0] + c0[1]) + (c0[2] + c0[3]); const float one = tid >= 160 ? 1.f : 0.f;
        *(f32x4*)(cst + tid * 4) = (f32x4){rbf(one + rbf(v[0])), rbf(one + rbf(v[1])), rbf(one + rbf(v[2])), rbf(one + rbf(v[3]))};
        if (wid == 0) { v = (c1[0] + c1[1]) + (c1[2] + c1[3]); *(f32x4*)(cst + 1024 + tid * 4) = (f32x4){rbf(1.f + rbf(v[0])), rbf(1.f + rbf(v[1])), rbf(1.f + rbf(v[2])), rbf(1.f + rbf(v[3]))}; }
      }
      BAR();
      const float* cp = cst + c * 8;
      f32x4 kc[10][4];
#define K1_KC(s) do { kc[s][0] = *(const f32x4*)(cp + (s) * 64); kc[s][1] = *(const f32x4*)(cp + (s) * 64 + 4); kc[s][2] = *(const f32x4*)(cp + (s) * 64 + 640); kc[s][3] = *(const f32x4*)(cp + (s) * 64 + 644); } while (0)
#pragma unroll
      for (int s = 0; s < 6; ++s) K1_KC(s);
      SCHED();
      u32x4 o[2][4];
#define K1_XF(s, m) (o[(s) & 1][m] = modulate8(a[s][m], kc[s][2], kc[s][3], kc[s][0], kc[s][1]))
#define K1_WR(s, m) (*(u32x4*)(wdst + ((s) % 3) * 16384 + ((m) >> 1) * 1024 + ((m) & 1) * 128) = o[(s) & 1][m])
#pragma unroll
      for (int m = 0; m < 4; ++m) { K1_XF(0, m); K1_WR(0, m); }
      BAR();
#pragma unroll
      for (int m = 0; m < 4; ++m) a[K1_W][m] = *(const u32x4*)(src + m * 10240 + K1_W * 128);
#pragma unroll
      for (int m = 0; m < 4; ++m) K1_XF(1, m);
      SCHED();
#pragma unroll
      for (int s = 1; s < 10; ++s) {
#pragma unroll
        for (int m = 0; m < 4; ++m) {
          if (m == 0) K1_WR(s, 0);
          if (m < 3) K1_WR(s, m + 1);
          if (s + K1_W < 10) a[s + K1_W][m] = *(const u32x4*)(src + m * 10240 + (s + K1_W) * 128);
          if (s < 5) kc[s + 5][m] = *(const f32x4*)(cp + (s + 5) * 64 + (m & 1) * 4 + (m >> 1) * 640);
          SCHED();
          if (s < 9) K1_XF(s + 1, m);
          SCHED();
        }
        asm volatile("" :: "v"(o[s & 1][0]), "v"(o[s & 1][1]), "v"(o[s & 1][2]), "v"(o[s & 1][3]));
        BAR();
      }
    } else {
      const int cw = wid - 4;
      const char* bsrc = ws + OFF_WQKV + (size_t)(nt * 4 + cw) * 40 * 1024 + lane * 16;
      const int r = lane & 15, g = lane >> 4;
      if (nt >= 10) {
        const unsigned so = OFF_KV + (mt >> 1) * 20480 + 10240 + (mt & 1) * 5120;
        unsigned vo[2];
#pragma unroll
        for (int x = 0; x < 2; ++x) { const unsigned c = (nt - 10) * 128 + cw * 32 + x * 16 + r; vo[x] = c / 20 * 163840u + ((g & 1) * 20 + c % 20) * 16 + (g >> 1) * 8; }
        k1_mma<true>(bsrc, smem, lane, [&](int m, const f32x4 (&a)[2]) __attribute__((always_inline)) {
#pragma unroll
          for (int x = 0; x < 2; ++x) stc8(rs, vo[x] + (m & 3) * 640, so + (m >> 2) * 2560, (u32x2){pack2(a[x][0], a[x][1]), pack2(a[x][2], a[x][3])});
        });
      } else {
        const bool isQ = nt < 5; const float sc = isQ ? QSCALE : 1.f;
        const unsigned so = isQ ? OFF_Q + mt * 5120 : OFF_KV + (mt >> 1) * 20480 + (mt & 1) * 5120, hs = isQ ? 81920u : 163840u;
        const int n5 = isQ ? nt : nt - 5;
        const bool wide = n5 < 4;
        const unsigned vo = wide ? (2 * (n5 * 4 + cw) + (g >> 1)) * hs + (g & 1) * 512 + r * 16 : (8 * cw + g) * hs + 1024 + r * 8;
        const f32x4 sc4 = (f32x4){sc, sc, sc, sc};
        k1_mma<false>(bsrc, smem, lane, [&](int m, const f32x4 (&a)[2]) __attribute__((always_inline)) {
          const f32x4 v0 = mul4s(a[0], sc4), v1 = mul4s(a[1], sc4);
          const u32x4 w = (u32x4){pack2(v0[0], v0[1]), pack2(v0[2], v0[3]), pack2(v1[0], v1[1]), pack2(v1[2], v1[3])};
          if (wide) stc16(rs, vo + (m & 1) * 256, so + (m >> 1) * 1280, w);
          else { stc8(rs, vo + (m & 1) * 128, so + (m >> 1) * 1280, (u32x2){w[0], w[1]}); stc8(rs, vo + (m & 1) * 128, so + 4 * hs + (m >> 1) * 1280, (u32x2){w[2], w[3]}); }
        });
      }
    }
  }
  waitv<0>();
  BAR();
  if (wid == 7 && lane < 8) __hip_atomic_fetch_add((GAS unsigned*)(ws + OFF_CNT + (lane * 16 + ctr) * 256), 1u, __ATOMIC_RELAXED, __HIP_MEMORY_SCOPE_AGENT);
  if (ctr == C_MOD && wid == 7) { __builtin_amdgcn_s_sleep(127); __builtin_amdgcn_s_sleep(60); }
}

constexpr int proj_pos(int f) { return 1024 + f * 1024; }
template <bool FINAL> DI u32x4 proj_ld(rsrc_t rs, int i, int wid, int lane) {
  constexpr int NF = FINAL ? 135 : 100;
  return ldc16(rs, lane * 16, (FINAL ? OFF_WOUT : OFF_WO) + (((blockIdx.x >> 3) & 7) * NF + (i * 8 + 7 < NF ? i * 8 + wid : min(i * 8 + wid, NF - 1))) * 1024);
}
template <bool FINAL> DI void proj_small(rsrc_t rs, const u16* __restrict__ x, int wid, int lane, u32x2 (&res)[3], f32x4 (&gate)[3], f32x4 (&cbv)[3]) {
  const int idx = blockIdx.x >> 3, mt = ((blockIdx.x & 7) * 4 + (idx >> 3)) * 4 + (wid & 3), g = lane >> 4;
#pragma unroll
  for (int i = 0; i < 3; ++i) {
    const int n = ((idx & 7) * 5 + min((wid >> 2) * 3 + i, 4)) * 16;
    if (!FINAL) { res[i] = *(const u32x2*)(x + (size_t)(mt * 16 + (lane & 15)) * D_ + n + g * 4); cbv[i] = ldc16f(rs, g * 16, OFF_MOD + (4 * 640 + n) * 4); }
    gate[i] = ldc16f(rs, g * 16, OFF_MOD + ((FINAL ? 5 : 2) * 640 + n) * 4);
  }
}
DI void proj_park(char* smem, int i, int tid, u32x4 v) { *(u32x4*)(smem + proj_pos(i * 8) + tid * 16) = v; }

DI void split3(float m, unsigned& w_h0, unsigned& w_h1) {
  const float n = -m; const float a = rbf(n), b = rbf(n - a), c = rbf(n - a - b);
  w_h0 = pack2(a, b); w_h1 = pack2(c, 0.f);
}

DI void phase_attn(char* __restrict__ ws, rsrc_t rs, const char* __restrict__ wsp, const u16* __restrict__ win, const u16* __restrict__ wout, const u16* __restrict__ x,
                   char* smem, int wid, u32x2 (&res)[3], f32x4 (&gate)[3], f32x4 (&cbv)[3]) {
  const int tid = fresh_tid(), lane = tid & 63;
  const int xcd = blockIdx.x & 7, idx = blockIdx.x >> 3, head = xcd * 4 + (idx >> 3), qb = idx & 7;
  const int r = lane & 31, h = lane >> 5, qg = wid & 3, kh = wid >> 2;
  unsigned kvo = OFF_KV + head * 163840, qo = OFF_Q + head * 81920 + (qb * 8 + qg * 2) * 1280;
  const unsigned kconst = h == 0 ? 0x3f803f80u : 0x00003f80u;
  const unsigned so16 = (tid & 255) * 16, l16 = lane * 16, l2 = 1024 + r * 8 + h * 4;
  u32x4 wq[13];
  for (int attempt = 0; attempt < 2; ++attempt) {
    asm volatile("" : "+s"(kvo), "+s"(qo));
    const char* kvs = ws + kvo + kh * 81920 + so16;
    const unsigned ldst = lds_addr(smem) + kh * 81920 + qg * 1024;
    u32x4 qA1w, qB1w, k1w; unsigned qA2w, qB2w, k2, m0sv;
    const char* d0 = kvs + (wid == 0 ? 4096 : 0); const char* d1 = kvs + 4096; const char* d2 = kvs + 8192; const char* d3 = kvs + 12288; const char* d4 = kvs + 16384;
    PIN(d0); PIN(d1); PIN(d2); PIN(d3); PIN(d4);
    if (attempt == 0) {
      if (wid == 7) {
        const int l7 = lane & 7;
        const int c = l7 == 0 ? head >> 3 : l7 == 1 ? 4 : l7 == 2 ? 5 + (head >> 3) : l7 == 3 ? 9 : l7 == 4 ? 10 + (20 * head) / 128 : l7 == 5 ? 10 + (20 * head + 19) / 128 : C_MOD;
        poll_ge(ws, xcd * 16 + c, 16);
      }
      BAR();
    }
    asm volatile(
        "s_nop 4\n\tbuffer_load_dwordx4 %0, %7, %9, %19 offen sc1\n\tbuffer_load_dwordx4 %1, %7, %9, %10 offen sc1\n\tbuffer_load_dwordx4 %2, %7, %9, %11 offen sc1\n\t"
        "buffer_load_dword %3, %8, %9, %19 offen sc1\n\tbuffer_load_dword %4, %8, %9, %10 offen sc1\n\tbuffer_load_dword %5, %8, %9, %11 offen sc1\n\t"
        "s_mov_b32 %6, m0\n\ts_mov_b32 m0, %17\n\ts_nop 0\n\tglobal_load_lds_dwordx4 %12, off sc1\n\t"
        "s_add_u32 m0, %18, 0x1000\n\ts_nop 0\n\tglobal_load_lds_dwordx4 %13, off sc1\n\t"
        "s_add_u32 m0, %18, 0x2000\n\ts_nop 0\n\tglobal_load_lds_dwordx4 %14, off sc1\n\t"
        "s_add_u32 m0, %18, 0x3000\n\ts_nop 0\n\tglobal_load_lds_dwordx4 %15, off sc1\n\t"
        "s_add_u32 m0, %18, 0x4000\n\ts_nop 0\n\tglobal_load_lds_dwordx4 %16, off sc1\n\t"
        "s_mov_b32 m0, %6\n\ts_waitcnt vmcnt(5)"
        : "=&v"(qA1w), "=&v"(qB1w), "=&v"(k1w), "=&v"(qA2w), "=&v"(qB2w), "=&v"(k2), "=&s"(m0sv)
        : "v"(l16), "v"(l2), "s"(rs), "s"(qo + kh * 1280), "s"(kvo), "v"(d0), "v"(d1), "v"(d2), "v"(d3), "v"(d4),
          "s"(ldst + (wid == 0 ? 4096 : 0)), "s"(ldst), "s"(qo + (kh ^ 1) * 1280)
        : "memory", "scc");
    if (wid == 0) { const unsigned f = (fresh_tid() & 1) ? 0u : 0x3f803f80u; *(u32x4*)(smem + l16) = (u32x4){f, f, f, f}; }
    const bf16x8 qA1 = BF8(qA1w), qB1 = BF8(qB1w);
    bf16x8 k1 = BF8(k1w);
    float mA = -INFINITY, mB = -INFINITY;
    {
      const bf16x4 qA2 = BF4(((u32x2){qA2w, 0u})), qB2 = BF4(((u32x2){qB2w, 0u}));
      const int nt = attempt ? 64 : 1;
      bf16x8 t1 = k1; unsigned t2 = k2;
      for (int t = 0; t < nt; ++t) {
        if (t) { const unsigned to = kvo + (t >> 3) * 20480 + (t & 7) * 1280; t1 = BF8(ldc16(rs, l16, to)); t2 = ldc4(rs, l2, to); }
        const bf16x4 t2v = BF4(((u32x2){t2, kconst}));
        f32x16 a = MFMA32(t1, qA1, (f32x16){}); a = MFMA32K8(t2v, qA2, a);
        f32x16 b = MFMA32(t1, qB1, (f32x16){}); b = MFMA32K8(t2v, qB2, b);
#pragma unroll
        for (int i = 0; i < 16; ++i) { mA = fmaxf(mA, a[i]); mB = fmaxf(mB, b[i]); }
      }
      mA = fmaxf(mA, xhalf(mA)); mB = fmaxf(mB, xhalf(mB));
    }
    unsigned a0, a1, b0, b1; split3(mA, a0, a1); split3(mB, b0, b1);
    const bf16x4 qA2 = BF4(((u32x2){qA2w, h ? a1 : a0})), qB2 = BF4(((u32x2){qB2w, h ? b1 : b0}));
    char* lh = smem + kh * 81920;
    waitv<0>();
    BAR();
    f32x16 oA = (f32x16){}, oB = (f32x16){};
    const char* kp1 = lh + l16;
    const char* kp2 = lh + l2;
    const bool vreal = r < 20;
    const char* vp = vreal ? lh + 10240 + (h * 20 + r) * 16 : smem + (r == 20 ? 0 : 16);
    const int vinc = vreal ? 1280 : 0, vjump = vreal ? 10240 : 0;
    if (kh) { k1 = *(const bf16x8*)kp1; k2 = *(const unsigned*)kp2; }
    bf16x8 kf1[2], vf[2][2]; u32x2 kf2[2];
    kf1[0] = k1; kf2[0] = (u32x2){k2, kconst}; kf2[1] = (u32x2){0u, kconst};
    vf[1][0] = (bf16x8){}; vf[1][1] = (bf16x8){};
    u32x4 pA0, pA1, pB0 = (u32x4){}, pB1 = (u32x4){};
    f32x4 e0 = (f32x4){}, e1;
    f32x16 sA = MFMA32(kf1[0], qA1, (f32x16){}), sB;
    sA = MFMA32K8(BF4(kf2[0]), qA2, sA);
#define EX2(x) __builtin_amdgcn_exp2f(x)
#define GRP(MF, S, i, EN, EP, P, w) do { MF; SCHED(); EN[0] = EX2(S[i]); P[w] = pack2(EP[0], EP[1]); EN[1] = EX2(S[i + 1]); P[w + 1] = pack2(EP[2], EP[3]); \
      EN[2] = EX2(S[i + 2]); EN[3] = EX2(S[i + 3]); PIN(P); PIN(EN); SCHED(); } while (0)
    const unsigned relsc = lds_addr(smem) + 768;
    auto group = [&](auto FIRST, int jump, int j) __attribute__((always_inline)) {
#pragma unroll
      for (int t = 0; t < 8; ++t) {
        const int c = t & 1, n = c ^ 1;
        if (FIRST) { if (t == 0) __builtin_amdgcn_s_setprio(3); if (t == 3) __builtin_amdgcn_s_setprio(2); if (t == 5) __builtin_amdgcn_s_setprio(1); if (t == 6) __builtin_amdgcn_s_setprio(0); if (t == 7) __builtin_amdgcn_s_setprio(3); }
        else {
          if (t == 4 && j == 2) __builtin_amdgcn_s_setprio(2);
          if (t == 3 && j == 3) {
            __builtin_amdgcn_s_setprio(1);
            const int wf = fresh_s(wid);
#pragma unroll
            for (int i = 0; i < 13; ++i) wq[i] = proj_ld<false>(rs, i, wf, lane);
            proj_small<false>(rs, x, wf, fresh_tid() & 63, res, gate, cbv);
          }
          if (t == 6 && j == 3) { __builtin_amdgcn_s_setprio(0); if (wid == 7) glds4c(ws + OFF_CNT + (C_REL + (lane & 7)) * 256, relsc); }
        }
        if (FIRST && t < 5) {
#pragma unroll
          for (int i = 5 + 3 * t; i < 8 + 3 * t; ++i) glds16c(kvs + i * 4096, fresh_s(ldst) + i * 4096);
        }
        if (t == 7) {
          if (FIRST) { waitv<0>(); BAR(); }
          kp1 += jump; kp2 += jump;
        }
        SCHED();
        kf1[n] = *(const bf16x8*)(kp1 + ((t + 1) & 7) * 1280); kf2[n][0] = *(const unsigned*)(kp2 + ((t + 1) & 7) * 1280);
        GRP(sB = MFMA32(kf1[c], qB1, (f32x16){}), sA, 0, e1, e0, pB1, 2);
        vf[c][0] = *(const bf16x8*)vp; vf[c][1] = *(const bf16x8*)(vp + 640); vp += vinc;
        GRP(sB = MFMA32K8(BF4(kf2[c]), qB2, sB), sA, 4, e0, e1, pA0, 0);
        GRP(oB = MFMA32(vf[n][0], BF8(pB0), oB), sA, 8, e1, e0, pA0, 2);
        GRP(oB = MFMA32(vf[n][1], BF8(pB1), oB), sA, 12, e0, e1, pA1, 0);
        GRP(sA = MFMA32(kf1[n], qA1, (f32x16){}), sB, 0, e1, e0, pA1, 2);
        GRP(sA = MFMA32K8(BF4(kf2[n]), qA2, sA), sB, 4, e0, e1, pB0, 0);
        GRP(oA = MFMA32(vf[c][0], BF8(pA0), oA), sB, 8, e1, e0, pB0, 2);
        GRP(oA = MFMA32(vf[c][1], BF8(pA1), oA), sB, 12, e0, e1, pB1, 0);
      }
      vp += vjump;
    };
    group(std::true_type{}, 20480, 0);
    const int blk = fresh_s(blockIdx.x);
    const int j2 = (blk - 135) * 4 + qg;
    const bool side = kh == 0 && attempt == 0 && j2 < 270;
    char* stile = smem + 1024 + qg * 4096;
    const char* svec = smem + 81920 + qg * 256;
    const int kt = blk < 135 ? 4 * (blk % 5) + qg : j2 / 10, ng = blk < 135 ? blk / 5 : j2 % 10;
    if (side) {
      const u16* W = blk < 135 ? win : wout; const int N = blk < 135 ? F_ : D_;
      const int lf = fresh_tid() & 63, j8 = lf >> 3;
#pragma unroll
      for (int i = 0; i < 4; ++i) {
        const int row = kt * 32 + i * 8 + j8;
        glds16(W + (size_t)row * N + ng * 64 + ((lf & 7) ^ j8) * 8, lds_addr(stile) + i * 1024);
      }
      glds4c((const float*)(ws + OFF_MOD) + (4 - h) * 640 + (kt < 20 ? kt : 0) * 32 + r, lds_addr(svec));
    }
#pragma nounroll
    for (int j = 1; j < 4; ++j) {
      if (j == 2) {
        if (kh) l2touch(wsp + ((size_t)(blk * 4 + qg) * 64 + lane) * 128, lds_addr(smem) + 256);
        else if (side) {
          waitv<0>();
          const int lf = fresh_tid() & 63, g4 = lf >> 4, r4 = lf & 15, row = 8 * g4 + (r4 >> 2);
          const f32x4 sv0 = *(const f32x4*)(svec + (r4 & 1) * 128 + g4 * 32), sv1 = *(const f32x4*)(svec + (r4 & 1) * 128 + g4 * 32 + 16);
          const bf16x8 vec = BF8(((u32x4){pack2(sv0[0], sv0[1]), pack2(sv0[2], sv0[3]), pack2(sv1[0], sv1[1]), pack2(sv1[2], sv1[3])}));
          const unsigned nks = blk < 135 ? 20 : 27, dso = (blk < 135 ? OFF_WIN : OFF_WOUT) + (ng * 4 * nks + kt) * 1024;
          bf16x8 fr[4]; f32x4 pr[4];
#pragma unroll
          for (int f = 0; f < 4; ++f) {
            const int ch = 2 * f + ((r4 & 3) >> 1);
            const bf16x4 lo = lds_tr(stile + row * 128 + (ch ^ (row & 7)) * 16 + (r4 & 1) * 8), hi = lds_tr(stile + (row + 4) * 128 + (ch ^ ((row + 4) & 7)) * 16 + (r4 & 1) * 8);
            fr[f] = __builtin_shufflevector(lo, hi, 0, 1, 2, 3, 4, 5, 6, 7);
          }
#pragma unroll
          for (int f = 0; f < 4; ++f) { stc16(rs, l16, dso + f * nks * 1024, __builtin_bit_cast(u32x4, fr[f])); pr[f] = MFMA16(fr[f], vec, (f32x4){}); }
          if (r4 < 2) {
#pragma unroll
            for (int f = 0; f < 4; ++f) *(f32x4*)(stile + 3072 + r4 * 256 + f * 64 + g4 * 16) = pr[f];
          }
        }
      }
      if (j == 3 && kh == 0 && attempt == 0) { waitv<0>(); arrive(ws, C_REL + xcd, lane); }
      group(std::false_type{}, j < 3 ? 20480 : 0, j);
    }
    pB1[2] = pack2(e0[0], e0[1]); pB1[3] = pack2(e0[2], e0[3]);
    oB = MFMA32(vf[1][0], BF8(pB0), oB); oB = MFMA32(vf[1][1], BF8(pB1), oB);
    {
      f32x4* ex = (f32x4*)(lh + 1024 + qg * 4096) + lane;
#pragma unroll
      for (int g = 0; g < 3; ++g) ex[g * 64] = (f32x4){oA[4 * g], oA[4 * g + 1], oA[4 * g + 2], oA[4 * g + 3]};
    }
    int* flag = (int*)(smem + 528);
    BAR();
    float o[12];
    {
      const f32x4* ex = (const f32x4*)(smem + (kh ^ 1) * 81920 + 1024 + qg * 4096) + lane;
#pragma unroll
      for (int g = 0; g < 3; ++g) { const f32x4 t = ex[g * 64];
#pragma unroll
        for (int e = 0; e < 4; ++e) o[4 * g + e] = oB[4 * g + e] + t[e]; }
    }
    const float lo = xhalf(o[8]); const float l = h ? o[8] : lo;
    const float rl = __builtin_amdgcn_rcpf(l);
    const int tok = qb * 256 + qg * 64 + kh * 32 + r;
    const unsigned vo = (tok >> 4) * 20480 + (tok & 15) * 16;
    stc16(rs, vo + h * 256, OFF_O + (head >> 1) * 1024 + (head & 1) * 512,
          (u32x4){pack2(o[0] * rl, o[1] * rl), pack2(o[2] * rl, o[3] * rl), pack2(o[4] * rl, o[5] * rl), pack2(o[6] * rl, o[7] * rl)});
    if (h == 0) stc8(rs, vo, OFF_O + 16384 + (head >> 3) * 1024 + ((head >> 1) & 3) * 256 + (head & 1) * 8, (u32x2){pack2(o[8] * rl, o[9] * rl), pack2(o[10] * rl, o[11] * rl)});
    if (wid == 4 && blk < 135 && attempt == 0 && lane < 32) {
      const char* pp = smem + 1024 + 3072 + lane * 16;
      const f32x4 v = (*(const f32x4*)pp + *(const f32x4*)(pp + 4096)) + (*(const f32x4*)(pp + 8192) + *(const f32x4*)(pp + 12288));
      stc16(rs, (lane >> 4) * 8192 + (lane & 15) * 16, OFF_CBP + (blk % 5) * 16384 + (blk / 5) * 256, __builtin_bit_cast(u32x4, v));
    }
    bool ok = (l > 1e-30f) && (l < 1e30f);
#pragma unroll
    for (int i = 0; i < 12; ++i) ok = ok && (fabsf(o[i]) < 3e38f);
    if (!ok && attempt == 0) *flag = 1;
    drain();
    BAR();
    const int redo = __builtin_amdgcn_readfirstlane(*(volatile LAS int*)flag);
    if (!redo) break;
    BAR();
  }
  if (wid == 7) arrive(ws, C_ATT + qb, lane);
#pragma unroll
  for (int i = 0; i < 13; ++i) proj_park(smem, i, tid, wq[i]);
  if (!__all(*(volatile LAS unsigned*)(smem + 768 + (lane & 7) * 4) >= 128u)) poll_ge(ws, C_REL + (lane & 7), 128);
}

template <bool FINAL, int KH, class HOOK>
DI void proj_body(char* __restrict__ ws, rsrc_t rs, const u16* __restrict__ x, u16* __restrict__ out, char* smem, int wid, u32x2 (&res)[3], const f32x4 (&gate)[3], const f32x4 (&cbv)[3], HOOK&& after_mma) {
  const int lane = fresh_tid() & 63;
  const int xcd = blockIdx.x & 7, idx = blockIdx.x >> 3, panel = xcd * 4 + (idx >> 3), cb = idx & 7;
  constexpr int NKS = FINAL ? 27 : 20;
  constexpr int K0 = KH ? (NKS + 1) / 2 : 0, NK = KH ? NKS / 2 : (NKS + 1) / 2, NKEEP = KH ? 2 : 3, UK = KH ? 3 : 0, UG = KH ? 0 : 3;
  const int m = wid & 3, mt = panel * 4 + m, g = lane >> 4, r = lane & 15;
  unsigned l16 = lane * 16; PIN(l16);
  const unsigned aso = fresh_s((FINAL ? OFF_U : OFF_O) + (mt * NKS + K0) * 1024);
  if (wid == 7) poll_ge(ws, FINAL ? C_U + panel : C_ATT + (panel >> 2), FINAL ? 8 : 32);
  BAR();
  bf16x8 a[NK];
#pragma unroll
  for (int k = 0; k < NK; ++k) a[k] = BF8(ldc16(rs, l16 + (k & 3) * 1024, aso + (k >> 2) * 4096));
  SCHED();
  constexpr int PD = FINAL ? 5 : 6;
  const char* bl = smem + lane * 16;
  bf16x8 bf[PD + 1][5]; f32x4 acc[5];
#pragma unroll
  for (int u = 0; u < 5; ++u) acc[u] = (f32x4){};
#pragma unroll
  for (int k = 0; k < PD; ++k)
#pragma unroll
    for (int u = 0; u < 5; ++u) bf[k][u] = *(const bf16x8*)(bl + proj_pos(u * NKS + K0 + k));
  SCHED();
#pragma unroll
  for (int k = 0; k < NK; ++k) {
#pragma unroll
    for (int u = 0; u < 5; ++u) {
      if (k + PD < NK) bf[(k + PD) % (PD + 1)][u] = *(const bf16x8*)(bl + proj_pos(u * NKS + K0 + k + PD));
      acc[u] = MFMA16(bf[k % (PD + 1)][u], a[k], acc[u]);
      SCHED();
    }
  }
  f32x4* ex = (f32x4*)(smem + proj_pos(FINAL ? 136 : 104)) + m * 5 * 64 + lane;
#pragma unroll
  for (int i = 0; i < 5 - NKEEP; ++i) ex[(UG + i) * 64] = acc[UG + i];
  f32x4 part[NKEEP];
#pragma unroll
  for (int i = 0; i < NKEEP; ++i) {
    part[i] = fma4s(gate[i], acc[UK + i], (f32x4){bflo(res[i][0]), bfhi(res[i][0]), bflo(res[i][1]), bfhi(res[i][1])});
    PIN(part[i]);
  }
  SCHED();
  after_mma();
  BAR();
  f32x4 oth[NKEEP];
#pragma unroll
  for (int i = 0; i < NKEEP; ++i) oth[i] = ex[(UK + i) * 64];
  f32x4 sv = (f32x4){}, qv = (f32x4){};
#pragma unroll
  for (int i = 0; i < NKEEP; ++i) {
    const f32x4 v = part[i] + gate[i] * oth[i];
    const u32x2 o = (u32x2){pack2(v[0], v[1]), pack2(v[2], v[3])};
    if (FINAL) *(u32x2*)(out + (size_t)(mt * 16 + r) * D_ + (cb * 5 + UK + i) * 16 + g * 4) = o;
    else {
      res[i] = o; PIN(res[i]);
      const f32x4 w = v * cbv[i]; const int un = cb * 5 + UK + i;
      stc8(rs, ((g >> 1) * 16 + r) * 16 + (g & 1) * 8, OFF_X1S + mt * 20480 + un * 512, (u32x2){pack2(w[0], w[1]), pack2(w[2], w[3])});
      sv += v; qv += v * v;
    }
  }
  if (!FINAL) {
    const float s = sum_g((sv[0] + sv[1]) + (sv[2] + sv[3])), q = sum_g((qv[0] + qv[1]) + (qv[2] + qv[3]));
    if (g == 0) stc8(rs, r * 8, OFF_ST1 + ((panel * 16 + cb * 2 + KH) * 64 + m * 16) * 8, (u32x2){__float_as_uint(s), __float_as_uint(q)});
    drain();
    BAR();
    if (wid == 7) arrive(ws, C_X1 + panel, lane);
  }
}

constexpr float GC1 = -2.8853900817779268f * 0.7978845608028654f, GC2 = GC1 * 0.044715f;
DI void gelu8(f32x4& a0, const f32x4& l0, f32x4& a1, const f32x4& l1) {
  float t[8];
#define G8(expr0, expr1) do { _Pragma("unroll") for (int i = 0; i < 4; ++i) { t[i] = opq(expr0); } _Pragma("unroll") for (int i = 0; i < 4; ++i) { t[4 + i] = opq(expr1); } SCHED(); } while (0)
  G8(a0[i] * a0[i], a1[i] * a1[i]);
  G8(__builtin_fmaf(t[i], l0[3], l0[2]), __builtin_fmaf(t[4 + i], l1[3], l1[2]));
  G8(a0[i] * t[i], a1[i] * t[4 + i]);
  G8(__builtin_amdgcn_exp2f(t[i]), __builtin_amdgcn_exp2f(t[4 + i]));
  G8(__builtin_fmaf(t[i], l0[0], l0[0]), __builtin_fmaf(t[4 + i], l1[0], l1[0]));
  G8(__builtin_amdgcn_rcpf(t[i]), __builtin_amdgcn_rcpf(t[4 + i]));
  G8(a0[i] * t[i], a1[i] * t[4 + i]);
  a0 = (f32x4){t[0], t[1], t[2], t[3]}; a1 = (f32x4){t[4], t[5], t[6], t[7]};
}
constexpr int L4_Z1 = 114688, L4_TR = L4_Z1 + 16384, L4_LN = L4_TR + 16384, L4_ST2 = L4_LN + 1024, L4_FIN = L4_ST2 + 3584, L4_CB = L4_FIN + 512, L4_CNT = L4_CB + 2048;
constexpr int K5_R = 7;
constexpr unsigned DEAD = 0x80000000u;
DI int sp_stage(int s) { const int idx = blockIdx.x >> 3; return (s + (idx & 7) * 2) & 15; }
DI unsigned sp_voff(int wid, int lane, int i) { return ((wid & 3) * 16 + i * 8 + (lane >> 3)) * 4096 + (wid >> 2) * 128 + ((lane & 7) ^ (4 * i + (lane >> 4))) * 16; }
DI const char* sp_base(const u16* wsp, int panel, int s) { return (const char*)wsp + (size_t)panel * 262144 + sp_stage(s) * 256; }
DI void sp_prefetch(u32x4 (&wr)[16 - K5_R][2], const u16* __restrict__ wsp, int panel, int wid, int lane) {
  const unsigned v0 = sp_voff(wid, lane, 0), v1 = sp_voff(wid, lane, 1);
#pragma unroll
  for (int s = K5_R; s < 16; ++s) { wr[s - K5_R][0] = *(const u32x4*)(sp_base(wsp, panel, s) + v0); wr[s - K5_R][1] = *(const u32x4*)(sp_base(wsp, panel, s) + v1); }
  asm volatile("" ::: "memory");
}
template <int S0, int S1>
DI void sp_ring(const u16* __restrict__ wsp, char* smem, int panel, int wid, int lane) {
  const unsigned v0 = sp_voff(wid, lane, 0), v1 = sp_voff(wid, lane, 1);
#pragma unroll
  for (int s = S0; s < S1; ++s) { glds16(sp_base(wsp, panel, s) + v0, lds_addr(smem) + wid * 2048 + s * 16384); glds16(sp_base(wsp, panel, s) + v1, lds_addr(smem) + wid * 2048 + s * 16384 + 1024); }
}
DI unsigned win_boff(int wid, int lane) { const int cb = (blockIdx.x >> 3) & 7; return lane * 16 + (wid == 6 && cb >= 6 ? DEAD : 0u); }
DI unsigned win_soff(int wid, bool z2) { return OFF_WIN + ((z2 ? 54 : 0) + unit0_of((blockIdx.x >> 3) & 7) + wid) * 20480; }
#define WIN_LD(so, k) BF8(ldc16(rs, bo + ((k) & 3) * 1024, (so) + ((k) >> 2) * 4096))
DI void win_prefetch(rsrc_t rs, int wid, bf16x8 (&b2)[20]) {
  const unsigned bo = win_boff(wid, fresh_tid() & 63), so = win_soff(wid, true);
#pragma unroll
  for (int k = 0; k < 20; ++k) b2[k] = WIN_LD(so, k);
}
template <class HOOK>
DI void win_mma(const char* al, const bf16x8 (&b)[20], f32x4 (&acc)[4], HOOK&& hook) {
  constexpr int PD = 4;
  bf16x8 af[PD + 1][2];
#define WIN_RD(i) do { af[(i) % (PD + 1)][0] = *(const bf16x8*)(al + ((i) & 3) * 20480 + ((i) >> 2) * 2048); af[(i) % (PD + 1)][1] = *(const bf16x8*)(al + ((i) & 3) * 20480 + ((i) >> 2) * 2048 + 1024); } while (0)
#pragma unroll
  for (int i = 0; i < PD; ++i) WIN_RD(i);
  SCHED();
  sfor<40>([&](auto I) __attribute__((always_inline)) {
    constexpr int i = I, m = i & 3, k = (i >> 2) * 2;
    if constexpr (i + PD < 40) WIN_RD(i + PD);
    acc[m] = MFMA16(b[k], af[i % (PD + 1)][0], acc[m]);
    acc[m] = MFMA16(b[k + 1], af[i % (PD + 1)][1], acc[m]); PIN(acc[m]);
    hook(I);
    SCHED();
  });
}
DI void stage_x1s(char* __restrict__ ws, char* smem, int panel, int wid, int tid) {
  const char* src = ws + OFF_X1S + (size_t)panel * 81920 + tid * 16;
#pragma unroll
  for (int i = 0; i < 10; ++i) glds16c(src + i * 8192, lds_addr(smem) + wid * 1024 + i * 8192);
}
DI void load_bb(const u16* __restrict__ bsp, int panel, int wid, int lane, u16 (&bb)[2]) { const u16* p = bsp + panel * 64 + (wid >> 2) * 32 + (lane & 15); bb[0] = p[0]; bb[1] = p[16]; asm volatile("" ::: "memory"); }
DI void win_tail(char* __restrict__ ws, const u16* __restrict__ wsp, char* smem, int panel, int cb, int wid, int lane) {
  BAR();
  if (wid == 7 && lane < 4) __hip_atomic_fetch_add((GAS unsigned*)(ws + OFF_CNT + (C_Z2 + lane * 8 + cb) * 256), 1u, __ATOMIC_RELAXED, __HIP_MEMORY_SCOPE_AGENT);
}

template <bool EARLY>
DI void win_body(char* __restrict__ ws, rsrc_t rs, const u16* __restrict__ wsp, char* smem, int wid, const bf16x8 (&b2)[20], u32x4 (&wr)[16 - K5_R][2], const u16* __restrict__ bsp, u16 (&bb)[2]) {
  const int tid = fresh_tid(), lane = tid & 63, g = lane >> 4, r = lane & 15;
  const int xcd = blockIdx.x & 7, idx = blockIdx.x >> 3, panel = xcd * 4 + (idx >> 3), cb = idx & 7, u0 = unit0_of(cb);
  float* lnp = (float*)(smem + L4_LN); float* st2 = (float*)(smem + L4_ST2); float* fin = (float*)(smem + L4_FIN); float* cbl = (float*)(smem + L4_CB);
  int* flg = (int*)(smem + L4_CNT);
  __builtin_amdgcn_s_setprio(1);
  const unsigned bo = win_boff(wid, lane), so1 = win_soff(wid, false);
  bf16x8 b1[20];
  BAR();
  stage_x1s(ws, smem, panel, wid, tid);
#pragma unroll
  for (int k = 0; k < 4; ++k) b1[k] = WIN_LD(so1, k);
  waitv<4>();
  BAR();
  load_bb(bsp, panel, wid, lane, bb);
  f32x4 ln[4], z2[4], z1[4];
#pragma unroll
  for (int m = 0; m < 4; ++m) ln[m] = *(const f32x4*)(lnp + (m * 16 + r) * 4);
  {
    const f32x4 cs2 = *(const f32x4*)(cbl + 112 + wid * 16 + g * 4), bi2 = *(const f32x4*)(cbl + 256 + 112 + wid * 16 + g * 4);
    const f32x4 cs1 = *(const f32x4*)(cbl + wid * 16 + g * 4), bi1 = *(const f32x4*)(cbl + 256 + wid * 16 + g * 4);
#pragma unroll
    for (int m = 0; m < 4; ++m) {
#pragma unroll
      for (int e = 0; e < 4; ++e) { z2[m][e] = opq(__builtin_fmaf(ln[m][1], cs2[e], opq(ln[m][0] * bi2[e]))); z1[m][e] = opq(__builtin_fmaf(ln[m][1], cs1[e], opq(ln[m][0] * bi1[e]))); }
      PIN(z1[m]);
    }
  }
  win_mma(smem + lane * 16, b2, z2, [&](auto I) __attribute__((always_inline)) { constexpr int i = I; if constexpr (i < 32 && (i & 1)) b1[4 + i / 2] = WIN_LD(so1, 4 + i / 2); });
  gelu8(z2[0], ln[0], z2[1], ln[1]); gelu8(z2[2], ln[2], z2[3], ln[3]);
  {
    float s[4], q[4];
#pragma unroll
    for (int m = 0; m < 4; ++m) { s[m] = opq(opq(z2[m][0] + z2[m][1]) + opq(z2[m][2] + z2[m][3])); q[m] = opq(__builtin_fmaf(z2[m][0], z2[m][0], opq(z2[m][1] * z2[m][1])) + opq(__builtin_fmaf(z2[m][2], z2[m][2], opq(z2[m][3] * z2[m][3])))); }
    auto pa = __builtin_amdgcn_permlane32_swap(__float_as_uint(s[0]), __float_as_uint(s[2]), false, false);
    auto pb = __builtin_amdgcn_permlane32_swap(__float_as_uint(s[1]), __float_as_uint(s[3]), false, false);
    auto pc = __builtin_amdgcn_permlane32_swap(__float_as_uint(q[0]), __float_as_uint(q[2]), false, false);
    auto pd = __builtin_amdgcn_permlane32_swap(__float_as_uint(q[1]), __float_as_uint(q[3]), false, false);
    const float sa = opq(__uint_as_float(pa[0]) + __uint_as_float(pa[1])), sb = opq(__uint_as_float(pb[0]) + __uint_as_float(pb[1]));
    const float qa = opq(__uint_as_float(pc[0]) + __uint_as_float(pc[1])), qb = opq(__uint_as_float(pd[0]) + __uint_as_float(pd[1]));
    auto ps = __builtin_amdgcn_permlane16_swap(__float_as_uint(sa), __float_as_uint(sb), false, false);
    auto pq = __builtin_amdgcn_permlane16_swap(__float_as_uint(qa), __float_as_uint(qb), false, false);
    *(f32x2*)(st2 + (wid * 64 + lane) * 2) = (f32x2){opq(__uint_as_float(ps[0]) + __uint_as_float(ps[1])), opq(__uint_as_float(pq[0]) + __uint_as_float(pq[1]))};
  }
  asm volatile("" ::: "memory");
  if (lane == 0) __hip_atomic_fetch_add(flg, 1, __ATOMIC_RELAXED, __HIP_MEMORY_SCOPE_WORKGROUP);
  asm volatile("" ::: "memory");
  __builtin_amdgcn_s_setprio(0);
  const unsigned wvo[2] = {sp_voff(wid, lane, 0), sp_voff(wid, lane, 1)};
  win_mma(smem + lane * 16, b1, z1, [&](auto I) __attribute__((always_inline)) {
    constexpr int i = I, j = i < 8 ? i : (i & 1) ? -1 : 4 + i / 2;
    if constexpr (EARLY && j >= 0 && j < 18) wr[j >> 1][j & 1] = *(const u32x4*)(sp_base(wsp, panel, K5_R + (j >> 1)) + wvo[j & 1]);
  });
  gelu8(z1[0], ln[0], z1[1], ln[1]); gelu8(z1[2], ln[2], z1[3], ln[3]);
#pragma unroll
  for (int mm = 0; mm < 2; ++mm)
    *(u32x4*)(smem + L4_Z1 + wid * 2048 + mm * 1024 + lane * 16) = (u32x4){pack2(z1[2 * mm][0], z1[2 * mm][1]), pack2(z1[2 * mm][2], z1[2 * mm][3]), pack2(z1[2 * mm + 1][0], z1[2 * mm + 1][1]), pack2(z1[2 * mm + 1][2], z1[2 * mm + 1][3])};
  BAR();
  char* tr = smem + L4_TR + wid * 2048;
#pragma unroll
  for (int m = 0; m < 4; ++m) {
    const f32x2 f = *(const f32x2*)(fin + (m * 16 + r) * 2);
    const f32x4 v = z2[m] * f[0] - f[1];
    *(u32x2*)(tr + (m * 16 + r) * 32 + g * 8) = (u32x2){pack2(v[0], v[1]), pack2(v[2], v[3])};
  }
  {
    const char* p = tr + (8 * g + (r >> 2)) * 32 + (r & 3) * 8;
    const bf16x4 lo0 = lds_tr(p), hi0 = lds_tr(p + 128), lo1 = lds_tr(p + 1024), hi1 = lds_tr(p + 1152);
    stc16(rs, bo, OFF_Z2 + (panel * 2 * 54 + u0 + wid) * 1024, __builtin_bit_cast(u32x4, (bf16x8)__builtin_shufflevector(lo0, hi0, 0, 1, 2, 3, 4, 5, 6, 7)));
    stc16(rs, bo, OFF_Z2 + ((panel * 2 + 1) * 54 + u0 + wid) * 1024, __builtin_bit_cast(u32x4, (bf16x8)__builtin_shufflevector(lo1, hi1, 0, 1, 2, 3, 4, 5, 6, 7)));
  }
  SCHED();
  waitv<0>();
  win_tail(ws, wsp, smem, panel, cb, wid, lane);
  sp_ring<0, 5>(wsp, smem, panel, wid, lane);
  if (!EARLY) sp_prefetch(wr, wsp, panel, wid, lane);
}

DI void win_agent(char* __restrict__ ws, rsrc_t rs, const u16* __restrict__ wsp, char* smem, u32x4 (&wr)[16 - K5_R][2], const u16* __restrict__ bsp, u16 (&bb)[2]) {
  const int tid = fresh_tid(), lane = tid & 63, wid = 7;
  const int xcd = blockIdx.x & 7, idx = blockIdx.x >> 3, panel = xcd * 4 + (idx >> 3), cb = idx & 7, u0 = unit0_of(cb);
  float* lnp = (float*)(smem + L4_LN); float* st2 = (float*)(smem + L4_ST2); float* fin = (float*)(smem + L4_FIN); float* cbl = (float*)(smem + L4_CB);
  int* cnt = (int*)(smem + L4_CNT);
  __builtin_amdgcn_s_setprio(2);
  if (lane == 0) *cnt = 0;
  poll_ge(ws, lane < 8 ? C_ATT + lane : C_X1 + panel, lane < 8 ? 32 : 8);
  BAR();
  stage_x1s(ws, smem, panel, wid, tid);
  {
    u32x2 t[16]; f32x4 cp[5][2];
#pragma unroll
    for (int i = 0; i < 16; ++i) t[i] = __builtin_amdgcn_raw_buffer_load_b64(rs, lane * 8, OFF_ST1 + (panel * 16 + i) * 512, 16);
    const unsigned co = lane < 28 ? (u0 * 16 + lane * 4) * 4 : min((54 + u0) * 16 + min(lane - 28, 27) * 4, F_ - 4) * 4;
#pragma unroll
    for (int i = 0; i < 5; ++i) { cp[i][0] = ldc16f(rs, co, OFF_CBP + i * 16384); cp[i][1] = ldc16f(rs, co, OFF_CBP + i * 16384 + 8192); }
    float s = 0.f, q = 0.f;
#pragma unroll
    for (int i = 0; i < 16; ++i) { s += __uint_as_float(t[i][0]); q += __uint_as_float(t[i][1]); }
    const float mean = s * (1.f / 640.f), var = fmaxf(q * (1.f / 640.f) - mean * mean, 0.f);
    const float den = ln_den(var), ri = __builtin_amdgcn_rcpf(den);
    *(f32x4*)(lnp + lane * 4) = (f32x4){den, -rbf(mean), GC1 * ri, GC2 * ri * ri * ri};
#pragma unroll
    for (int v = 0; v < 2; ++v) *(f32x4*)(cbl + v * 256 + lane * 4) = ((cp[0][v] + cp[1][v]) + (cp[2][v] + cp[3][v])) + cp[4][v];
  }
  waitv<0>();
  BAR();
  __builtin_amdgcn_s_setprio(0);
  __builtin_amdgcn_s_sleep(8);
  load_bb(bsp, panel, wid, lane, bb);
  sp_prefetch(wr, wsp, panel, wid, lane);
#pragma nounroll
  for (int w = 0; w < 8; ++w) sp_ring<5, 7>(wsp, smem, panel, w, lane);
  __builtin_amdgcn_s_setprio(2);
#pragma nounroll
  for (unsigned spin = 0; spin < (1u << 22); ++spin) {
    if (__hip_atomic_load(cnt, __ATOMIC_RELAXED, __HIP_MEMORY_SCOPE_WORKGROUP) >= 7) break;
    __builtin_amdgcn_s_sleep(1);
  }
  asm volatile("" ::: "memory");
  float s, q;
  {
    f32x2 t[7];
#pragma unroll
    for (int i = 0; i < 7; ++i) t[i] = *(const f32x2*)(st2 + (i * 64 + lane) * 2);
    s = t[0][0]; q = t[0][1];
#pragma unroll
    for (int i = 1; i < 6; ++i) { s = opq(s + t[i][0]); q = opq(q + t[i][1]); }
    if (cb < 6) { s = opq(s + t[6][0]); q = opq(q + t[6][1]); }
  }
  GAS u64* xch = (GAS u64*)(ws + OFF_XCH) + panel * 512;
  unsigned qb = __float_as_uint(q); if (qb == ~0u) qb = 0x7fc00000u;
  __hip_atomic_store(xch + cb * 64 + lane, ((u64)qb << 32) | __float_as_uint(s), __ATOMIC_RELAXED, __HIP_MEMORY_SCOPE_AGENT);
  u64 v[8];
#pragma nounroll
  for (unsigned spin = 0; spin < (1u << 19); ++spin) {
    bool ok = true;
#pragma unroll
    for (int i = 0; i < 8; ++i) { v[i] = __hip_atomic_load(xch + i * 64 + lane, __ATOMIC_RELAXED, __HIP_MEMORY_SCOPE_AGENT); ok = ok && ((unsigned)(v[i] >> 32) != ~0u); }
    if (__all(ok)) break;
    __builtin_amdgcn_s_sleep(1);
  }
  s = 0.f; q = 0.f;
#pragma unroll
  for (int i = 0; i < 8; ++i) { s = opq(s + __uint_as_float((unsigned)v[i])); q = opq(q + __uint_as_float((unsigned)(v[i] >> 32))); }
  const float mean = opq(s * (1.f / 864.f)), var = fmaxf(opq(q * (1.f / 864.f)) - mean * mean, 0.f);
  const float ri = __builtin_amdgcn_rcpf(ln_den(var));
  *(f32x2*)(fin + lane * 2) = (f32x2){ri, rbf(mean) * ri};
  __builtin_amdgcn_s_setprio(0);
  BAR();
  __builtin_amdgcn_s_sleep(6);
  sp_ring<0, 5>(wsp, smem, panel, wid, lane);
  win_tail(ws, wsp, smem, panel, cb, wid, lane);
}

constexpr int sp_q(int t) { const int w = 4 * 2 + t / 3; return t + (w < 4 * 4 ? w : 4 * 4); }
DI void phase_sp(char* __restrict__ ws, rsrc_t rs, const u16 (&bb)[2], char* smem, int wid, const u32x4 (&wr)[16 - K5_R][2], u32x2 (&res)[3], f32x4 (&gate)[3]) {
  const int tid = fresh_tid(), lane = tid & 63;
  const int xcd = blockIdx.x & 7, idx = blockIdx.x >> 3, panel = xcd * 4 + (idx >> 3), cb = idx & 7;
  const int kh = wid >> 2, up = wid & 3, u0 = unit0_of(cb) + 2 * up, nu = cb < 6 ? 7 : 6, g = lane >> 4, r = lane & 15;
  const unsigned dead[2] = {2 * up < nu ? 0u : DEAD, 2 * up + 1 < nu ? 0u : DEAD};
  unsigned bvo[2] = {lane * 16 + dead[0], lane * 16 + 1024 + dead[1]};
  const char* al[2][2];
#pragma unroll
  for (int k = 0; k < 2; ++k)
#pragma unroll
    for (int j = 0; j < 2; ++j) al[k][j] = smem + kh * 8192 + (j ^ kh) * 4096 + r * 128 + ((4 * k + g) ^ (r >> 1)) * 16;
  constexpr int LB = 4, PD = 3;
  bf16x8 bq[LB + 1][4], af[PD + 1][2]; f32x4 acc[2][4]; u32x4 wq[17];
#pragma unroll
  for (int i = 0; i < 2; ++i) {
    const float b0 = bf2f(bb[0]), b1 = bf2f(bb[1]);
    acc[i][0] = (f32x4){b0, b0, b0, b0}; acc[i][1] = (f32x4){b1, b1, b1, b1}; acc[i][2] = (f32x4){}; acc[i][3] = (f32x4){};
  }
  const unsigned bso = fresh_s(OFF_Z2 + (kh * 2 * 54 + u0) * 1024);
  PIN(bvo[0]); PIN(bvo[1]);
  if (wid == 7) poll_ge(ws, C_Z2 + (panel >> 3) * 8 + cb, 32);
  else if (kh && up < 3) waitv<2 * (16 - K5_R)>(); else waitv<0>();
  BAR();
#define SP_LD(s, j) (bq[(s) % (LB + 1)][j] = BF8(ldc16(rs, bvo[(j) & 1], bso + (sp_stage(s) * 4 + ((j) >> 1)) * 55296)))
#define SP_RD(t, k) (af[(t) % (PD + 1)][k] = *(const bf16x8*)(al[k][((t) >> 1) & 1] + (((t) >> 2) % K5_R) * 16384 + ((t) & 1) * 2048))
#pragma unroll
  for (int q = 0; q < sp_q(0); ++q) SP_LD(q >> 2, q & 3);
#pragma unroll
  for (int t = 0; t < PD; ++t) { SP_RD(t, 0); SP_RD(t, 1); }
  SCHED();
  sfor<64>([&](auto T) __attribute__((always_inline)) {
    constexpr int t = T, nx = t + PD, s = t >> 2, m = t & 3, tl = sp_q(t), t2 = sp_q(t + 1) - 1;
    if constexpr (nx == 16 || nx == 28 || nx == 44 || nx == 56) BAR();
    constexpr int c = nx >= 16 && nx < 24 ? nx - 16 : nx >= 28 && nx < 34 ? nx - 20 : nx >= 44 && nx < 48 ? nx - 30 : -1;
    if constexpr (nx < 64) SP_RD(nx, 0);
    acc[0][m] = MFMA16(bq[s % (LB + 1)][0], af[t % (PD + 1)][0], acc[0][m]);
    acc[0][m] = MFMA16(bq[s % (LB + 1)][2], af[t % (PD + 1)][1], acc[0][m]); PIN(acc[0][m]);
    if constexpr (tl < 64) SP_LD(tl >> 2, tl & 3); else if constexpr (tl - 64 < 17) wq[tl - 64] = proj_ld<true>(rs, tl - 64, wid, lane);
    SCHED();
    if constexpr (nx < 64) SP_RD(nx, 1);
    acc[1][m] = MFMA16(bq[s % (LB + 1)][1], af[t % (PD + 1)][0], acc[1][m]);
    acc[1][m] = MFMA16(bq[s % (LB + 1)][3], af[t % (PD + 1)][1], acc[1][m]); PIN(acc[1][m]);
    if constexpr (t2 > tl && t2 < 64) SP_LD(t2 >> 2, t2 & 3);
    if constexpr (c >= 0) *(u32x4*)(smem + ((c >> 1) % K5_R) * 16384 + wid * 2048 + (c & 1) * 1024 + lane * 16) = wr[c >> 1][c & 1];
    if constexpr (t == 63 && 4 * LB < 17) { _Pragma("unroll") for (int i = 4 * LB; i < 17; ++i) wq[i] = proj_ld<true>(rs, i, wid, lane); }
    if constexpr (t == 62) proj_small<true>(rs, nullptr, wid, lane, res, gate, gate);
    SCHED();
  });
  f32x4* exw = (f32x4*)(smem + 32768 + wid * 4096) + lane;
  const f32x4* exr = (const f32x4*)(smem + 32768 + (wid ^ 4) * 4096) + lane;
  u32x4 z[2];
#pragma unroll
  for (int i = 0; i < 2; ++i) z[i] = *(const u32x4*)(smem + L4_Z1 + (2 * up + i) * 2048 + kh * 1024 + lane * 16);
#pragma unroll
  for (int i = 0; i < 2; ++i)
#pragma unroll
    for (int j = 0; j < 2; ++j) exw[(i * 2 + j) * 64] = acc[i][2 + j];
  f32x4 zf[2][2];
#pragma unroll
  for (int i = 0; i < 2; ++i)
#pragma unroll
    for (int j = 0; j < 2; ++j) {
      zf[i][j] = (f32x4){bflo(z[i][2 * j]), bfhi(z[i][2 * j]), bflo(z[i][2 * j + 1]), bfhi(z[i][2 * j + 1])};
      acc[i][j] = mul4s(acc[i][j], zf[i][j]); PIN(zf[i][j]); PIN(acc[i][j]);
    }
  BAR();
  f32x4 oth[2][2];
#pragma unroll
  for (int i = 0; i < 2; ++i)
#pragma unroll
    for (int j = 0; j < 2; ++j) oth[i][j] = exr[(i * 2 + j) * 64];
#pragma unroll
  for (int i = 0; i < 2; ++i)
#pragma unroll
    for (int j = 0; j < 2; ++j) {
      const f32x4 v = acc[i][j] + zf[i][j] * oth[i][j];
      stc8(rs, ((g >> 1) * 16 + r) * 16 + (g & 1) * 8 + dead[i], OFF_U + (panel * 4 + 2 * kh + j) * 27648 + (u0 + i) * 512, (u32x2){pack2(v[0], v[1]), pack2(v[2], v[3])});
    }
  drain();
  BAR();
  if (wid == 7) arrive(ws, C_U + panel, lane);
#pragma unroll
  for (int i = 0; i < 17; ++i) proj_park(smem, i, tid, wq[i]);
}

extern "C" __global__ __launch_bounds__(512, 2) OCC2 void k_mega(char* __restrict__ ws, const u16* __restrict__ x, const u16* __restrict__ win,
                                                     const u16* __restrict__ wsp, const u16* __restrict__ bsp, const u16* __restrict__ wout, u16* __restrict__ out) {
  extern __shared__ __attribute__((aligned(16))) char smem[];
  const int wid = __builtin_amdgcn_readfirstlane(threadIdx.x >> 6);
  const rsrc_t rs = __builtin_amdgcn_make_buffer_rsrc(ws, 0, WS_USED, 0x00020000);
  u32x2 res[3]; f32x4 gate[3], cbv[3];
  phase_qkv(ws, rs, smem, wid);
  phase_attn(ws, rs, (const char*)wsp, win, wout, x, smem, wid, res, gate, cbv);
  u32x4 wr[16 - K5_R][2];
  auto nohook = []() __attribute__((always_inline)) {};
  u16 bb[2];
  if (wid >= 4) {
    proj_body<false, 1>(ws, rs, x, out, smem, wid, res, gate, cbv, nohook);
    if (wid == 7) win_agent(ws, rs, wsp, smem, wr, bsp, bb);
    else { bf16x8 b2[20]; win_prefetch(rs, wid, b2); win_body<false>(ws, rs, wsp, smem, wid, b2, wr, bsp, bb); }
#pragma unroll
    for (int i = 0; i < 3; ++i) { gate[i] = (f32x4){}; cbv[i] = (f32x4){}; }
  }
  const int w2 = fresh_s(wid);
  if (w2 < 4) {
    bf16x8 b2[20];
    proj_body<false, 0>(ws, rs, x, out, smem, w2, res, gate, cbv, [&]() __attribute__((always_inline)) { win_prefetch(rs, w2, b2); });
    win_body<true>(ws, rs, wsp, smem, w2, b2, wr, bsp, bb);
  }
  phase_sp(ws, rs, bb, smem, wid, wr, res, gate);
  if (wid < 4) proj_body<true, 0>(ws, rs, x, out, smem, wid, res, gate, cbv, nohook);
  else proj_body<true, 1>(ws, rs, x, out, smem, wid, res, gate, cbv, nohook);
  asm volatile("k_mega_code_end:");
}

extern "C" void kernel_launch(void* const* d_in, const int* in_sizes, int n_in, void* d_out, int out_size, void* d_ws, size_t ws_size,
                              hipStream_t stream) {
  const u16* x = (const u16*)d_in[0];
  const u16* wqkv = (const u16*)d_in[1];
  const u16* wo = (const u16*)d_in[2];
  const u16* win = (const u16*)d_in[4];
  const u16* wsp = (const u16*)d_in[5];
  const u16* bsp = (const u16*)d_in[6];
  const u16* wout = (const u16*)d_in[7];
  const u16* cvec = (const u16*)d_in[8];
  const u16* wmod = (const u16*)d_in[9];
  char* ws = (char*)d_ws;
  (void)hipFuncSetAttribute((const void*)k_mega, hipFuncAttributeMaxDynamicSharedMemorySize, 163840);
  k0_prep<<<K0_GRID, 256, 0, stream>>>(x, wqkv, wo, cvec, wmod, ws);
  k_mega<<<256, 512, 163840, stream>>>(ws, x, win, wsp, bsp, wout, (u16*)d_out);
}
```

```cpp
#include <hip/hip_runtime.h>
#include <hip/hip_bf16.h>
#include <stdint.h>
#include <utility>

typedef __attribute__((ext_vector_type(8))) short bf16x8;
typedef __attribute__((ext_vector_type(4))) short bf16x4;
typedef __attribute__((ext_vector_type(2))) float f32x2;
typedef __attribute__((ext_vector_type(4))) float f32x4;
typedef __attribute__((ext_vector_type(16))) float f32x16;
typedef __attribute__((ext_vector_type(2))) unsigned u32x2;
typedef __attribute__((ext_vector_type(4))) unsigned u32x4;
typedef __attribute__((ext_vector_type(2))) __bf16 bf2_t;
typedef __attribute__((ext_vector_type(2))) _Float16 h2_t;
typedef unsigned short u16;
typedef unsigned long long u64;
#define LAS __attribute__((address_space(3)))
#define GAS __attribute__((address_space(1)))
#define DI __device__ __forceinline__

constexpr int D_ = 640, NQKV = 1920, F_ = 1728;

constexpr unsigned OFF_CNT = 0;
constexpr unsigned OFF_MOD = 65536;
constexpr unsigned OFF_MODP = OFF_MOD + 16384;
constexpr unsigned OFF_CBP = OFF_MODP + 61440;
constexpr unsigned OFF_XCH = OFF_CBP + 5 * 16384;
constexpr unsigned OFF_ST1 = OFF_XCH + 131072;
constexpr unsigned OFF_XHAT = OFF_ST1 + 262144;
constexpr unsigned OFF_WQKV = OFF_XHAT + 2621440;
constexpr unsigned OFF_WO = OFF_WQKV + 2457600;
constexpr unsigned OFF_WIN = OFF_WO + 819200;
constexpr unsigned OFF_WOUT = OFF_WIN + 2211840;
constexpr unsigned OFF_Q = OFF_WOUT + 1105920;
constexpr unsigned OFF_KV = OFF_Q + 32 * 81920;
constexpr unsigned OFF_O = OFF_KV + 32 * 163840;
constexpr unsigned OFF_X1S = OFF_O + 2621440;
constexpr unsigned OFF_Z2 = OFF_X1S + 2621440;
constexpr unsigned OFF_U = OFF_Z2 + 3538944;
constexpr unsigned WS_USED = OFF_U + 3538944;
constexpr int C_QKV = 0, C_MOD = 15  , C_ATT = 128, C_REL = 136, C_X1 = 144, C_U = 176, C_Z2 = 208  ;

DI unsigned pack2(float lo, float hi) { return __builtin_bit_cast(unsigned, __builtin_convertvector((f32x2){lo, hi}, bf2_t)); }
DI float opq(float x) { asm("" : "+v"(x)); return x; }
DI f32x4 fma4s(f32x4 a, f32x4 b, f32x4 c) { return (f32x4){opq(__builtin_fmaf(a[0], b[0], c[0])), opq(__builtin_fmaf(a[1], b[1], c[1])), opq(__builtin_fmaf(a[2], b[2], c[2])), opq(__builtin_fmaf(a[3], b[3], c[3]))}; }
DI f32x4 mul4s(f32x4 a, f32x4 b) { return (f32x4){opq(a[0] * b[0]), opq(a[1] * b[1]), opq(a[2] * b[2]), opq(a[3] * b[3])}; }
DI unsigned pack2h(float lo, float hi) { return __builtin_bit_cast(unsigned, __builtin_convertvector((f32x2){lo, hi}, h2_t)); }
DI float bflo(unsigned u) { return __uint_as_float(u << 16); }
DI float bfhi(unsigned u) { return __uint_as_float(u & 0xffff0000u); }
DI float rbf(float x) { return bflo(pack2(x, x)); }
DI float bf2f(u16 b) { return __uint_as_float((unsigned)b << 16); }
#define MFMA32(a, b, c) __builtin_amdgcn_mfma_f32_32x32x16_bf16((a), (b), (c), 0, 0, 0)
#define MFMA32K8(a, b, c) __builtin_amdgcn_mfma_f32_32x32x8bf16_1k((a), (b), (c), 0, 0, 0)
#define MFMA16(a, b, c) __builtin_amdgcn_mfma_f32_16x16x32_bf16((a), (b), (c), 0, 0, 0)
#define BAR() asm volatile("s_waitcnt lgkmcnt(0)\n\ts_barrier" ::: "memory")
#define SCHED() __builtin_amdgcn_sched_barrier(0)
#define PIN(x) asm volatile("" : "+v"(x))
#define OCC2 __attribute__((amdgpu_waves_per_eu(2, 2)))
#define BF8(x) __builtin_bit_cast(bf16x8, x)
#define BF4(x) __builtin_bit_cast(bf16x4, x)
template <int N> DI void waitv() { asm volatile("s_waitcnt vmcnt(%0)" ::"i"(N) : "memory"); }
DI void drain() { waitv<0>(); __builtin_amdgcn_s_waitcnt(0x0F70); }
template <class F, int... I> DI void sfor_impl(F&& f, std::integer_sequence<int, I...>) { (f(std::integral_constant<int, I>{}), ...); }
template <int N, class F> DI void sfor(F&& f) { sfor_impl(f, std::make_integer_sequence<int, N>{}); }
typedef short v4i16_t __attribute__((ext_vector_type(4)));
DI bf16x4 lds_tr(const char* p) { return __builtin_bit_cast(bf16x4, __builtin_amdgcn_ds_read_tr16_b64_v4i16((LAS v4i16_t*)p)); }
DI void glds16(const void* g, unsigned lds_base) {
  unsigned sv;
  asm volatile("s_mov_b32 %0, m0\n\ts_mov_b32 m0, %2\n\ts_nop 0\n\tglobal_load_lds_dwordx4 %1, off\n\ts_mov_b32 m0, %0" : "=&s"(sv) : "v"(g), "s"(lds_base) : "memory");
}
DI void glds16c(const void* g, unsigned lds_base) {
  unsigned sv;
  asm volatile("s_mov_b32 %0, m0\n\ts_mov_b32 m0, %2\n\ts_nop 0\n\tglobal_load_lds_dwordx4 %1, off sc1\n\ts_mov_b32 m0, %0" : "=&s"(sv) : "v"(g), "s"(lds_base) : "memory");
}
DI void glds4c(const void* g, unsigned lds_base) {
  unsigned sv;
  asm volatile("s_mov_b32 %0, m0\n\ts_mov_b32 m0, %2\n\ts_nop 0\n\tglobal_load_lds_dword %1, off sc1\n\ts_mov_b32 m0, %0" : "=&s"(sv) : "v"(g), "s"(lds_base) : "memory");
}
typedef __amdgpu_buffer_rsrc_t rsrc_t;
DI u32x4 ldc16(rsrc_t rs, unsigned voff, unsigned soff) { return __builtin_amdgcn_raw_buffer_load_b128(rs, voff, soff, 16); }
DI u32x2 ldc8(rsrc_t rs, unsigned voff, unsigned soff) { return __builtin_amdgcn_raw_buffer_load_b64(rs, voff, soff, 16); }
DI unsigned ldc4(rsrc_t rs, unsigned voff, unsigned soff) { return __builtin_amdgcn_raw_buffer_load_b32(rs, voff, soff, 16); }
DI void stc16(rsrc_t rs, unsigned voff, unsigned soff, u32x4 v) { __builtin_amdgcn_raw_buffer_store_b128(v, rs, voff, soff, 16); asm volatile("s_nop 1" ::"v"(v) : "memory"); }
DI void stc8(rsrc_t rs, unsigned voff, unsigned soff, u32x2 v) { __builtin_amdgcn_raw_buffer_store_b64(v, rs, voff, soff, 16); }
DI void stc4(rsrc_t rs, unsigned voff, unsigned soff, unsigned v) { __builtin_amdgcn_raw_buffer_store_b32(v, rs, voff, soff, 16); }
DI f32x4 ldc16f(rsrc_t rs, unsigned voff, unsigned soff) { return __builtin_bit_cast(f32x4, ldc16(rs, voff, soff)); }
DI void arrive(char* ws, int c, int lane) { if (lane == 0) __hip_atomic_fetch_add((GAS unsigned*)(ws + OFF_CNT + c * 256), 1u, __ATOMIC_RELAXED, __HIP_MEMORY_SCOPE_AGENT); }
DI void poll_ge(char* ws, int c, unsigned thr) {
  const GAS unsigned* p = (const GAS unsigned*)(ws + OFF_CNT + c * 256);
#pragma nounroll
  for (unsigned spin = 0; spin < (1u << 19); ++spin) {
    if (__all(__hip_atomic_load(p, __ATOMIC_RELAXED, __HIP_MEMORY_SCOPE_AGENT) >= thr)) break;
    __builtin_amdgcn_s_sleep(1);
  }
  asm volatile("" ::: "memory");
}
DI int fresh_s(int v) { asm volatile("" : "+s"(v)); return v; }
DI int fresh_tid() { int t = threadIdx.x; asm volatile("" : "+v"(t)); return t; }
DI void l2touch(const void* g, unsigned lds_scratch) {
  unsigned sv;
  asm volatile("s_mov_b32 %0, m0\n\ts_mov_b32 m0, %2\n\ts_nop 0\n\tglobal_load_lds_dword %1, off\n\ts_mov_b32 m0, %0" : "=&s"(sv) : "v"(g), "s"(lds_scratch) : "memory");
}
#define LDNT(p) __builtin_nontemporal_load(p)
DI float silu_bf(u16 c) { const float f = __uint_as_float((unsigned)c << 16); const float v = f / (1.f + __expf(-f)); return __uint_as_float(__builtin_bit_cast(unsigned, __builtin_convertvector((f32x2){v, v}, bf2_t)) << 16); }
DI unsigned lds_addr(const void* p) { return (unsigned)(uintptr_t)(const LAS void*)p; }
DI float xhalf(float v) {
  auto rr = __builtin_amdgcn_permlane32_swap(__float_as_uint(v), __float_as_uint(v), false, false);
  return __uint_as_float((threadIdx.x & 32) ? rr[0] : rr[1]);
}
DI float sum_g(float v) {
  auto a = __builtin_amdgcn_permlane16_swap(__float_as_uint(v), __float_as_uint(v), false, false);
  v = __uint_as_float(a[0]) + __uint_as_float(a[1]);
  auto b = __builtin_amdgcn_permlane32_swap(__float_as_uint(v), __float_as_uint(v), false, false);
  return __uint_as_float(b[0]) + __uint_as_float(b[1]);
}
template <int CTRL> DI float dpp_add(float v) { return v + __uint_as_float(__builtin_amdgcn_update_dpp(0u, __float_as_uint(v), CTRL, 0xf, 0xf, true)); }
DI float sum16(float v) {
  v = dpp_add<0x128>(v); v = dpp_add<0x124>(v); v = dpp_add<0x122>(v); return dpp_add<0x121>(v);
}
DI u32x4 modulate8(u32x4 v, f32x4 sc0, f32x4 sc1, f32x4 sh0, f32x4 sh1) {
  unsigned o0, o1, o2, o3; float t0, t1, t2, t3;
  asm volatile(
      "v_fma_mix_f32 %0, %8, %12, %20 op_sel_hi:[1,0,0]\n\tv_fma_mix_f32 %4, %8, %13, %21 op_sel:[1,0,0] op_sel_hi:[1,0,0]\n\t"
      "v_fma_mix_f32 %1, %9, %14, %22 op_sel_hi:[1,0,0]\n\tv_fma_mix_f32 %5, %9, %15, %23 op_sel:[1,0,0] op_sel_hi:[1,0,0]\n\t"
      "v_fma_mix_f32 %2, %10, %16, %24 op_sel_hi:[1,0,0]\n\tv_fma_mix_f32 %6, %10, %17, %25 op_sel:[1,0,0] op_sel_hi:[1,0,0]\n\t"
      "v_fma_mix_f32 %3, %11, %18, %26 op_sel_hi:[1,0,0]\n\tv_fma_mix_f32 %7, %11, %19, %27 op_sel:[1,0,0] op_sel_hi:[1,0,0]\n\t"
      "v_cvt_pk_bf16_f32 %0, %0, %4\n\tv_cvt_pk_bf16_f32 %1, %1, %5\n\tv_cvt_pk_bf16_f32 %2, %2, %6\n\tv_cvt_pk_bf16_f32 %3, %3, %7"
      : "=&v"(o0), "=&v"(o1), "=&v"(o2), "=&v"(o3), "=&v"(t0), "=&v"(t1), "=&v"(t2), "=&v"(t3)
      : "v"(v[0]), "v"(v[1]), "v"(v[2]), "v"(v[3]), "v"(sc0[0]), "v"(sc0[1]), "v"(sc0[2]), "v"(sc0[3]), "v"(sc1[0]), "v"(sc1[1]), "v"(sc1[2]), "v"(sc1[3]),
        "v"(sh0[0]), "v"(sh0[1]), "v"(sh0[2]), "v"(sh0[3]), "v"(sh1[0]), "v"(sh1[1]), "v"(sh1[2]), "v"(sh1[3]));
  return (u32x4){o0, o1, o2, o3};
}
DI float ln_den(float var) { return rbf(__builtin_amdgcn_sqrtf(rbf(rbf(var) + 1e-5f))); }
DI int unit0_of(int j) { return j < 6 ? 7 * j : 42 + 6 * (j - 6); }

DI void K0ST(char* p, u32x4 v) { *(u32x4*)p = v; }
constexpr int K0_MOD = 240, K0_LN = 128, K0_RL = 240, K0_WO = 50, K0_INIT = 12, K0_WARM = 24;
constexpr int K0_GRID = K0_WARM + K0_MOD + K0_LN + K0_RL + K0_WO + K0_INIT;

__global__ __launch_bounds__(256) void k0_prep(const u16* __restrict__ x, const u16* __restrict__ wqkv, const u16* __restrict__ wo, const u16* __restrict__ cvec,
                                               const u16* __restrict__ wmod, char* __restrict__ ws) {
  __shared__ __attribute__((aligned(16))) char smem[4 * 4608];
  const int tid = threadIdx.x, lane = tid & 63, wid = __builtin_amdgcn_readfirstlane(tid >> 6);
  int b = blockIdx.x;
  if (b < K0_WARM) {
    u64 pc; unsigned len;
    asm volatile("s_getpc_b64 s[30:31]\n\ts_add_u32 s30, s30, k_mega@rel32@lo+4\n\ts_addc_u32 s31, s31, k_mega@rel32@hi+12\n\ts_mov_b64 %0, s[30:31]\n\t"
                 "s_mov_b32 %1, k_mega_code_end-k_mega" : "=s"(pc), "=s"(len) :: "scc", "s30", "s31");
    l2touch((const char*)pc + min(((b >> 3) * 256 + tid) * 128u, len - 4u), lds_addr(smem) + wid * 256);
    return;
  }
  b -= K0_WARM;
  if (b < K0_MOD) {
    const int cg = b >> 2, ks = b & 3, c = tid & 7, row0 = ks * 160 + (tid >> 3), col0 = cg * 64;
    u32x4 w[5]; u16 cv[5];
#pragma unroll
    for (int i = 0; i < 5; ++i) { w[i] = LDNT((const u32x4*)(wmod + (size_t)(row0 + 32 * i) * 3840 + col0 + c * 8)); cv[i] = cvec[row0 + 32 * i]; }
    float acc[8] = {0, 0, 0, 0, 0, 0, 0, 0};
#pragma unroll
    for (int i = 0; i < 5; ++i) {
      const float s = silu_bf(cv[i]);
#pragma unroll
      for (int e = 0; e < 4; ++e) { acc[2 * e] += s * bflo(w[i][e]); acc[2 * e + 1] += s * bfhi(w[i][e]); }
    }
#pragma unroll
    for (int e = 0; e < 8; ++e) acc[e] = sum_g(dpp_add<0x128>(acc[e]));
    float* red = (float*)smem;
    if (lane < 8) { *(f32x4*)(red + wid * 64 + lane * 8) = (f32x4){acc[0], acc[1], acc[2], acc[3]}; *(f32x4*)(red + wid * 64 + lane * 8 + 4) = (f32x4){acc[4], acc[5], acc[6], acc[7]}; }
    __syncthreads();
    if (tid < 64) ((float*)(ws + OFF_MODP))[ks * 3840 + cg * 64 + tid] = (red[tid] + red[64 + tid]) + (red[128 + tid] + red[192 + tid]);
    return;
  }
  b -= K0_MOD;
  if (b < K0_LN) {
    b = (b & 7) * 16 + (b >> 3);
    const int row = b * 16 + wid * 4 + (lane >> 4), c = lane & 15;
    const u16* xr = x + (size_t)row * D_ + c * 8;
    u32x4 v[5];
#pragma unroll
    for (int i = 0; i < 5; ++i) v[i] = *(const u32x4*)(xr + i * 128);
    float s = 0.f, q = 0.f;
#pragma unroll
    for (int i = 0; i < 5; ++i)
#pragma unroll
      for (int e = 0; e < 4; ++e) s += bflo(v[i][e]) + bfhi(v[i][e]);
    const float mean = sum16(s) * (1.f / 640.f);
#pragma unroll
    for (int i = 0; i < 5; ++i)
#pragma unroll
      for (int e = 0; e < 4; ++e) { const float a = bflo(v[i][e]) - mean, d = bfhi(v[i][e]) - mean; q += a * a + d * d; }
    const float var = sum16(q) * (1.f / 640.f);
    const float rinv = __builtin_amdgcn_rcpf(ln_den(var)), mb = rbf(mean);
    char* dst = ws + OFF_XHAT + (size_t)row * 1280 + c * 16;
#pragma unroll
    for (int i = 0; i < 5; ++i) {
      u32x4 o;
#pragma unroll
      for (int e = 0; e < 4; ++e) o[e] = pack2h((bflo(v[i][e]) - mb) * rinv, (bfhi(v[i][e]) - mb) * rinv);
      K0ST(dst + i * 256, o);
    }
    return;
  }
  b -= K0_LN;
  if (b < K0_RL) {
    const int kt = b / 12, G = b % 12, sec = G >> 2, gq = G & 3;
    u32x4 v[3];
#pragma unroll
    for (int i = 0; i < 3; ++i) { const int ci = min(i * 256 + tid, 639); v[i] = LDNT((const u32x4*)(wqkv + (size_t)(kt * 32 + ci / 20) * NQKV + G * 160 + (ci % 20) * 8)); }
#pragma unroll
    for (int i = 0; i < 3; ++i) { const int ci = min(i * 256 + tid, 639); *(u32x4*)(smem + (ci / 20) * 336 + (ci % 20) * 16) = v[i]; }
    __syncthreads();
    const int g = lane >> 4, q = (lane & 15) >> 2, p = lane & 3;
    for (int f = wid; f < 10; f += 4) {
      const int t = f >> 1, x2 = f & 1;
      const int col = t == 4 ? 20 * (4 * x2 + p) + 16 : sec == 2 ? 20 * f + 4 * p : 20 * (2 * t + (p >> 1)) + 8 * (p & 1) + 4 * x2;
      const int T = sec * 20 + (t < 4 ? 4 * gq + t : 16 + gq);
      const char* pp = smem + (8 * g + q) * 336 + col * 2;
      const bf16x4 lo = lds_tr(pp), hi = lds_tr(pp + 4 * 336);
      K0ST(ws + OFF_WQKV + (((size_t)T * 20 + kt) * 2 + x2) * 1024 + lane * 16, __builtin_bit_cast(u32x4, (bf16x8)__builtin_shufflevector(lo, hi, 0, 1, 2, 3, 4, 5, 6, 7)));
    }
    return;
  }
  b -= K0_RL;
  if (b < K0_WO) {
    const int job = b * 4 + wid, kt = job / 10, ng = job % 10, j8 = lane >> 3;
    char* t = smem + wid * 4608;
    u32x4 v[4];
#pragma unroll
    for (int i = 0; i < 4; ++i) {
      const int c = 4 * kt + i, row = c < 64 ? 20 * (c >> 1) + 4 * (c & 1) + (j8 & 3) + 8 * (j8 >> 2) : 20 * (2 * (c - 64) + (j8 >> 2)) + 16 + (j8 & 3);
      v[i] = LDNT((const u32x4*)(wo + (size_t)row * D_ + ng * 64 + (lane & 7) * 8));
    }
#pragma unroll
    for (int i = 0; i < 4; ++i) *(u32x4*)(t + (i * 8 + j8) * 144 + (lane & 7) * 16) = v[i];
    const char* pp = t + (8 * (lane >> 4) + ((lane & 15) >> 2)) * 144 + (lane & 3) * 8;
#pragma unroll
    for (int f = 0; f < 4; ++f) {
      const bf16x4 lo = lds_tr(pp + f * 32), hi = lds_tr(pp + 4 * 144 + f * 32);
      K0ST(ws + OFF_WO + ((size_t)(ng * 4 + f) * 20 + kt) * 1024 + lane * 16, __builtin_bit_cast(u32x4, (bf16x8)__builtin_shufflevector(lo, hi, 0, 1, 2, 3, 4, 5, 6, 7)));
    }
    return;
  }
  b -= K0_WO;
  {
    u32x4* p = (u32x4*)(ws + (b < 8 ? OFF_XCH + b * 16384 : OFF_CNT + (b - 8) * 16384)) + tid;
    const unsigned f = b < 8 ? ~0u : 0u;
#pragma unroll
    for (int i = 0; i < 4; ++i) p[i * 256] = (u32x4){f, f, f, f};
  }
}

constexpr float QSCALE = 0.22360679774997896f * 1.4426950408889634f;
constexpr int K1_W = 3, K1_WB = 5, K1_WB0 = 1;

template <bool ISV, class FIN>
DI void k1_mma(const char* __restrict__ bsrc, const char* smem, int lane, FIN&& fin) {
  f32x4 acc[8][2];
  constexpr int PD = 4;
  bf16x8 bfr[K1_WB + 1][4], af[PD + 1][2];
  const char* lds[2];
#pragma unroll
  for (int k = 0; k < 2; ++k) lds[k] = smem + k * 8192 + (lane ^ (4 * k + (lane >> 4))) * 16;
#pragma unroll
  for (int i = 0; i < 4 * K1_WB0; ++i) bfr[i >> 2][i & 3] = *(const bf16x8*)(bsrc + i * 1024);
#pragma unroll
  for (int m = 0; m < 8; ++m) { acc[m][0] = (f32x4){}; acc[m][1] = (f32x4){}; }
  BAR();
#pragma unroll
  for (int i = 4 * K1_WB0; i < 4 * K1_WB; ++i) bfr[i >> 2][i & 3] = *(const bf16x8*)(bsrc + i * 1024);
  BAR();
  __builtin_amdgcn_s_setprio(1);
#define K1_RD(t, k) (af[(t) % (PD + 1)][k] = *(const bf16x8*)(lds[k] + (((t) >> 3) % 3) * 16384 + ((t) & 7) * 1024))
#define K1_MM(c, b, a) (c = ISV ? MFMA16(a, b, c) : MFMA16(b, a, c))
#pragma unroll
  for (int t = 0; t < PD; ++t) { K1_RD(t, 0); K1_RD(t, 1); }
  SCHED();
  sfor<80>([&](auto T) __attribute__((always_inline)) {
    constexpr int t = T, s = t >> 3, m = t & 7, nx = t + PD;
    if constexpr (nx < 80 && (nx & 7) == 0) asm volatile("s_barrier" ::: "memory");
    if constexpr (nx < 80) K1_RD(nx, 0);
    K1_MM(acc[m][0], bfr[s % (K1_WB + 1)][0], af[t % (PD + 1)][0]); K1_MM(acc[m][0], bfr[s % (K1_WB + 1)][2], af[t % (PD + 1)][1]); PIN(acc[m][0]);
    SCHED();
    if constexpr (nx < 80) K1_RD(nx, 1);
    if constexpr (s + K1_WB < 10 && (m & 1)) bfr[(s + K1_WB) % (K1_WB + 1)][m >> 1] = *(const bf16x8*)(bsrc + ((s + K1_WB) * 4 + (m >> 1)) * 1024);
    K1_MM(acc[m][1], bfr[s % (K1_WB + 1)][1], af[t % (PD + 1)][0]); K1_MM(acc[m][1], bfr[s % (K1_WB + 1)][3], af[t % (PD + 1)][1]); PIN(acc[m][1]);
    SCHED();
    if constexpr (t >= 74) { fin(t - 74, acc[t - 74]); SCHED(); }
  });
  fin(6, acc[6]); fin(7, acc[7]);
  __builtin_amdgcn_s_setprio(0);
}


DI void phase_qkv(char* __restrict__ ws, rsrc_t rs, char* smem, int wid) {
  const int tid = fresh_tid(), lane = tid & 63;
  const int xcd = blockIdx.x & 7, idx = blockIdx.x >> 3;
  int ctr;
  if ((xcd & 1) && idx >= 28) {
    ctr = C_MOD;
    if (tid < 160) {
      const int col = 1280 + ((xcd >> 1) * 4 + idx - 28) * 160 + tid;
      const float* pp = (const float*)(ws + OFF_MODP) + col;
      float v = rbf((pp[0] + pp[3840]) + (pp[7680] + pp[11520]));
      if (col / 640 == 4) v = rbf(1.f + v);
      stc4(rs, col * 4, OFF_MOD, __float_as_uint(v));
    }
  } else {
    const int mt = (xcd >> 1) * 4 + (idx & 3), nt = (xcd & 1) * 8 + (idx >> 2);
    ctr = C_QKV + nt;
    if (wid < 4) {
      float* cst = (float*)(smem + 49152);
      const int c = lane & 7, rr = lane >> 3;
      const char* src = ws + OFF_XHAT + (size_t)(mt * 128 + wid * 32 + rr) * 1280 + c * 16;
      char* wdst = smem + (c >> 2) * 8192 + wid * 2048 + (c & 3) * 256 + (rr ^ c) * 16;
      u32x4 a[10][4];
      f32x4 c0[4], c1[4];
#pragma unroll
      for (int i = 0; i < 4; ++i) { c0[i] = *(const f32x4*)(ws + OFF_MODP + i * 15360 + tid * 16); if (wid == 0) c1[i] = *(const f32x4*)(ws + OFF_MODP + i * 15360 + 4096 + tid * 16); }
#pragma unroll
      for (int s = 0; s < K1_W; ++s)
#pragma unroll
        for (int m = 0; m < 4; ++m) a[s][m] = *(const u32x4*)(src + m * 10240 + s * 128);
      {
        f32x4 v = (c0[0] + c0[1]) + (c0[2] + c0[3]); const float one = tid >= 160 ? 1.f : 0.f;
        *(f32x4*)(cst + tid * 4) = (f32x4){rbf(one + rbf(v[0])), rbf(one + rbf(v[1])), rbf(one + rbf(v[2])), rbf(one + rbf(v[3]))};
        if (wid == 0) { v = (c1[0] + c1[1]) + (c1[2] + c1[3]); *(f32x4*)(cst + 1024 + tid * 4) = (f32x4){rbf(1.f + rbf(v[0])), rbf(1.f + rbf(v[1])), rbf(1.f + rbf(v[2])), rbf(1.f + rbf(v[3]))}; }
      }
      BAR();
      const float* cp = cst + c * 8;
      f32x4 kc[10][4];
#define K1_KC(s) do { kc[s][0] = *(const f32x4*)(cp + (s) * 64); kc[s][1] = *(const f32x4*)(cp + (s) * 64 + 4); kc[s][2] = *(const f32x4*)(cp + (s) * 64 + 640); kc[s][3] = *(const f32x4*)(cp + (s) * 64 + 644); } while (0)
#pragma unroll
      for (int s = 0; s < 6; ++s) K1_KC(s);
      SCHED();
      u32x4 o[2][4];
#define K1_XF(s, m) (o[(s) & 1][m] = modulate8(a[s][m], kc[s][2], kc[s][3], kc[s][0], kc[s][1]))
#define K1_WR(s, m) (*(u32x4*)(wdst + ((s) % 3) * 16384 + ((m) >> 1) * 1024 + ((m) & 1) * 128) = o[(s) & 1][m])
#pragma unroll
      for (int m = 0; m < 4; ++m) { K1_XF(0, m); K1_WR(0, m); }
      BAR();
#pragma unroll
      for (int m = 0; m < 4; ++m) a[K1_W][m] = *(const u32x4*)(src + m * 10240 + K1_W * 128);
#pragma unroll
      for (int m = 0; m < 4; ++m) K1_XF(1, m);
      SCHED();
#pragma unroll
      for (int s = 1; s < 10; ++s) {
#pragma unroll
        for (int m = 0; m < 4; ++m) {
          if (m == 0) K1_WR(s, 0);
          if (m < 3) K1_WR(s, m + 1);
          if (s + K1_W < 10) a[s + K1_W][m] = *(const u32x4*)(src + m * 10240 + (s + K1_W) * 128);
          if (s < 5) kc[s + 5][m] = *(const f32x4*)(cp + (s + 5) * 64 + (m & 1) * 4 + (m >> 1) * 640);
          SCHED();
          if (s < 9) K1_XF(s + 1, m);
          SCHED();
        }
        asm volatile("" :: "v"(o[s & 1][0]), "v"(o[s & 1][1]), "v"(o[s & 1][2]), "v"(o[s & 1][3]));
        BAR();
      }
    } else {
      const int cw = wid - 4;
      const char* bsrc = ws + OFF_WQKV + (size_t)(nt * 4 + cw) * 40 * 1024 + lane * 16;
      const int r = lane & 15, g = lane >> 4;
      if (nt >= 10) {
        const unsigned so = OFF_KV + (mt >> 1) * 20480 + 10240 + (mt & 1) * 5120;
        unsigned vo[2];
#pragma unroll
        for (int x = 0; x < 2; ++x)
          vo[x] = (nt < 14 ? ((nt - 10) * 8 + cw * 2 + x) * 163840u + ((g & 1) * 16 + r) * 16 : (cw * 8 + x * 4 + (r >> 2)) * 163840u + 512 + ((g & 1) * 4 + (r & 3)) * 16) + (g >> 1) * 8;
        k1_mma<true>(bsrc, smem, lane, [&](int m, const f32x4 (&a)[2]) __attribute__((always_inline)) {
#pragma unroll
          for (int x = 0; x < 2; ++x) stc8(rs, vo[x] + (m & 3) * 640, so + (m >> 2) * 2560, (u32x2){pack2(a[x][0], a[x][1]), pack2(a[x][2], a[x][3])});
        });
      } else {
        const bool isQ = nt < 5; const float sc = isQ ? QSCALE : 1.f;
        const unsigned so = isQ ? OFF_Q + mt * 5120 : OFF_KV + (mt >> 1) * 20480 + (mt & 1) * 5120, hs = isQ ? 81920u : 163840u;
        const int n5 = isQ ? nt : nt - 5;
        const bool wide = n5 < 4;
        const unsigned vo = wide ? (2 * (n5 * 4 + cw) + (g >> 1)) * hs + (g & 1) * 512 + r * 16 : (8 * cw + g) * hs + 1024 + r * 8;
        const f32x4 sc4 = (f32x4){sc, sc, sc, sc};
        k1_mma<false>(bsrc, smem, lane, [&](int m, const f32x4 (&a)[2]) __attribute__((always_inline)) {
          const f32x4 v0 = mul4s(a[0], sc4), v1 = mul4s(a[1], sc4);
          const u32x4 w = (u32x4){pack2(v0[0], v0[1]), pack2(v0[2], v0[3]), pack2(v1[0], v1[1]), pack2(v1[2], v1[3])};
          if (wide) stc16(rs, vo + (m & 1) * 256, so + (m >> 1) * 1280, w);
          else { stc8(rs, vo + (m & 1) * 128, so + (m >> 1) * 1280, (u32x2){w[0], w[1]}); stc8(rs, vo + (m & 1) * 128, so + 4 * hs + (m >> 1) * 1280, (u32x2){w[2], w[3]}); }
        });
      }
    }
  }
  waitv<0>();
  BAR();
  if (wid == 7 && lane < 8) __hip_atomic_fetch_add((GAS unsigned*)(ws + OFF_CNT + (lane * 16 + ctr) * 256), 1u, __ATOMIC_RELAXED, __HIP_MEMORY_SCOPE_AGENT);
  if (ctr == C_MOD && wid == 7) { __builtin_amdgcn_s_sleep(127); __builtin_amdgcn_s_sleep(60); }
}

constexpr int proj_pos(int f) { return 1024 + f * 1024; }
template <bool FINAL> DI u32x4 proj_ld(rsrc_t rs, int i, int wid, int lane) {
  constexpr int NF = FINAL ? 135 : 100;
  return ldc16(rs, lane * 16, (FINAL ? OFF_WOUT : OFF_WO) + (((blockIdx.x >> 3) & 7) * NF + (i * 8 + 7 < NF ? i * 8 + wid : min(i * 8 + wid, NF - 1))) * 1024);
}
template <bool FINAL> DI void proj_small(rsrc_t rs, const u16* __restrict__ x, int wid, int lane, u32x2 (&res)[3], f32x4 (&gate)[3], f32x4 (&cbv)[3]) {
  const int idx = blockIdx.x >> 3, mt = ((blockIdx.x & 7) * 4 + (idx >> 3)) * 4 + (wid & 3), g = lane >> 4;
#pragma unroll
  for (int i = 0; i < 3; ++i) {
    const int n = ((idx & 7) * 5 + min((wid >> 2) * 3 + i, 4)) * 16;
    if (!FINAL) { res[i] = *(const u32x2*)(x + (size_t)(mt * 16 + (lane & 15)) * D_ + n + g * 4); cbv[i] = ldc16f(rs, g * 16, OFF_MOD + (4 * 640 + n) * 4); }
    gate[i] = ldc16f(rs, g * 16, OFF_MOD + ((FINAL ? 5 : 2) * 640 + n) * 4);
  }
}
DI void proj_park(char* smem, int i, int tid, u32x4 v) { *(u32x4*)(smem + proj_pos(i * 8) + tid * 16) = v; }

DI void split3(float m, unsigned& w_h0, unsigned& w_h1) {
  const float n = -m; const float a = rbf(n), b = rbf(n - a), c = rbf(n - a - b);
  w_h0 = pack2(a, b); w_h1 = pack2(c, 0.f);
}

DI void phase_attn(char* __restrict__ ws, rsrc_t rs, const char* __restrict__ wsp, const u16* __restrict__ win, const u16* __restrict__ wout, const u16* __restrict__ x,
                   char* smem, int wid, u32x2 (&res)[3], f32x4 (&gate)[3], f32x4 (&cbv)[3]) {
  const int tid = fresh_tid(), lane = tid & 63;
  const int xcd = blockIdx.x & 7, idx = blockIdx.x >> 3, head = xcd * 4 + (idx >> 3), qb = idx & 7;
  const int r = lane & 31, h = lane >> 5, qg = wid & 3, kh = wid >> 2;
  unsigned kvo = OFF_KV + head * 163840, qo = OFF_Q + head * 81920 + (qb * 8 + qg * 2) * 1280;
  const unsigned kconst = h == 0 ? 0x3f803f80u : 0x00003f80u;
  const unsigned so16 = (tid & 255) * 16, l16 = lane * 16, l2 = 1024 + r * 8 + h * 4;
  u32x4 wq[13];
  for (int attempt = 0; attempt < 2; ++attempt) {
    asm volatile("" : "+s"(kvo), "+s"(qo));
    const char* kvs = ws + kvo + kh * 81920 + so16;
    const unsigned ldst = lds_addr(smem) + kh * 81920 + qg * 1024;
    u32x4 qA1w, qB1w, k1w; unsigned qA2w, qB2w, k2, m0sv;
    const char* d0 = kvs + (wid == 0 ? 4096 : 0); const char* d1 = kvs + 4096; const char* d2 = kvs + 8192; const char* d3 = kvs + 12288; const char* d4 = kvs + 16384;
    PIN(d0); PIN(d1); PIN(d2); PIN(d3); PIN(d4);
    if (attempt == 0) {
      if (wid == 7) {
        const int l7 = lane & 7;
        const int c = l7 == 0 ? head >> 3 : l7 == 1 ? 4 : l7 == 2 ? 5 + (head >> 3) : l7 == 3 ? 9 : l7 == 4 ? 10 + (head >> 3) : l7 == 5 ? 14 : C_MOD;
        poll_ge(ws, xcd * 16 + c, 16);
      }
      BAR();
    }
    asm volatile(
        "s_nop 4\n\tbuffer_load_dwordx4 %0, %7, %9, %19 offen sc1\n\tbuffer_load_dwordx4 %1, %7, %9, %10 offen sc1\n\tbuffer_load_dwordx4 %2, %7, %9, %11 offen sc1\n\t"
        "buffer_load_dword %3, %8, %9, %19 offen sc1\n\tbuffer_load_dword %4, %8, %9, %10 offen sc1\n\tbuffer_load_dword %5, %8, %9, %11 offen sc1\n\t"
        "s_mov_b32 %6, m0\n\ts_mov_b32 m0, %17\n\ts_nop 0\n\tglobal_load_lds_dwordx4 %12, off sc1\n\t"
        "s_add_u32 m0, %18, 0x1000\n\ts_nop 0\n\tglobal_load_lds_dwordx4 %13, off sc1\n\t"
        "s_add_u32 m0, %18, 0x2000\n\ts_nop 0\n\tglobal_load_lds_dwordx4 %14, off sc1\n\t"
        "s_add_u32 m0, %18, 0x3000\n\ts_nop 0\n\tglobal_load_lds_dwordx4 %15, off sc1\n\t"
        "s_add_u32 m0, %18, 0x4000\n\ts_nop 0\n\tglobal_load_lds_dwordx4 %16, off sc1\n\t"
        "s_mov_b32 m0, %6\n\ts_waitcnt vmcnt(5)"
        : "=&v"(qA1w), "=&v"(qB1w), "=&v"(k1w), "=&v"(qA2w), "=&v"(qB2w), "=&v"(k2), "=&s"(m0sv)
        : "v"(l16), "v"(l2), "s"(rs), "s"(qo + kh * 1280), "s"(kvo), "v"(d0), "v"(d1), "v"(d2), "v"(d3), "v"(d4),
          "s"(ldst + (wid == 0 ? 4096 : 0)), "s"(ldst), "s"(qo + (kh ^ 1) * 1280)
        : "memory", "scc");
    if (wid == 0) { const unsigned f = (fresh_tid() & 1) ? 0u : 0x3f803f80u; *(u32x4*)(smem + l16) = (u32x4){f, f, f, f}; }
    const bf16x8 qA1 = BF8(qA1w), qB1 = BF8(qB1w);
    bf16x8 k1 = BF8(k1w);
    float mA = -INFINITY, mB = -INFINITY;
    {
      const bf16x4 qA2 = BF4(((u32x2){qA2w, 0u})), qB2 = BF4(((u32x2){qB2w, 0u}));
      const int nt = attempt ? 64 : 1;
      bf16x8 t1 = k1; unsigned t2 = k2;
      for (int t = 0; t < nt; ++t) {
        if (t) { const unsigned to = kvo + (t >> 3) * 20480 + (t & 7) * 1280; t1 = BF8(ldc16(rs, l16, to)); t2 = ldc4(rs, l2, to); }
        const bf16x4 t2v = BF4(((u32x2){t2, kconst}));
        f32x16 a = MFMA32(t1, qA1, (f32x16){}); a = MFMA32K8(t2v, qA2, a);
        f32x16 b = MFMA32(t1, qB1, (f32x16){}); b = MFMA32K8(t2v, qB2, b);
#pragma unroll
        for (int i = 0; i < 16; ++i) { mA = fmaxf(mA, a[i]); mB = fmaxf(mB, b[i]); }
      }
      mA = fmaxf(mA, xhalf(mA)); mB = fmaxf(mB, xhalf(mB));
    }
    unsigned a0, a1, b0, b1; split3(mA, a0, a1); split3(mB, b0, b1);
    const bf16x4 qA2 = BF4(((u32x2){qA2w, h ? a1 : a0})), qB2 = BF4(((u32x2){qB2w, h ? b1 : b0}));
    char* lh = smem + kh * 81920;
    waitv<0>();
    BAR();
    f32x16 oA = (f32x16){}, oB = (f32x16){};
    const char* kp1 = lh + l16;
    const char* kp2 = lh + l2;
    const bool vreal = r < 20;
    const char* vp = vreal ? lh + 10240 + (r < 16 ? h * 16 + r : 16 + h * 4 + r) * 16 : smem + (r == 20 ? 0 : 16);
    const int vinc = vreal ? 1280 : 0, vjump = vreal ? 10240 : 0;
    if (kh) { k1 = *(const bf16x8*)kp1; k2 = *(const unsigned*)kp2; }
    bf16x8 kf1[2], vf[2][2]; u32x2 kf2[2];
    kf1[0] = k1; kf2[0] = (u32x2){k2, kconst}; kf2[1] = (u32x2){0u, kconst};
    vf[1][0] = (bf16x8){}; vf[1][1] = (bf16x8){};
    u32x4 pA0, pA1, pB0 = (u32x4){}, pB1 = (u32x4){};
    f32x4 e0 = (f32x4){}, e1;
    f32x16 sA = MFMA32(kf1[0], qA1, (f32x16){}), sB;
    sA = MFMA32K8(BF4(kf2[0]), qA2, sA);
#define EX2(x) __builtin_amdgcn_exp2f(x)
#define GRP(MF, S, i, EN, EP, P, w) do { MF; SCHED(); EN[0] = EX2(S[i]); P[w] = pack2(EP[0], EP[1]); EN[1] = EX2(S[i + 1]); P[w + 1] = pack2(EP[2], EP[3]); \
      EN[2] = EX2(S[i + 2]); EN[3] = EX2(S[i + 3]); PIN(P); PIN(EN); SCHED(); } while (0)
    const unsigned relsc = lds_addr(smem) + 768;
    auto group = [&](auto FIRST, int jump, int j) __attribute__((always_inline)) {
#pragma unroll
      for (int t = 0; t < 8; ++t) {
        const int c = t & 1, n = c ^ 1;
        if (FIRST) { if (t == 0) __builtin_amdgcn_s_setprio(3); if (t == 3) __builtin_amdgcn_s_setprio(2); if (t == 5) __builtin_amdgcn_s_setprio(1); if (t == 6) __builtin_amdgcn_s_setprio(0); if (t == 7) __builtin_amdgcn_s_setprio(3); }
        else {
          if (t == 4 && j == 2) __builtin_amdgcn_s_setprio(2);
          if (t == 3 && j == 3) {
            __builtin_amdgcn_s_setprio(1);
            const int wf = fresh_s(wid);
#pragma unroll
            for (int i = 0; i < 13; ++i) wq[i] = proj_ld<false>(rs, i, wf, lane);
            proj_small<false>(rs, x, wf, fresh_tid() & 63, res, gate, cbv);
          }
          if (t == 6 && j == 3) { __builtin_amdgcn_s_setprio(0); if (wid == 7) glds4c(ws + OFF_CNT + (C_REL + (lane & 7)) * 256, relsc); }
        }
        if (FIRST && t < 5) {
#pragma unroll
          for (int i = 5 + 3 * t; i < 8 + 3 * t; ++i) glds16c(kvs + i * 4096, fresh_s(ldst) + i * 4096);
        }
        if (t == 7) {
          if (FIRST) { waitv<0>(); BAR(); }
          kp1 += jump; kp2 += jump;
        }
        SCHED();
        kf1[n] = *(const bf16x8*)(kp1 + ((t + 1) & 7) * 1280); kf2[n][0] = *(const unsigned*)(kp2 + ((t + 1) & 7) * 1280);
        GRP(sB = MFMA32(kf1[c], qB1, (f32x16){}), sA, 0, e1, e0, pB1, 2);
        vf[c][0] = *(const bf16x8*)vp; vf[c][1] = *(const bf16x8*)(vp + 640); vp += vinc;
        GRP(sB = MFMA32K8(BF4(kf2[c]), qB2, sB), sA, 4, e0, e1, pA0, 0);
        GRP(oB = MFMA32(vf[n][0], BF8(pB0), oB), sA, 8, e1, e0, pA0, 2);
        GRP(oB = MFMA32(vf[n][1], BF8(pB1), oB), sA, 12, e0, e1, pA1, 0);
        GRP(sA = MFMA32(kf1[n], qA1, (f32x16){}), sB, 0, e1, e0, pA1, 2);
        GRP(sA = MFMA32K8(BF4(kf2[n]), qA2, sA), sB, 4, e0, e1, pB0, 0);
        GRP(oA = MFMA32(vf[c][0], BF8(pA0), oA), sB, 8, e1, e0, pB0, 2);
        GRP(oA = MFMA32(vf[c][1], BF8(pA1), oA), sB, 12, e0, e1, pB1, 0);
      }
      vp += vjump;
    };
    group(std::true_type{}, 20480, 0);
    const int blk = fresh_s(blockIdx.x);
    const int j2 = (blk - 135) * 4 + qg;
    const bool side = kh == 0 && attempt == 0 && j2 < 270;
    char* stile = smem + 1024 + qg * 4096;
    const char* svec = smem + 81920 + qg * 256;
    const int kt = blk < 135 ? 4 * (blk % 5) + qg : j2 / 10, ng = blk < 135 ? blk / 5 : j2 % 10;
    if (side) {
      const u16* W = blk < 135 ? win : wout; const int N = blk < 135 ? F_ : D_;
      const int lf = fresh_tid() & 63, j8 = lf >> 3;
#pragma unroll
      for (int i = 0; i < 4; ++i) {
        const int row = kt * 32 + i * 8 + j8;
        glds16(W + (size_t)row * N + ng * 64 + ((lf & 7) ^ j8) * 8, lds_addr(stile) + i * 1024);
      }
      glds4c((const float*)(ws + OFF_MOD) + (4 - h) * 640 + (kt < 20 ? kt : 0) * 32 + r, lds_addr(svec));
    }
#pragma nounroll
    for (int j = 1; j < 4; ++j) {
      if (j == 2) {
        if (kh) l2touch(wsp + ((size_t)(blk * 4 + qg) * 64 + lane) * 128, lds_addr(smem) + 256);
        else if (side) {
          waitv<0>();
          const int lf = fresh_tid() & 63, g4 = lf >> 4, r4 = lf & 15, row = 8 * g4 + (r4 >> 2);
          const f32x4 sv0 = *(const f32x4*)(svec + (r4 & 1) * 128 + g4 * 32), sv1 = *(const f32x4*)(svec + (r4 & 1) * 128 + g4 * 32 + 16);
          const bf16x8 vec = BF8(((u32x4){pack2(sv0[0], sv0[1]), pack2(sv0[2], sv0[3]), pack2(sv1[0], sv1[1]), pack2(sv1[2], sv1[3])}));
          const unsigned nks = blk < 135 ? 20 : 27, dso = (blk < 135 ? OFF_WIN : OFF_WOUT) + (ng * 4 * nks + kt) * 1024;
          bf16x8 fr[4]; f32x4 pr[4];
#pragma unroll
          for (int f = 0; f < 4; ++f) {
            const int ch = 2 * f + ((r4 & 3) >> 1);
            const bf16x4 lo = lds_tr(stile + row * 128 + (ch ^ (row & 7)) * 16 + (r4 & 1) * 8), hi = lds_tr(stile + (row + 4) * 128 + (ch ^ ((row + 4) & 7)) * 16 + (r4 & 1) * 8);
            fr[f] = __builtin_shufflevector(lo, hi, 0, 1, 2, 3, 4, 5, 6, 7);
          }
#pragma unroll
          for (int f = 0; f < 4; ++f) { stc16(rs, l16, dso + f * nks * 1024, __builtin_bit_cast(u32x4, fr[f])); pr[f] = MFMA16(fr[f], vec, (f32x4){}); }
          if (r4 < 2) {
#pragma unroll
            for (int f = 0; f < 4; ++f) *(f32x4*)(stile + 3072 + r4 * 256 + f * 64 + g4 * 16) = pr[f];
          }
        }
      }
      if (j == 3 && kh == 0 && attempt == 0) { waitv<0>(); arrive(ws, C_REL + xcd, lane); }
      group(std::false_type{}, j < 3 ? 20480 : 0, j);
    }
    pB1[2] = pack2(e0[0], e0[1]); pB1[3] = pack2(e0[2], e0[3]);
    oB = MFMA32(vf[1][0], BF8(pB0), oB); oB = MFMA32(vf[1][1], BF8(pB1), oB);
    {
      f32x4* ex = (f32x4*)(lh + 1024 + qg * 4096) + lane;
#pragma unroll
      for (int g = 0; g < 3; ++g) ex[g * 64] = (f32x4){oA[4 * g], oA[4 * g + 1], oA[4 * g + 2], oA[4 * g + 3]};
    }
    int* flag = (int*)(smem + 528);
    BAR();
    float o[12];
    {
      const f32x4* ex = (const f32x4*)(smem + (kh ^ 1) * 81920 + 1024 + qg * 4096) + lane;
#pragma unroll
      for (int g = 0; g < 3; ++g) { const f32x4 t = ex[g * 64];
#pragma unroll
        for (int e = 0; e < 4; ++e) o[4 * g + e] = oB[4 * g + e] + t[e]; }
    }
    const float lo = xhalf(o[8]); const float l = h ? o[8] : lo;
    const float rl = __builtin_amdgcn_rcpf(l);
    const int tok = qb * 256 + qg * 64 + kh * 32 + r;
    const unsigned vo = (tok >> 4) * 20480 + (tok & 15) * 16;
    stc16(rs, vo + h * 256, OFF_O + (head >> 1) * 1024 + (head & 1) * 512,
          (u32x4){pack2(o[0] * rl, o[1] * rl), pack2(o[2] * rl, o[3] * rl), pack2(o[4] * rl, o[5] * rl), pack2(o[6] * rl, o[7] * rl)});
    if (h == 0) stc8(rs, vo - (tok & 15) * 8, OFF_O + 16384 + (head >> 3) * 1024 + (head & 1) * 512 + ((head >> 1) & 3) * 128, (u32x2){pack2(o[8] * rl, o[9] * rl), pack2(o[10] * rl, o[11] * rl)});
    if (wid == 4 && blk < 135 && attempt == 0 && lane < 32) {
      const char* pp = smem + 1024 + 3072 + lane * 16;
      const f32x4 v = (*(const f32x4*)pp + *(const f32x4*)(pp + 4096)) + (*(const f32x4*)(pp + 8192) + *(const f32x4*)(pp + 12288));
      stc16(rs, (lane >> 4) * 8192 + (lane & 15) * 16, OFF_CBP + (blk % 5) * 16384 + (blk / 5) * 256, __builtin_bit_cast(u32x4, v));
    }
    bool ok = (l > 1e-30f) && (l < 1e30f);
#pragma unroll
    for (int i = 0; i < 12; ++i) ok = ok && (fabsf(o[i]) < 3e38f);
    if (!ok && attempt == 0) *flag = 1;
    drain();
    BAR();
    const int redo = __builtin_amdgcn_readfirstlane(*(volatile LAS int*)flag);
    if (!redo) break;
    BAR();
  }
  if (wid == 7) arrive(ws, C_ATT + qb, lane);
#pragma unroll
  for (int i = 0; i < 13; ++i) proj_park(smem, i, tid, wq[i]);
  if (!__all(*(volatile LAS unsigned*)(smem + 768 + (lane & 7) * 4) >= 128u)) poll_ge(ws, C_REL + (lane & 7), 128);
}

template <bool FINAL, int KH, class HOOK>
DI void proj_body(char* __restrict__ ws, rsrc_t rs, const u16* __restrict__ x, u16* __restrict__ out, char* smem, int wid, u32x2 (&res)[3], const f32x4 (&gate)[3], const f32x4 (&cbv)[3], HOOK&& after_mma) {
  const int lane = fresh_tid() & 63;
  const int xcd = blockIdx.x & 7, idx = blockIdx.x >> 3, panel = xcd * 4 + (idx >> 3), cb = idx & 7;
  constexpr int NKS = FINAL ? 27 : 20;
  constexpr int K0 = KH ? (NKS + 1) / 2 : 0, NK = KH ? NKS / 2 : (NKS + 1) / 2, NKEEP = KH ? 2 : 3, UK = KH ? 3 : 0, UG = KH ? 0 : 3;
  const int m = wid & 3, mt = panel * 4 + m, g = lane >> 4, r = lane & 15;
  unsigned l16 = lane * 16; PIN(l16);
  const unsigned aso = fresh_s((FINAL ? OFF_U : OFF_O) + (mt * NKS + K0) * 1024);
  if (wid == 7) poll_ge(ws, FINAL ? C_U + panel : C_ATT + (panel >> 2), FINAL ? 8 : 32);
  BAR();
  bf16x8 a[NK];
#pragma unroll
  for (int k = 0; k < NK; ++k) {
    if (!FINAL && K0 + k >= 16) {
      const u32x2 lo = ldc8(rs, (l16 >> 1) + (k & 3) * 1024, aso + (k >> 2) * 4096), hi = ldc8(rs, (l16 >> 1) + (k & 3) * 1024 + 512, aso + (k >> 2) * 4096);
      a[k] = BF8(((u32x4){lo[0], lo[1], hi[0], hi[1]}));
    } else a[k] = BF8(ldc16(rs, l16 + (k & 3) * 1024, aso + (k >> 2) * 4096));
  }
  SCHED();
  constexpr int PD = FINAL ? 5 : 6;
  const char* bl = smem + lane * 16;
  bf16x8 bf[PD + 1][5]; f32x4 acc[5];
#pragma unroll
  for (int u = 0; u < 5; ++u) acc[u] = (f32x4){};
#pragma unroll
  for (int k = 0; k < PD; ++k)
#pragma unroll
    for (int u = 0; u < 5; ++u) bf[k][u] = *(const bf16x8*)(bl + proj_pos(u * NKS + K0 + k));
  SCHED();
#pragma unroll
  for (int k = 0; k < NK; ++k) {
#pragma unroll
    for (int u = 0; u < 5; ++u) {
      if (k + PD < NK) bf[(k + PD) % (PD + 1)][u] = *(const bf16x8*)(bl + proj_pos(u * NKS + K0 + k + PD));
      acc[u] = MFMA16(bf[k % (PD + 1)][u], a[k], acc[u]);
      SCHED();
    }
  }
  f32x4* ex = (f32x4*)(smem + proj_pos(FINAL ? 136 : 104)) + m * 5 * 64 + lane;
#pragma unroll
  for (int i = 0; i < 5 - NKEEP; ++i) ex[(UG + i) * 64] = acc[UG + i];
  f32x4 part[NKEEP];
#pragma unroll
  for (int i = 0; i < NKEEP; ++i) {
    part[i] = fma4s(gate[i], acc[UK + i], (f32x4){bflo(res[i][0]), bfhi(res[i][0]), bflo(res[i][1]), bfhi(res[i][1])});
    PIN(part[i]);
  }
  SCHED();
  after_mma();
  BAR();
  f32x4 oth[NKEEP];
#pragma unroll
  for (int i = 0; i < NKEEP; ++i) oth[i] = ex[(UK + i) * 64];
  f32x4 sv = (f32x4){}, qv = (f32x4){};
#pragma unroll
  for (int i = 0; i < NKEEP; ++i) {
    const f32x4 v = part[i] + gate[i] * oth[i];
    const u32x2 o = (u32x2){pack2(v[0], v[1]), pack2(v[2], v[3])};
    if (FINAL) *(u32x2*)(out + (size_t)(mt * 16 + r) * D_ + (cb * 5 + UK + i) * 16 + g * 4) = o;
    else {
      res[i] = o; PIN(res[i]);
      const f32x4 w = v * cbv[i]; const int un = cb * 5 + UK + i;
      stc8(rs, ((g >> 1) * 16 + r) * 16 + (g & 1) * 8, OFF_X1S + mt * 20480 + un * 512, (u32x2){pack2(w[0], w[1]), pack2(w[2], w[3])});
      sv += v; qv += v * v;
    }
  }
  if (!FINAL) {
    const float s = sum_g((sv[0] + sv[1]) + (sv[2] + sv[3])), q = sum_g((qv[0] + qv[1]) + (qv[2] + qv[3]));
    if (g == 0) stc8(rs, r * 8, OFF_ST1 + ((panel * 16 + cb * 2 + KH) * 64 + m * 16) * 8, (u32x2){__float_as_uint(s), __float_as_uint(q)});
    drain();
    BAR();
    if (wid == 7) arrive(ws, C_X1 + panel, lane);
  }
}

constexpr float GC1 = -2.8853900817779268f * 0.7978845608028654f, GC2 = GC1 * 0.044715f;
DI void gelu8(f32x4& a0, const f32x4& l0, f32x4& a1, const f32x4& l1) {
  float t[8];
#define G8(expr0, expr1) do { _Pragma("unroll") for (int i = 0; i < 4; ++i) { t[i] = opq(expr0); } _Pragma("unroll") for (int i = 0; i < 4; ++i) { t[4 + i] = opq(expr1); } SCHED(); } while (0)
  G8(a0[i] * a0[i], a1[i] * a1[i]);
  G8(__builtin_fmaf(t[i], l0[3], l0[2]), __builtin_fmaf(t[4 + i], l1[3], l1[2]));
  G8(a0[i] * t[i], a1[i] * t[4 + i]);
  G8(__builtin_amdgcn_exp2f(t[i]), __builtin_amdgcn_exp2f(t[4 + i]));
  G8(__builtin_fmaf(t[i], l0[0], l0[0]), __builtin_fmaf(t[4 + i], l1[0], l1[0]));
  G8(__builtin_amdgcn_rcpf(t[i]), __builtin_amdgcn_rcpf(t[4 + i]));
  G8(a0[i] * t[i], a1[i] * t[4 + i]);
  a0 = (f32x4){t[0], t[1], t[2], t[3]}; a1 = (f32x4){t[4], t[5], t[6], t[7]};
}
constexpr int L4_Z1 = 114688, L4_TR = L4_Z1 + 16384, L4_LN = L4_TR + 16384, L4_ST2 = L4_LN + 1024, L4_FIN = L4_ST2 + 3584, L4_CB = L4_FIN + 512, L4_CNT = L4_CB + 2048;
constexpr int K5_R = 7;
constexpr unsigned DEAD = 0x80000000u;
DI int sp_stage(int s) { const int idx = blockIdx.x >> 3; return (s + (idx & 7) * 2) & 15; }
DI unsigned sp_voff(int wid, int lane, int i) { return ((wid & 3) * 16 + i * 8 + (lane >> 3)) * 4096 + (wid >> 2) * 128 + ((lane & 7) ^ (4 * i + (lane >> 4))) * 16; }
DI const char* sp_base(const u16* wsp, int panel, int s) { return (const char*)wsp + (size_t)panel * 262144 + sp_stage(s) * 256; }
DI void sp_prefetch(u32x4 (&wr)[16 - K5_R][2], const u16* __restrict__ wsp, int panel, int wid, int lane) {
  const unsigned v0 = sp_voff(wid, lane, 0), v1 = sp_voff(wid, lane, 1);
#pragma unroll
  for (int s = K5_R; s < 16; ++s) { wr[s - K5_R][0] = *(const u32x4*)(sp_base(wsp, panel, s) + v0); wr[s - K5_R][1] = *(const u32x4*)(sp_base(wsp, panel, s) + v1); }
  asm volatile("" ::: "memory");
}
template <int S0, int S1>
DI void sp_ring(const u16* __restrict__ wsp, char* smem, int panel, int wid, int lane) {
  const unsigned v0 = sp_voff(wid, lane, 0), v1 = sp_voff(wid, lane, 1);
#pragma unroll
  for (int s = S0; s < S1; ++s) { glds16(sp_base(wsp, panel, s) + v0, lds_addr(smem) + wid * 2048 + s * 16384); glds16(sp_base(wsp, panel, s) + v1, lds_addr(smem) + wid * 2048 + s * 16384 + 1024); }
}
DI unsigned win_boff(int wid, int lane) { const int cb = (blockIdx.x >> 3) & 7; return lane * 16 + (wid == 6 && cb >= 6 ? DEAD : 0u); }
DI unsigned win_soff(int wid, bool z2) { return OFF_WIN + ((z2 ? 54 : 0) + unit0_of((blockIdx.x >> 3) & 7) + wid) * 20480; }
#define WIN_LD(so, k) BF8(ldc16(rs, bo + ((k) & 3) * 1024, (so) + ((k) >> 2) * 4096))
DI void win_prefetch(rsrc_t rs, int wid, bf16x8 (&b2)[20]) {
  const unsigned bo = win_boff(wid, fresh_tid() & 63), so = win_soff(wid, true);
#pragma unroll
  for (int k = 0; k < 20; ++k) b2[k] = WIN_LD(so, k);
}
template <class HOOK>
DI void win_mma(const char* al, const bf16x8 (&b)[20], f32x4 (&acc)[4], HOOK&& hook) {
  constexpr int PD = 4;
  bf16x8 af[PD + 1][2];
#define WIN_RD(i) do { af[(i) % (PD + 1)][0] = *(const bf16x8*)(al + ((i) & 3) * 20480 + ((i) >> 2) * 2048); af[(i) % (PD + 1)][1] = *(const bf16x8*)(al + ((i) & 3) * 20480 + ((i) >> 2) * 2048 + 1024); } while (0)
#pragma unroll
  for (int i = 0; i < PD; ++i) WIN_RD(i);
  SCHED();
  sfor<40>([&](auto I) __attribute__((always_inline)) {
    constexpr int i = I, m = i & 3, k = (i >> 2) * 2;
    if constexpr (i + PD < 40) WIN_RD(i + PD);
    acc[m] = MFMA16(b[k], af[i % (PD + 1)][0], acc[m]);
    acc[m] = MFMA16(b[k + 1], af[i % (PD + 1)][1], acc[m]); PIN(acc[m]);
    hook(I);
    SCHED();
  });
}
DI void stage_x1s(char* __restrict__ ws, char* smem, int panel, int wid, int lane) {
  const char* src = ws + OFF_X1S + (size_t)panel * 81920 + lane * 16;
#pragma unroll
  for (int i = 0; i < 12; ++i) { const int f = i < 11 ? i * 7 + wid : min(77 + wid, 79); glds16c(src + f * 1024, lds_addr(smem) + f * 1024); }
}
DI void load_bb(const u16* __restrict__ bsp, int panel, int wid, int lane, u16 (&bb)[2]) { const u16* p = bsp + panel * 64 + (wid >> 2) * 32 + (lane & 15); bb[0] = p[0]; bb[1] = p[16]; asm volatile("" ::: "memory"); }
DI void win_tail(char* __restrict__ ws, const u16* __restrict__ wsp, char* smem, int panel, int cb, int wid, int lane) {
  BAR();
  if (wid == 7 && lane < 4) __hip_atomic_fetch_add((GAS unsigned*)(ws + OFF_CNT + (C_Z2 + lane * 8 + cb) * 256), 1u, __ATOMIC_RELAXED, __HIP_MEMORY_SCOPE_AGENT);
}

template <bool EARLY>
DI void win_body(char* __restrict__ ws, rsrc_t rs, const u16* __restrict__ wsp, char* smem, int wid, const bf16x8 (&b2)[20], u32x4 (&wr)[16 - K5_R][2], const u16* __restrict__ bsp, u16 (&bb)[2]) {
  const int tid = fresh_tid(), lane = tid & 63, g = lane >> 4, r = lane & 15;
  const int xcd = blockIdx.x & 7, idx = blockIdx.x >> 3, panel = xcd * 4 + (idx >> 3), cb = idx & 7, u0 = unit0_of(cb);
  float* lnp = (float*)(smem + L4_LN); float* st2 = (float*)(smem + L4_ST2); float* fin = (float*)(smem + L4_FIN); float* cbl = (float*)(smem + L4_CB);
  int* flg = (int*)(smem + L4_CNT);
  __builtin_amdgcn_s_setprio(1);
  const unsigned bo = win_boff(wid, lane), so1 = win_soff(wid, false);
  bf16x8 b1[20];
  BAR();
  stage_x1s(ws, smem, panel, wid, lane);
  BAR();
  f32x4 ln[4], z2[4], z1[4];
#pragma unroll
  for (int m = 0; m < 4; ++m) ln[m] = *(const f32x4*)(lnp + (m * 16 + r) * 4);
  {
    const f32x4 cs2 = *(const f32x4*)(cbl + 112 + wid * 16 + g * 4), bi2 = *(const f32x4*)(cbl + 256 + 112 + wid * 16 + g * 4);
    const f32x4 cs1 = *(const f32x4*)(cbl + wid * 16 + g * 4), bi1 = *(const f32x4*)(cbl + 256 + wid * 16 + g * 4);
#pragma unroll
    for (int m = 0; m < 4; ++m) {
#pragma unroll
      for (int e = 0; e < 4; ++e) { z2[m][e] = opq(__builtin_fmaf(ln[m][1], cs2[e], opq(ln[m][0] * bi2[e]))); z1[m][e] = opq(__builtin_fmaf(ln[m][1], cs1[e], opq(ln[m][0] * bi1[e]))); }
      PIN(z1[m]); PIN(z2[m]);
    }
  }
#pragma unroll
  for (int k = 0; k < 4; ++k) b1[k] = WIN_LD(so1, k);
  waitv<4>();
  BAR();
  load_bb(bsp, panel, wid, lane, bb);
  win_mma(smem + lane * 16, b2, z2, [&](auto I) __attribute__((always_inline)) { constexpr int i = I; if constexpr (i < 32 && (i & 1)) b1[4 + i / 2] = WIN_LD(so1, 4 + i / 2); });
  gelu8(z2[0], ln[0], z2[1], ln[1]); gelu8(z2[2], ln[2], z2[3], ln[3]);
  {
    float s[4], q[4];
#pragma unroll
    for (int m = 0; m < 4; ++m) { s[m] = opq(opq(z2[m][0] + z2[m][1]) + opq(z2[m][2] + z2[m][3])); q[m] = opq(__builtin_fmaf(z2[m][0], z2[m][0], opq(z2[m][1] * z2[m][1])) + opq(__builtin_fmaf(z2[m][2], z2[m][2], opq(z2[m][3] * z2[m][3])))); }
    auto pa = __builtin_amdgcn_permlane32_swap(__float_as_uint(s[0]), __float_as_uint(s[2]), false, false);
    auto pb = __builtin_amdgcn_permlane32_swap(__float_as_uint(s[1]), __float_as_uint(s[3]), false, false);
    auto pc = __builtin_amdgcn_permlane32_swap(__float_as_uint(q[0]), __float_as_uint(q[2]), false, false);
    auto pd = __builtin_amdgcn_permlane32_swap(__float_as_uint(q[1]), __float_as_uint(q[3]), false, false);
    const float sa = opq(__uint_as_float(pa[0]) + __uint_as_float(pa[1])), sb = opq(__uint_as_float(pb[0]) + __uint_as_float(pb[1]));
    const float qa = opq(__uint_as_float(pc[0]) + __uint_as_float(pc[1])), qb = opq(__uint_as_float(pd[0]) + __uint_as_float(pd[1]));
    auto ps = __builtin_amdgcn_permlane16_swap(__float_as_uint(sa), __float_as_uint(sb), false, false);
    auto pq = __builtin_amdgcn_permlane16_swap(__float_as_uint(qa), __float_as_uint(qb), false, false);
    *(f32x2*)(st2 + (wid * 64 + lane) * 2) = (f32x2){opq(__uint_as_float(ps[0]) + __uint_as_float(ps[1])), opq(__uint_as_float(pq[0]) + __uint_as_float(pq[1]))};
  }
  asm volatile("" ::: "memory");
  if (lane == 0) __hip_atomic_fetch_add(flg, 1, __ATOMIC_RELAXED, __HIP_MEMORY_SCOPE_WORKGROUP);
  asm volatile("" ::: "memory");
  __builtin_amdgcn_s_setprio(0);
  const unsigned wvo[2] = {sp_voff(wid, lane, 0), sp_voff(wid, lane, 1)};
  win_mma(smem + lane * 16, b1, z1, [&](auto I) __attribute__((always_inline)) {
    constexpr int i = I, j = i < 8 ? i : (i & 1) ? -1 : 4 + i / 2;
    if constexpr (EARLY && j >= 0 && j < 18) wr[j >> 1][j & 1] = *(const u32x4*)(sp_base(wsp, panel, K5_R + (j >> 1)) + wvo[j & 1]);
  });
  gelu8(z1[0], ln[0], z1[1], ln[1]); gelu8(z1[2], ln[2], z1[3], ln[3]);
#pragma unroll
  for (int mm = 0; mm < 2; ++mm)
    *(u32x4*)(smem + L4_Z1 + wid * 2048 + mm * 1024 + lane * 16) = (u32x4){pack2(z1[2 * mm][0], z1[2 * mm][1]), pack2(z1[2 * mm][2], z1[2 * mm][3]), pack2(z1[2 * mm + 1][0], z1[2 * mm + 1][1]), pack2(z1[2 * mm + 1][2], z1[2 * mm + 1][3])};
  BAR();
  char* tr = smem + L4_TR + wid * 2048;
#pragma unroll
  for (int m = 0; m < 4; ++m) {
    const f32x2 f = *(const f32x2*)(fin + (m * 16 + r) * 2);
    const f32x4 v = z2[m] * f[0] - f[1];
    *(u32x2*)(tr + (m * 16 + r) * 32 + g * 8) = (u32x2){pack2(v[0], v[1]), pack2(v[2], v[3])};
  }
  {
    const char* p = tr + (8 * g + (r >> 2)) * 32 + (r & 3) * 8;
    const bf16x4 lo0 = lds_tr(p), hi0 = lds_tr(p + 128), lo1 = lds_tr(p + 1024), hi1 = lds_tr(p + 1152);
    stc16(rs, bo, OFF_Z2 + (panel * 2 * 54 + u0 + wid) * 1024, __builtin_bit_cast(u32x4, (bf16x8)__builtin_shufflevector(lo0, hi0, 0, 1, 2, 3, 4, 5, 6, 7)));
    stc16(rs, bo, OFF_Z2 + ((panel * 2 + 1) * 54 + u0 + wid) * 1024, __builtin_bit_cast(u32x4, (bf16x8)__builtin_shufflevector(lo1, hi1, 0, 1, 2, 3, 4, 5, 6, 7)));
  }
  SCHED();
  waitv<0>();
  win_tail(ws, wsp, smem, panel, cb, wid, lane);
  sp_ring<0, 5>(wsp, smem, panel, wid, lane);
  if (!EARLY) sp_prefetch(wr, wsp, panel, wid, lane);
}

DI void win_agent(char* __restrict__ ws, rsrc_t rs, const u16* __restrict__ wsp, char* smem, u32x4 (&wr)[16 - K5_R][2], const u16* __restrict__ bsp, u16 (&bb)[2]) {
  const int tid = fresh_tid(), lane = tid & 63, wid = 7;
  const int xcd = blockIdx.x & 7, idx = blockIdx.x >> 3, panel = xcd * 4 + (idx >> 3), cb = idx & 7, u0 = unit0_of(cb);
  float* lnp = (float*)(smem + L4_LN); float* st2 = (float*)(smem + L4_ST2); float* fin = (float*)(smem + L4_FIN); float* cbl = (float*)(smem + L4_CB);
  int* cnt = (int*)(smem + L4_CNT);
  __builtin_amdgcn_s_setprio(2);
  if (lane == 0) *cnt = 0;
  poll_ge(ws, lane < 8 ? C_ATT + lane : C_X1 + panel, lane < 8 ? 32 : 8);
  BAR();
  {
    u32x2 t[16]; f32x4 cp[5][2];
#pragma unroll
    for (int i = 0; i < 16; ++i) t[i] = ldc8(rs, lane * 8 + (i & 7) * 512, OFF_ST1 + (panel * 16 + (i & 8)) * 512);
    const unsigned co = lane < 28 ? (u0 * 16 + lane * 4) * 4 : min((54 + u0) * 16 + min(lane - 28, 27) * 4, F_ - 4) * 4;
#pragma unroll
    for (int i = 0; i < 5; ++i) { cp[i][0] = ldc16f(rs, co, OFF_CBP + i * 16384); cp[i][1] = ldc16f(rs, co, OFF_CBP + i * 16384 + 8192); }
    float s = 0.f, q = 0.f;
#pragma unroll
    for (int i = 0; i < 16; ++i) { s += __uint_as_float(t[i][0]); q += __uint_as_float(t[i][1]); }
    const float mean = s * (1.f / 640.f), var = fmaxf(q * (1.f / 640.f) - mean * mean, 0.f);
    const float den = ln_den(var), ri = __builtin_amdgcn_rcpf(den);
    *(f32x4*)(lnp + lane * 4) = (f32x4){den, -rbf(mean), GC1 * ri, GC2 * ri * ri * ri};
#pragma unroll
    for (int v = 0; v < 2; ++v) *(f32x4*)(cbl + v * 256 + lane * 4) = ((cp[0][v] + cp[1][v]) + (cp[2][v] + cp[3][v])) + cp[4][v];
  }
  BAR();
  BAR();
  __builtin_amdgcn_s_setprio(0);
  __builtin_amdgcn_s_sleep(8);
  load_bb(bsp, panel, wid, lane, bb);
  sp_prefetch(wr, wsp, panel, wid, lane);
#pragma nounroll
  for (int w = 0; w < 8; ++w) sp_ring<5, 7>(wsp, smem, panel, w, lane);
  __builtin_amdgcn_s_setprio(2);
#pragma nounroll
  for (unsigned spin = 0; spin < (1u << 22); ++spin) {
    if (__hip_atomic_load(cnt, __ATOMIC_RELAXED, __HIP_MEMORY_SCOPE_WORKGROUP) >= 7) break;
    __builtin_amdgcn_s_sleep(1);
  }
  asm volatile("" ::: "memory");
  float s, q;
  {
    f32x2 t[7];
#pragma unroll
    for (int i = 0; i < 7; ++i) t[i] = *(const f32x2*)(st2 + (i * 64 + lane) * 2);
    s = t[0][0]; q = t[0][1];
#pragma unroll
    for (int i = 1; i < 6; ++i) { s = opq(s + t[i][0]); q = opq(q + t[i][1]); }
    if (cb < 6) { s = opq(s + t[6][0]); q = opq(q + t[6][1]); }
  }
  GAS u64* xch = (GAS u64*)(ws + OFF_XCH) + panel * 512;
  unsigned qb = __float_as_uint(q); if (qb == ~0u) qb = 0x7fc00000u;
  __hip_atomic_store(xch + cb * 64 + lane, ((u64)qb << 32) | __float_as_uint(s), __ATOMIC_RELAXED, __HIP_MEMORY_SCOPE_AGENT);
  u64 v[8];
#pragma nounroll
  for (unsigned spin = 0; spin < (1u << 19); ++spin) {
    bool ok = true;
#pragma unroll
    for (int i = 0; i < 8; ++i) { v[i] = __hip_atomic_load(xch + i * 64 + lane, __ATOMIC_RELAXED, __HIP_MEMORY_SCOPE_AGENT); ok = ok && ((unsigned)(v[i] >> 32) != ~0u); }
    if (__all(ok)) break;
    __builtin_amdgcn_s_sleep(1);
  }
  s = 0.f; q = 0.f;
#pragma unroll
  for (int i = 0; i < 8; ++i) { s = opq(s + __uint_as_float((unsigned)v[i])); q = opq(q + __uint_as_float((unsigned)(v[i] >> 32))); }
  const float mean = opq(s * (1.f / 864.f)), var = fmaxf(opq(q * (1.f / 864.f)) - mean * mean, 0.f);
  const float ri = __builtin_amdgcn_rcpf(ln_den(var));
  *(f32x2*)(fin + lane * 2) = (f32x2){ri, rbf(mean) * ri};
  __builtin_amdgcn_s_setprio(0);
  BAR();
  __builtin_amdgcn_s_sleep(6);
  sp_ring<0, 5>(wsp, smem, panel, wid, lane);
  win_tail(ws, wsp, smem, panel, cb, wid, lane);
}

constexpr int sp_q(int t) { const int w = 4 * 2 + t / 3; return t + (w < 4 * 4 ? w : 4 * 4); }
DI void phase_sp(char* __restrict__ ws, rsrc_t rs, const u16 (&bb)[2], char* smem, int wid, const u32x4 (&wr)[16 - K5_R][2], u32x2 (&res)[3], f32x4 (&gate)[3]) {
  const int tid = fresh_tid(), lane = tid & 63;
  const int xcd = blockIdx.x & 7, idx = blockIdx.x >> 3, panel = xcd * 4 + (idx >> 3), cb = idx & 7;
  const int kh = wid >> 2, up = wid & 3, u0 = unit0_of(cb) + 2 * up, nu = cb < 6 ? 7 : 6, g = lane >> 4, r = lane & 15;
  const unsigned dead[2] = {2 * up < nu ? 0u : DEAD, 2 * up + 1 < nu ? 0u : DEAD};
  unsigned bvo[2] = {lane * 16 + dead[0], lane * 16 + 1024 + dead[1]};
  const char* al[2][2];
#pragma unroll
  for (int k = 0; k < 2; ++k)
#pragma unroll
    for (int j = 0; j < 2; ++j) al[k][j] = smem + kh * 8192 + (j ^ kh) * 4096 + r * 128 + ((4 * k + g) ^ (r >> 1)) * 16;
  constexpr int LB = 4, PD = 3;
  bf16x8 bq[LB + 1][4], af[PD + 1][2]; f32x4 acc[2][4]; u32x4 wq[17];
#pragma unroll
  for (int i = 0; i < 2; ++i) {
    const float b0 = bf2f(bb[0]), b1 = bf2f(bb[1]);
    acc[i][0] = (f32x4){b0, b0, b0, b0}; acc[i][1] = (f32x4){b1, b1, b1, b1}; acc[i][2] = (f32x4){}; acc[i][3] = (f32x4){};
  }
  const unsigned bso = fresh_s(OFF_Z2 + (kh * 2 * 54 + u0) * 1024);
  PIN(bvo[0]); PIN(bvo[1]);
  if (wid == 7) poll_ge(ws, C_Z2 + (panel >> 3) * 8 + cb, 32);
  else if (kh && up < 3) waitv<2 * (16 - K5_R)>(); else waitv<0>();
  BAR();
#define SP_LD(s, j) (bq[(s) % (LB + 1)][j] = BF8(ldc16(rs, bvo[(j) & 1], bso + (sp_stage(s) * 4 + ((j) >> 1)) * 55296)))
#define SP_RD(t, k) (af[(t) % (PD + 1)][k] = *(const bf16x8*)(al[k][((t) >> 1) & 1] + (((t) >> 2) % K5_R) * 16384 + ((t) & 1) * 2048))
#pragma unroll
  for (int q = 0; q < sp_q(0); ++q) SP_LD(q >> 2, q & 3);
#pragma unroll
  for (int t = 0; t < PD; ++t) { SP_RD(t, 0); SP_RD(t, 1); }
  SCHED();
  sfor<64>([&](auto T) __attribute__((always_inline)) {
    constexpr int t = T, nx = t + PD, s = t >> 2, m = t & 3, tl = sp_q(t), t2 = sp_q(t + 1) - 1;
    if constexpr (nx == 16 || nx == 28 || nx == 44 || nx == 56) BAR();
    constexpr int c = nx >= 16 && nx < 24 ? nx - 16 : nx >= 28 && nx < 34 ? nx - 20 : nx >= 44 && nx < 48 ? nx - 30 : -1;
    if constexpr (nx < 64) SP_RD(nx, 0);
    acc[0][m] = MFMA16(bq[s % (LB + 1)][0], af[t % (PD + 1)][0], acc[0][m]);
    acc[0][m] = MFMA16(bq[s % (LB + 1)][2], af[t % (PD + 1)][1], acc[0][m]); PIN(acc[0][m]);
    if constexpr (tl < 64) SP_LD(tl >> 2, tl & 3); else if constexpr (tl - 64 < 17) wq[tl - 64] = proj_ld<true>(rs, tl - 64, wid, lane);
    SCHED();
    if constexpr (nx < 64) SP_RD(nx, 1);
    acc[1][m] = MFMA16(bq[s % (LB + 1)][1], af[t % (PD + 1)][0], acc[1][m]);
    acc[1][m] = MFMA16(bq[s % (LB + 1)][3], af[t % (PD + 1)][1], acc[1][m]); PIN(acc[1][m]);
    if constexpr (t2 > tl && t2 < 64) SP_LD(t2 >> 2, t2 & 3);
    if constexpr (c >= 0) *(u32x4*)(smem + ((c >> 1) % K5_R) * 16384 + wid * 2048 + (c & 1) * 1024 + lane * 16) = wr[c >> 1][c & 1];
    if constexpr (t == 63 && 4 * LB < 17) { _Pragma("unroll") for (int i = 4 * LB; i < 17; ++i) wq[i] = proj_ld<true>(rs, i, wid, lane); }
    if constexpr (t == 62) proj_small<true>(rs, nullptr, wid, lane, res, gate, gate);
    SCHED();
  });
  f32x4* exw = (f32x4*)(smem + 32768 + wid * 4096) + lane;
  const f32x4* exr = (const f32x4*)(smem + 32768 + (wid ^ 4) * 4096) + lane;
  u32x4 z[2];
#pragma unroll
  for (int i = 0; i < 2; ++i) z[i] = *(const u32x4*)(smem + L4_Z1 + (2 * up + i) * 2048 + kh * 1024 + lane * 16);
#pragma unroll
  for (int i = 0; i < 2; ++i)
#pragma unroll
    for (int j = 0; j < 2; ++j) exw[(i * 2 + j) * 64] = acc[i][2 + j];
  f32x4 zf[2][2];
#pragma unroll
  for (int i = 0; i < 2; ++i)
#pragma unroll
    for (int j = 0; j < 2; ++j) {
      zf[i][j] = (f32x4){bflo(z[i][2 * j]), bfhi(z[i][2 * j]), bflo(z[i][2 * j + 1]), bfhi(z[i][2 * j + 1])};
      acc[i][j] = mul4s(acc[i][j], zf[i][j]); PIN(zf[i][j]); PIN(acc[i][j]);
    }
  BAR();
  f32x4 oth[2][2];
#pragma unroll
  for (int i = 0; i < 2; ++i)
#pragma unroll
    for (int j = 0; j < 2; ++j) oth[i][j] = exr[(i * 2 + j) * 64];
#pragma unroll
  for (int i = 0; i < 2; ++i)
#pragma unroll
    for (int j = 0; j < 2; ++j) {
      const f32x4 v = acc[i][j] + zf[i][j] * oth[i][j];
      stc8(rs, ((g >> 1) * 16 + r) * 16 + (g & 1) * 8 + dead[i], OFF_U + (panel * 4 + 2 * kh + j) * 27648 + (u0 + i) * 512, (u32x2){pack2(v[0], v[1]), pack2(v[2], v[3])});
    }
  drain();
  BAR();
  if (wid == 7) arrive(ws, C_U + panel, lane);
#pragma unroll
  for (int i = 0; i < 17; ++i) proj_park(smem, i, tid, wq[i]);
}

extern "C" __global__ __launch_bounds__(512, 2) OCC2 void k_mega(char* __restrict__ ws, const u16* __restrict__ x, const u16* __restrict__ win,
                                                     const u16* __restrict__ wsp, const u16* __restrict__ bsp, const u16* __restrict__ wout, u16* __restrict__ out) {
  extern __shared__ __attribute__((aligned(16))) char smem[];
  const int wid = __builtin_amdgcn_readfirstlane(threadIdx.x >> 6);
  const rsrc_t rs = __builtin_amdgcn_make_buffer_rsrc(ws, 0, WS_USED, 0x00020000);
  u32x2 res[3]; f32x4 gate[3], cbv[3];
  phase_qkv(ws, rs, smem, wid);
  phase_attn(ws, rs, (const char*)wsp, win, wout, x, smem, wid, res, gate, cbv);
  u32x4 wr[16 - K5_R][2];
  auto nohook = []() __attribute__((always_inline)) {};
  u16 bb[2];
  if (wid >= 4) {
    proj_body<false, 1>(ws, rs, x, out, smem, wid, res, gate, cbv, nohook);
    if (wid == 7) win_agent(ws, rs, wsp, smem, wr, bsp, bb);
    else { bf16x8 b2[20]; win_prefetch(rs, wid, b2); win_body<false>(ws, rs, wsp, smem, wid, b2, wr, bsp, bb); }
#pragma unroll
    for (int i = 0; i < 3; ++i) { gate[i] = (f32x4){}; cbv[i] = (f32x4){}; }
  }
  const int w2 = fresh_s(wid);
  if (w2 < 4) {
    bf16x8 b2[20];
    proj_body<false, 0>(ws, rs, x, out, smem, w2, res, gate, cbv, [&]() __attribute__((always_inline)) { win_prefetch(rs, w2, b2); });
    win_body<true>(ws, rs, wsp, smem, w2, b2, wr, bsp, bb);
  }
  phase_sp(ws, rs, bb, smem, wid, wr, res, gate);
  if (wid < 4) proj_body<true, 0>(ws, rs, x, out, smem, wid, res, gate, cbv, nohook);
  else proj_body<true, 1>(ws, rs, x, out, smem, wid, res, gate, cbv, nohook);
  asm volatile("k_mega_code_end:");
}

extern "C" void kernel_launch(void* const* d_in, const int* in_sizes, int n_in, void* d_out, int out_size, void* d_ws, size_t ws_size,
                              hipStream_t stream) {
  const u16* x = (const u16*)d_in[0];
  const u16* wqkv = (const u16*)d_in[1];
  const u16* wo = (const u16*)d_in[2];
  const u16* win = (const u16*)d_in[4];
  const u16* wsp = (const u16*)d_in[5];
  const u16* bsp = (const u16*)d_in[6];
  const u16* wout = (const u16*)d_in[7];
  const u16* cvec = (const u16*)d_in[8];
  const u16* wmod = (const u16*)d_in[9];
  char* ws = (char*)d_ws;
  (void)hipFuncSetAttribute((const void*)k_mega, hipFuncAttributeMaxDynamicSharedMemorySize, 163840);
  k0_prep<<<K0_GRID, 256, 0, stream>>>(x, wqkv, wo, cvec, wmod, ws);
  k_mega<<<256, 512, 163840, stream>>>(ws, x, win, wsp, bsp, wout, (u16*)d_out);
}
```

```cpp
#include <hip/hip_runtime.h>
#include <hip/hip_bf16.h>
#include <stdint.h>
#include <utility>

typedef __attribute__((ext_vector_type(8))) short bf16x8;
typedef __attribute__((ext_vector_type(4))) short bf16x4;
typedef __attribute__((ext_vector_type(2))) float f32x2;
typedef __attribute__((ext_vector_type(4))) float f32x4;
typedef __attribute__((ext_vector_type(16))) float f32x16;
typedef __attribute__((ext_vector_type(2))) unsigned u32x2;
typedef __attribute__((ext_vector_type(4))) unsigned u32x4;
typedef __attribute__((ext_vector_type(2))) __bf16 bf2_t;
typedef __attribute__((ext_vector_type(2))) _Float16 h2_t;
typedef unsigned short u16;
typedef unsigned long long u64;
#define LAS __attribute__((address_space(3)))
#define GAS __attribute__((address_space(1)))
#define DI __device__ __forceinline__

constexpr int D_ = 640, NQKV = 1920, F_ = 1728;

constexpr unsigned OFF_CNT = 0;
constexpr unsigned OFF_MOD = 65536;
constexpr unsigned OFF_MODP = OFF_MOD + 16384;
constexpr unsigned OFF_CBP = OFF_MODP + 61440;
constexpr unsigned OFF_XCH = OFF_CBP + 5 * 16384;
constexpr unsigned OFF_ST1 = OFF_XCH + 131072;
constexpr unsigned OFF_XHAT = OFF_ST1 + 262144;
constexpr unsigned OFF_WQKV = OFF_XHAT + 2621440;
constexpr unsigned OFF_WO = OFF_WQKV + 2457600;
constexpr unsigned OFF_WIN = OFF_WO + 819200;
constexpr unsigned OFF_WOUT = OFF_WIN + 2211840;
constexpr unsigned OFF_Q = OFF_WOUT + 1105920;
constexpr unsigned OFF_KV = OFF_Q + 32 * 81920;
constexpr unsigned OFF_O = OFF_KV + 32 * 163840;
constexpr unsigned OFF_X1S = OFF_O + 2621440;
constexpr unsigned OFF_Z2 = OFF_X1S + 2621440;
constexpr unsigned OFF_U = OFF_Z2 + 3538944;
constexpr unsigned WS_USED = OFF_U + 3538944;
constexpr int C_QKV = 0, C_MOD = 15  , C_ATT = 128, C_REL = 136, C_X1 = 144, C_U = 176, C_Z2 = 208  ;

DI unsigned pack2(float lo, float hi) { return __builtin_bit_cast(unsigned, __builtin_convertvector((f32x2){lo, hi}, bf2_t)); }
DI float opq(float x) { asm("" : "+v"(x)); return x; }
DI f32x4 fma4s(f32x4 a, f32x4 b, f32x4 c) { return (f32x4){opq(__builtin_fmaf(a[0], b[0], c[0])), opq(__builtin_fmaf(a[1], b[1], c[1])), opq(__builtin_fmaf(a[2], b[2], c[2])), opq(__builtin_fmaf(a[3], b[3], c[3]))}; }
DI f32x4 mul4s(f32x4 a, f32x4 b) { return (f32x4){opq(a[0] * b[0]), opq(a[1] * b[1]), opq(a[2] * b[2]), opq(a[3] * b[3])}; }
DI unsigned pack2h(float lo, float hi) { return __builtin_bit_cast(unsigned, __builtin_convertvector((f32x2){lo, hi}, h2_t)); }
DI float bflo(unsigned u) { return __uint_as_float(u << 16); }
DI float bfhi(unsigned u) { return __uint_as_float(u & 0xffff0000u); }
DI float rbf(float x) { return bflo(pack2(x, x)); }
DI float bf2f(u16 b) { return __uint_as_float((unsigned)b << 16); }
#define MFMA32(a, b, c) __builtin_amdgcn_mfma_f32_32x32x16_bf16((a), (b), (c), 0, 0, 0)
#define MFMA32K8(a, b, c) __builtin_amdgcn_mfma_f32_32x32x8bf16_1k((a), (b), (c), 0, 0, 0)
#define MFMA16(a, b, c) __builtin_amdgcn_mfma_f32_16x16x32_bf16((a), (b), (c), 0, 0, 0)
#define BAR() asm volatile("s_waitcnt lgkmcnt(0)\n\ts_barrier" ::: "memory")
#define SCHED() __builtin_amdgcn_sched_barrier(0)
#define PIN(x) asm volatile("" : "+v"(x))
#define OCC2 __attribute__((amdgpu_waves_per_eu(2, 2)))
#define BF8(x) __builtin_bit_cast(bf16x8, x)
#define BF4(x) __builtin_bit_cast(bf16x4, x)
template <int N> DI void waitv() { asm volatile("s_waitcnt vmcnt(%0)" ::"i"(N) : "memory"); }
DI void drain() { waitv<0>(); __builtin_amdgcn_s_waitcnt(0x0F70); }
template <class F, int... I> DI void sfor_impl(F&& f, std::integer_sequence<int, I...>) { (f(std::integral_constant<int, I>{}), ...); }
template <int N, class F> DI void sfor(F&& f) { sfor_impl(f, std::make_integer_sequence<int, N>{}); }
typedef short v4i16_t __attribute__((ext_vector_type(4)));
DI bf16x4 lds_tr(const char* p) { return __builtin_bit_cast(bf16x4, __builtin_amdgcn_ds_read_tr16_b64_v4i16((LAS v4i16_t*)p)); }
DI void glds16(const void* g, unsigned lds_base) {
  unsigned sv;
  asm volatile("s_mov_b32 %0, m0\n\ts_mov_b32 m0, %2\n\ts_nop 0\n\tglobal_load_lds_dwordx4 %1, off\n\ts_mov_b32 m0, %0" : "=&s"(sv) : "v"(g), "s"(lds_base) : "memory");
}
DI void glds16c(const void* g, unsigned lds_base) {
  unsigned sv;
  asm volatile("s_mov_b32 %0, m0\n\ts_mov_b32 m0, %2\n\ts_nop 0\n\tglobal_load_lds_dwordx4 %1, off sc1\n\ts_mov_b32 m0, %0" : "=&s"(sv) : "v"(g), "s"(lds_base) : "memory");
}
DI void glds4c(const void* g, unsigned lds_base) {
  unsigned sv;
  asm volatile("s_mov_b32 %0, m0\n\ts_mov_b32 m0, %2\n\ts_nop 0\n\tglobal_load_lds_dword %1, off sc1\n\ts_mov_b32 m0, %0" : "=&s"(sv) : "v"(g), "s"(lds_base) : "memory");
}
typedef __amdgpu_buffer_rsrc_t rsrc_t;
DI u32x4 ldc16(rsrc_t rs, unsigned voff, unsigned soff) { return __builtin_amdgcn_raw_buffer_load_b128(rs, voff, soff, 16); }
DI u32x2 ldc8(rsrc_t rs, unsigned voff, unsigned soff) { return __builtin_amdgcn_raw_buffer_load_b64(rs, voff, soff, 16); }
DI unsigned ldc4(rsrc_t rs, unsigned voff, unsigned soff) { return __builtin_amdgcn_raw_buffer_load_b32(rs, voff, soff, 16); }
DI void stc16(rsrc_t rs, unsigned voff, unsigned soff, u32x4 v) { __builtin_amdgcn_raw_buffer_store_b128(v, rs, voff, soff, 16); asm volatile("s_nop 1" ::"v"(v) : "memory"); }
DI void stc8(rsrc_t rs, unsigned voff, unsigned soff, u32x2 v) { __builtin_amdgcn_raw_buffer_store_b64(v, rs, voff, soff, 16); }
DI void stc4(rsrc_t rs, unsigned voff, unsigned soff, unsigned v) { __builtin_amdgcn_raw_buffer_store_b32(v, rs, voff, soff, 16); }
DI f32x4 ldc16f(rsrc_t rs, unsigned voff, unsigned soff) { return __builtin_bit_cast(f32x4, ldc16(rs, voff, soff)); }
DI void arrive(char* ws, int c, int lane) { if (lane == 0) __hip_atomic_fetch_add((GAS unsigned*)(ws + OFF_CNT + c * 256), 1u, __ATOMIC_RELAXED, __HIP_MEMORY_SCOPE_AGENT); }
DI void poll_ge(char* ws, int c, unsigned thr) {
  const GAS unsigned* p = (const GAS unsigned*)(ws + OFF_CNT + c * 256);
#pragma nounroll
  for (unsigned spin = 0; spin < (1u << 19); ++spin) {
    if (__all(__hip_atomic_load(p, __ATOMIC_RELAXED, __HIP_MEMORY_SCOPE_AGENT) >= thr)) break;
    __builtin_amdgcn_s_sleep(1);
  }
  asm volatile("" ::: "memory");
}
DI int fresh_s(int v) { asm volatile("" : "+s"(v)); return v; }
DI int fresh_tid() { int t = threadIdx.x; asm volatile("" : "+v"(t)); return t; }
DI void l2touch(const void* g, unsigned lds_scratch) {
  unsigned sv;
  asm volatile("s_mov_b32 %0, m0\n\ts_mov_b32 m0, %2\n\ts_nop 0\n\tglobal_load_lds_dword %1, off\n\ts_mov_b32 m0, %0" : "=&s"(sv) : "v"(g), "s"(lds_scratch) : "memory");
}
#define LDNT(p) __builtin_nontemporal_load(p)
DI float silu_bf(u16 c) { const float f = __uint_as_float((unsigned)c << 16); const float v = f / (1.f + __expf(-f)); return __uint_as_float(__builtin_bit_cast(unsigned, __builtin_convertvector((f32x2){v, v}, bf2_t)) << 16); }
DI unsigned lds_addr(const void* p) { return (unsigned)(uintptr_t)(const LAS void*)p; }
DI float xhalf(float v) {
  auto rr = __builtin_amdgcn_permlane32_swap(__float_as_uint(v), __float_as_uint(v), false, false);
  return __uint_as_float((threadIdx.x & 32) ? rr[0] : rr[1]);
}
DI float sum_g(float v) {
  auto a = __builtin_amdgcn_permlane16_swap(__float_as_uint(v), __float_as_uint(v), false, false);
  v = __uint_as_float(a[0]) + __uint_as_float(a[1]);
  auto b = __builtin_amdgcn_permlane32_swap(__float_as_uint(v), __float_as_uint(v), false, false);
  return __uint_as_float(b[0]) + __uint_as_float(b[1]);
}
template <int CTRL> DI float dpp_add(float v) { return v + __uint_as_float(__builtin_amdgcn_update_dpp(0u, __float_as_uint(v), CTRL, 0xf, 0xf, true)); }
DI float sum16(float v) {
  v = dpp_add<0x128>(v); v = dpp_add<0x124>(v); v = dpp_add<0x122>(v); return dpp_add<0x121>(v);
}
DI u32x4 modulate8(u32x4 v, f32x4 sc0, f32x4 sc1, f32x4 sh0, f32x4 sh1) {
  unsigned o0, o1, o2, o3; float t0, t1, t2, t3;
  asm volatile(
      "v_fma_mix_f32 %0, %8, %12, %20 op_sel_hi:[1,0,0]\n\tv_fma_mix_f32 %4, %8, %13, %21 op_sel:[1,0,0] op_sel_hi:[1,0,0]\n\t"
      "v_fma_mix_f32 %1, %9, %14, %22 op_sel_hi:[1,0,0]\n\tv_fma_mix_f32 %5, %9, %15, %23 op_sel:[1,0,0] op_sel_hi:[1,0,0]\n\t"
      "v_fma_mix_f32 %2, %10, %16, %24 op_sel_hi:[1,0,0]\n\tv_fma_mix_f32 %6, %10, %17, %25 op_sel:[1,0,0] op_sel_hi:[1,0,0]\n\t"
      "v_fma_mix_f32 %3, %11, %18, %26 op_sel_hi:[1,0,0]\n\tv_fma_mix_f32 %7, %11, %19, %27 op_sel:[1,0,0] op_sel_hi:[1,0,0]\n\t"
      "v_cvt_pk_bf16_f32 %0, %0, %4\n\tv_cvt_pk_bf16_f32 %1, %1, %5\n\tv_cvt_pk_bf16_f32 %2, %2, %6\n\tv_cvt_pk_bf16_f32 %3, %3, %7"
      : "=&v"(o0), "=&v"(o1), "=&v"(o2), "=&v"(o3), "=&v"(t0), "=&v"(t1), "=&v"(t2), "=&v"(t3)
      : "v"(v[0]), "v"(v[1]), "v"(v[2]), "v"(v[3]), "v"(sc0[0]), "v"(sc0[1]), "v"(sc0[2]), "v"(sc0[3]), "v"(sc1[0]), "v"(sc1[1]), "v"(sc1[2]), "v"(sc1[3]),
        "v"(sh0[0]), "v"(sh0[1]), "v"(sh0[2]), "v"(sh0[3]), "v"(sh1[0]), "v"(sh1[1]), "v"(sh1[2]), "v"(sh1[3]));
  return (u32x4){o0, o1, o2, o3};
}
DI float ln_den(float var) { return rbf(__builtin_amdgcn_sqrtf(rbf(rbf(var) + 1e-5f))); }
DI int blk_panel() { return ((blockIdx.x & 7) >> 1) * (4 << 1) + (blockIdx.x >> (6 - 1)); }
DI int blk_cb() { return (blockIdx.x & ((1 << 1) - 1)) * (8 >> 1) + ((blockIdx.x >> 3) & ((8 >> 1) - 1)); }
DI int unit0_of(int j) { return j < 6 ? 7 * j : 42 + 6 * (j - 6); }

DI void K0ST(char* p, u32x4 v) { *(u32x4*)p = v; }
constexpr int K0_MOD = 240, K0_LN = 128, K0_RL = 240, K0_WO = 50, K0_INIT = 12, K0_WARM = 24;
constexpr int K0_GRID = K0_WARM + K0_MOD + K0_LN + K0_RL + K0_WO + K0_INIT;

__global__ __launch_bounds__(256) void k0_prep(const u16* __restrict__ x, const u16* __restrict__ wqkv, const u16* __restrict__ wo, const u16* __restrict__ cvec,
                                               const u16* __restrict__ wmod, char* __restrict__ ws) {
  __shared__ __attribute__((aligned(16))) char smem[4 * 4608];
  const int tid = threadIdx.x, lane = tid & 63, wid = __builtin_amdgcn_readfirstlane(tid >> 6);
  int b = blockIdx.x;
  if (b < K0_WARM) {
    u64 pc; unsigned len;
    asm volatile("s_getpc_b64 s[30:31]\n\ts_add_u32 s30, s30, k_mega@rel32@lo+4\n\ts_addc_u32 s31, s31, k_mega@rel32@hi+12\n\ts_mov_b64 %0, s[30:31]\n\t"
                 "s_mov_b32 %1, k_mega_code_end-k_mega" : "=s"(pc), "=s"(len) :: "scc", "s30", "s31");
    l2touch((const char*)pc + min(((b >> 3) * 256 + tid) * 128u, len - 4u), lds_addr(smem) + wid * 256);
    return;
  }
  b -= K0_WARM;
  if (b < K0_MOD) {
    const int cg = b >> 2, ks = b & 3, c = tid & 7, row0 = ks * 160 + (tid >> 3), col0 = cg * 64;
    u32x4 w[5]; u16 cv[5];
#pragma unroll
    for (int i = 0; i < 5; ++i) { w[i] = LDNT((const u32x4*)(wmod + (size_t)(row0 + 32 * i) * 3840 + col0 + c * 8)); cv[i] = cvec[row0 + 32 * i]; }
    float acc[8] = {0, 0, 0, 0, 0, 0, 0, 0};
#pragma unroll
    for (int i = 0; i < 5; ++i) {
      const float s = silu_bf(cv[i]);
#pragma unroll
      for (int e = 0; e < 4; ++e) { acc[2 * e] += s * bflo(w[i][e]); acc[2 * e + 1] += s * bfhi(w[i][e]); }
    }
#pragma unroll
    for (int e = 0; e < 8; ++e) acc[e] = sum_g(dpp_add<0x128>(acc[e]));
    float* red = (float*)smem;
    if (lane < 8) { *(f32x4*)(red + wid * 64 + lane * 8) = (f32x4){acc[0], acc[1], acc[2], acc[3]}; *(f32x4*)(red + wid * 64 + lane * 8 + 4) = (f32x4){acc[4], acc[5], acc[6], acc[7]}; }
    __syncthreads();
    if (tid < 64) ((float*)(ws + OFF_MODP))[ks * 3840 + cg * 64 + tid] = (red[tid] + red[64 + tid]) + (red[128 + tid] + red[192 + tid]);
    return;
  }
  b -= K0_MOD;
  if (b < K0_LN) {
    b = (b & 7) * 16 + (b >> 3);
    const int row = b * 16 + wid * 4 + (lane >> 4), c = lane & 15;
    const u16* xr = x + (size_t)row * D_ + c * 8;
    u32x4 v[5];
#pragma unroll
    for (int i = 0; i < 5; ++i) v[i] = *(const u32x4*)(xr + i * 128);
    float s = 0.f, q = 0.f;
#pragma unroll
    for (int i = 0; i < 5; ++i)
#pragma unroll
      for (int e = 0; e < 4; ++e) s += bflo(v[i][e]) + bfhi(v[i][e]);
    const float mean = sum16(s) * (1.f / 640.f);
#pragma unroll
    for (int i = 0; i < 5; ++i)
#pragma unroll
      for (int e = 0; e < 4; ++e) { const float a = bflo(v[i][e]) - mean, d = bfhi(v[i][e]) - mean; q += a * a + d * d; }
    const float var = sum16(q) * (1.f / 640.f);
    const float rinv = __builtin_amdgcn_rcpf(ln_den(var)), mb = rbf(mean);
    char* dst = ws + OFF_XHAT + (size_t)row * 1280 + c * 16;
#pragma unroll
    for (int i = 0; i < 5; ++i) {
      u32x4 o;
#pragma unroll
      for (int e = 0; e < 4; ++e) o[e] = pack2h((bflo(v[i][e]) - mb) * rinv, (bfhi(v[i][e]) - mb) * rinv);
      K0ST(dst + i * 256, o);
    }
    return;
  }
  b -= K0_LN;
  if (b < K0_RL) {
    const int kt = b / 12, G = b % 12, sec = G >> 2, gq = G & 3;
    u32x4 v[3];
#pragma unroll
    for (int i = 0; i < 3; ++i) { const int ci = min(i * 256 + tid, 639); v[i] = LDNT((const u32x4*)(wqkv + (size_t)(kt * 32 + ci / 20) * NQKV + G * 160 + (ci % 20) * 8)); }
#pragma unroll
    for (int i = 0; i < 3; ++i) { const int ci = min(i * 256 + tid, 639); *(u32x4*)(smem + (ci / 20) * 336 + (ci % 20) * 16) = v[i]; }
    __syncthreads();
    const int g = lane >> 4, q = (lane & 15) >> 2, p = lane & 3;
    for (int f = wid; f < 10; f += 4) {
      const int t = f >> 1, x2 = f & 1;
      const int col = t == 4 ? 20 * (4 * x2 + p) + 16 : sec == 2 ? 20 * f + 4 * p : 20 * (2 * t + (p >> 1)) + 8 * (p & 1) + 4 * x2;
      const int T = sec * 20 + (t < 4 ? 4 * gq + t : 16 + gq);
      const char* pp = smem + (8 * g + q) * 336 + col * 2;
      const bf16x4 lo = lds_tr(pp), hi = lds_tr(pp + 4 * 336);
      K0ST(ws + OFF_WQKV + (((size_t)T * 20 + kt) * 2 + x2) * 1024 + lane * 16, __builtin_bit_cast(u32x4, (bf16x8)__builtin_shufflevector(lo, hi, 0, 1, 2, 3, 4, 5, 6, 7)));
    }
    return;
  }
  b -= K0_RL;
  if (b < K0_WO) {
    const int job = b * 4 + wid, kt = job / 10, ng = job % 10, j8 = lane >> 3;
    char* t = smem + wid * 4608;
    u32x4 v[4];
#pragma unroll
    for (int i = 0; i < 4; ++i) {
      const int c = 4 * kt + i, row = c < 64 ? 20 * (c >> 1) + 4 * (c & 1) + (j8 & 3) + 8 * (j8 >> 2) : 20 * (2 * (c - 64) + (j8 >> 2)) + 16 + (j8 & 3);
      v[i] = LDNT((const u32x4*)(wo + (size_t)row * D_ + ng * 64 + (lane & 7) * 8));
    }
#pragma unroll
    for (int i = 0; i < 4; ++i) *(u32x4*)(t + (i * 8 + j8) * 144 + (lane & 7) * 16) = v[i];
    const char* pp = t + (8 * (lane >> 4) + ((lane & 15) >> 2)) * 144 + (lane & 3) * 8;
#pragma unroll
    for (int f = 0; f < 4; ++f) {
      const bf16x4 lo = lds_tr(pp + f * 32), hi = lds_tr(pp + 4 * 144 + f * 32);
      K0ST(ws + OFF_WO + ((size_t)(ng * 4 + f) * 20 + kt) * 1024 + lane * 16, __builtin_bit_cast(u32x4, (bf16x8)__builtin_shufflevector(lo, hi, 0, 1, 2, 3, 4, 5, 6, 7)));
    }
    return;
  }
  b -= K0_WO;
  {
    u32x4* p = (u32x4*)(ws + (b < 8 ? OFF_XCH + b * 16384 : OFF_CNT + (b - 8) * 16384)) + tid;
    const unsigned f = b < 8 ? ~0u : 0u;
#pragma unroll
    for (int i = 0; i < 4; ++i) p[i * 256] = (u32x4){f, f, f, f};
  }
}

constexpr float QSCALE = 0.22360679774997896f * 1.4426950408889634f;
constexpr int K1_W = 3, K1_WB = 5, K1_WB0 = 1;

template <bool ISV, class FIN>
DI void k1_mma(const char* __restrict__ bsrc, const char* smem, int lane, FIN&& fin) {
  f32x4 acc[8][2];
  constexpr int PD = 4;
  bf16x8 bfr[K1_WB + 1][4], af[PD + 1][2];
  const char* lds[2];
#pragma unroll
  for (int k = 0; k < 2; ++k) lds[k] = smem + k * 8192 + (lane ^ (4 * k + (lane >> 4))) * 16;
#pragma unroll
  for (int i = 0; i < 4 * K1_WB0; ++i) bfr[i >> 2][i & 3] = *(const bf16x8*)(bsrc + i * 1024);
#pragma unroll
  for (int m = 0; m < 8; ++m) { acc[m][0] = (f32x4){}; acc[m][1] = (f32x4){}; }
  BAR();
#pragma unroll
  for (int i = 4 * K1_WB0; i < 4 * K1_WB; ++i) bfr[i >> 2][i & 3] = *(const bf16x8*)(bsrc + i * 1024);
  BAR();
  __builtin_amdgcn_s_setprio(1);
#define K1_RD(t, k) (af[(t) % (PD + 1)][k] = *(const bf16x8*)(lds[k] + (((t) >> 3) % 3) * 16384 + ((t) & 7) * 1024))
#define K1_MM(c, b, a) (c = ISV ? MFMA16(a, b, c) : MFMA16(b, a, c))
#pragma unroll
  for (int t = 0; t < PD; ++t) { K1_RD(t, 0); K1_RD(t, 1); }
  SCHED();
  sfor<80>([&](auto T) __attribute__((always_inline)) {
    constexpr int t = T, s = t >> 3, m = t & 7, nx = t + PD;
    if constexpr (nx < 80 && (nx & 7) == 0) asm volatile("s_barrier" ::: "memory");
    if constexpr (nx < 80) K1_RD(nx, 0);
    K1_MM(acc[m][0], bfr[s % (K1_WB + 1)][0], af[t % (PD + 1)][0]); K1_MM(acc[m][0], bfr[s % (K1_WB + 1)][2], af[t % (PD + 1)][1]); PIN(acc[m][0]);
    SCHED();
    if constexpr (nx < 80) K1_RD(nx, 1);
    if constexpr (s + K1_WB < 10 && (m & 1)) bfr[(s + K1_WB) % (K1_WB + 1)][m >> 1] = *(const bf16x8*)(bsrc + ((s + K1_WB) * 4 + (m >> 1)) * 1024);
    K1_MM(acc[m][1], bfr[s % (K1_WB + 1)][1], af[t % (PD + 1)][0]); K1_MM(acc[m][1], bfr[s % (K1_WB + 1)][3], af[t % (PD + 1)][1]); PIN(acc[m][1]);
    SCHED();
    if constexpr (t >= 74) { fin(t - 74, acc[t - 74]); SCHED(); }
  });
  fin(6, acc[6]); fin(7, acc[7]);
  __builtin_amdgcn_s_setprio(0);
}


DI void phase_qkv(char* __restrict__ ws, rsrc_t rs, char* smem, int wid) {
  const int tid = fresh_tid(), lane = tid & 63;
  const int xcd = blockIdx.x & 7, idx = blockIdx.x >> 3;
  int ctr;
  if ((xcd & 1) && idx >= 28) {
    ctr = C_MOD;
    if (tid < 160) {
      const int col = 1280 + ((xcd >> 1) * 4 + idx - 28) * 160 + tid;
      const float* pp = (const float*)(ws + OFF_MODP) + col;
      float v = rbf((pp[0] + pp[3840]) + (pp[7680] + pp[11520]));
      if (col / 640 == 4) v = rbf(1.f + v);
      stc4(rs, col * 4, OFF_MOD, __float_as_uint(v));
    }
  } else {
    const int mt = (xcd >> 1) * 4 + (idx & 3), nt = (xcd & 1) * 8 + (idx >> 2);
    ctr = C_QKV + nt;
    if (wid < 4) {
      float* cst = (float*)(smem + 49152);
      const int c = lane & 7, rr = lane >> 3;
      const char* src = ws + OFF_XHAT + (size_t)(mt * 128 + wid * 32 + rr) * 1280 + c * 16;
      char* wdst = smem + (c >> 2) * 8192 + wid * 2048 + (c & 3) * 256 + (rr ^ c) * 16;
      u32x4 a[10][4];
      f32x4 c0[4], c1[4];
#pragma unroll
      for (int i = 0; i < 4; ++i) { c0[i] = *(const f32x4*)(ws + OFF_MODP + i * 15360 + tid * 16); if (wid == 0) c1[i] = *(const f32x4*)(ws + OFF_MODP + i * 15360 + 4096 + tid * 16); }
#pragma unroll
      for (int s = 0; s < K1_W; ++s)
#pragma unroll
        for (int m = 0; m < 4; ++m) a[s][m] = *(const u32x4*)(src + m * 10240 + s * 128);
      {
        f32x4 v = (c0[0] + c0[1]) + (c0[2] + c0[3]); const float one = tid >= 160 ? 1.f : 0.f;
        *(f32x4*)(cst + tid * 4) = (f32x4){rbf(one + rbf(v[0])), rbf(one + rbf(v[1])), rbf(one + rbf(v[2])), rbf(one + rbf(v[3]))};
        if (wid == 0) { v = (c1[0] + c1[1]) + (c1[2] + c1[3]); *(f32x4*)(cst + 1024 + tid * 4) = (f32x4){rbf(1.f + rbf(v[0])), rbf(1.f + rbf(v[1])), rbf(1.f + rbf(v[2])), rbf(1.f + rbf(v[3]))}; }
      }
      BAR();
      const float* cp = cst + c * 8;
      f32x4 kc[10][4];
#define K1_KC(s) do { kc[s][0] = *(const f32x4*)(cp + (s) * 64); kc[s][1] = *(const f32x4*)(cp + (s) * 64 + 4); kc[s][2] = *(const f32x4*)(cp + (s) * 64 + 640); kc[s][3] = *(const f32x4*)(cp + (s) * 64 + 644); } while (0)
#pragma unroll
      for (int s = 0; s < 6; ++s) K1_KC(s);
      SCHED();
      u32x4 o[2][4];
#define K1_XF(s, m) (o[(s) & 1][m] = modulate8(a[s][m], kc[s][2], kc[s][3], kc[s][0], kc[s][1]))
#define K1_WR(s, m) (*(u32x4*)(wdst + ((s) % 3) * 16384 + ((m) >> 1) * 1024 + ((m) & 1) * 128) = o[(s) & 1][m])
#pragma unroll
      for (int m = 0; m < 4; ++m) { K1_XF(0, m); K1_WR(0, m); }
      BAR();
#pragma unroll
      for (int m = 0; m < 4; ++m) a[K1_W][m] = *(const u32x4*)(src + m * 10240 + K1_W * 128);
#pragma unroll
      for (int m = 0; m < 4; ++m) K1_XF(1, m);
      SCHED();
#pragma unroll
      for (int s = 1; s < 10; ++s) {
#pragma unroll
        for (int m = 0; m < 4; ++m) {
          if (m == 0) K1_WR(s, 0);
          if (m < 3) K1_WR(s, m + 1);
          if (s + K1_W < 10) a[s + K1_W][m] = *(const u32x4*)(src + m * 10240 + (s + K1_W) * 128);
          if (s < 5) kc[s + 5][m] = *(const f32x4*)(cp + (s + 5) * 64 + (m & 1) * 4 + (m >> 1) * 640);
          SCHED();
          if (s < 9) K1_XF(s + 1, m);
          SCHED();
        }
        asm volatile("" :: "v"(o[s & 1][0]), "v"(o[s & 1][1]), "v"(o[s & 1][2]), "v"(o[s & 1][3]));
        BAR();
      }
    } else {
      const int cw = wid - 4;
      const char* bsrc = ws + OFF_WQKV + (size_t)(nt * 4 + cw) * 40 * 1024 + lane * 16;
      const int r = lane & 15, g = lane >> 4;
      if (nt >= 10) {
        const unsigned so = OFF_KV + (mt >> 1) * 20480 + 10240 + (mt & 1) * 5120;
        unsigned vo[2];
#pragma unroll
        for (int x = 0; x < 2; ++x)
          vo[x] = (nt < 14 ? ((nt - 10) * 8 + cw * 2 + x) * 163840u + ((g & 1) * 16 + r) * 16 : (cw * 8 + x * 4 + (r >> 2)) * 163840u + 512 + ((g & 1) * 4 + (r & 3)) * 16) + (g >> 1) * 8;
        k1_mma<true>(bsrc, smem, lane, [&](int m, const f32x4 (&a)[2]) __attribute__((always_inline)) {
#pragma unroll
          for (int x = 0; x < 2; ++x) stc8(rs, vo[x] + (m & 3) * 640, so + (m >> 2) * 2560, (u32x2){pack2(a[x][0], a[x][1]), pack2(a[x][2], a[x][3])});
        });
      } else {
        const bool isQ = nt < 5; const float sc = isQ ? QSCALE : 1.f;
        const unsigned so = isQ ? OFF_Q + mt * 5120 : OFF_KV + (mt >> 1) * 20480 + (mt & 1) * 5120, hs = isQ ? 81920u : 163840u;
        const int n5 = isQ ? nt : nt - 5;
        const bool wide = n5 < 4;
        const unsigned vo = wide ? (2 * (n5 * 4 + cw) + (g >> 1)) * hs + (g & 1) * 512 + r * 16 : (8 * cw + g) * hs + 1024 + r * 8;
        const f32x4 sc4 = (f32x4){sc, sc, sc, sc};
        k1_mma<false>(bsrc, smem, lane, [&](int m, const f32x4 (&a)[2]) __attribute__((always_inline)) {
          const f32x4 v0 = mul4s(a[0], sc4), v1 = mul4s(a[1], sc4);
          const u32x4 w = (u32x4){pack2(v0[0], v0[1]), pack2(v0[2], v0[3]), pack2(v1[0], v1[1]), pack2(v1[2], v1[3])};
          if (wide) stc16(rs, vo + (m & 1) * 256, so + (m >> 1) * 1280, w);
          else { stc8(rs, vo + (m & 1) * 128, so + (m >> 1) * 1280, (u32x2){w[0], w[1]}); stc8(rs, vo + (m & 1) * 128, so + 4 * hs + (m >> 1) * 1280, (u32x2){w[2], w[3]}); }
        });
      }
    }
  }
  waitv<0>();
  BAR();
  if (wid == 7 && lane < 8) __hip_atomic_fetch_add((GAS unsigned*)(ws + OFF_CNT + (lane * 16 + ctr) * 256), 1u, __ATOMIC_RELAXED, __HIP_MEMORY_SCOPE_AGENT);
  if (ctr == C_MOD && wid == 7) { __builtin_amdgcn_s_sleep(127); __builtin_amdgcn_s_sleep(60); }
}

constexpr int proj_pos(int f) { return 1024 + f * 1024; }
template <bool FINAL> DI u32x4 proj_ld(rsrc_t rs, int i, int wid, int lane) {
  constexpr int NF = FINAL ? 135 : 100;
  return ldc16(rs, lane * 16, (FINAL ? OFF_WOUT : OFF_WO) + (blk_cb() * NF + (i * 8 + 7 < NF ? i * 8 + wid : min(i * 8 + wid, NF - 1))) * 1024);
}
template <bool FINAL> DI void proj_small(rsrc_t rs, const u16* __restrict__ x, int wid, int lane, u32x2 (&res)[3], f32x4 (&gate)[3], f32x4 (&cbv)[3]) {
  const int mt = blk_panel() * 4 + (wid & 3), g = lane >> 4;
#pragma unroll
  for (int i = 0; i < 3; ++i) {
    const int n = (blk_cb() * 5 + min((wid >> 2) * 3 + i, 4)) * 16;
    if (!FINAL) { res[i] = *(const u32x2*)(x + (size_t)(mt * 16 + (lane & 15)) * D_ + n + g * 4); cbv[i] = ldc16f(rs, g * 16, OFF_MOD + (4 * 640 + n) * 4); }
    gate[i] = ldc16f(rs, g * 16, OFF_MOD + ((FINAL ? 5 : 2) * 640 + n) * 4);
  }
}
DI void proj_park(char* smem, int i, int tid, u32x4 v) { *(u32x4*)(smem + proj_pos(i * 8) + tid * 16) = v; }

DI void split3(float m, unsigned& w_h0, unsigned& w_h1) {
  const float n = -m; const float a = rbf(n), b = rbf(n - a), c = rbf(n - a - b);
  w_h0 = pack2(a, b); w_h1 = pack2(c, 0.f);
}

DI void phase_attn(char* __restrict__ ws, rsrc_t rs, const char* __restrict__ wsp, const u16* __restrict__ win, const u16* __restrict__ wout, const u16* __restrict__ x,
                   char* smem, int wid, u32x2 (&res)[3], f32x4 (&gate)[3], f32x4 (&cbv)[3]) {
  const int tid = fresh_tid(), lane = tid & 63;
  const int xcd = blockIdx.x & 7, idx = blockIdx.x >> 3, head = xcd * 4 + (idx >> 3), qb = idx & 7;
  const int r = lane & 31, h = lane >> 5, qg = wid & 3, kh = wid >> 2;
  unsigned kvo = OFF_KV + head * 163840, qo = OFF_Q + head * 81920 + (qb * 8 + qg * 2) * 1280;
  const unsigned kconst = h == 0 ? 0x3f803f80u : 0x00003f80u;
  const unsigned so16 = (tid & 255) * 16, l16 = lane * 16, l2 = 1024 + r * 8 + h * 4;
  u32x4 wq[13];
  for (int attempt = 0; attempt < 2; ++attempt) {
    asm volatile("" : "+s"(kvo), "+s"(qo));
    const char* kvs = ws + kvo + kh * 81920 + so16;
    const unsigned ldst = lds_addr(smem) + kh * 81920 + qg * 1024;
    u32x4 qA1w, qB1w, k1w; unsigned qA2w, qB2w, k2, m0sv;
    const char* d0 = kvs + (wid == 0 ? 4096 : 0); const char* d1 = kvs + 4096; const char* d2 = kvs + 8192; const char* d3 = kvs + 12288; const char* d4 = kvs + 16384;
    PIN(d0); PIN(d1); PIN(d2); PIN(d3); PIN(d4);
    if (attempt == 0) {
      if (wid == 7) {
        const int l7 = lane & 7;
        const int c = l7 == 0 ? head >> 3 : l7 == 1 ? 4 : l7 == 2 ? 5 + (head >> 3) : l7 == 3 ? 9 : l7 == 4 ? 10 + (head >> 3) : l7 == 5 ? 14 : C_MOD;
        poll_ge(ws, xcd * 16 + c, 16);
      }
      BAR();
    }
    asm volatile(
        "s_nop 4\n\tbuffer_load_dwordx4 %0, %7, %9, %19 offen sc1\n\tbuffer_load_dwordx4 %1, %7, %9, %10 offen sc1\n\tbuffer_load_dwordx4 %2, %7, %9, %11 offen sc1\n\t"
        "buffer_load_dword %3, %8, %9, %19 offen sc1\n\tbuffer_load_dword %4, %8, %9, %10 offen sc1\n\tbuffer_load_dword %5, %8, %9, %11 offen sc1\n\t"
        "s_mov_b32 %6, m0\n\ts_mov_b32 m0, %17\n\ts_nop 0\n\tglobal_load_lds_dwordx4 %12, off sc1\n\t"
        "s_add_u32 m0, %18, 0x1000\n\ts_nop 0\n\tglobal_load_lds_dwordx4 %13, off sc1\n\t"
        "s_add_u32 m0, %18, 0x2000\n\ts_nop 0\n\tglobal_load_lds_dwordx4 %14, off sc1\n\t"
        "s_add_u32 m0, %18, 0x3000\n\ts_nop 0\n\tglobal_load_lds_dwordx4 %15, off sc1\n\t"
        "s_add_u32 m0, %18, 0x4000\n\ts_nop 0\n\tglobal_load_lds_dwordx4 %16, off sc1\n\t"
        "s_mov_b32 m0, %6\n\ts_waitcnt vmcnt(5)"
        : "=&v"(qA1w), "=&v"(qB1w), "=&v"(k1w), "=&v"(qA2w), "=&v"(qB2w), "=&v"(k2), "=&s"(m0sv)
        : "v"(l16), "v"(l2), "s"(rs), "s"(qo + kh * 1280), "s"(kvo), "v"(d0), "v"(d1), "v"(d2), "v"(d3), "v"(d4),
          "s"(ldst + (wid == 0 ? 4096 : 0)), "s"(ldst), "s"(qo + (kh ^ 1) * 1280)
        : "memory", "scc");
    if (wid == 0) { const unsigned f = (fresh_tid() & 1) ? 0u : 0x3f803f80u; *(u32x4*)(smem + l16) = (u32x4){f, f, f, f}; }
    const bf16x8 qA1 = BF8(qA1w), qB1 = BF8(qB1w);
    bf16x8 k1 = BF8(k1w);
    float mA = -INFINITY, mB = -INFINITY;
    {
      const bf16x4 qA2 = BF4(((u32x2){qA2w, 0u})), qB2 = BF4(((u32x2){qB2w, 0u}));
      const int nt = attempt ? 64 : 1;
      bf16x8 t1 = k1; unsigned t2 = k2;
      for (int t = 0; t < nt; ++t) {
        if (t) { const unsigned to = kvo + (t >> 3) * 20480 + (t & 7) * 1280; t1 = BF8(ldc16(rs, l16, to)); t2 = ldc4(rs, l2, to); }
        const bf16x4 t2v = BF4(((u32x2){t2, kconst}));
        f32x16 a = MFMA32(t1, qA1, (f32x16){}); a = MFMA32K8(t2v, qA2, a);
        f32x16 b = MFMA32(t1, qB1, (f32x16){}); b = MFMA32K8(t2v, qB2, b);
#pragma unroll
        for (int i = 0; i < 16; ++i) { mA = fmaxf(mA, a[i]); mB = fmaxf(mB, b[i]); }
      }
      mA = fmaxf(mA, xhalf(mA)); mB = fmaxf(mB, xhalf(mB));
    }
    unsigned a0, a1, b0, b1; split3(mA, a0, a1); split3(mB, b0, b1);
    const bf16x4 qA2 = BF4(((u32x2){qA2w, h ? a1 : a0})), qB2 = BF4(((u32x2){qB2w, h ? b1 : b0}));
    char* lh = smem + kh * 81920;
    waitv<0>();
    BAR();
    f32x16 oA = (f32x16){}, oB = (f32x16){};
    const char* kp1 = lh + l16;
    const char* kp2 = lh + l2;
    const bool vreal = r < 20;
    const char* vp = vreal ? lh + 10240 + (r < 16 ? h * 16 + r : 16 + h * 4 + r) * 16 : smem + (r == 20 ? 0 : 16);
    const int vinc = vreal ? 1280 : 0, vjump = vreal ? 10240 : 0;
    if (kh) { k1 = *(const bf16x8*)kp1; k2 = *(const unsigned*)kp2; }
    bf16x8 kf1[2], vf[2][2]; u32x2 kf2[2];
    kf1[0] = k1; kf2[0] = (u32x2){k2, kconst}; kf2[1] = (u32x2){0u, kconst};
    vf[1][0] = (bf16x8){}; vf[1][1] = (bf16x8){};
    u32x4 pA0, pA1, pB0 = (u32x4){}, pB1 = (u32x4){};
    f32x4 e0 = (f32x4){}, e1;
    f32x16 sA = MFMA32(kf1[0], qA1, (f32x16){}), sB;
    sA = MFMA32K8(BF4(kf2[0]), qA2, sA);
#define EX2(x) __builtin_amdgcn_exp2f(x)
#define GRP(MF, S, i, EN, EP, P, w) do { MF; SCHED(); EN[0] = EX2(S[i]); P[w] = pack2(EP[0], EP[1]); EN[1] = EX2(S[i + 1]); P[w + 1] = pack2(EP[2], EP[3]); \
      EN[2] = EX2(S[i + 2]); EN[3] = EX2(S[i + 3]); PIN(P); PIN(EN); SCHED(); } while (0)
    const unsigned relsc = lds_addr(smem) + 768;
    auto group = [&](auto FIRST, int jump, int j) __attribute__((always_inline)) {
#pragma unroll
      for (int t = 0; t < 8; ++t) {
        const int c = t & 1, n = c ^ 1;
        if (FIRST) { if (t == 0) __builtin_amdgcn_s_setprio(3); if (t == 3) __builtin_amdgcn_s_setprio(2); if (t == 5) __builtin_amdgcn_s_setprio(1); if (t == 6) __builtin_amdgcn_s_setprio(0); if (t == 7) __builtin_amdgcn_s_setprio(3); }
        else {
          if (t == 4 && j == 2) __builtin_amdgcn_s_setprio(2);
          if (t == 3 && j == 3) {
            __builtin_amdgcn_s_setprio(1);
            const int wf = fresh_s(wid);
#pragma unroll
            for (int i = 0; i < 13; ++i) wq[i] = proj_ld<false>(rs, i, wf, lane);
            proj_small<false>(rs, x, wf, fresh_tid() & 63, res, gate, cbv);
          }
          if (t == 6 && j == 3) { __builtin_amdgcn_s_setprio(0); if (wid == 7) glds4c(ws + OFF_CNT + (C_REL + (lane & 7)) * 256, relsc); }
        }
        if (FIRST && t < 5) {
#pragma unroll
          for (int i = 5 + 3 * t; i < 8 + 3 * t; ++i) glds16c(kvs + i * 4096, fresh_s(ldst) + i * 4096);
        }
        if (t == 7) {
          if (FIRST) { waitv<0>(); BAR(); }
          kp1 += jump; kp2 += jump;
        }
        SCHED();
        kf1[n] = *(const bf16x8*)(kp1 + ((t + 1) & 7) * 1280); kf2[n][0] = *(const unsigned*)(kp2 + ((t + 1) & 7) * 1280);
        GRP(sB = MFMA32(kf1[c], qB1, (f32x16){}), sA, 0, e1, e0, pB1, 2);
        vf[c][0] = *(const bf16x8*)vp; vf[c][1] = *(const bf16x8*)(vp + 640); vp += vinc;
        GRP(sB = MFMA32K8(BF4(kf2[c]), qB2, sB), sA, 4, e0, e1, pA0, 0);
        GRP(oB = MFMA32(vf[n][0], BF8(pB0), oB), sA, 8, e1, e0, pA0, 2);
        GRP(oB = MFMA32(vf[n][1], BF8(pB1), oB), sA, 12, e0, e1, pA1, 0);
        GRP(sA = MFMA32(kf1[n], qA1, (f32x16){}), sB, 0, e1, e0, pA1, 2);
        GRP(sA = MFMA32K8(BF4(kf2[n]), qA2, sA), sB, 4, e0, e1, pB0, 0);
        GRP(oA = MFMA32(vf[c][0], BF8(pA0), oA), sB, 8, e1, e0, pB0, 2);
        GRP(oA = MFMA32(vf[c][1], BF8(pA1), oA), sB, 12, e0, e1, pB1, 0);
      }
      vp += vjump;
    };
    group(std::true_type{}, 20480, 0);
    const int blk = fresh_s(blockIdx.x);
    const int j2 = (blk - 135) * 4 + qg;
    const bool side = kh == 0 && attempt == 0 && j2 < 270;
    char* stile = smem + 1024 + qg * 4096;
    const char* svec = smem + 81920 + qg * 256;
    const int kt = blk < 135 ? 4 * (blk % 5) + qg : j2 / 10, ng = blk < 135 ? blk / 5 : j2 % 10;
    if (side) {
      const u16* W = blk < 135 ? win : wout; const int N = blk < 135 ? F_ : D_;
      const int lf = fresh_tid() & 63, j8 = lf >> 3;
#pragma unroll
      for (int i = 0; i < 4; ++i) {
        const int row = kt * 32 + i * 8 + j8;
        glds16(W + (size_t)row * N + ng * 64 + ((lf & 7) ^ j8) * 8, lds_addr(stile) + i * 1024);
      }
      glds4c((const float*)(ws + OFF_MOD) + (4 - h) * 640 + (kt < 20 ? kt : 0) * 32 + r, lds_addr(svec));
    }
#pragma nounroll
    for (int j = 1; j < 4; ++j) {
      if (j == 2) {
        if (kh) l2touch(wsp + ((size_t)(blk * 4 + qg) * 64 + lane) * 128, lds_addr(smem) + 256);
        else if (side) {
          waitv<0>();
          const int lf = fresh_tid() & 63, g4 = lf >> 4, r4 = lf & 15, row = 8 * g4 + (r4 >> 2);
          const f32x4 sv0 = *(const f32x4*)(svec + (r4 & 1) * 128 + g4 * 32), sv1 = *(const f32x4*)(svec + (r4 & 1) * 128 + g4 * 32 + 16);
          const bf16x8 vec = BF8(((u32x4){pack2(sv0[0], sv0[1]), pack2(sv0[2], sv0[3]), pack2(sv1[0], sv1[1]), pack2(sv1[2], sv1[3])}));
          const unsigned nks = blk < 135 ? 20 : 27, dso = (blk < 135 ? OFF_WIN : OFF_WOUT) + (ng * 4 * nks + kt) * 1024;
          bf16x8 fr[4]; f32x4 pr[4];
#pragma unroll
          for (int f = 0; f < 4; ++f) {
            const int ch = 2 * f + ((r4 & 3) >> 1);
            const bf16x4 lo = lds_tr(stile + row * 128 + (ch ^ (row & 7)) * 16 + (r4 & 1) * 8), hi = lds_tr(stile + (row + 4) * 128 + (ch ^ ((row + 4) & 7)) * 16 + (r4 & 1) * 8);
            fr[f] = __builtin_shufflevector(lo, hi, 0, 1, 2, 3, 4, 5, 6, 7);
          }
#pragma unroll
          for (int f = 0; f < 4; ++f) { stc16(rs, l16, dso + f * nks * 1024, __builtin_bit_cast(u32x4, fr[f])); pr[f] = MFMA16(fr[f], vec, (f32x4){}); }
          if (r4 < 2) {
#pragma unroll
            for (int f = 0; f < 4; ++f) *(f32x4*)(stile + 3072 + r4 * 256 + f * 64 + g4 * 16) = pr[f];
          }
        }
      }
      if (j == 3 && kh == 0 && attempt == 0) { waitv<0>(); arrive(ws, C_REL + xcd, lane); }
      group(std::false_type{}, j < 3 ? 20480 : 0, j);
    }
    pB1[2] = pack2(e0[0], e0[1]); pB1[3] = pack2(e0[2], e0[3]);
    oB = MFMA32(vf[1][0], BF8(pB0), oB); oB = MFMA32(vf[1][1], BF8(pB1), oB);
    {
      f32x4* ex = (f32x4*)(lh + 1024 + qg * 4096) + lane;
#pragma unroll
      for (int g = 0; g < 3; ++g) ex[g * 64] = (f32x4){oA[4 * g], oA[4 * g + 1], oA[4 * g + 2], oA[4 * g + 3]};
    }
    int* flag = (int*)(smem + 528);
    BAR();
    float o[12];
    {
      const f32x4* ex = (const f32x4*)(smem + (kh ^ 1) * 81920 + 1024 + qg * 4096) + lane;
#pragma unroll
      for (int g = 0; g < 3; ++g) { const f32x4 t = ex[g * 64];
#pragma unroll
        for (int e = 0; e < 4; ++e) o[4 * g + e] = oB[4 * g + e] + t[e]; }
    }
    const float lo = xhalf(o[8]); const float l = h ? o[8] : lo;
    const float rl = __builtin_amdgcn_rcpf(l);
    const int tok = qb * 256 + qg * 64 + kh * 32 + r;
    const unsigned vo = (tok >> 4) * 20480 + (tok & 15) * 16;
    stc16(rs, vo + h * 256, OFF_O + (head >> 1) * 1024 + (head & 1) * 512,
          (u32x4){pack2(o[0] * rl, o[1] * rl), pack2(o[2] * rl, o[3] * rl), pack2(o[4] * rl, o[5] * rl), pack2(o[6] * rl, o[7] * rl)});
    if (h == 0) stc8(rs, vo - (tok & 15) * 8, OFF_O + 16384 + (head >> 3) * 1024 + (head & 1) * 512 + ((head >> 1) & 3) * 128, (u32x2){pack2(o[8] * rl, o[9] * rl), pack2(o[10] * rl, o[11] * rl)});
    if (wid == 4 && blk < 135 && attempt == 0 && lane < 32) {
      const char* pp = smem + 1024 + 3072 + lane * 16;
      const f32x4 v = (*(const f32x4*)pp + *(const f32x4*)(pp + 4096)) + (*(const f32x4*)(pp + 8192) + *(const f32x4*)(pp + 12288));
      stc16(rs, (lane >> 4) * 8192 + (lane & 15) * 16, OFF_CBP + (blk % 5) * 16384 + (blk / 5) * 256, __builtin_bit_cast(u32x4, v));
    }
    bool ok = (l > 1e-30f) && (l < 1e30f);
#pragma unroll
    for (int i = 0; i < 12; ++i) ok = ok && (fabsf(o[i]) < 3e38f);
    if (!ok && attempt == 0) *flag = 1;
    drain();
    BAR();
    const int redo = __builtin_amdgcn_readfirstlane(*(volatile LAS int*)flag);
    if (!redo) break;
    BAR();
  }
  if (wid == 7) arrive(ws, C_ATT + qb, lane);
#pragma unroll
  for (int i = 0; i < 13; ++i) proj_park(smem, i, tid, wq[i]);
  if (!__all(*(volatile LAS unsigned*)(smem + 768 + (lane & 7) * 4) >= 128u)) poll_ge(ws, C_REL + (lane & 7), 128);
}

template <bool FINAL, int KH, class HOOK>
DI void proj_body(char* __restrict__ ws, rsrc_t rs, const u16* __restrict__ x, u16* __restrict__ out, char* smem, int wid, u32x2 (&res)[3], const f32x4 (&gate)[3], const f32x4 (&cbv)[3], HOOK&& after_mma) {
  const int lane = fresh_tid() & 63;
  const int panel = blk_panel(), cb = blk_cb();
  constexpr int NKS = FINAL ? 27 : 20;
  constexpr int K0 = KH ? (NKS + 1) / 2 : 0, NK = KH ? NKS / 2 : (NKS + 1) / 2, NKEEP = KH ? 2 : 3, UK = KH ? 3 : 0, UG = KH ? 0 : 3;
  const int m = wid & 3, mt = panel * 4 + m, g = lane >> 4, r = lane & 15;
  unsigned l16 = lane * 16; PIN(l16);
  const unsigned aso = fresh_s((FINAL ? OFF_U : OFF_O) + (mt * NKS + K0) * 1024);
  if (wid == 7) poll_ge(ws, FINAL ? C_U + panel : C_ATT + (panel >> 2), FINAL ? 8 : 32);
  BAR();
  bf16x8 a[NK];
#pragma unroll
  for (int k = 0; k < NK; ++k) {
    if (!FINAL && K0 + k >= 16) {
      const u32x2 lo = ldc8(rs, (l16 >> 1) + (k & 3) * 1024, aso + (k >> 2) * 4096), hi = ldc8(rs, (l16 >> 1) + (k & 3) * 1024 + 512, aso + (k >> 2) * 4096);
      a[k] = BF8(((u32x4){lo[0], lo[1], hi[0], hi[1]}));
    } else a[k] = BF8(ldc16(rs, l16 + (k & 3) * 1024, aso + (k >> 2) * 4096));
  }
  SCHED();
  constexpr int PD = FINAL ? 5 : 6;
  const char* bl = smem + lane * 16;
  bf16x8 bf[PD + 1][5]; f32x4 acc[5];
#pragma unroll
  for (int u = 0; u < 5; ++u) acc[u] = (f32x4){};
#pragma unroll
  for (int k = 0; k < PD; ++k)
#pragma unroll
    for (int u = 0; u < 5; ++u) bf[k][u] = *(const bf16x8*)(bl + proj_pos(u * NKS + K0 + k));
  SCHED();
#pragma unroll
  for (int k = 0; k < NK; ++k) {
#pragma unroll
    for (int u = 0; u < 5; ++u) {
      if (k + PD < NK) bf[(k + PD) % (PD + 1)][u] = *(const bf16x8*)(bl + proj_pos(u * NKS + K0 + k + PD));
      acc[u] = MFMA16(bf[k % (PD + 1)][u], a[k], acc[u]);
      SCHED();
    }
  }
  f32x4* ex = (f32x4*)(smem + proj_pos(FINAL ? 136 : 104)) + m * 5 * 64 + lane;
#pragma unroll
  for (int i = 0; i < 5 - NKEEP; ++i) ex[(UG + i) * 64] = acc[UG + i];
  f32x4 part[NKEEP];
#pragma unroll
  for (int i = 0; i < NKEEP; ++i) {
    part[i] = fma4s(gate[i], acc[UK + i], (f32x4){bflo(res[i][0]), bfhi(res[i][0]), bflo(res[i][1]), bfhi(res[i][1])});
    PIN(part[i]);
  }
  SCHED();
  after_mma();
  BAR();
  f32x4 oth[NKEEP];
#pragma unroll
  for (int i = 0; i < NKEEP; ++i) oth[i] = ex[(UK + i) * 64];
  f32x4 sv = (f32x4){}, qv = (f32x4){};
#pragma unroll
  for (int i = 0; i < NKEEP; ++i) {
    const f32x4 v = part[i] + gate[i] * oth[i];
    const u32x2 o = (u32x2){pack2(v[0], v[1]), pack2(v[2], v[3])};
    if (FINAL) *(u32x2*)(out + (size_t)(mt * 16 + r) * D_ + (cb * 5 + UK + i) * 16 + g * 4) = o;
    else {
      res[i] = o; PIN(res[i]);
      const f32x4 w = v * cbv[i]; const int un = cb * 5 + UK + i;
      stc8(rs, ((g >> 1) * 16 + r) * 16 + (g & 1) * 8, OFF_X1S + mt * 20480 + un * 512, (u32x2){pack2(w[0], w[1]), pack2(w[2], w[3])});
      sv += v; qv += v * v;
    }
  }
  if (!FINAL) {
    const float s = sum_g((sv[0] + sv[1]) + (sv[2] + sv[3])), q = sum_g((qv[0] + qv[1]) + (qv[2] + qv[3]));
    if (g == 0) stc8(rs, r * 8, OFF_ST1 + ((panel * 16 + cb * 2 + KH) * 64 + m * 16) * 8, (u32x2){__float_as_uint(s), __float_as_uint(q)});
    drain();
    BAR();
    if (wid == 7) arrive(ws, C_X1 + panel, lane);
  }
}

constexpr float GC1 = -2.8853900817779268f * 0.7978845608028654f, GC2 = GC1 * 0.044715f;
DI void gelu8(f32x4& a0, const f32x4& l0, f32x4& a1, const f32x4& l1) {
  float t[8];
#define G8(expr0, expr1) do { _Pragma("unroll") for (int i = 0; i < 4; ++i) { t[i] = opq(expr0); } _Pragma("unroll") for (int i = 0; i < 4; ++i) { t[4 + i] = opq(expr1); } SCHED(); } while (0)
  G8(a0[i] * a0[i], a1[i] * a1[i]);
  G8(__builtin_fmaf(t[i], l0[3], l0[2]), __builtin_fmaf(t[4 + i], l1[3], l1[2]));
  G8(a0[i] * t[i], a1[i] * t[4 + i]);
  G8(__builtin_amdgcn_exp2f(t[i]), __builtin_amdgcn_exp2f(t[4 + i]));
  G8(__builtin_fmaf(t[i], l0[0], l0[0]), __builtin_fmaf(t[4 + i], l1[0], l1[0]));
  G8(__builtin_amdgcn_rcpf(t[i]), __builtin_amdgcn_rcpf(t[4 + i]));
  G8(a0[i] * t[i], a1[i] * t[4 + i]);
  a0 = (f32x4){t[0], t[1], t[2], t[3]}; a1 = (f32x4){t[4], t[5], t[6], t[7]};
}
constexpr int L4_Z1 = 114688, L4_TR = L4_Z1 + 16384, L4_LN = L4_TR + 16384, L4_ST2 = L4_LN + 1024, L4_FIN = L4_ST2 + 3584, L4_CB = L4_FIN + 512, L4_CNT = L4_CB + 2048;
constexpr int K5_R = 7;
constexpr unsigned DEAD = 0x80000000u;
DI int sp_stage(int s) { const int cb = blk_cb(); return (s + cb * 2) & 15; }
DI unsigned sp_voff(int wid, int lane, int i) { return ((wid & 3) * 16 + i * 8 + (lane >> 3)) * 4096 + (wid >> 2) * 128 + ((lane & 7) ^ (4 * i + (lane >> 4))) * 16; }
DI const char* sp_base(const u16* wsp, int panel, int s) { return (const char*)wsp + (size_t)panel * 262144 + sp_stage(s) * 256; }
DI void sp_prefetch(u32x4 (&wr)[16 - K5_R][2], const u16* __restrict__ wsp, int panel, int wid, int lane) {
  const unsigned v0 = sp_voff(wid, lane, 0), v1 = sp_voff(wid, lane, 1);
#pragma unroll
  for (int s = K5_R; s < 16; ++s) { wr[s - K5_R][0] = *(const u32x4*)(sp_base(wsp, panel, s) + v0); wr[s - K5_R][1] = *(const u32x4*)(sp_base(wsp, panel, s) + v1); }
  asm volatile("" ::: "memory");
}
template <int S0, int S1>
DI void sp_ring(const u16* __restrict__ wsp, char* smem, int panel, int wid, int lane) {
  const unsigned v0 = sp_voff(wid, lane, 0), v1 = sp_voff(wid, lane, 1);
#pragma unroll
  for (int s = S0; s < S1; ++s) { glds16(sp_base(wsp, panel, s) + v0, lds_addr(smem) + wid * 2048 + s * 16384); glds16(sp_base(wsp, panel, s) + v1, lds_addr(smem) + wid * 2048 + s * 16384 + 1024); }
}
DI unsigned win_boff(int wid, int lane) { const int cb = blk_cb(); return lane * 16 + (wid == 6 && cb >= 6 ? DEAD : 0u); }
DI unsigned win_soff(int wid, bool z2) { return OFF_WIN + ((z2 ? 54 : 0) + unit0_of(blk_cb()) + wid) * 20480; }
#define WIN_LD(so, k) BF8(ldc16(rs, bo + ((k) & 3) * 1024, (so) + ((k) >> 2) * 4096))
DI void win_prefetch(rsrc_t rs, int wid, bf16x8 (&b2)[20]) {
  const unsigned bo = win_boff(wid, fresh_tid() & 63), so = win_soff(wid, true);
#pragma unroll
  for (int k = 0; k < 20; ++k) b2[k] = WIN_LD(so, k);
}
template <class HOOK>
DI void win_mma(const char* al, const bf16x8 (&b)[20], f32x4 (&acc)[4], HOOK&& hook) {
  constexpr int PD = 4;
  bf16x8 af[PD + 1][2];
#define WIN_RD(i) do { af[(i) % (PD + 1)][0] = *(const bf16x8*)(al + ((i) & 3) * 20480 + ((i) >> 2) * 2048); af[(i) % (PD + 1)][1] = *(const bf16x8*)(al + ((i) & 3) * 20480 + ((i) >> 2) * 2048 + 1024); } while (0)
#pragma unroll
  for (int i = 0; i < PD; ++i) WIN_RD(i);
  SCHED();
  sfor<40>([&](auto I) __attribute__((always_inline)) {
    constexpr int i = I, m = i & 3, k = (i >> 2) * 2;
    if constexpr (i + PD < 40) WIN_RD(i + PD);
    acc[m] = MFMA16(b[k], af[i % (PD + 1)][0], acc[m]);
    acc[m] = MFMA16(b[k + 1], af[i % (PD + 1)][1], acc[m]); PIN(acc[m]);
    hook(I);
    SCHED();
  });
}
DI void stage_x1s(char* __restrict__ ws, char* smem, int panel, int wid, int lane) {
  const char* src = ws + OFF_X1S + (size_t)panel * 81920 + lane * 16;
#pragma unroll
  for (int i = 0; i < 12; ++i) { const int f = i < 11 ? i * 7 + wid : min(77 + wid, 79); glds16c(src + f * 1024, lds_addr(smem) + f * 1024); }
}
DI void load_bb(const u16* __restrict__ bsp, int panel, int wid, int lane, u16 (&bb)[2]) { const u16* p = bsp + panel * 64 + (wid >> 2) * 32 + (lane & 15); bb[0] = p[0]; bb[1] = p[16]; asm volatile("" ::: "memory"); }
DI void win_tail(char* __restrict__ ws, const u16* __restrict__ wsp, char* smem, int panel, int cb, int wid, int lane) {
  BAR();
  if (wid == 7 && lane < 4) __hip_atomic_fetch_add((GAS unsigned*)(ws + OFF_CNT + (C_Z2 + lane * 8 + cb) * 256), 1u, __ATOMIC_RELAXED, __HIP_MEMORY_SCOPE_AGENT);
}

template <bool EARLY>
DI void win_body(char* __restrict__ ws, rsrc_t rs, const u16* __restrict__ wsp, char* smem, int wid, const bf16x8 (&b2)[20], u32x4 (&wr)[16 - K5_R][2], const u16* __restrict__ bsp, u16 (&bb)[2]) {
  const int tid = fresh_tid(), lane = tid & 63, g = lane >> 4, r = lane & 15;
  const int panel = blk_panel(), cb = blk_cb(), u0 = unit0_of(cb);
  float* lnp = (float*)(smem + L4_LN); float* st2 = (float*)(smem + L4_ST2); float* fin = (float*)(smem + L4_FIN); float* cbl = (float*)(smem + L4_CB);
  int* flg = (int*)(smem + L4_CNT);
  __builtin_amdgcn_s_setprio(1);
  const unsigned bo = win_boff(wid, lane), so1 = win_soff(wid, false);
  bf16x8 b1[20];
  BAR();
  stage_x1s(ws, smem, panel, wid, lane);
  BAR();
  f32x4 ln[4], z2[4], z1[4];
#pragma unroll
  for (int m = 0; m < 4; ++m) ln[m] = *(const f32x4*)(lnp + (m * 16 + r) * 4);
  {
    const f32x4 cs2 = *(const f32x4*)(cbl + 112 + wid * 16 + g * 4), bi2 = *(const f32x4*)(cbl + 256 + 112 + wid * 16 + g * 4);
    const f32x4 cs1 = *(const f32x4*)(cbl + wid * 16 + g * 4), bi1 = *(const f32x4*)(cbl + 256 + wid * 16 + g * 4);
#pragma unroll
    for (int m = 0; m < 4; ++m) {
#pragma unroll
      for (int e = 0; e < 4; ++e) { z2[m][e] = opq(__builtin_fmaf(ln[m][1], cs2[e], opq(ln[m][0] * bi2[e]))); z1[m][e] = opq(__builtin_fmaf(ln[m][1], cs1[e], opq(ln[m][0] * bi1[e]))); }
      PIN(z1[m]); PIN(z2[m]);
    }
  }
#pragma unroll
  for (int k = 0; k < 4; ++k) b1[k] = WIN_LD(so1, k);
  waitv<4>();
  BAR();
  load_bb(bsp, panel, wid, lane, bb);
  win_mma(smem + lane * 16, b2, z2, [&](auto I) __attribute__((always_inline)) { constexpr int i = I; if constexpr (i < 32 && (i & 1)) b1[4 + i / 2] = WIN_LD(so1, 4 + i / 2); });
  gelu8(z2[0], ln[0], z2[1], ln[1]); gelu8(z2[2], ln[2], z2[3], ln[3]);
  {
    float s[4], q[4];
#pragma unroll
    for (int m = 0; m < 4; ++m) { s[m] = opq(opq(z2[m][0] + z2[m][1]) + opq(z2[m][2] + z2[m][3])); q[m] = opq(__builtin_fmaf(z2[m][0], z2[m][0], opq(z2[m][1] * z2[m][1])) + opq(__builtin_fmaf(z2[m][2], z2[m][2], opq(z2[m][3] * z2[m][3])))); }
    auto pa = __builtin_amdgcn_permlane32_swap(__float_as_uint(s[0]), __float_as_uint(s[2]), false, false);
    auto pb = __builtin_amdgcn_permlane32_swap(__float_as_uint(s[1]), __float_as_uint(s[3]), false, false);
    auto pc = __builtin_amdgcn_permlane32_swap(__float_as_uint(q[0]), __float_as_uint(q[2]), false, false);
    auto pd = __builtin_amdgcn_permlane32_swap(__float_as_uint(q[1]), __float_as_uint(q[3]), false, false);
    const float sa = opq(__uint_as_float(pa[0]) + __uint_as_float(pa[1])), sb = opq(__uint_as_float(pb[0]) + __uint_as_float(pb[1]));
    const float qa = opq(__uint_as_float(pc[0]) + __uint_as_float(pc[1])), qb = opq(__uint_as_float(pd[0]) + __uint_as_float(pd[1]));
    auto ps = __builtin_amdgcn_permlane16_swap(__float_as_uint(sa), __float_as_uint(sb), false, false);
    auto pq = __builtin_amdgcn_permlane16_swap(__float_as_uint(qa), __float_as_uint(qb), false, false);
    *(f32x2*)(st2 + (wid * 64 + lane) * 2) = (f32x2){opq(__uint_as_float(ps[0]) + __uint_as_float(ps[1])), opq(__uint_as_float(pq[0]) + __uint_as_float(pq[1]))};
  }
  asm volatile("" ::: "memory");
  if (lane == 0) __hip_atomic_fetch_add(flg, 1, __ATOMIC_RELAXED, __HIP_MEMORY_SCOPE_WORKGROUP);
  asm volatile("" ::: "memory");
  __builtin_amdgcn_s_setprio(0);
  const unsigned wvo[2] = {sp_voff(wid, lane, 0), sp_voff(wid, lane, 1)};
  win_mma(smem + lane * 16, b1, z1, [&](auto I) __attribute__((always_inline)) {
    constexpr int i = I, j = i < 8 ? i : (i & 1) ? -1 : 4 + i / 2;
    if constexpr (EARLY && j >= 0 && j < 18) wr[j >> 1][j & 1] = *(const u32x4*)(sp_base(wsp, panel, K5_R + (j >> 1)) + wvo[j & 1]);
  });
  gelu8(z1[0], ln[0], z1[1], ln[1]); gelu8(z1[2], ln[2], z1[3], ln[3]);
#pragma unroll
  for (int mm = 0; mm < 2; ++mm)
    *(u32x4*)(smem + L4_Z1 + wid * 2048 + mm * 1024 + lane * 16) = (u32x4){pack2(z1[2 * mm][0], z1[2 * mm][1]), pack2(z1[2 * mm][2], z1[2 * mm][3]), pack2(z1[2 * mm + 1][0], z1[2 * mm + 1][1]), pack2(z1[2 * mm + 1][2], z1[2 * mm + 1][3])};
  BAR();
  char* tr = smem + L4_TR + wid * 2048;
#pragma unroll
  for (int m = 0; m < 4; ++m) {
    const f32x2 f = *(const f32x2*)(fin + (m * 16 + r) * 2);
    const f32x4 v = z2[m] * f[0] - f[1];
    *(u32x2*)(tr + (m * 16 + r) * 32 + g * 8) = (u32x2){pack2(v[0], v[1]), pack2(v[2], v[3])};
  }
  {
    const char* p = tr + (8 * g + (r >> 2)) * 32 + (r & 3) * 8;
    const bf16x4 lo0 = lds_tr(p), hi0 = lds_tr(p + 128), lo1 = lds_tr(p + 1024), hi1 = lds_tr(p + 1152);
    stc16(rs, bo, OFF_Z2 + (panel * 2 * 54 + u0 + wid) * 1024, __builtin_bit_cast(u32x4, (bf16x8)__builtin_shufflevector(lo0, hi0, 0, 1, 2, 3, 4, 5, 6, 7)));
    stc16(rs, bo, OFF_Z2 + ((panel * 2 + 1) * 54 + u0 + wid) * 1024, __builtin_bit_cast(u32x4, (bf16x8)__builtin_shufflevector(lo1, hi1, 0, 1, 2, 3, 4, 5, 6, 7)));
  }
  SCHED();
  waitv<0>();
  win_tail(ws, wsp, smem, panel, cb, wid, lane);
  sp_ring<0, 5>(wsp, smem, panel, wid, lane);
  if (!EARLY) sp_prefetch(wr, wsp, panel, wid, lane);
}

DI void win_agent(char* __restrict__ ws, rsrc_t rs, const u16* __restrict__ wsp, char* smem, u32x4 (&wr)[16 - K5_R][2], const u16* __restrict__ bsp, u16 (&bb)[2]) {
  const int tid = fresh_tid(), lane = tid & 63, wid = 7;
  const int panel = blk_panel(), cb = blk_cb(), u0 = unit0_of(cb);
  float* lnp = (float*)(smem + L4_LN); float* st2 = (float*)(smem + L4_ST2); float* fin = (float*)(smem + L4_FIN); float* cbl = (float*)(smem + L4_CB);
  int* cnt = (int*)(smem + L4_CNT);
  __builtin_amdgcn_s_setprio(2);
  if (lane == 0) *cnt = 0;
  poll_ge(ws, lane < 8 ? C_ATT + lane : C_X1 + panel, lane < 8 ? 32 : 8);
  BAR();
  {
    u32x2 t[16]; f32x4 cp[5][2];
#pragma unroll
    for (int i = 0; i < 16; ++i) t[i] = ldc8(rs, lane * 8 + (i & 7) * 512, OFF_ST1 + (panel * 16 + (i & 8)) * 512);
    const unsigned co = lane < 28 ? (u0 * 16 + lane * 4) * 4 : min((54 + u0) * 16 + min(lane - 28, 27) * 4, F_ - 4) * 4;
#pragma unroll
    for (int i = 0; i < 5; ++i) { cp[i][0] = ldc16f(rs, co, OFF_CBP + i * 16384); cp[i][1] = ldc16f(rs, co, OFF_CBP + i * 16384 + 8192); }
    float s = 0.f, q = 0.f;
#pragma unroll
    for (int i = 0; i < 16; ++i) { s += __uint_as_float(t[i][0]); q += __uint_as_float(t[i][1]); }
    const float mean = s * (1.f / 640.f), var = fmaxf(q * (1.f / 640.f) - mean * mean, 0.f);
    const float den = ln_den(var), ri = __builtin_amdgcn_rcpf(den);
    *(f32x4*)(lnp + lane * 4) = (f32x4){den, -rbf(mean), GC1 * ri, GC2 * ri * ri * ri};
#pragma unroll
    for (int v = 0; v < 2; ++v) *(f32x4*)(cbl + v * 256 + lane * 4) = ((cp[0][v] + cp[1][v]) + (cp[2][v] + cp[3][v])) + cp[4][v];
  }
  BAR();
  BAR();
  __builtin_amdgcn_s_setprio(0);
  __builtin_amdgcn_s_sleep(8);
  load_bb(bsp, panel, wid, lane, bb);
  sp_prefetch(wr, wsp, panel, wid, lane);
#pragma nounroll
  for (int w = 0; w < 8; ++w) sp_ring<5, 7>(wsp, smem, panel, w, lane);
  __builtin_amdgcn_s_setprio(2);
#pragma nounroll
  for (unsigned spin = 0; spin < (1u << 22); ++spin) {
    if (__hip_atomic_load(cnt, __ATOMIC_RELAXED, __HIP_MEMORY_SCOPE_WORKGROUP) >= 7) break;
    __builtin_amdgcn_s_sleep(1);
  }
  asm volatile("" ::: "memory");
  float s, q;
  {
    f32x2 t[7];
#pragma unroll
    for (int i = 0; i < 7; ++i) t[i] = *(const f32x2*)(st2 + (i * 64 + lane) * 2);
    s = t[0][0]; q = t[0][1];
#pragma unroll
    for (int i = 1; i < 6; ++i) { s = opq(s + t[i][0]); q = opq(q + t[i][1]); }
    if (cb < 6) { s = opq(s + t[6][0]); q = opq(q + t[6][1]); }
  }
  GAS u64* xch = (GAS u64*)(ws + OFF_XCH) + panel * 512;
  unsigned qb = __float_as_uint(q); if (qb == ~0u) qb = 0x7fc00000u;
  __hip_atomic_store(xch + cb * 64 + lane, ((u64)qb << 32) | __float_as_uint(s), __ATOMIC_RELAXED, __HIP_MEMORY_SCOPE_AGENT);
  u64 v[8];
#pragma nounroll
  for (unsigned spin = 0; spin < (1u << 19); ++spin) {
    bool ok = true;
#pragma unroll
    for (int i = 0; i < 8; ++i) { v[i] = __hip_atomic_load(xch + i * 64 + lane, __ATOMIC_RELAXED, __HIP_MEMORY_SCOPE_AGENT); ok = ok && ((unsigned)(v[i] >> 32) != ~0u); }
    if (__all(ok)) break;
    __builtin_amdgcn_s_sleep(1);
  }
  s = 0.f; q = 0.f;
#pragma unroll
  for (int i = 0; i < 8; ++i) { s = opq(s + __uint_as_float((unsigned)v[i])); q = opq(q + __uint_as_float((unsigned)(v[i] >> 32))); }
  const float mean = opq(s * (1.f / 864.f)), var = fmaxf(opq(q * (1.f / 864.f)) - mean * mean, 0.f);
  const float ri = __builtin_amdgcn_rcpf(ln_den(var));
  *(f32x2*)(fin + lane * 2) = (f32x2){ri, rbf(mean) * ri};
  __builtin_amdgcn_s_setprio(0);
  BAR();
  __builtin_amdgcn_s_sleep(6);
  sp_ring<0, 5>(wsp, smem, panel, wid, lane);
  win_tail(ws, wsp, smem, panel, cb, wid, lane);
}

constexpr int sp_q(int t) { const int w = 4 * 2 + t / 3; return t + (w < 4 * 4 ? w : 4 * 4); }
DI void phase_sp(char* __restrict__ ws, rsrc_t rs, const u16 (&bb)[2], char* smem, int wid, const u32x4 (&wr)[16 - K5_R][2], u32x2 (&res)[3], f32x4 (&gate)[3]) {
  const int tid = fresh_tid(), lane = tid & 63;
  const int panel = blk_panel(), cb = blk_cb();
  const int kh = wid >> 2, up = wid & 3, u0 = unit0_of(cb) + 2 * up, nu = cb < 6 ? 7 : 6, g = lane >> 4, r = lane & 15;
  const unsigned dead[2] = {2 * up < nu ? 0u : DEAD, 2 * up + 1 < nu ? 0u : DEAD};
  unsigned bvo[2] = {lane * 16 + dead[0], lane * 16 + 1024 + dead[1]};
  const char* al[2][2];
#pragma unroll
  for (int k = 0; k < 2; ++k)
#pragma unroll
    for (int j = 0; j < 2; ++j) al[k][j] = smem + kh * 8192 + (j ^ kh) * 4096 + r * 128 + ((4 * k + g) ^ (r >> 1)) * 16;
  constexpr int LB = 4, PD = 3;
  bf16x8 bq[LB + 1][4], af[PD + 1][2]; f32x4 acc[2][4]; u32x4 wq[17];
#pragma unroll
  for (int i = 0; i < 2; ++i) {
    const float b0 = bf2f(bb[0]), b1 = bf2f(bb[1]);
    acc[i][0] = (f32x4){b0, b0, b0, b0}; acc[i][1] = (f32x4){b1, b1, b1, b1}; acc[i][2] = (f32x4){}; acc[i][3] = (f32x4){};
  }
  const unsigned bso = fresh_s(OFF_Z2 + (kh * 2 * 54 + u0) * 1024);
  PIN(bvo[0]); PIN(bvo[1]);
  if (wid == 7) poll_ge(ws, C_Z2 + (panel >> 3) * 8 + cb, 32);
  else if (kh && up < 3) waitv<2 * (16 - K5_R) + 6>(); else waitv<6>();
  BAR();
#define SP_LD(s, j) (bq[(s) % (LB + 1)][j] = BF8(ldc16(rs, bvo[(j) & 1], bso + (sp_stage(s) * 4 + ((j) >> 1)) * 55296)))
#define SP_RD(t, k) (af[(t) % (PD + 1)][k] = *(const bf16x8*)(al[k][((t) >> 1) & 1] + (((t) >> 2) % K5_R) * 16384 + ((t) & 1) * 2048))
#pragma unroll
  for (int q = 0; q < sp_q(0); ++q) SP_LD(q >> 2, q & 3);
#pragma unroll
  for (int t = 0; t < PD; ++t) { SP_RD(t, 0); SP_RD(t, 1); }
  SCHED();
  sfor<64>([&](auto T) __attribute__((always_inline)) {
    constexpr int t = T, nx = t + PD, s = t >> 2, m = t & 3, tl = sp_q(t), t2 = sp_q(t + 1) - 1;
    if constexpr (6 && nx == 8) asm volatile("s_barrier" ::: "memory");
    if constexpr (nx == 16 || nx == 28 || nx == 44 || nx == 56) BAR();
    constexpr int c = nx >= 16 && nx < 24 ? nx - 16 : nx >= 28 && nx < 34 ? nx - 20 : nx >= 44 && nx < 48 ? nx - 30 : -1;
    if constexpr (nx < 64) SP_RD(nx, 0);
    acc[0][m] = MFMA16(bq[s % (LB + 1)][0], af[t % (PD + 1)][0], acc[0][m]);
    acc[0][m] = MFMA16(bq[s % (LB + 1)][2], af[t % (PD + 1)][1], acc[0][m]); PIN(acc[0][m]);
    if constexpr (tl < 64) SP_LD(tl >> 2, tl & 3); else if constexpr (tl - 64 < 17) wq[tl - 64] = proj_ld<true>(rs, tl - 64, wid, lane);
    SCHED();
    if constexpr (nx < 64) SP_RD(nx, 1);
    acc[1][m] = MFMA16(bq[s % (LB + 1)][1], af[t % (PD + 1)][0], acc[1][m]);
    acc[1][m] = MFMA16(bq[s % (LB + 1)][3], af[t % (PD + 1)][1], acc[1][m]); PIN(acc[1][m]);
    if constexpr (t2 > tl && t2 < 64) SP_LD(t2 >> 2, t2 & 3);
    if constexpr (c >= 0) *(u32x4*)(smem + ((c >> 1) % K5_R) * 16384 + wid * 2048 + (c & 1) * 1024 + lane * 16) = wr[c >> 1][c & 1];
    if constexpr (t == 63 && 4 * LB < 17) { _Pragma("unroll") for (int i = 4 * LB; i < 17; ++i) wq[i] = proj_ld<true>(rs, i, wid, lane); }
    if constexpr (t == 62) proj_small<true>(rs, nullptr, wid, lane, res, gate, gate);
    SCHED();
  });
  f32x4* exw = (f32x4*)(smem + 32768 + wid * 4096) + lane;
  const f32x4* exr = (const f32x4*)(smem + 32768 + (wid ^ 4) * 4096) + lane;
  u32x4 z[2];
#pragma unroll
  for (int i = 0; i < 2; ++i) z[i] = *(const u32x4*)(smem + L4_Z1 + (2 * up + i) * 2048 + kh * 1024 + lane * 16);
#pragma unroll
  for (int i = 0; i < 2; ++i)
#pragma unroll
    for (int j = 0; j < 2; ++j) exw[(i * 2 + j) * 64] = acc[i][2 + j];
  f32x4 zf[2][2];
#pragma unroll
  for (int i = 0; i < 2; ++i)
#pragma unroll
    for (int j = 0; j < 2; ++j) {
      zf[i][j] = (f32x4){bflo(z[i][2 * j]), bfhi(z[i][2 * j]), bflo(z[i][2 * j + 1]), bfhi(z[i][2 * j + 1])};
      acc[i][j] = mul4s(acc[i][j], zf[i][j]); PIN(zf[i][j]); PIN(acc[i][j]);
    }
  BAR();
  f32x4 oth[2][2];
#pragma unroll
  for (int i = 0; i < 2; ++i)
#pragma unroll
    for (int j = 0; j < 2; ++j) oth[i][j] = exr[(i * 2 + j) * 64];
#pragma unroll
  for (int i = 0; i < 2; ++i)
#pragma unroll
    for (int j = 0; j < 2; ++j) {
      const f32x4 v = acc[i][j] + zf[i][j] * oth[i][j];
      stc8(rs, ((g >> 1) * 16 + r) * 16 + (g & 1) * 8 + dead[i], OFF_U + (panel * 4 + 2 * kh + j) * 27648 + (u0 + i) * 512, (u32x2){pack2(v[0], v[1]), pack2(v[2], v[3])});
    }
  drain();
  BAR();
  if (wid == 7) arrive(ws, C_U + panel, lane);
#pragma unroll
  for (int i = 0; i < 17; ++i) proj_park(smem, i, tid, wq[i]);
}

extern "C" __global__ __launch_bounds__(512, 2) OCC2 void k_mega(char* __restrict__ ws, const u16* __restrict__ x, const u16* __restrict__ win,
                                                     const u16* __restrict__ wsp, const u16* __restrict__ bsp, const u16* __restrict__ wout, u16* __restrict__ out) {
  extern __shared__ __attribute__((aligned(16))) char smem[];
  const int wid = __builtin_amdgcn_readfirstlane(threadIdx.x >> 6);
  const rsrc_t rs = __builtin_amdgcn_make_buffer_rsrc(ws, 0, WS_USED, 0x00020000);
  u32x2 res[3]; f32x4 gate[3], cbv[3];
  phase_qkv(ws, rs, smem, wid);
  phase_attn(ws, rs, (const char*)wsp, win, wout, x, smem, wid, res, gate, cbv);
  u32x4 wr[16 - K5_R][2];
  auto nohook = []() __attribute__((always_inline)) {};
  u16 bb[2];
  if (wid >= 4) {
    proj_body<false, 1>(ws, rs, x, out, smem, wid, res, gate, cbv, nohook);
    if (wid == 7) win_agent(ws, rs, wsp, smem, wr, bsp, bb);
    else { bf16x8 b2[20]; win_prefetch(rs, wid, b2); win_body<false>(ws, rs, wsp, smem, wid, b2, wr, bsp, bb); }
#pragma unroll
    for (int i = 0; i < 3; ++i) { gate[i] = (f32x4){}; cbv[i] = (f32x4){}; }
  }
  const int w2 = fresh_s(wid);
  if (w2 < 4) {
    bf16x8 b2[20];
    proj_body<false, 0>(ws, rs, x, out, smem, w2, res, gate, cbv, [&]() __attribute__((always_inline)) { win_prefetch(rs, w2, b2); });
    win_body<true>(ws, rs, wsp, smem, w2, b2, wr, bsp, bb);
  }
  phase_sp(ws, rs, bb, smem, wid, wr, res, gate);
  if (wid < 4) proj_body<true, 0>(ws, rs, x, out, smem, wid, res, gate, cbv, nohook);
  else proj_body<true, 1>(ws, rs, x, out, smem, wid, res, gate, cbv, nohook);
  asm volatile("k_mega_code_end:");
}

extern "C" void kernel_launch(void* const* d_in, const int* in_sizes, int n_in, void* d_out, int out_size, void* d_ws, size_t ws_size,
                              hipStream_t stream) {
  const u16* x = (const u16*)d_in[0];
  const u16* wqkv = (const u16*)d_in[1];
  const u16* wo = (const u16*)d_in[2];
  const u16* win = (const u16*)d_in[4];
  const u16* wsp = (const u16*)d_in[5];
  const u16* bsp = (const u16*)d_in[6];
  const u16* wout = (const u16*)d_in[7];
  const u16* cvec = (const u16*)d_in[8];
  const u16* wmod = (const u16*)d_in[9];
  char* ws = (char*)d_ws;
  (void)hipFuncSetAttribute((const void*)k_mega, hipFuncAttributeMaxDynamicSharedMemorySize, 163840);
  k0_prep<<<K0_GRID, 256, 0, stream>>>(x, wqkv, wo, cvec, wmod, ws);
  k_mega<<<256, 512, 163840, stream>>>(ws, x, win, wsp, bsp, wout, (u16*)d_out);
}
```

```cpp
#include <hip/hip_runtime.h>
#include <hip/hip_bf16.h>
#include <stdint.h>
#include <utility>

typedef __attribute__((ext_vector_type(8))) short bf16x8;
typedef __attribute__((ext_vector_type(4))) short bf16x4;
typedef __attribute__((ext_vector_type(2))) float f32x2;
typedef __attribute__((ext_vector_type(4))) float f32x4;
typedef __attribute__((ext_vector_type(16))) float f32x16;
typedef __attribute__((ext_vector_type(2))) unsigned u32x2;
typedef __attribute__((ext_vector_type(4))) unsigned u32x4;
typedef __attribute__((ext_vector_type(2))) __bf16 bf2_t;
typedef __attribute__((ext_vector_type(2))) _Float16 h2_t;
typedef unsigned short u16;
typedef unsigned long long u64;
#define LAS __attribute__((address_space(3)))
#define GAS __attribute__((address_space(1)))
#define DI __device__ __forceinline__

constexpr int D_ = 640, NQKV = 1920, F_ = 1728;

constexpr unsigned OFF_CNT = 0;
constexpr unsigned OFF_MOD = 65536;
constexpr unsigned OFF_MODP = OFF_MOD + 16384;
constexpr unsigned OFF_CBP = OFF_MODP + 61440;
constexpr unsigned OFF_XCH = OFF_CBP + 5 * 16384;
constexpr unsigned OFF_ST1 = OFF_XCH + 131072;
constexpr unsigned OFF_XHAT = OFF_ST1 + 262144;
constexpr unsigned OFF_WQKV = OFF_XHAT + 2621440;
constexpr unsigned OFF_WO = OFF_WQKV + 2457600;
constexpr unsigned OFF_WIN = OFF_WO + 819200;
constexpr unsigned OFF_WOUT = OFF_WIN + 2211840;
constexpr unsigned OFF_Q = OFF_WOUT + 1105920;
constexpr unsigned OFF_KV = OFF_Q + 32 * 81920;
constexpr unsigned OFF_O = OFF_KV + 32 * 163840;
constexpr unsigned OFF_X1S = OFF_O + 2621440;
constexpr unsigned OFF_Z2 = OFF_X1S + 2621440;
constexpr unsigned OFF_U = OFF_Z2 + 3538944;
constexpr unsigned WS_USED = OFF_U + 3538944;
constexpr int C_QKV = 0, C_MOD = 15  , C_ATT = 128, C_REL = 136, C_X1 = 144, C_U = 176, C_Z2 = 208  ;

#define STAMP(kid, k)
#define TDECL
#define TS(k)
#define TDUMP()
DI unsigned pack2(float lo, float hi) { return __builtin_bit_cast(unsigned, __builtin_convertvector((f32x2){lo, hi}, bf2_t)); }
DI float opq(float x) { asm("" : "+v"(x)); return x; }
DI f32x4 fma4s(f32x4 a, f32x4 b, f32x4 c) { return (f32x4){opq(__builtin_fmaf(a[0], b[0], c[0])), opq(__builtin_fmaf(a[1], b[1], c[1])), opq(__builtin_fmaf(a[2], b[2], c[2])), opq(__builtin_fmaf(a[3], b[3], c[3]))}; }
DI f32x4 mul4s(f32x4 a, f32x4 b) { return (f32x4){opq(a[0] * b[0]), opq(a[1] * b[1]), opq(a[2] * b[2]), opq(a[3] * b[3])}; }
DI unsigned pack2h(float lo, float hi) { return __builtin_bit_cast(unsigned, __builtin_convertvector((f32x2){lo, hi}, h2_t)); }
DI float bflo(unsigned u) { return __uint_as_float(u << 16); }
DI float bfhi(unsigned u) { return __uint_as_float(u & 0xffff0000u); }
DI float rbf(float x) { return bflo(pack2(x, x)); }
DI float bf2f(u16 b) { return __uint_as_float((unsigned)b << 16); }
#define MFMA32(a, b, c) __builtin_amdgcn_mfma_f32_32x32x16_bf16((a), (b), (c), 0, 0, 0)
#define MFMA32K8(a, b, c) __builtin_amdgcn_mfma_f32_32x32x8bf16_1k((a), (b), (c), 0, 0, 0)
#define MFMA16(a, b, c) __builtin_amdgcn_mfma_f32_16x16x32_bf16((a), (b), (c), 0, 0, 0)
#define BAR() asm volatile("s_waitcnt lgkmcnt(0)\n\ts_barrier" ::: "memory")
#define SCHED() __builtin_amdgcn_sched_barrier(0)
#define PIN(x) asm volatile("" : "+v"(x))
#define OCC2 __attribute__((amdgpu_waves_per_eu(2, 2)))
#define BF8(x) __builtin_bit_cast(bf16x8, x)
#define BF4(x) __builtin_bit_cast(bf16x4, x)
template <int N> DI void waitv() { asm volatile("s_waitcnt vmcnt(%0)" ::"i"(N) : "memory"); }
DI void drain() { waitv<0>(); __builtin_amdgcn_s_waitcnt(0x0F70); }
template <class F, int... I> DI void sfor_impl(F&& f, std::integer_sequence<int, I...>) { (f(std::integral_constant<int, I>{}), ...); }
template <int N, class F> DI void sfor(F&& f) { sfor_impl(f, std::make_integer_sequence<int, N>{}); }
typedef short v4i16_t __attribute__((ext_vector_type(4)));
DI bf16x4 lds_tr(const char* p) { return __builtin_bit_cast(bf16x4, __builtin_amdgcn_ds_read_tr16_b64_v4i16((LAS v4i16_t*)p)); }
DI void glds16(const void* g, unsigned lds_base) {
  unsigned sv;
  asm volatile("s_mov_b32 %0, m0\n\ts_mov_b32 m0, %2\n\ts_nop 0\n\tglobal_load_lds_dwordx4 %1, off\n\ts_mov_b32 m0, %0" : "=&s"(sv) : "v"(g), "s"(lds_base) : "memory");
}
DI void glds16c(const void* g, unsigned lds_base) {
  unsigned sv;
  asm volatile("s_mov_b32 %0, m0\n\ts_mov_b32 m0, %2\n\ts_nop 0\n\tglobal_load_lds_dwordx4 %1, off sc1\n\ts_mov_b32 m0, %0" : "=&s"(sv) : "v"(g), "s"(lds_base) : "memory");
}
DI void glds4c(const void* g, unsigned lds_base) {
  unsigned sv;
  asm volatile("s_mov_b32 %0, m0\n\ts_mov_b32 m0, %2\n\ts_nop 0\n\tglobal_load_lds_dword %1, off sc1\n\ts_mov_b32 m0, %0" : "=&s"(sv) : "v"(g), "s"(lds_base) : "memory");
}
#define PSLEEP 1
typedef __amdgpu_buffer_rsrc_t rsrc_t;
DI u32x4 ldc16(rsrc_t rs, unsigned voff, unsigned soff) { return __builtin_amdgcn_raw_buffer_load_b128(rs, voff, soff, 16); }
DI u32x2 ldc8(rsrc_t rs, unsigned voff, unsigned soff) { return __builtin_amdgcn_raw_buffer_load_b64(rs, voff, soff, 16); }
DI unsigned ldc4(rsrc_t rs, unsigned voff, unsigned soff) { return __builtin_amdgcn_raw_buffer_load_b32(rs, voff, soff, 16); }
DI void stc16(rsrc_t rs, unsigned voff, unsigned soff, u32x4 v) { __builtin_amdgcn_raw_buffer_store_b128(v, rs, voff, soff, 16); asm volatile("s_nop 1" ::"v"(v) : "memory"); }
DI void stc8(rsrc_t rs, unsigned voff, unsigned soff, u32x2 v) { __builtin_amdgcn_raw_buffer_store_b64(v, rs, voff, soff, 16); }
DI void stc4(rsrc_t rs, unsigned voff, unsigned soff, unsigned v) { __builtin_amdgcn_raw_buffer_store_b32(v, rs, voff, soff, 16); }
DI f32x4 ldc16f(rsrc_t rs, unsigned voff, unsigned soff) { return __builtin_bit_cast(f32x4, ldc16(rs, voff, soff)); }
DI void arrive(char* ws, int c, int lane) { if (lane == 0) __hip_atomic_fetch_add((GAS unsigned*)(ws + OFF_CNT + c * 256), 1u, __ATOMIC_RELAXED, __HIP_MEMORY_SCOPE_AGENT); }
DI void poll_ge(char* ws, int c, unsigned thr) {
  const GAS unsigned* p = (const GAS unsigned*)(ws + OFF_CNT + c * 256);
#pragma nounroll
  for (unsigned spin = 0; spin < (1u << 19); ++spin) {
    if (__all(__hip_atomic_load(p, __ATOMIC_RELAXED, __HIP_MEMORY_SCOPE_AGENT) >= thr)) break;
    __builtin_amdgcn_s_sleep(PSLEEP);
  }
  asm volatile("" ::: "memory");
}
DI int fresh_s(int v) { asm volatile("" : "+s"(v)); return v; }
DI int fresh_tid() { int t = threadIdx.x; asm volatile("" : "+v"(t)); return t; }
DI void l2touch(const void* g, unsigned lds_scratch) {
  unsigned sv;
  asm volatile("s_mov_b32 %0, m0\n\ts_mov_b32 m0, %2\n\ts_nop 0\n\tglobal_load_lds_dword %1, off\n\ts_mov_b32 m0, %0" : "=&s"(sv) : "v"(g), "s"(lds_scratch) : "memory");
}
#define LDNT(p) __builtin_nontemporal_load(p)
DI float silu_bf(u16 c) { const float f = __uint_as_float((unsigned)c << 16); const float v = f / (1.f + __expf(-f)); return __uint_as_float(__builtin_bit_cast(unsigned, __builtin_convertvector((f32x2){v, v}, bf2_t)) << 16); }
DI unsigned lds_addr(const void* p) { return (unsigned)(uintptr_t)(const LAS void*)p; }
DI float xhalf(float v) {
  auto rr = __builtin_amdgcn_permlane32_swap(__float_as_uint(v), __float_as_uint(v), false, false);
  return __uint_as_float((threadIdx.x & 32) ? rr[0] : rr[1]);
}
DI float sum_g(float v) {
  auto a = __builtin_amdgcn_permlane16_swap(__float_as_uint(v), __float_as_uint(v), false, false);
  v = __uint_as_float(a[0]) + __uint_as_float(a[1]);
  auto b = __builtin_amdgcn_permlane32_swap(__float_as_uint(v), __float_as_uint(v), false, false);
  return __uint_as_float(b[0]) + __uint_as_float(b[1]);
}
template <int CTRL> DI float dpp_add(float v) { return v + __uint_as_float(__builtin_amdgcn_update_dpp(0u, __float_as_uint(v), CTRL, 0xf, 0xf, true)); }
DI float sum16(float v) {
  v = dpp_add<0x128>(v); v = dpp_add<0x124>(v); v = dpp_add<0x122>(v); return dpp_add<0x121>(v);
}
DI u32x4 modulate8(u32x4 v, f32x4 sc0, f32x4 sc1, f32x4 sh0, f32x4 sh1) {
  unsigned o0, o1, o2, o3; float t0, t1, t2, t3;
  asm volatile(
      "v_fma_mix_f32 %0, %8, %12, %20 op_sel_hi:[1,0,0]\n\tv_fma_mix_f32 %4, %8, %13, %21 op_sel:[1,0,0] op_sel_hi:[1,0,0]\n\t"
      "v_fma_mix_f32 %1, %9, %14, %22 op_sel_hi:[1,0,0]\n\tv_fma_mix_f32 %5, %9, %15, %23 op_sel:[1,0,0] op_sel_hi:[1,0,0]\n\t"
      "v_fma_mix_f32 %2, %10, %16, %24 op_sel_hi:[1,0,0]\n\tv_fma_mix_f32 %6, %10, %17, %25 op_sel:[1,0,0] op_sel_hi:[1,0,0]\n\t"
      "v_fma_mix_f32 %3, %11, %18, %26 op_sel_hi:[1,0,0]\n\tv_fma_mix_f32 %7, %11, %19, %27 op_sel:[1,0,0] op_sel_hi:[1,0,0]\n\t"
      "v_cvt_pk_bf16_f32 %0, %0, %4\n\tv_cvt_pk_bf16_f32 %1, %1, %5\n\tv_cvt_pk_bf16_f32 %2, %2, %6\n\tv_cvt_pk_bf16_f32 %3, %3, %7"
      : "=&v"(o0), "=&v"(o1), "=&v"(o2), "=&v"(o3), "=&v"(t0), "=&v"(t1), "=&v"(t2), "=&v"(t3)
      : "v"(v[0]), "v"(v[1]), "v"(v[2]), "v"(v[3]), "v"(sc0[0]), "v"(sc0[1]), "v"(sc0[2]), "v"(sc0[3]), "v"(sc1[0]), "v"(sc1[1]), "v"(sc1[2]), "v"(sc1[3]),
        "v"(sh0[0]), "v"(sh0[1]), "v"(sh0[2]), "v"(sh0[3]), "v"(sh1[0]), "v"(sh1[1]), "v"(sh1[2]), "v"(sh1[3]));
  return (u32x4){o0, o1, o2, o3};
}
DI float ln_den(float var) { return rbf(__builtin_amdgcn_sqrtf(rbf(rbf(var) + 1e-5f))); }
DI int unit0_of(int j) { return j < 6 ? 7 * j : 42 + 6 * (j - 6); }

DI void K0ST(char* p, u32x4 v) { *(u32x4*)p = v; }
constexpr int K0_MOD = 240, K0_LN = 128, K0_RL = 240, K0_WO = 50, K0_INIT = 12, K0_WARM = 24;
constexpr int K0_GRID = K0_WARM + K0_MOD + K0_LN + K0_RL + K0_WO + K0_INIT;

__global__ __launch_bounds__(256) void k0_prep(const u16* __restrict__ x, const u16* __restrict__ wqkv, const u16* __restrict__ wo, const u16* __restrict__ cvec,
                                               const u16* __restrict__ wmod, char* __restrict__ ws) {
  __shared__ __attribute__((aligned(16))) char smem[4 * 4608];
  const int tid = threadIdx.x, lane = tid & 63, wid = __builtin_amdgcn_readfirstlane(tid >> 6);
  int b = blockIdx.x;
  STAMP(0, 0);
  if (b < K0_WARM) {
    u64 pc; unsigned len;
    asm volatile("s_getpc_b64 s[30:31]\n\ts_add_u32 s30, s30, k_mega@rel32@lo+4\n\ts_addc_u32 s31, s31, k_mega@rel32@hi+12\n\ts_mov_b64 %0, s[30:31]\n\t"
                 "s_mov_b32 %1, k_mega_code_end-k_mega" : "=s"(pc), "=s"(len) :: "scc", "s30", "s31");
    l2touch((const char*)pc + min(((b >> 3) * 256 + tid) * 128u, len - 4u), lds_addr(smem) + wid * 256);
    return;
  }
  b -= K0_WARM;
  if (b < K0_MOD) {
    const int cg = b >> 2, ks = b & 3, c = tid & 7, row0 = ks * 160 + (tid >> 3), col0 = cg * 64;
    u32x4 w[5]; u16 cv[5];
#pragma unroll
    for (int i = 0; i < 5; ++i) { w[i] = LDNT((const u32x4*)(wmod + (size_t)(row0 + 32 * i) * 3840 + col0 + c * 8)); cv[i] = cvec[row0 + 32 * i]; }
    float acc[8] = {0, 0, 0, 0, 0, 0, 0, 0};
#pragma unroll
    for (int i = 0; i < 5; ++i) {
      const float s = silu_bf(cv[i]);
#pragma unroll
      for (int e = 0; e < 4; ++e) { acc[2 * e] += s * bflo(w[i][e]); acc[2 * e + 1] += s * bfhi(w[i][e]); }
    }
#pragma unroll
    for (int e = 0; e < 8; ++e) acc[e] = sum_g(dpp_add<0x128>(acc[e]));
    float* red = (float*)smem;
    if (lane < 8) { *(f32x4*)(red + wid * 64 + lane * 8) = (f32x4){acc[0], acc[1], acc[2], acc[3]}; *(f32x4*)(red + wid * 64 + lane * 8 + 4) = (f32x4){acc[4], acc[5], acc[6], acc[7]}; }
    __syncthreads();
    if (tid < 64) ((float*)(ws + OFF_MODP))[ks * 3840 + cg * 64 + tid] = (red[tid] + red[64 + tid]) + (red[128 + tid] + red[192 + tid]);
    STAMP(0, 1); STAMP(0, 13);
    return;
  }
  b -= K0_MOD;
  if (b < K0_LN) {
    b = (b & 7) * 16 + (b >> 3);
    const int row = b * 16 + wid * 4 + (lane >> 4), c = lane & 15;
    const u16* xr = x + (size_t)row * D_ + c * 8;
    u32x4 v[5];
#pragma unroll
    for (int i = 0; i < 5; ++i) v[i] = *(const u32x4*)(xr + i * 128);
    float s = 0.f, q = 0.f;
#pragma unroll
    for (int i = 0; i < 5; ++i)
#pragma unroll
      for (int e = 0; e < 4; ++e) s += bflo(v[i][e]) + bfhi(v[i][e]);
    const float mean = sum16(s) * (1.f / 640.f);
#pragma unroll
    for (int i = 0; i < 5; ++i)
#pragma unroll
      for (int e = 0; e < 4; ++e) { const float a = bflo(v[i][e]) - mean, d = bfhi(v[i][e]) - mean; q += a * a + d * d; }
    const float var = sum16(q) * (1.f / 640.f);
    const float rinv = __builtin_amdgcn_rcpf(ln_den(var)), mb = rbf(mean);
    char* dst = ws + OFF_XHAT + (size_t)row * 1280 + c * 16;
#pragma unroll
    for (int i = 0; i < 5; ++i) {
      u32x4 o;
#pragma unroll
      for (int e = 0; e < 4; ++e) o[e] = pack2h((bflo(v[i][e]) - mb) * rinv, (bfhi(v[i][e]) - mb) * rinv);
      K0ST(dst + i * 256, o);
    }
    STAMP(0, 2); STAMP(0, 13);
    return;
  }
  b -= K0_LN;
  if (b < K0_RL) {
    const int kt = b / 12, G = b % 12, sec = G >> 2, gq = G & 3;
    u32x4 v[3];
#pragma unroll
    for (int i = 0; i < 3; ++i) { const int ci = min(i * 256 + tid, 639); v[i] = LDNT((const u32x4*)(wqkv + (size_t)(kt * 32 + ci / 20) * NQKV + G * 160 + (ci % 20) * 8)); }
#pragma unroll
    for (int i = 0; i < 3; ++i) { const int ci = min(i * 256 + tid, 639); *(u32x4*)(smem + (ci / 20) * 336 + (ci % 20) * 16) = v[i]; }
    __syncthreads();
    const int g = lane >> 4, q = (lane & 15) >> 2, p = lane & 3;
    for (int f = wid; f < 10; f += 4) {
      const int t = f >> 1, x2 = f & 1;
      const int col = t == 4 ? 20 * (4 * x2 + p) + 16 : sec == 2 ? 20 * f + 4 * p : 20 * (2 * t + (p >> 1)) + 8 * (p & 1) + 4 * x2;
      const int T = sec * 20 + (t < 4 ? 4 * gq + t : 16 + gq);
      const char* pp = smem + (8 * g + q) * 336 + col * 2;
      const bf16x4 lo = lds_tr(pp), hi = lds_tr(pp + 4 * 336);
      K0ST(ws + OFF_WQKV + (((size_t)T * 20 + kt) * 2 + x2) * 1024 + lane * 16, __builtin_bit_cast(u32x4, (bf16x8)__builtin_shufflevector(lo, hi, 0, 1, 2, 3, 4, 5, 6, 7)));
    }
    STAMP(0, 3); STAMP(0, 13);
    return;
  }
  b -= K0_RL;
  if (b < K0_WO) {
    const int job = b * 4 + wid, kt = job / 10, ng = job % 10, j8 = lane >> 3;
    char* t = smem + wid * 4608;
    u32x4 v[4];
#pragma unroll
    for (int i = 0; i < 4; ++i) {
      const int c = 4 * kt + i, row = c < 64 ? 20 * (c >> 1) + 4 * (c & 1) + (j8 & 3) + 8 * (j8 >> 2) : 20 * (2 * (c - 64) + (j8 >> 2)) + 16 + (j8 & 3);
      v[i] = LDNT((const u32x4*)(wo + (size_t)row * D_ + ng * 64 + (lane & 7) * 8));
    }
#pragma unroll
    for (int i = 0; i < 4; ++i) *(u32x4*)(t + (i * 8 + j8) * 144 + (lane & 7) * 16) = v[i];
    const char* pp = t + (8 * (lane >> 4) + ((lane & 15) >> 2)) * 144 + (lane & 3) * 8;
#pragma unroll
    for (int f = 0; f < 4; ++f) {
      const bf16x4 lo = lds_tr(pp + f * 32), hi = lds_tr(pp + 4 * 144 + f * 32);
      K0ST(ws + OFF_WO + ((size_t)(ng * 4 + f) * 20 + kt) * 1024 + lane * 16, __builtin_bit_cast(u32x4, (bf16x8)__builtin_shufflevector(lo, hi, 0, 1, 2, 3, 4, 5, 6, 7)));
    }
    return;
  }
  b -= K0_WO;
  {
    u32x4* p = (u32x4*)(ws + (b < 8 ? OFF_XCH + b * 16384 : OFF_CNT + (b - 8) * 16384)) + tid;
    const unsigned f = b < 8 ? ~0u : 0u;
#pragma unroll
    for (int i = 0; i < 4; ++i) p[i * 256] = (u32x4){f, f, f, f};
  }
}

constexpr float QSCALE = 0.22360679774997896f * 1.4426950408889634f;
#define K1W 3
#define K1WB 5
#define K1WB0 1
constexpr int K1_W = K1W, K1_WB = K1WB, K1_WB0 = K1WB0;

template <bool ISV, class FIN>
DI void k1_mma(const char* __restrict__ bsrc, const char* smem, int lane, FIN&& fin) {
  f32x4 acc[8][2];
  constexpr int PD = 4;
  bf16x8 bfr[K1_WB + 1][4], af[PD + 1][2];
  const char* lds[2];
#pragma unroll
  for (int k = 0; k < 2; ++k) lds[k] = smem + k * 8192 + (lane ^ (4 * k + (lane >> 4))) * 16;
#pragma unroll
  for (int i = 0; i < 4 * K1_WB0; ++i) bfr[i >> 2][i & 3] = *(const bf16x8*)(bsrc + i * 1024);
#pragma unroll
  for (int m = 0; m < 8; ++m) { acc[m][0] = (f32x4){}; acc[m][1] = (f32x4){}; }
  BAR();
#pragma unroll
  for (int i = 4 * K1_WB0; i < 4 * K1_WB; ++i) bfr[i >> 2][i & 3] = *(const bf16x8*)(bsrc + i * 1024);
  BAR();
  __builtin_amdgcn_s_setprio(1);
#define K1_RD(t, k) (af[(t) % (PD + 1)][k] = *(const bf16x8*)(lds[k] + (((t) >> 3) % 3) * 16384 + ((t) & 7) * 1024))
#define K1_MM(c, b, a) (c = ISV ? MFMA16(a, b, c) : MFMA16(b, a, c))
#pragma unroll
  for (int t = 0; t < PD; ++t) { K1_RD(t, 0); K1_RD(t, 1); }
  SCHED();
  sfor<80>([&](auto T) __attribute__((always_inline)) {
    constexpr int t = T, s = t >> 3, m = t & 7, nx = t + PD;
    if constexpr (nx < 80 && (nx & 7) == 0) asm volatile("s_barrier" ::: "memory");
    if constexpr (nx < 80) K1_RD(nx, 0);
    K1_MM(acc[m][0], bfr[s % (K1_WB + 1)][0], af[t % (PD + 1)][0]); K1_MM(acc[m][0], bfr[s % (K1_WB + 1)][2], af[t % (PD + 1)][1]); PIN(acc[m][0]);
    SCHED();
    if constexpr (nx < 80) K1_RD(nx, 1);
    if constexpr (s + K1_WB < 10 && (m & 1)) bfr[(s + K1_WB) % (K1_WB + 1)][m >> 1] = *(const bf16x8*)(bsrc + ((s + K1_WB) * 4 + (m >> 1)) * 1024);
    K1_MM(acc[m][1], bfr[s % (K1_WB + 1)][1], af[t % (PD + 1)][0]); K1_MM(acc[m][1], bfr[s % (K1_WB + 1)][3], af[t % (PD + 1)][1]); PIN(acc[m][1]);
    SCHED();
    if constexpr (t >= 74) { fin(t - 74, acc[t - 74]); SCHED(); }
  });
  fin(6, acc[6]); fin(7, acc[7]);
  __builtin_amdgcn_s_setprio(0);
}

#define TARG
#define TPASS

DI void phase_qkv(char* __restrict__ ws, rsrc_t rs, char* smem, int wid TARG) {
  const int tid = fresh_tid(), lane = tid & 63;
  const int xcd = blockIdx.x & 7, idx = blockIdx.x >> 3;
  int ctr;
  if ((xcd & 1) && idx >= 28) {
    ctr = C_MOD;
    if (tid < 160) {
      const int col = 1280 + ((xcd >> 1) * 4 + idx - 28) * 160 + tid;
      const float* pp = (const float*)(ws + OFF_MODP) + col;
      float v = rbf((pp[0] + pp[3840]) + (pp[7680] + pp[11520]));
      if (col / 640 == 4) v = rbf(1.f + v);
      stc4(rs, col * 4, OFF_MOD, __float_as_uint(v));
    }
  } else {
    const int mt = (xcd >> 1) * 4 + (idx & 3), nt = (xcd & 1) * 8 + (idx >> 2);
    ctr = C_QKV + nt;
    if (wid < 4) {
      float* cst = (float*)(smem + 49152);
      const int c = lane & 7, rr = lane >> 3;
      const char* src = ws + OFF_XHAT + (size_t)(mt * 128 + wid * 32 + rr) * 1280 + c * 16;
      char* wdst = smem + (c >> 2) * 8192 + wid * 2048 + (c & 3) * 256 + (rr ^ c) * 16;
      u32x4 a[10][4];
      f32x4 c0[4], c1[4];
#pragma unroll
      for (int i = 0; i < 4; ++i) { c0[i] = *(const f32x4*)(ws + OFF_MODP + i * 15360 + tid * 16); if (wid == 0) c1[i] = *(const f32x4*)(ws + OFF_MODP + i * 15360 + 4096 + tid * 16); }
#pragma unroll
      for (int s = 0; s < K1_W; ++s)
#pragma unroll
        for (int m = 0; m < 4; ++m) a[s][m] = *(const u32x4*)(src + m * 10240 + s * 128);
      {
        f32x4 v = (c0[0] + c0[1]) + (c0[2] + c0[3]); const float one = tid >= 160 ? 1.f : 0.f;
        *(f32x4*)(cst + tid * 4) = (f32x4){rbf(one + rbf(v[0])), rbf(one + rbf(v[1])), rbf(one + rbf(v[2])), rbf(one + rbf(v[3]))};
        if (wid == 0) { v = (c1[0] + c1[1]) + (c1[2] + c1[3]); *(f32x4*)(cst + 1024 + tid * 4) = (f32x4){rbf(1.f + rbf(v[0])), rbf(1.f + rbf(v[1])), rbf(1.f + rbf(v[2])), rbf(1.f + rbf(v[3]))}; }
      }
      BAR();
      const float* cp = cst + c * 8;
      f32x4 kc[10][4];
#define K1_KC(s) do { kc[s][0] = *(const f32x4*)(cp + (s) * 64); kc[s][1] = *(const f32x4*)(cp + (s) * 64 + 4); kc[s][2] = *(const f32x4*)(cp + (s) * 64 + 640); kc[s][3] = *(const f32x4*)(cp + (s) * 64 + 644); } while (0)
#pragma unroll
      for (int s = 0; s < 6; ++s) K1_KC(s);
      SCHED();
      u32x4 o[2][4];
#define K1_XF(s, m) (o[(s) & 1][m] = modulate8(a[s][m], kc[s][2], kc[s][3], kc[s][0], kc[s][1]))
#define K1_WR(s, m) (*(u32x4*)(wdst + ((s) % 3) * 16384 + ((m) >> 1) * 1024 + ((m) & 1) * 128) = o[(s) & 1][m])
#pragma unroll
      for (int m = 0; m < 4; ++m) { K1_XF(0, m); K1_WR(0, m); }
      BAR();
      TS(1);
#pragma unroll
      for (int m = 0; m < 4; ++m) a[K1_W][m] = *(const u32x4*)(src + m * 10240 + K1_W * 128);
#pragma unroll
      for (int m = 0; m < 4; ++m) K1_XF(1, m);
      SCHED();
#pragma unroll
      for (int s = 1; s < 10; ++s) {
#pragma unroll
        for (int m = 0; m < 4; ++m) {
          if (m == 0) K1_WR(s, 0);
          if (m < 3) K1_WR(s, m + 1);
          if (s + K1_W < 10) a[s + K1_W][m] = *(const u32x4*)(src + m * 10240 + (s + K1_W) * 128);
          if (s < 5) kc[s + 5][m] = *(const f32x4*)(cp + (s + 5) * 64 + (m & 1) * 4 + (m >> 1) * 640);
          SCHED();
          if (s < 9) K1_XF(s + 1, m);
          SCHED();
        }
        asm volatile("" :: "v"(o[s & 1][0]), "v"(o[s & 1][1]), "v"(o[s & 1][2]), "v"(o[s & 1][3]));
        BAR();
      }
    } else {
      const int cw = wid - 4;
      const char* bsrc = ws + OFF_WQKV + (size_t)(nt * 4 + cw) * 40 * 1024 + lane * 16;
      const int r = lane & 15, g = lane >> 4;
      if (nt >= 10) {
        const unsigned so = OFF_KV + (mt >> 1) * 20480 + 10240 + (mt & 1) * 5120;
        unsigned vo[2];
#pragma unroll
        for (int x = 0; x < 2; ++x)
          vo[x] = (nt < 14 ? ((nt - 10) * 8 + cw * 2 + x) * 163840u + ((g & 1) * 16 + r) * 16 : (cw * 8 + x * 4 + (r >> 2)) * 163840u + 512 + ((g & 1) * 4 + (r & 3)) * 16) + (g >> 1) * 8;
        k1_mma<true>(bsrc, smem, lane, [&](int m, const f32x4 (&a)[2]) __attribute__((always_inline)) {
#pragma unroll
          for (int x = 0; x < 2; ++x) stc8(rs, vo[x] + (m & 3) * 640, so + (m >> 2) * 2560, (u32x2){pack2(a[x][0], a[x][1]), pack2(a[x][2], a[x][3])});
        });
      } else {
        const bool isQ = nt < 5; const float sc = isQ ? QSCALE : 1.f;
        const unsigned so = isQ ? OFF_Q + mt * 5120 : OFF_KV + (mt >> 1) * 20480 + (mt & 1) * 5120, hs = isQ ? 81920u : 163840u;
        const int n5 = isQ ? nt : nt - 5;
        const bool wide = n5 < 4;
        const unsigned vo = wide ? (2 * (n5 * 4 + cw) + (g >> 1)) * hs + (g & 1) * 512 + r * 16 : (8 * cw + g) * hs + 1024 + r * 8;
        const f32x4 sc4 = (f32x4){sc, sc, sc, sc};
        k1_mma<false>(bsrc, smem, lane, [&](int m, const f32x4 (&a)[2]) __attribute__((always_inline)) {
          const f32x4 v0 = mul4s(a[0], sc4), v1 = mul4s(a[1], sc4);
          const u32x4 w = (u32x4){pack2(v0[0], v0[1]), pack2(v0[2], v0[3]), pack2(v1[0], v1[1]), pack2(v1[2], v1[3])};
          if (wide) stc16(rs, vo + (m & 1) * 256, so + (m >> 1) * 1280, w);
          else { stc8(rs, vo + (m & 1) * 128, so + (m >> 1) * 1280, (u32x2){w[0], w[1]}); stc8(rs, vo + (m & 1) * 128, so + 4 * hs + (m >> 1) * 1280, (u32x2){w[2], w[3]}); }
        });
      }
    }
  }
  TS(3);
  waitv<0>();
  BAR();
  TS(4);
  if (wid == 7 && lane < 8) __hip_atomic_fetch_add((GAS unsigned*)(ws + OFF_CNT + (lane * 16 + ctr) * 256), 1u, __ATOMIC_RELAXED, __HIP_MEMORY_SCOPE_AGENT);
  if (ctr == C_MOD && wid == 7) { __builtin_amdgcn_s_sleep(127); __builtin_amdgcn_s_sleep(60); }
}

constexpr int proj_pos(int f) { return 1024 + f * 1024; }
template <bool FINAL> DI u32x4 proj_ld(rsrc_t rs, int i, int wid, int lane) {
  constexpr int NF = FINAL ? 135 : 100;
  return ldc16(rs, lane * 16, (FINAL ? OFF_WOUT : OFF_WO) + (((blockIdx.x >> 3) & 7) * NF + (i * 8 + 7 < NF ? i * 8 + wid : min(i * 8 + wid, NF - 1))) * 1024);
}
template <bool FINAL> DI void proj_small(rsrc_t rs, const u16* __restrict__ x, int wid, int lane, u32x2 (&res)[3], f32x4 (&gate)[3], f32x4 (&cbv)[3]) {
  const int idx = blockIdx.x >> 3, mt = ((blockIdx.x & 7) * 4 + (idx >> 3)) * 4 + (wid & 3), g = lane >> 4;
#pragma unroll
  for (int i = 0; i < 3; ++i) {
    const int n = ((idx & 7) * 5 + min((wid >> 2) * 3 + i, 4)) * 16;
    if (!FINAL) { res[i] = *(const u32x2*)(x + (size_t)(mt * 16 + (lane & 15)) * D_ + n + g * 4); cbv[i] = ldc16f(rs, g * 16, OFF_MOD + (4 * 640 + n) * 4); }
    gate[i] = ldc16f(rs, g * 16, OFF_MOD + ((FINAL ? 5 : 2) * 640 + n) * 4);
  }
}
DI void proj_park(char* smem, int i, int tid, u32x4 v) { *(u32x4*)(smem + proj_pos(i * 8) + tid * 16) = v; }

DI void split3(float m, unsigned& w_h0, unsigned& w_h1) {
  const float n = -m; const float a = rbf(n), b = rbf(n - a), c = rbf(n - a - b);
  w_h0 = pack2(a, b); w_h1 = pack2(c, 0.f);
}

DI void phase_attn(char* __restrict__ ws, rsrc_t rs, const char* __restrict__ wsp, const u16* __restrict__ win, const u16* __restrict__ wout, const u16* __restrict__ x,
                   char* smem, int wid, u32x2 (&res)[3], f32x4 (&gate)[3], f32x4 (&cbv)[3] TARG) {
  const int tid = fresh_tid(), lane = tid & 63;
  const int xcd = blockIdx.x & 7, idx = blockIdx.x >> 3, head = xcd * 4 + (idx >> 3), qb = idx & 7;
  const int r = lane & 31, h = lane >> 5, qg = wid & 3, kh = wid >> 2;
  unsigned kvo = OFF_KV + head * 163840, qo = OFF_Q + head * 81920 + (qb * 8 + qg * 2) * 1280;
  const unsigned kconst = h == 0 ? 0x3f803f80u : 0x00003f80u;
  const unsigned so16 = (tid & 255) * 16, l16 = lane * 16, l2 = 1024 + r * 8 + h * 4;
  u32x4 wq[13];
  for (int attempt = 0; attempt < 2; ++attempt) {
    asm volatile("" : "+s"(kvo), "+s"(qo));
    const char* kvs = ws + kvo + kh * 81920 + so16;
    const unsigned ldst = lds_addr(smem) + kh * 81920 + qg * 1024;
    u32x4 qA1w, qB1w, k1w; unsigned qA2w, qB2w, k2, m0sv;
    const char* d0 = kvs + (wid == 0 ? 4096 : 0); const char* d1 = kvs + 4096; const char* d2 = kvs + 8192; const char* d3 = kvs + 12288; const char* d4 = kvs + 16384;
    PIN(d0); PIN(d1); PIN(d2); PIN(d3); PIN(d4);
    if (attempt == 0) {
      if (wid == 7) {
        const int l7 = lane & 7;
        const int c = l7 == 0 ? head >> 3 : l7 == 1 ? 4 : l7 == 2 ? 5 + (head >> 3) : l7 == 3 ? 9 : l7 == 4 ? 10 + (head >> 3) : l7 == 5 ? 14 : C_MOD;
        poll_ge(ws, xcd * 16 + c, 16);
      }
      BAR();
      TS(5);
    }
    asm volatile(
        "s_nop 4\n\tbuffer_load_dwordx4 %0, %7, %9, %19 offen sc1\n\tbuffer_load_dwordx4 %1, %7, %9, %10 offen sc1\n\tbuffer_load_dwordx4 %2, %7, %9, %11 offen sc1\n\t"
        "buffer_load_dword %3, %8, %9, %19 offen sc1\n\tbuffer_load_dword %4, %8, %9, %10 offen sc1\n\tbuffer_load_dword %5, %8, %9, %11 offen sc1\n\t"
        "s_mov_b32 %6, m0\n\ts_mov_b32 m0, %17\n\ts_nop 0\n\tglobal_load_lds_dwordx4 %12, off sc1\n\t"
        "s_add_u32 m0, %18, 0x1000\n\ts_nop 0\n\tglobal_load_lds_dwordx4 %13, off sc1\n\t"
        "s_add_u32 m0, %18, 0x2000\n\ts_nop 0\n\tglobal_load_lds_dwordx4 %14, off sc1\n\t"
        "s_add_u32 m0, %18, 0x3000\n\ts_nop 0\n\tglobal_load_lds_dwordx4 %15, off sc1\n\t"
        "s_add_u32 m0, %18, 0x4000\n\ts_nop 0\n\tglobal_load_lds_dwordx4 %16, off sc1\n\t"
        "s_mov_b32 m0, %6\n\ts_waitcnt vmcnt(5)"
        : "=&v"(qA1w), "=&v"(qB1w), "=&v"(k1w), "=&v"(qA2w), "=&v"(qB2w), "=&v"(k2), "=&s"(m0sv)
        : "v"(l16), "v"(l2), "s"(rs), "s"(qo + kh * 1280), "s"(kvo), "v"(d0), "v"(d1), "v"(d2), "v"(d3), "v"(d4),
          "s"(ldst + (wid == 0 ? 4096 : 0)), "s"(ldst), "s"(qo + (kh ^ 1) * 1280)
        : "memory", "scc");
    if (wid == 0) { const unsigned f = (fresh_tid() & 1) ? 0u : 0x3f803f80u; *(u32x4*)(smem + l16) = (u32x4){f, f, f, f}; }
    const bf16x8 qA1 = BF8(qA1w), qB1 = BF8(qB1w);
    bf16x8 k1 = BF8(k1w);
    TS(30);
    float mA = -INFINITY, mB = -INFINITY;
    {
      const bf16x4 qA2 = BF4(((u32x2){qA2w, 0u})), qB2 = BF4(((u32x2){qB2w, 0u}));
      const int nt = attempt ? 64 : 1;
      bf16x8 t1 = k1; unsigned t2 = k2;
      for (int t = 0; t < nt; ++t) {
        if (t) { const unsigned to = kvo + (t >> 3) * 20480 + (t & 7) * 1280; t1 = BF8(ldc16(rs, l16, to)); t2 = ldc4(rs, l2, to); }
        const bf16x4 t2v = BF4(((u32x2){t2, kconst}));
        f32x16 a = MFMA32(t1, qA1, (f32x16){}); a = MFMA32K8(t2v, qA2, a);
        f32x16 b = MFMA32(t1, qB1, (f32x16){}); b = MFMA32K8(t2v, qB2, b);
#pragma unroll
        for (int i = 0; i < 16; ++i) { mA = fmaxf(mA, a[i]); mB = fmaxf(mB, b[i]); }
      }
      mA = fmaxf(mA, xhalf(mA)); mB = fmaxf(mB, xhalf(mB));
    }
    unsigned a0, a1, b0, b1; split3(mA, a0, a1); split3(mB, b0, b1);
    const bf16x4 qA2 = BF4(((u32x2){qA2w, h ? a1 : a0})), qB2 = BF4(((u32x2){qB2w, h ? b1 : b0}));
    char* lh = smem + kh * 81920;
    TS(31);
    waitv<0>();
    BAR();
    TS(6);
    f32x16 oA = (f32x16){}, oB = (f32x16){};
    const char* kp1 = lh + l16;
    const char* kp2 = lh + l2;
    const bool vreal = r < 20;
    const char* vp = vreal ? lh + 10240 + (r < 16 ? h * 16 + r : 16 + h * 4 + r) * 16 : smem + (r == 20 ? 0 : 16);
    const int vinc = vreal ? 1280 : 0, vjump = vreal ? 10240 : 0;
    if (kh) { k1 = *(const bf16x8*)kp1; k2 = *(const unsigned*)kp2; }
    bf16x8 kf1[2], vf[2][2]; u32x2 kf2[2];
    kf1[0] = k1; kf2[0] = (u32x2){k2, kconst}; kf2[1] = (u32x2){0u, kconst};
    vf[1][0] = (bf16x8){}; vf[1][1] = (bf16x8){};
    u32x4 pA0, pA1, pB0 = (u32x4){}, pB1 = (u32x4){};
    f32x4 e0 = (f32x4){}, e1;
    f32x16 sA = MFMA32(kf1[0], qA1, (f32x16){}), sB;
    sA = MFMA32K8(BF4(kf2[0]), qA2, sA);
#define EX2(x) __builtin_amdgcn_exp2f(x)
#define GRP(MF, S, i, EN, EP, P, w) do { MF; SCHED(); EN[0] = EX2(S[i]); P[w] = pack2(EP[0], EP[1]); EN[1] = EX2(S[i + 1]); P[w + 1] = pack2(EP[2], EP[3]); \
      EN[2] = EX2(S[i + 2]); EN[3] = EX2(S[i + 3]); PIN(P); PIN(EN); SCHED(); } while (0)
    const unsigned relsc = lds_addr(smem) + 768;
    auto group = [&](auto FIRST, int jump, int j) __attribute__((always_inline)) {
#pragma unroll
      for (int t = 0; t < 8; ++t) {
        const int c = t & 1, n = c ^ 1;
        if (FIRST) { if (t == 0) __builtin_amdgcn_s_setprio(3); if (t == 3) __builtin_amdgcn_s_setprio(2); if (t == 5) __builtin_amdgcn_s_setprio(1); if (t == 6) __builtin_amdgcn_s_setprio(0); if (t == 7) __builtin_amdgcn_s_setprio(3); }
        else {
          if (t == 4 && j == 2) __builtin_amdgcn_s_setprio(2);
          if (t == 3 && j == 3) {
            __builtin_amdgcn_s_setprio(1);
            const int wf = fresh_s(wid);
#pragma unroll
            for (int i = 0; i < 13; ++i) wq[i] = proj_ld<false>(rs, i, wf, lane);
            proj_small<false>(rs, x, wf, fresh_tid() & 63, res, gate, cbv);
          }
          if (t == 6 && j == 3) { __builtin_amdgcn_s_setprio(0); if (wid == 7) glds4c(ws + OFF_CNT + (C_REL + (lane & 7)) * 256, relsc); }
        }
        if (FIRST && t < 5) {
#pragma unroll
          for (int i = 5 + 3 * t; i < 8 + 3 * t; ++i) glds16c(kvs + i * 4096, fresh_s(ldst) + i * 4096);
        }
        if (t == 7) {
          if (FIRST) { waitv<0>(); BAR(); }
          kp1 += jump; kp2 += jump;
        }
        SCHED();
        kf1[n] = *(const bf16x8*)(kp1 + ((t + 1) & 7) * 1280); kf2[n][0] = *(const unsigned*)(kp2 + ((t + 1) & 7) * 1280);
        GRP(sB = MFMA32(kf1[c], qB1, (f32x16){}), sA, 0, e1, e0, pB1, 2);
        vf[c][0] = *(const bf16x8*)vp; vf[c][1] = *(const bf16x8*)(vp + 640); vp += vinc;
        GRP(sB = MFMA32K8(BF4(kf2[c]), qB2, sB), sA, 4, e0, e1, pA0, 0);
        GRP(oB = MFMA32(vf[n][0], BF8(pB0), oB), sA, 8, e1, e0, pA0, 2);
        GRP(oB = MFMA32(vf[n][1], BF8(pB1), oB), sA, 12, e0, e1, pA1, 0);
        GRP(sA = MFMA32(kf1[n], qA1, (f32x16){}), sB, 0, e1, e0, pA1, 2);
        GRP(sA = MFMA32K8(BF4(kf2[n]), qA2, sA), sB, 4, e0, e1, pB0, 0);
        GRP(oA = MFMA32(vf[c][0], BF8(pA0), oA), sB, 8, e1, e0, pB0, 2);
        GRP(oA = MFMA32(vf[c][1], BF8(pA1), oA), sB, 12, e0, e1, pB1, 0);
      }
      vp += vjump;
    };
    group(std::true_type{}, 20480, 0);
    TS(7);
    const int blk = fresh_s(blockIdx.x);
    const int j2 = (blk - 135) * 4 + qg;
    const bool side = kh == 0 && attempt == 0 && j2 < 270;
    char* stile = smem + 1024 + qg * 4096;
    const char* svec = smem + 81920 + qg * 256;
    const int kt = blk < 135 ? 4 * (blk % 5) + qg : j2 / 10, ng = blk < 135 ? blk / 5 : j2 % 10;
    if (side) {
      const u16* W = blk < 135 ? win : wout; const int N = blk < 135 ? F_ : D_;
      const int lf = fresh_tid() & 63, j8 = lf >> 3;
#pragma unroll
      for (int i = 0; i < 4; ++i) {
        const int row = kt * 32 + i * 8 + j8;
        glds16(W + (size_t)row * N + ng * 64 + ((lf & 7) ^ j8) * 8, lds_addr(stile) + i * 1024);
      }
      glds4c((const float*)(ws + OFF_MOD) + (4 - h) * 640 + (kt < 20 ? kt : 0) * 32 + r, lds_addr(svec));
    }
#pragma nounroll
    for (int j = 1; j < 4; ++j) {
      if (j == 2) {
        if (kh) l2touch(wsp + ((size_t)(blk * 4 + qg) * 64 + lane) * 128, lds_addr(smem) + 256);
        else if (side) {
          waitv<0>();
          const int lf = fresh_tid() & 63, g4 = lf >> 4, r4 = lf & 15, row = 8 * g4 + (r4 >> 2);
          const f32x4 sv0 = *(const f32x4*)(svec + (r4 & 1) * 128 + g4 * 32), sv1 = *(const f32x4*)(svec + (r4 & 1) * 128 + g4 * 32 + 16);
          const bf16x8 vec = BF8(((u32x4){pack2(sv0[0], sv0[1]), pack2(sv0[2], sv0[3]), pack2(sv1[0], sv1[1]), pack2(sv1[2], sv1[3])}));
          const unsigned nks = blk < 135 ? 20 : 27, dso = (blk < 135 ? OFF_WIN : OFF_WOUT) + (ng * 4 * nks + kt) * 1024;
          bf16x8 fr[4]; f32x4 pr[4];
#pragma unroll
          for (int f = 0; f < 4; ++f) {
            const int ch = 2 * f + ((r4 & 3) >> 1);
            const bf16x4 lo = lds_tr(stile + row * 128 + (ch ^ (row & 7)) * 16 + (r4 & 1) * 8), hi = lds_tr(stile + (row + 4) * 128 + (ch ^ ((row + 4) & 7)) * 16 + (r4 & 1) * 8);
            fr[f] = __builtin_shufflevector(lo, hi, 0, 1, 2, 3, 4, 5, 6, 7);
          }
#pragma unroll
          for (int f = 0; f < 4; ++f) { stc16(rs, l16, dso + f * nks * 1024, __builtin_bit_cast(u32x4, fr[f])); pr[f] = MFMA16(fr[f], vec, (f32x4){}); }
          if (r4 < 2) {
#pragma unroll
            for (int f = 0; f < 4; ++f) *(f32x4*)(stile + 3072 + r4 * 256 + f * 64 + g4 * 16) = pr[f];
          }
        }
      }
      if (j == 3 && kh == 0 && attempt == 0) { waitv<0>(); arrive(ws, C_REL + xcd, lane); }
      group(std::false_type{}, j < 3 ? 20480 : 0, j);
    }
    TS(8);
    pB1[2] = pack2(e0[0], e0[1]); pB1[3] = pack2(e0[2], e0[3]);
    oB = MFMA32(vf[1][0], BF8(pB0), oB); oB = MFMA32(vf[1][1], BF8(pB1), oB);
    {
      f32x4* ex = (f32x4*)(lh + 1024 + qg * 4096) + lane;
#pragma unroll
      for (int g = 0; g < 3; ++g) ex[g * 64] = (f32x4){oA[4 * g], oA[4 * g + 1], oA[4 * g + 2], oA[4 * g + 3]};
    }
    int* flag = (int*)(smem + 528);
    BAR();
    TS(9);
    float o[12];
    {
      const f32x4* ex = (const f32x4*)(smem + (kh ^ 1) * 81920 + 1024 + qg * 4096) + lane;
#pragma unroll
      for (int g = 0; g < 3; ++g) { const f32x4 t = ex[g * 64];
#pragma unroll
        for (int e = 0; e < 4; ++e) o[4 * g + e] = oB[4 * g + e] + t[e]; }
    }
    const float lo = xhalf(o[8]); const float l = h ? o[8] : lo;
    const float rl = __builtin_amdgcn_rcpf(l);
    const int tok = qb * 256 + qg * 64 + kh * 32 + r;
    const unsigned vo = (tok >> 4) * 20480 + (tok & 15) * 16;
    stc16(rs, vo + h * 256, OFF_O + (head >> 1) * 1024 + (head & 1) * 512,
          (u32x4){pack2(o[0] * rl, o[1] * rl), pack2(o[2] * rl, o[3] * rl), pack2(o[4] * rl, o[5] * rl), pack2(o[6] * rl, o[7] * rl)});
    if (h == 0) stc8(rs, vo - (tok & 15) * 8, OFF_O + 16384 + (head >> 3) * 1024 + (head & 1) * 512 + ((head >> 1) & 3) * 128, (u32x2){pack2(o[8] * rl, o[9] * rl), pack2(o[10] * rl, o[11] * rl)});
    if (wid == 4 && blk < 135 && attempt == 0 && lane < 32) {
      const char* pp = smem + 1024 + 3072 + lane * 16;
      const f32x4 v = (*(const f32x4*)pp + *(const f32x4*)(pp + 4096)) + (*(const f32x4*)(pp + 8192) + *(const f32x4*)(pp + 12288));
      stc16(rs, (lane >> 4) * 8192 + (lane & 15) * 16, OFF_CBP + (blk % 5) * 16384 + (blk / 5) * 256, __builtin_bit_cast(u32x4, v));
    }
    bool ok = (l > 1e-30f) && (l < 1e30f);
#pragma unroll
    for (int i = 0; i < 12; ++i) ok = ok && (fabsf(o[i]) < 3e38f);
    if (!ok && attempt == 0) *flag = 1;
    drain();
    BAR();
    const int redo = __builtin_amdgcn_readfirstlane(*(volatile LAS int*)flag);
    if (!redo) break;
    BAR();
  }
  TS(10);
  if (wid == 7) arrive(ws, C_ATT + qb, lane);
#pragma unroll
  for (int i = 0; i < 13; ++i) proj_park(smem, i, tid, wq[i]);
  if (!__all(*(volatile LAS unsigned*)(smem + 768 + (lane & 7) * 4) >= 128u)) poll_ge(ws, C_REL + (lane & 7), 128);
}

template <bool FINAL, int KH, class HOOK>
DI void proj_body(char* __restrict__ ws, rsrc_t rs, const u16* __restrict__ x, u16* __restrict__ out, char* smem, int wid, u32x2 (&res)[3], const f32x4 (&gate)[3], const f32x4 (&cbv)[3], HOOK&& after_mma TARG) {
  const int lane = fresh_tid() & 63;
  const int xcd = blockIdx.x & 7, idx = blockIdx.x >> 3, panel = xcd * 4 + (idx >> 3), cb = idx & 7;
  constexpr int NKS = FINAL ? 27 : 20;
  constexpr int K0 = KH ? (NKS + 1) / 2 : 0, NK = KH ? NKS / 2 : (NKS + 1) / 2, NKEEP = KH ? 2 : 3, UK = KH ? 3 : 0, UG = KH ? 0 : 3;
  const int m = wid & 3, mt = panel * 4 + m, g = lane >> 4, r = lane & 15;
  unsigned l16 = lane * 16; PIN(l16);
  const unsigned aso = fresh_s((FINAL ? OFF_U : OFF_O) + (mt * NKS + K0) * 1024);
  if (wid == 7) poll_ge(ws, FINAL ? C_U + panel : C_ATT + (panel >> 2), FINAL ? 8 : 32);
  BAR();
  TS(FINAL ? 27 : 11);
  bf16x8 a[NK];
#pragma unroll
  for (int k = 0; k < NK; ++k) {
    if (!FINAL && K0 + k >= 16) {
      const u32x2 lo = ldc8(rs, (l16 >> 1) + (k & 3) * 1024, aso + (k >> 2) * 4096), hi = ldc8(rs, (l16 >> 1) + (k & 3) * 1024 + 512, aso + (k >> 2) * 4096);
      a[k] = BF8(((u32x4){lo[0], lo[1], hi[0], hi[1]}));
    } else a[k] = BF8(ldc16(rs, l16 + (k & 3) * 1024, aso + (k >> 2) * 4096));
  }
  SCHED();
  constexpr int PD = FINAL ? 5 : 6;
  const char* bl = smem + lane * 16;
  bf16x8 bf[PD + 1][5]; f32x4 acc[5];
#pragma unroll
  for (int u = 0; u < 5; ++u) acc[u] = (f32x4){};
#pragma unroll
  for (int k = 0; k < PD; ++k)
#pragma unroll
    for (int u = 0; u < 5; ++u) bf[k][u] = *(const bf16x8*)(bl + proj_pos(u * NKS + K0 + k));
  SCHED();
#pragma unroll
  for (int k = 0; k < NK; ++k) {
#pragma unroll
    for (int u = 0; u < 5; ++u) {
      if (k + PD < NK) bf[(k + PD) % (PD + 1)][u] = *(const bf16x8*)(bl + proj_pos(u * NKS + K0 + k + PD));
      acc[u] = MFMA16(bf[k % (PD + 1)][u], a[k], acc[u]);
      SCHED();
    }
  }
  TS(FINAL ? 28 : 12);
  f32x4* ex = (f32x4*)(smem + proj_pos(FINAL ? 136 : 104)) + m * 5 * 64 + lane;
#pragma unroll
  for (int i = 0; i < 5 - NKEEP; ++i) ex[(UG + i) * 64] = acc[UG + i];
  f32x4 part[NKEEP];
#pragma unroll
  for (int i = 0; i < NKEEP; ++i) {
    part[i] = fma4s(gate[i], acc[UK + i], (f32x4){bflo(res[i][0]), bfhi(res[i][0]), bflo(res[i][1]), bfhi(res[i][1])});
    PIN(part[i]);
  }
  SCHED();
  after_mma();
  BAR();
  f32x4 oth[NKEEP];
#pragma unroll
  for (int i = 0; i < NKEEP; ++i) oth[i] = ex[(UK + i) * 64];
  f32x4 sv = (f32x4){}, qv = (f32x4){};
#pragma unroll
  for (int i = 0; i < NKEEP; ++i) {
    const f32x4 v = part[i] + gate[i] * oth[i];
    const u32x2 o = (u32x2){pack2(v[0], v[1]), pack2(v[2], v[3])};
    if (FINAL) *(u32x2*)(out + (size_t)(mt * 16 + r) * D_ + (cb * 5 + UK + i) * 16 + g * 4) = o;
    else {
      res[i] = o; PIN(res[i]);
      const f32x4 w = v * cbv[i]; const int un = cb * 5 + UK + i;
      stc8(rs, ((g >> 1) * 16 + r) * 16 + (g & 1) * 8, OFF_X1S + mt * 20480 + un * 512, (u32x2){pack2(w[0], w[1]), pack2(w[2], w[3])});
      sv += v; qv += v * v;
    }
  }
  if (!FINAL) {
    const float s = sum_g((sv[0] + sv[1]) + (sv[2] + sv[3])), q = sum_g((qv[0] + qv[1]) + (qv[2] + qv[3]));
    if (g == 0) stc8(rs, r * 8, OFF_ST1 + ((panel * 16 + cb * 2 + KH) * 64 + m * 16) * 8, (u32x2){__float_as_uint(s), __float_as_uint(q)});
    TS(13);
    drain();
    BAR();
    TS(14);
    if (wid == 7) arrive(ws, C_X1 + panel, lane);
  }
}

constexpr float GC1 = -2.8853900817779268f * 0.7978845608028654f, GC2 = GC1 * 0.044715f;
DI void gelu8(f32x4& a0, const f32x4& l0, f32x4& a1, const f32x4& l1) {
  float t[8];
#define G8(expr0, expr1) do { _Pragma("unroll") for (int i = 0; i < 4; ++i) { t[i] = opq(expr0); } _Pragma("unroll") for (int i = 0; i < 4; ++i) { t[4 + i] = opq(expr1); } SCHED(); } while (0)
  G8(a0[i] * a0[i], a1[i] * a1[i]);
  G8(__builtin_fmaf(t[i], l0[3], l0[2]), __builtin_fmaf(t[4 + i], l1[3], l1[2]));
  G8(a0[i] * t[i], a1[i] * t[4 + i]);
  G8(__builtin_amdgcn_exp2f(t[i]), __builtin_amdgcn_exp2f(t[4 + i]));
  G8(__builtin_fmaf(t[i], l0[0], l0[0]), __builtin_fmaf(t[4 + i], l1[0], l1[0]));
  G8(__builtin_amdgcn_rcpf(t[i]), __builtin_amdgcn_rcpf(t[4 + i]));
  G8(a0[i] * t[i], a1[i] * t[4 + i]);
  a0 = (f32x4){t[0], t[1], t[2], t[3]}; a1 = (f32x4){t[4], t[5], t[6], t[7]};
}
constexpr int L4_Z1 = 114688, L4_TR = L4_Z1 + 16384, L4_LN = L4_TR + 16384, L4_ST2 = L4_LN + 1024, L4_FIN = L4_ST2 + 3584, L4_CB = L4_FIN + 512, L4_CNT = L4_CB + 2048;
constexpr int K5_R = 7;
constexpr unsigned DEAD = 0x80000000u;
DI int sp_stage(int s) { const int idx = blockIdx.x >> 3; return (s + (idx & 7) * 2) & 15; }
DI unsigned sp_voff(int wid, int lane, int i) { return ((wid & 3) * 16 + i * 8 + (lane >> 3)) * 4096 + (wid >> 2) * 128 + ((lane & 7) ^ (4 * i + (lane >> 4))) * 16; }
DI const char* sp_base(const u16* wsp, int panel, int s) { return (const char*)wsp + (size_t)panel * 262144 + sp_stage(s) * 256; }
DI void sp_prefetch(u32x4 (&wr)[16 - K5_R][2], const u16* __restrict__ wsp, int panel, int wid, int lane) {
  const unsigned v0 = sp_voff(wid, lane, 0), v1 = sp_voff(wid, lane, 1);
#pragma unroll
  for (int s = K5_R; s < 16; ++s) { wr[s - K5_R][0] = *(const u32x4*)(sp_base(wsp, panel, s) + v0); wr[s - K5_R][1] = *(const u32x4*)(sp_base(wsp, panel, s) + v1); }
  asm volatile("" ::: "memory");
}
template <int S0, int S1>
DI void sp_ring(const u16* __restrict__ wsp, char* smem, int panel, int wid, int lane) {
  const unsigned v0 = sp_voff(wid, lane, 0), v1 = sp_voff(wid, lane, 1);
#pragma unroll
  for (int s = S0; s < S1; ++s) { glds16(sp_base(wsp, panel, s) + v0, lds_addr(smem) + wid * 2048 + s * 16384); glds16(sp_base(wsp, panel, s) + v1, lds_addr(smem) + wid * 2048 + s * 16384 + 1024); }
}
DI unsigned win_boff(int wid, int lane) { const int cb = (blockIdx.x >> 3) & 7; return lane * 16 + (wid == 6 && cb >= 6 ? DEAD : 0u); }
DI unsigned win_soff(int wid, bool z2) { return OFF_WIN + ((z2 ? 54 : 0) + unit0_of((blockIdx.x >> 3) & 7) + wid) * 20480; }
#define WIN_LD(so, k) BF8(ldc16(rs, bo + ((k) & 3) * 1024, (so) + ((k) >> 2) * 4096))
DI void win_prefetch(rsrc_t rs, int wid, bf16x8 (&b2)[20]) {
  const unsigned bo = win_boff(wid, fresh_tid() & 63), so = win_soff(wid, true);
#pragma unroll
  for (int k = 0; k < 20; ++k) b2[k] = WIN_LD(so, k);
}
template <class HOOK>
DI void win_mma(const char* al, const bf16x8 (&b)[20], f32x4 (&acc)[4], HOOK&& hook) {
  constexpr int PD = 4;
  bf16x8 af[PD + 1][2];
#define WIN_RD(i) do { af[(i) % (PD + 1)][0] = *(const bf16x8*)(al + ((i) & 3) * 20480 + ((i) >> 2) * 2048); af[(i) % (PD + 1)][1] = *(const bf16x8*)(al + ((i) & 3) * 20480 + ((i) >> 2) * 2048 + 1024); } while (0)
#pragma unroll
  for (int i = 0; i < PD; ++i) WIN_RD(i);
  SCHED();
  sfor<40>([&](auto I) __attribute__((always_inline)) {
    constexpr int i = I, m = i & 3, k = (i >> 2) * 2;
    if constexpr (i + PD < 40) WIN_RD(i + PD);
    acc[m] = MFMA16(b[k], af[i % (PD + 1)][0], acc[m]);
    acc[m] = MFMA16(b[k + 1], af[i % (PD + 1)][1], acc[m]); PIN(acc[m]);
    hook(I);
    SCHED();
  });
}
DI void stage_x1s(char* __restrict__ ws, char* smem, int panel, int wid, int lane) {
  const char* src = ws + OFF_X1S + (size_t)panel * 81920 + lane * 16;
#pragma unroll
  for (int i = 0; i < 12; ++i) { const int f = i < 11 ? i * 7 + wid : min(77 + wid, 79); glds16c(src + f * 1024, lds_addr(smem) + f * 1024); }
}
DI void load_bb(const u16* __restrict__ bsp, int panel, int wid, int lane, u16 (&bb)[2]) { const u16* p = bsp + panel * 64 + (wid >> 2) * 32 + (lane & 15); bb[0] = p[0]; bb[1] = p[16]; asm volatile("" ::: "memory"); }
DI void win_tail(char* __restrict__ ws, const u16* __restrict__ wsp, char* smem, int panel, int cb, int wid, int lane TARG) {
  BAR();
  TS(22);
  if (wid == 7 && lane < 4) __hip_atomic_fetch_add((GAS unsigned*)(ws + OFF_CNT + (C_Z2 + lane * 8 + cb) * 256), 1u, __ATOMIC_RELAXED, __HIP_MEMORY_SCOPE_AGENT);
}

template <bool EARLY>
DI void win_body(char* __restrict__ ws, rsrc_t rs, const u16* __restrict__ wsp, char* smem, int wid, const bf16x8 (&b2)[20], u32x4 (&wr)[16 - K5_R][2], const u16* __restrict__ bsp, u16 (&bb)[2] TARG) {
  const int tid = fresh_tid(), lane = tid & 63, g = lane >> 4, r = lane & 15;
  const int xcd = blockIdx.x & 7, idx = blockIdx.x >> 3, panel = xcd * 4 + (idx >> 3), cb = idx & 7, u0 = unit0_of(cb);
  float* lnp = (float*)(smem + L4_LN); float* st2 = (float*)(smem + L4_ST2); float* fin = (float*)(smem + L4_FIN); float* cbl = (float*)(smem + L4_CB);
  int* flg = (int*)(smem + L4_CNT);
  __builtin_amdgcn_s_setprio(1);
  const unsigned bo = win_boff(wid, lane), so1 = win_soff(wid, false);
  bf16x8 b1[20];
  BAR();
  TS(15);
  stage_x1s(ws, smem, panel, wid, lane);
  BAR();
  f32x4 ln[4], z2[4], z1[4];
#pragma unroll
  for (int m = 0; m < 4; ++m) ln[m] = *(const f32x4*)(lnp + (m * 16 + r) * 4);
  {
    const f32x4 cs2 = *(const f32x4*)(cbl + 112 + wid * 16 + g * 4), bi2 = *(const f32x4*)(cbl + 256 + 112 + wid * 16 + g * 4);
    const f32x4 cs1 = *(const f32x4*)(cbl + wid * 16 + g * 4), bi1 = *(const f32x4*)(cbl + 256 + wid * 16 + g * 4);
#pragma unroll
    for (int m = 0; m < 4; ++m) {
#pragma unroll
      for (int e = 0; e < 4; ++e) { z2[m][e] = opq(__builtin_fmaf(ln[m][1], cs2[e], opq(ln[m][0] * bi2[e]))); z1[m][e] = opq(__builtin_fmaf(ln[m][1], cs1[e], opq(ln[m][0] * bi1[e]))); }
      PIN(z1[m]); PIN(z2[m]);
    }
  }
#pragma unroll
  for (int k = 0; k < 4; ++k) b1[k] = WIN_LD(so1, k);
  waitv<4>();
  BAR();
  TS(16);
  load_bb(bsp, panel, wid, lane, bb);
  win_mma(smem + lane * 16, b2, z2, [&](auto I) __attribute__((always_inline)) { constexpr int i = I; if constexpr (i < 32 && (i & 1)) b1[4 + i / 2] = WIN_LD(so1, 4 + i / 2); });
  TS(17);
  gelu8(z2[0], ln[0], z2[1], ln[1]); gelu8(z2[2], ln[2], z2[3], ln[3]);
  {
    float s[4], q[4];
#pragma unroll
    for (int m = 0; m < 4; ++m) { s[m] = opq(opq(z2[m][0] + z2[m][1]) + opq(z2[m][2] + z2[m][3])); q[m] = opq(__builtin_fmaf(z2[m][0], z2[m][0], opq(z2[m][1] * z2[m][1])) + opq(__builtin_fmaf(z2[m][2], z2[m][2], opq(z2[m][3] * z2[m][3])))); }
    auto pa = __builtin_amdgcn_permlane32_swap(__float_as_uint(s[0]), __float_as_uint(s[2]), false, false);
    auto pb = __builtin_amdgcn_permlane32_swap(__float_as_uint(s[1]), __float_as_uint(s[3]), false, false);
    auto pc = __builtin_amdgcn_permlane32_swap(__float_as_uint(q[0]), __float_as_uint(q[2]), false, false);
    auto pd = __builtin_amdgcn_permlane32_swap(__float_as_uint(q[1]), __float_as_uint(q[3]), false, false);
    const float sa = opq(__uint_as_float(pa[0]) + __uint_as_float(pa[1])), sb = opq(__uint_as_float(pb[0]) + __uint_as_float(pb[1]));
    const float qa = opq(__uint_as_float(pc[0]) + __uint_as_float(pc[1])), qb = opq(__uint_as_float(pd[0]) + __uint_as_float(pd[1]));
    auto ps = __builtin_amdgcn_permlane16_swap(__float_as_uint(sa), __float_as_uint(sb), false, false);
    auto pq = __builtin_amdgcn_permlane16_swap(__float_as_uint(qa), __float_as_uint(qb), false, false);
    *(f32x2*)(st2 + (wid * 64 + lane) * 2) = (f32x2){opq(__uint_as_float(ps[0]) + __uint_as_float(ps[1])), opq(__uint_as_float(pq[0]) + __uint_as_float(pq[1]))};
  }
  asm volatile("" ::: "memory");
  if (lane == 0) __hip_atomic_fetch_add(flg, 1, __ATOMIC_RELAXED, __HIP_MEMORY_SCOPE_WORKGROUP);
  asm volatile("" ::: "memory");
  __builtin_amdgcn_s_setprio(0);
  TS(18);
  const unsigned wvo[2] = {sp_voff(wid, lane, 0), sp_voff(wid, lane, 1)};
  win_mma(smem + lane * 16, b1, z1, [&](auto I) __attribute__((always_inline)) {
    constexpr int i = I, j = i < 8 ? i : (i & 1) ? -1 : 4 + i / 2;
    if constexpr (EARLY && j >= 0 && j < 18) wr[j >> 1][j & 1] = *(const u32x4*)(sp_base(wsp, panel, K5_R + (j >> 1)) + wvo[j & 1]);
  });
  gelu8(z1[0], ln[0], z1[1], ln[1]); gelu8(z1[2], ln[2], z1[3], ln[3]);
#pragma unroll
  for (int mm = 0; mm < 2; ++mm)
    *(u32x4*)(smem + L4_Z1 + wid * 2048 + mm * 1024 + lane * 16) = (u32x4){pack2(z1[2 * mm][0], z1[2 * mm][1]), pack2(z1[2 * mm][2], z1[2 * mm][3]), pack2(z1[2 * mm + 1][0], z1[2 * mm + 1][1]), pack2(z1[2 * mm + 1][2], z1[2 * mm + 1][3])};
  TS(20);
  BAR();
  TS(21);
  char* tr = smem + L4_TR + wid * 2048;
#pragma unroll
  for (int m = 0; m < 4; ++m) {
    const f32x2 f = *(const f32x2*)(fin + (m * 16 + r) * 2);
    const f32x4 v = z2[m] * f[0] - f[1];
    *(u32x2*)(tr + (m * 16 + r) * 32 + g * 8) = (u32x2){pack2(v[0], v[1]), pack2(v[2], v[3])};
  }
  {
    const char* p = tr + (8 * g + (r >> 2)) * 32 + (r & 3) * 8;
    const bf16x4 lo0 = lds_tr(p), hi0 = lds_tr(p + 128), lo1 = lds_tr(p + 1024), hi1 = lds_tr(p + 1152);
    stc16(rs, bo, OFF_Z2 + (panel * 2 * 54 + u0 + wid) * 1024, __builtin_bit_cast(u32x4, (bf16x8)__builtin_shufflevector(lo0, hi0, 0, 1, 2, 3, 4, 5, 6, 7)));
    stc16(rs, bo, OFF_Z2 + ((panel * 2 + 1) * 54 + u0 + wid) * 1024, __builtin_bit_cast(u32x4, (bf16x8)__builtin_shufflevector(lo1, hi1, 0, 1, 2, 3, 4, 5, 6, 7)));
  }
  SCHED();
  waitv<0>();
  win_tail(ws, wsp, smem, panel, cb, wid, lane TPASS);
  sp_ring<0, 5>(wsp, smem, panel, wid, lane);
  if (!EARLY) sp_prefetch(wr, wsp, panel, wid, lane);
}

DI void win_agent(char* __restrict__ ws, rsrc_t rs, const u16* __restrict__ wsp, char* smem, u32x4 (&wr)[16 - K5_R][2], const u16* __restrict__ bsp, u16 (&bb)[2] TARG) {
  const int tid = fresh_tid(), lane = tid & 63, wid = 7;
  const int xcd = blockIdx.x & 7, idx = blockIdx.x >> 3, panel = xcd * 4 + (idx >> 3), cb = idx & 7, u0 = unit0_of(cb);
  float* lnp = (float*)(smem + L4_LN); float* st2 = (float*)(smem + L4_ST2); float* fin = (float*)(smem + L4_FIN); float* cbl = (float*)(smem + L4_CB);
  int* cnt = (int*)(smem + L4_CNT);
  __builtin_amdgcn_s_setprio(2);
  if (lane == 0) *cnt = 0;
  poll_ge(ws, lane < 8 ? C_ATT + lane : C_X1 + panel, lane < 8 ? 32 : 8);
  BAR();
  {
    u32x2 t[16]; f32x4 cp[5][2];
#pragma unroll
    for (int i = 0; i < 16; ++i) t[i] = ldc8(rs, lane * 8 + (i & 7) * 512, OFF_ST1 + (panel * 16 + (i & 8)) * 512);
    const unsigned co = lane < 28 ? (u0 * 16 + lane * 4) * 4 : min((54 + u0) * 16 + min(lane - 28, 27) * 4, F_ - 4) * 4;
#pragma unroll
    for (int i = 0; i < 5; ++i) { cp[i][0] = ldc16f(rs, co, OFF_CBP + i * 16384); cp[i][1] = ldc16f(rs, co, OFF_CBP + i * 16384 + 8192); }
    float s = 0.f, q = 0.f;
#pragma unroll
    for (int i = 0; i < 16; ++i) { s += __uint_as_float(t[i][0]); q += __uint_as_float(t[i][1]); }
    const float mean = s * (1.f / 640.f), var = fmaxf(q * (1.f / 640.f) - mean * mean, 0.f);
    const float den = ln_den(var), ri = __builtin_amdgcn_rcpf(den);
    *(f32x4*)(lnp + lane * 4) = (f32x4){den, -rbf(mean), GC1 * ri, GC2 * ri * ri * ri};
#pragma unroll
    for (int v = 0; v < 2; ++v) *(f32x4*)(cbl + v * 256 + lane * 4) = ((cp[0][v] + cp[1][v]) + (cp[2][v] + cp[3][v])) + cp[4][v];
  }
  TS(47);
  BAR();
  BAR();
  __builtin_amdgcn_s_setprio(0);
  __builtin_amdgcn_s_sleep(8);
  load_bb(bsp, panel, wid, lane, bb);
  sp_prefetch(wr, wsp, panel, wid, lane);
#pragma nounroll
  for (int w = 0; w < 8; ++w) sp_ring<5, 7>(wsp, smem, panel, w, lane);
  __builtin_amdgcn_s_setprio(2);
#pragma nounroll
  for (unsigned spin = 0; spin < (1u << 22); ++spin) {
    if (__hip_atomic_load(cnt, __ATOMIC_RELAXED, __HIP_MEMORY_SCOPE_WORKGROUP) >= 7) break;
    __builtin_amdgcn_s_sleep(1);
  }
  asm volatile("" ::: "memory");
  float s, q;
  {
    f32x2 t[7];
#pragma unroll
    for (int i = 0; i < 7; ++i) t[i] = *(const f32x2*)(st2 + (i * 64 + lane) * 2);
    s = t[0][0]; q = t[0][1];
#pragma unroll
    for (int i = 1; i < 6; ++i) { s = opq(s + t[i][0]); q = opq(q + t[i][1]); }
    if (cb < 6) { s = opq(s + t[6][0]); q = opq(q + t[6][1]); }
  }
  GAS u64* xch = (GAS u64*)(ws + OFF_XCH) + panel * 512;
  unsigned qb = __float_as_uint(q); if (qb == ~0u) qb = 0x7fc00000u;
  __hip_atomic_store(xch + cb * 64 + lane, ((u64)qb << 32) | __float_as_uint(s), __ATOMIC_RELAXED, __HIP_MEMORY_SCOPE_AGENT);
  TS(19);
  u64 v[8];
#pragma nounroll
  for (unsigned spin = 0; spin < (1u << 19); ++spin) {
    bool ok = true;
#pragma unroll
    for (int i = 0; i < 8; ++i) { v[i] = __hip_atomic_load(xch + i * 64 + lane, __ATOMIC_RELAXED, __HIP_MEMORY_SCOPE_AGENT); ok = ok && ((unsigned)(v[i] >> 32) != ~0u); }
    if (__all(ok)) break;
    __builtin_amdgcn_s_sleep(1);
  }
  s = 0.f; q = 0.f;
#pragma unroll
  for (int i = 0; i < 8; ++i) { s = opq(s + __uint_as_float((unsigned)v[i])); q = opq(q + __uint_as_float((unsigned)(v[i] >> 32))); }
  const float mean = opq(s * (1.f / 864.f)), var = fmaxf(opq(q * (1.f / 864.f)) - mean * mean, 0.f);
  const float ri = __builtin_amdgcn_rcpf(ln_den(var));
  *(f32x2*)(fin + lane * 2) = (f32x2){ri, rbf(mean) * ri};
  __builtin_amdgcn_s_setprio(0);
  TS(20);
  BAR();
  __builtin_amdgcn_s_sleep(6);
  sp_ring<0, 5>(wsp, smem, panel, wid, lane);
  win_tail(ws, wsp, smem, panel, cb, wid, lane TPASS);
}

#define SP_L0 2
#define SP_G 3
#define SP_LB 4
constexpr int sp_q(int t) { const int w = 4 * SP_L0 + t / SP_G; return t + (w < 4 * SP_LB ? w : 4 * SP_LB); }
DI void phase_sp(char* __restrict__ ws, rsrc_t rs, const u16 (&bb)[2], char* smem, int wid, const u32x4 (&wr)[16 - K5_R][2], u32x2 (&res)[3], f32x4 (&gate)[3] TARG) {
  const int tid = fresh_tid(), lane = tid & 63;
  const int xcd = blockIdx.x & 7, idx = blockIdx.x >> 3, panel = xcd * 4 + (idx >> 3), cb = idx & 7;
  const int kh = wid >> 2, up = wid & 3, u0 = unit0_of(cb) + 2 * up, nu = cb < 6 ? 7 : 6, g = lane >> 4, r = lane & 15;
  const unsigned dead[2] = {2 * up < nu ? 0u : DEAD, 2 * up + 1 < nu ? 0u : DEAD};
  unsigned bvo[2] = {lane * 16 + dead[0], lane * 16 + 1024 + dead[1]};
  const char* al[2][2];
#pragma unroll
  for (int k = 0; k < 2; ++k)
#pragma unroll
    for (int j = 0; j < 2; ++j) al[k][j] = smem + kh * 8192 + (j ^ kh) * 4096 + r * 128 + ((4 * k + g) ^ (r >> 1)) * 16;
  constexpr int LB = SP_LB, PD = 3;
  bf16x8 bq[LB + 1][4], af[PD + 1][2]; f32x4 acc[2][4]; u32x4 wq[17];
#pragma unroll
  for (int i = 0; i < 2; ++i) {
    const float b0 = bf2f(bb[0]), b1 = bf2f(bb[1]);
    acc[i][0] = (f32x4){b0, b0, b0, b0}; acc[i][1] = (f32x4){b1, b1, b1, b1}; acc[i][2] = (f32x4){}; acc[i][3] = (f32x4){};
  }
  const unsigned bso = fresh_s(OFF_Z2 + (kh * 2 * 54 + u0) * 1024);
  PIN(bvo[0]); PIN(bvo[1]);
  if (wid == 7) poll_ge(ws, C_Z2 + (panel >> 3) * 8 + cb, 32);
  else if (kh && up < 3) waitv<2 * (16 - K5_R)>(); else waitv<0>();
  BAR();
  TS(23);
#define SP_LD(s, j) (bq[(s) % (LB + 1)][j] = BF8(ldc16(rs, bvo[(j) & 1], bso + (sp_stage(s) * 4 + ((j) >> 1)) * 55296)))
#define SP_RD(t, k) (af[(t) % (PD + 1)][k] = *(const bf16x8*)(al[k][((t) >> 1) & 1] + (((t) >> 2) % K5_R) * 16384 + ((t) & 1) * 2048))
#pragma unroll
  for (int q = 0; q < sp_q(0); ++q) SP_LD(q >> 2, q & 3);
#pragma unroll
  for (int t = 0; t < PD; ++t) { SP_RD(t, 0); SP_RD(t, 1); }
  SCHED();
  sfor<64>([&](auto T) __attribute__((always_inline)) {
    constexpr int t = T, nx = t + PD, s = t >> 2, m = t & 3, tl = sp_q(t), t2 = sp_q(t + 1) - 1;
    if constexpr (t == 0) TS(43);
    if constexpr (t == 1) TS(44);
    if constexpr (t == 4) TS(45);
    if constexpr (t == 8) TS(46);
    if constexpr (nx == 16 || nx == 28 || nx == 44 || nx == 56) { BAR(); TS(32 + (nx == 28) + 2 * (nx == 44) + 3 * (nx == 56)); }
    constexpr int c = nx >= 16 && nx < 24 ? nx - 16 : nx >= 28 && nx < 34 ? nx - 20 : nx >= 44 && nx < 48 ? nx - 30 : -1;
    if constexpr (nx < 64) SP_RD(nx, 0);
    acc[0][m] = MFMA16(bq[s % (LB + 1)][0], af[t % (PD + 1)][0], acc[0][m]);
    acc[0][m] = MFMA16(bq[s % (LB + 1)][2], af[t % (PD + 1)][1], acc[0][m]); PIN(acc[0][m]);
    if constexpr (tl < 64) SP_LD(tl >> 2, tl & 3); else if constexpr (tl - 64 < 17) wq[tl - 64] = proj_ld<true>(rs, tl - 64, wid, lane);
    SCHED();
    if constexpr (nx < 64) SP_RD(nx, 1);
    acc[1][m] = MFMA16(bq[s % (LB + 1)][1], af[t % (PD + 1)][0], acc[1][m]);
    acc[1][m] = MFMA16(bq[s % (LB + 1)][3], af[t % (PD + 1)][1], acc[1][m]); PIN(acc[1][m]);
    if constexpr (t2 > tl && t2 < 64) SP_LD(t2 >> 2, t2 & 3);
    if constexpr (c >= 0) *(u32x4*)(smem + ((c >> 1) % K5_R) * 16384 + wid * 2048 + (c & 1) * 1024 + lane * 16) = wr[c >> 1][c & 1];
    if constexpr (t == 63 && 4 * LB < 17) { _Pragma("unroll") for (int i = 4 * LB; i < 17; ++i) wq[i] = proj_ld<true>(rs, i, wid, lane); }
    if constexpr (t == 62) proj_small<true>(rs, nullptr, wid, lane, res, gate, gate);
    SCHED();
  });
  TS(24);
  f32x4* exw = (f32x4*)(smem + 32768 + wid * 4096) + lane;
  const f32x4* exr = (const f32x4*)(smem + 32768 + (wid ^ 4) * 4096) + lane;
  u32x4 z[2];
#pragma unroll
  for (int i = 0; i < 2; ++i) z[i] = *(const u32x4*)(smem + L4_Z1 + (2 * up + i) * 2048 + kh * 1024 + lane * 16);
#pragma unroll
  for (int i = 0; i < 2; ++i)
#pragma unroll
    for (int j = 0; j < 2; ++j) exw[(i * 2 + j) * 64] = acc[i][2 + j];
  f32x4 zf[2][2];
#pragma unroll
  for (int i = 0; i < 2; ++i)
#pragma unroll
    for (int j = 0; j < 2; ++j) {
      zf[i][j] = (f32x4){bflo(z[i][2 * j]), bfhi(z[i][2 * j]), bflo(z[i][2 * j + 1]), bfhi(z[i][2 * j + 1])};
      acc[i][j] = mul4s(acc[i][j], zf[i][j]); PIN(zf[i][j]); PIN(acc[i][j]);
    }
  BAR();
  f32x4 oth[2][2];
#pragma unroll
  for (int i = 0; i < 2; ++i)
#pragma unroll
    for (int j = 0; j < 2; ++j) oth[i][j] = exr[(i * 2 + j) * 64];
#pragma unroll
  for (int i = 0; i < 2; ++i)
#pragma unroll
    for (int j = 0; j < 2; ++j) {
      const f32x4 v = acc[i][j] + zf[i][j] * oth[i][j];
      stc8(rs, ((g >> 1) * 16 + r) * 16 + (g & 1) * 8 + dead[i], OFF_U + (panel * 4 + 2 * kh + j) * 27648 + (u0 + i) * 512, (u32x2){pack2(v[0], v[1]), pack2(v[2], v[3])});
    }
  TS(25);
  drain();
  BAR();
  TS(26);
  if (wid == 7) arrive(ws, C_U + panel, lane);
#pragma unroll
  for (int i = 0; i < 17; ++i) proj_park(smem, i, tid, wq[i]);
}

extern "C" __global__ __launch_bounds__(512, 2) OCC2 void k_mega(char* __restrict__ ws, const u16* __restrict__ x, const u16* __restrict__ win,
                                                     const u16* __restrict__ wsp, const u16* __restrict__ bsp, const u16* __restrict__ wout, u16* __restrict__ out) {
  extern __shared__ __attribute__((aligned(16))) char smem[];
  const int wid = __builtin_amdgcn_readfirstlane(threadIdx.x >> 6);
  const rsrc_t rs = __builtin_amdgcn_make_buffer_rsrc(ws, 0, WS_USED, 0x00020000);
  TDECL; TS(0);
  u32x2 res[3]; f32x4 gate[3], cbv[3];
  phase_qkv(ws, rs, smem, wid TPASS);
  phase_attn(ws, rs, (const char*)wsp, win, wout, x, smem, wid, res, gate, cbv TPASS);
  u32x4 wr[16 - K5_R][2];
  auto nohook = []() __attribute__((always_inline)) {};
  u16 bb[2];
  if (wid >= 4) {
    proj_body<false, 1>(ws, rs, x, out, smem, wid, res, gate, cbv, nohook TPASS);
    if (wid == 7) win_agent(ws, rs, wsp, smem, wr, bsp, bb TPASS);
    else { bf16x8 b2[20]; win_prefetch(rs, wid, b2); win_body<false>(ws, rs, wsp, smem, wid, b2, wr, bsp, bb TPASS); }
#pragma unroll
    for (int i = 0; i < 3; ++i) { gate[i] = (f32x4){}; cbv[i] = (f32x4){}; }
  }
  const int w2 = fresh_s(wid);
  if (w2 < 4) {
    bf16x8 b2[20];
    proj_body<false, 0>(ws, rs, x, out, smem, w2, res, gate, cbv, [&]() __attribute__((always_inline)) { win_prefetch(rs, w2, b2); } TPASS);
    win_body<true>(ws, rs, wsp, smem, w2, b2, wr, bsp, bb TPASS);
  }
  phase_sp(ws, rs, bb, smem, wid, wr, res, gate TPASS);
  if (wid < 4) proj_body<true, 0>(ws, rs, x, out, smem, wid, res, gate, cbv, nohook TPASS);
  else proj_body<true, 1>(ws, rs, x, out, smem, wid, res, gate, cbv, nohook TPASS);
  TS(29);
  TDUMP();
  asm volatile("k_mega_code_end:");
}

extern "C" void kernel_launch(void* const* d_in, const int* in_sizes, int n_in, void* d_out, int out_size, void* d_ws, size_t ws_size,
                              hipStream_t stream) {
  const u16* x = (const u16*)d_in[0];
  const u16* wqkv = (const u16*)d_in[1];
  const u16* wo = (const u16*)d_in[2];
  const u16* win = (const u16*)d_in[4];
  const u16* wsp = (const u16*)d_in[5];
  const u16* bsp = (const u16*)d_in[6];
  const u16* wout = (const u16*)d_in[7];
  const u16* cvec = (const u16*)d_in[8];
  const u16* wmod = (const u16*)d_in[9];
  char* ws = (char*)d_ws;
  (void)hipFuncSetAttribute((const void*)k_mega, hipFuncAttributeMaxDynamicSharedMemorySize, 163840);
  k0_prep<<<K0_GRID, 256, 0, stream>>>(x, wqkv, wo, cvec, wmod, ws);
  k_mega<<<256, 512, 163840, stream>>>(ws, x, win, wsp, bsp, wout, (u16*)d_out);
}
```

```cpp
#include <hip/hip_runtime.h>
#include <hip/hip_bf16.h>
#include <stdint.h>
#include <utility>

typedef __attribute__((ext_vector_type(8))) short bf16x8;
typedef __attribute__((ext_vector_type(4))) short bf16x4;
typedef __attribute__((ext_vector_type(2))) float f32x2;
typedef __attribute__((ext_vector_type(4))) float f32x4;
typedef __attribute__((ext_vector_type(16))) float f32x16;
typedef __attribute__((ext_vector_type(2))) unsigned u32x2;
typedef __attribute__((ext_vector_type(4))) unsigned u32x4;
typedef __attribute__((ext_vector_type(2))) __bf16 bf2_t;
typedef __attribute__((ext_vector_type(2))) _Float16 h2_t;
typedef unsigned short u16;
typedef unsigned long long u64;
#define LAS __attribute__((address_space(3)))
#define GAS __attribute__((address_space(1)))
#define DI __device__ __forceinline__

constexpr int D_ = 640, NQKV = 1920, F_ = 1728;

constexpr unsigned OFF_CNT = 0;
constexpr unsigned OFF_MOD = 65536;
constexpr unsigned OFF_MODP = OFF_MOD + 16384;
constexpr unsigned OFF_CBP = OFF_MODP + 61440;
constexpr unsigned OFF_XCH = OFF_CBP + 5 * 16384;
constexpr unsigned OFF_ST1 = OFF_XCH + 131072;
constexpr unsigned OFF_XHAT = OFF_ST1 + 262144;
constexpr unsigned OFF_WQKV = OFF_XHAT + 2621440;
constexpr unsigned OFF_WO = OFF_WQKV + 2457600;
constexpr unsigned OFF_WIN = OFF_WO + 819200;
constexpr unsigned OFF_WOUT = OFF_WIN + 2211840;
constexpr unsigned OFF_Q = OFF_WOUT + 1105920;
constexpr unsigned OFF_KV = OFF_Q + 32 * 81920;
constexpr unsigned OFF_O = OFF_KV + 32 * 163840;
constexpr unsigned OFF_X1S = OFF_O + 2621440;
constexpr unsigned OFF_Z2 = OFF_X1S + 2621440;
constexpr unsigned OFF_U = OFF_Z2 + 3538944;
constexpr unsigned WS_USED = OFF_U + 3538944;
constexpr int C_QKV = 0, C_MOD = 15  , C_ATT = 128, C_REL = 136, C_X1 = 144, C_U = 176, C_Z2 = 208  ;

DI unsigned pack2(float lo, float hi) { return __builtin_bit_cast(unsigned, __builtin_convertvector((f32x2){lo, hi}, bf2_t)); }
DI float opq(float x) { asm("" : "+v"(x)); return x; }
DI f32x4 fma4s(f32x4 a, f32x4 b, f32x4 c) { return (f32x4){opq(__builtin_fmaf(a[0], b[0], c[0])), opq(__builtin_fmaf(a[1], b[1], c[1])), opq(__builtin_fmaf(a[2], b[2], c[2])), opq(__builtin_fmaf(a[3], b[3], c[3]))}; }
DI f32x4 mul4s(f32x4 a, f32x4 b) { return (f32x4){opq(a[0] * b[0]), opq(a[1] * b[1]), opq(a[2] * b[2]), opq(a[3] * b[3])}; }
DI unsigned pack2h(float lo, float hi) { return __builtin_bit_cast(unsigned, __builtin_convertvector((f32x2){lo, hi}, h2_t)); }
DI float bflo(unsigned u) { return __uint_as_float(u << 16); }
DI float bfhi(unsigned u) { return __uint_as_float(u & 0xffff0000u); }
DI float rbf(float x) { return bflo(pack2(x, x)); }
DI float bf2f(u16 b) { return __uint_as_float((unsigned)b << 16); }
#define MFMA32(a, b, c) __builtin_amdgcn_mfma_f32_32x32x16_bf16((a), (b), (c), 0, 0, 0)
#define MFMA32K8(a, b, c) __builtin_amdgcn_mfma_f32_32x32x8bf16_1k((a), (b), (c), 0, 0, 0)
#define MFMA16(a, b, c) __builtin_amdgcn_mfma_f32_16x16x32_bf16((a), (b), (c), 0, 0, 0)
#define BAR() asm volatile("s_waitcnt lgkmcnt(0)\n\ts_barrier" ::: "memory")
#define SCHED() __builtin_amdgcn_sched_barrier(0)
#define PIN(x) asm volatile("" : "+v"(x))
#define OCC2 __attribute__((amdgpu_waves_per_eu(2, 2)))
#define BF8(x) __builtin_bit_cast(bf16x8, x)
#define BF4(x) __builtin_bit_cast(bf16x4, x)
template <int N> DI void waitv() { asm volatile("s_waitcnt vmcnt(%0)" ::"i"(N) : "memory"); }
DI void drain() { waitv<0>(); __builtin_amdgcn_s_waitcnt(0x0F70); }
template <class F, int... I> DI void sfor_impl(F&& f, std::integer_sequence<int, I...>) { (f(std::integral_constant<int, I>{}), ...); }
template <int N, class F> DI void sfor(F&& f) { sfor_impl(f, std::make_integer_sequence<int, N>{}); }
typedef short v4i16_t __attribute__((ext_vector_type(4)));
DI bf16x4 lds_tr(const char* p) { return __builtin_bit_cast(bf16x4, __builtin_amdgcn_ds_read_tr16_b64_v4i16((LAS v4i16_t*)p)); }
DI void glds16(const void* g, unsigned lds_base) {
  unsigned sv;
  asm volatile("s_mov_b32 %0, m0\n\ts_mov_b32 m0, %2\n\ts_nop 0\n\tglobal_load_lds_dwordx4 %1, off\n\ts_mov_b32 m0, %0" : "=&s"(sv) : "v"(g), "s"(lds_base) : "memory");
}
DI void glds16c(const void* g, unsigned lds_base) {
  unsigned sv;
  asm volatile("s_mov_b32 %0, m0\n\ts_mov_b32 m0, %2\n\ts_nop 0\n\tglobal_load_lds_dwordx4 %1, off sc1\n\ts_mov_b32 m0, %0" : "=&s"(sv) : "v"(g), "s"(lds_base) : "memory");
}
DI void glds4c(const void* g, unsigned lds_base) {
  unsigned sv;
  asm volatile("s_mov_b32 %0, m0\n\ts_mov_b32 m0, %2\n\ts_nop 0\n\tglobal_load_lds_dword %1, off sc1\n\ts_mov_b32 m0, %0" : "=&s"(sv) : "v"(g), "s"(lds_base) : "memory");
}
typedef __amdgpu_buffer_rsrc_t rsrc_t;
DI u32x4 ldc16(rsrc_t rs, unsigned voff, unsigned soff) { return __builtin_amdgcn_raw_buffer_load_b128(rs, voff, soff, 16); }
DI u32x2 ldc8(rsrc_t rs, unsigned voff, unsigned soff) { return __builtin_amdgcn_raw_buffer_load_b64(rs, voff, soff, 16); }
DI unsigned ldc4(rsrc_t rs, unsigned voff, unsigned soff) { return __builtin_amdgcn_raw_buffer_load_b32(rs, voff, soff, 16); }
DI void stc16(rsrc_t rs, unsigned voff, unsigned soff, u32x4 v) { __builtin_amdgcn_raw_buffer_store_b128(v, rs, voff, soff, 16); asm volatile("s_nop 1" ::"v"(v) : "memory"); }
DI void stc8(rsrc_t rs, unsigned voff, unsigned soff, u32x2 v) { __builtin_amdgcn_raw_buffer_store_b64(v, rs, voff, soff, 16); }
DI void stc4(rsrc_t rs, unsigned voff, unsigned soff, unsigned v) { __builtin_amdgcn_raw_buffer_store_b32(v, rs, voff, soff, 16); }
DI f32x4 ldc16f(rsrc_t rs, unsigned voff, unsigned soff) { return __builtin_bit_cast(f32x4, ldc16(rs, voff, soff)); }
DI void arrive(char* ws, int c, int lane) { if (lane == 0) __hip_atomic_fetch_add((GAS unsigned*)(ws + OFF_CNT + c * 256), 1u, __ATOMIC_RELAXED, __HIP_MEMORY_SCOPE_AGENT); }
DI void poll_ge(char* ws, int c, unsigned thr) {
  const GAS unsigned* p = (const GAS unsigned*)(ws + OFF_CNT + c * 256);
#pragma nounroll
  for (unsigned spin = 0; spin < (1u << 19); ++spin) {
    if (__all(__hip_atomic_load(p, __ATOMIC_RELAXED, __HIP_MEMORY_SCOPE_AGENT) >= thr)) break;
    __builtin_amdgcn_s_sleep(1);
  }
  asm volatile("" ::: "memory");
}
DI int fresh_s(int v) { asm volatile("" : "+s"(v)); return v; }
DI int fresh_tid() { int t = threadIdx.x; asm volatile("" : "+v"(t)); return t; }
DI void l2touch(const void* g, unsigned lds_scratch) {
  unsigned sv;
  asm volatile("s_mov_b32 %0, m0\n\ts_mov_b32 m0, %2\n\ts_nop 0\n\tglobal_load_lds_dword %1, off\n\ts_mov_b32 m0, %0" : "=&s"(sv) : "v"(g), "s"(lds_scratch) : "memory");
}
#define LDNT(p) __builtin_nontemporal_load(p)
DI float silu_bf(u16 c) { const float f = __uint_as_float((unsigned)c << 16); const float v = f / (1.f + __expf(-f)); return __uint_as_float(__builtin_bit_cast(unsigned, __builtin_convertvector((f32x2){v, v}, bf2_t)) << 16); }
DI unsigned lds_addr(const void* p) { return (unsigned)(uintptr_t)(const LAS void*)p; }
DI float xhalf(float v) {
  auto rr = __builtin_amdgcn_permlane32_swap(__float_as_uint(v), __float_as_uint(v), false, false);
  return __uint_as_float((threadIdx.x & 32) ? rr[0] : rr[1]);
}
DI float sum_g(float v) {
  auto a = __builtin_amdgcn_permlane16_swap(__float_as_uint(v), __float_as_uint(v), false, false);
  v = __uint_as_float(a[0]) + __uint_as_float(a[1]);
  auto b = __builtin_amdgcn_permlane32_swap(__float_as_uint(v), __float_as_uint(v), false, false);
  return __uint_as_float(b[0]) + __uint_as_float(b[1]);
}
template <int CTRL> DI float dpp_add(float v) { return v + __uint_as_float(__builtin_amdgcn_update_dpp(0u, __float_as_uint(v), CTRL, 0xf, 0xf, true)); }
DI float sum16(float v) {
  v = dpp_add<0x128>(v); v = dpp_add<0x124>(v); v = dpp_add<0x122>(v); return dpp_add<0x121>(v);
}
DI u32x4 modulate8(u32x4 v, f32x4 sc0, f32x4 sc1, f32x4 sh0, f32x4 sh1) {
  unsigned o0, o1, o2, o3; float t0, t1, t2, t3;
  asm volatile(
      "v_fma_mix_f32 %0, %8, %12, %20 op_sel_hi:[1,0,0]\n\tv_fma_mix_f32 %4, %8, %13, %21 op_sel:[1,0,0] op_sel_hi:[1,0,0]\n\t"
      "v_fma_mix_f32 %1, %9, %14, %22 op_sel_hi:[1,0,0]\n\tv_fma_mix_f32 %5, %9, %15, %23 op_sel:[1,0,0] op_sel_hi:[1,0,0]\n\t"
      "v_fma_mix_f32 %2, %10, %16, %24 op_sel_hi:[1,0,0]\n\tv_fma_mix_f32 %6, %10, %17, %25 op_sel:[1,0,0] op_sel_hi:[1,0,0]\n\t"
      "v_fma_mix_f32 %3, %11, %18, %26 op_sel_hi:[1,0,0]\n\tv_fma_mix_f32 %7, %11, %19, %27 op_sel:[1,0,0] op_sel_hi:[1,0,0]\n\t"
      "v_cvt_pk_bf16_f32 %0, %0, %4\n\tv_cvt_pk_bf16_f32 %1, %1, %5\n\tv_cvt_pk_bf16_f32 %2, %2, %6\n\tv_cvt_pk_bf16_f32 %3, %3, %7"
      : "=&v"(o0), "=&v"(o1), "=&v"(o2), "=&v"(o3), "=&v"(t0), "=&v"(t1), "=&v"(t2), "=&v"(t3)
      : "v"(v[0]), "v"(v[1]), "v"(v[2]), "v"(v[3]), "v"(sc0[0]), "v"(sc0[1]), "v"(sc0[2]), "v"(sc0[3]), "v"(sc1[0]), "v"(sc1[1]), "v"(sc1[2]), "v"(sc1[3]),
        "v"(sh0[0]), "v"(sh0[1]), "v"(sh0[2]), "v"(sh0[3]), "v"(sh1[0]), "v"(sh1[1]), "v"(sh1[2]), "v"(sh1[3]));
  return (u32x4){o0, o1, o2, o3};
}
DI float ln_den(float var) { return rbf(__builtin_amdgcn_sqrtf(rbf(rbf(var) + 1e-5f))); }
DI int blk_panel() { return ((blockIdx.x & 7) >> 1) * 8 + (blockIdx.x >> 5); }
DI int blk_cb() { return (blockIdx.x & 1) * 4 + ((blockIdx.x >> 3) & 3); }
DI int unit0_of(int j) { return j < 6 ? 7 * j : 42 + 6 * (j - 6); }

DI void K0ST(char* p, u32x4 v) { *(u32x4*)p = v; }
constexpr int K0_MOD = 240, K0_LN = 128, K0_RL = 240, K0_WO = 50, K0_INIT = 12, K0_WARM = 24;
constexpr int K0_GRID = K0_WARM + K0_MOD + K0_LN + K0_RL + K0_WO + K0_INIT;

__global__ __launch_bounds__(256) void k0_prep(const u16* __restrict__ x, const u16* __restrict__ wqkv, const u16* __restrict__ wo, const u16* __restrict__ cvec,
                                               const u16* __restrict__ wmod, char* __restrict__ ws) {
  __shared__ __attribute__((aligned(16))) char smem[4 * 4608];
  const int tid = threadIdx.x, lane = tid & 63, wid = __builtin_amdgcn_readfirstlane(tid >> 6);
  int b = blockIdx.x;
  if (b < K0_WARM) {
    u64 pc; unsigned len;
    asm volatile("s_getpc_b64 s[30:31]\n\ts_add_u32 s30, s30, k_mega@rel32@lo+4\n\ts_addc_u32 s31, s31, k_mega@rel32@hi+12\n\ts_mov_b64 %0, s[30:31]\n\t"
                 "s_mov_b32 %1, k_mega_code_end-k_mega" : "=s"(pc), "=s"(len) :: "scc", "s30", "s31");
    l2touch((const char*)pc + min(((b >> 3) * 256 + tid) * 128u, len - 4u), lds_addr(smem) + wid * 256);
    return;
  }
  b -= K0_WARM;
  if (b < K0_MOD) {
    const int cg = b >> 2, ks = b & 3, c = tid & 7, row0 = ks * 160 + (tid >> 3), col0 = cg * 64;
    u32x4 w[5]; u16 cv[5];
#pragma unroll
    for (int i = 0; i < 5; ++i) { w[i] = LDNT((const u32x4*)(wmod + (size_t)(row0 + 32 * i) * 3840 + col0 + c * 8)); cv[i] = cvec[row0 + 32 * i]; }
    float acc[8] = {0, 0, 0, 0, 0, 0, 0, 0};
#pragma unroll
    for (int i = 0; i < 5; ++i) {
      const float s = silu_bf(cv[i]);
#pragma unroll
      for (int e = 0; e < 4; ++e) { acc[2 * e] += s * bflo(w[i][e]); acc[2 * e + 1] += s * bfhi(w[i][e]); }
    }
#pragma unroll
    for (int e = 0; e < 8; ++e) acc[e] = sum_g(dpp_add<0x128>(acc[e]));
    float* red = (float*)smem;
    if (lane < 8) { *(f32x4*)(red + wid * 64 + lane * 8) = (f32x4){acc[0], acc[1], acc[2], acc[3]}; *(f32x4*)(red + wid * 64 + lane * 8 + 4) = (f32x4){acc[4], acc[5], acc[6], acc[7]}; }
    __syncthreads();
    if (tid < 64) ((float*)(ws + OFF_MODP))[ks * 3840 + cg * 64 + tid] = (red[tid] + red[64 + tid]) + (red[128 + tid] + red[192 + tid]);
    return;
  }
  b -= K0_MOD;
  if (b < K0_LN) {
    b = (b & 7) * 16 + (b >> 3);
    const int row = b * 16 + wid * 4 + (lane >> 4), c = lane & 15;
    const u16* xr = x + (size_t)row * D_ + c * 8;
    u32x4 v[5];
#pragma unroll
    for (int i = 0; i < 5; ++i) v[i] = *(const u32x4*)(xr + i * 128);
    float s = 0.f, q = 0.f;
#pragma unroll
    for (int i = 0; i < 5; ++i)
#pragma unroll
      for (int e = 0; e < 4; ++e) s += bflo(v[i][e]) + bfhi(v[i][e]);
    const float mean = sum16(s) * (1.f / 640.f);
#pragma unroll
    for (int i = 0; i < 5; ++i)
#pragma unroll
      for (int e = 0; e < 4; ++e) { const float a = bflo(v[i][e]) - mean, d = bfhi(v[i][e]) - mean; q += a * a + d * d; }
    const float var = sum16(q) * (1.f / 640.f);
    const float rinv = __builtin_amdgcn_rcpf(ln_den(var)), mb = rbf(mean);
    char* dst = ws + OFF_XHAT + (size_t)row * 1280 + c * 16;
#pragma unroll
    for (int i = 0; i < 5; ++i) {
      u32x4 o;
#pragma unroll
      for (int e = 0; e < 4; ++e) o[e] = pack2h((bflo(v[i][e]) - mb) * rinv, (bfhi(v[i][e]) - mb) * rinv);
      K0ST(dst + i * 256, o);
    }
    return;
  }
  b -= K0_LN;
  if (b < K0_RL) {
    const int kt = b / 12, G = b % 12, sec = G >> 2, gq = G & 3;
    u32x4 v[3];
#pragma unroll
    for (int i = 0; i < 3; ++i) { const int ci = min(i * 256 + tid, 639); v[i] = LDNT((const u32x4*)(wqkv + (size_t)(kt * 32 + ci / 20) * NQKV + G * 160 + (ci % 20) * 8)); }
#pragma unroll
    for (int i = 0; i < 3; ++i) { const int ci = min(i * 256 + tid, 639); *(u32x4*)(smem + (ci / 20) * 336 + (ci % 20) * 16) = v[i]; }
    __syncthreads();
    const int g = lane >> 4, q = (lane & 15) >> 2, p = lane & 3;
    for (int f = wid; f < 10; f += 4) {
      const int t = f >> 1, x2 = f & 1;
      const int col = t == 4 ? 20 * (4 * x2 + p) + 16 : sec == 2 ? 20 * f + 4 * p : 20 * (2 * t + (p >> 1)) + 8 * (p & 1) + 4 * x2;
      const int T = sec * 20 + (t < 4 ? 4 * gq + t : 16 + gq);
      const char* pp = smem + (8 * g + q) * 336 + col * 2;
      const bf16x4 lo = lds_tr(pp), hi = lds_tr(pp + 4 * 336);
      K0ST(ws + OFF_WQKV + (((size_t)T * 20 + kt) * 2 + x2) * 1024 + lane * 16, __builtin_bit_cast(u32x4, (bf16x8)__builtin_shufflevector(lo, hi, 0, 1, 2, 3, 4, 5, 6, 7)));
    }
    return;
  }
  b -= K0_RL;
  if (b < K0_WO) {
    const int job = b * 4 + wid, kt = job / 10, ng = job % 10, j8 = lane >> 3;
    char* t = smem + wid * 4608;
    u32x4 v[4];
#pragma unroll
    for (int i = 0; i < 4; ++i) {
      const int c = 4 * kt + i, row = c < 64 ? 20 * (c >> 1) + 4 * (c & 1) + (j8 & 3) + 8 * (j8 >> 2) : 20 * (2 * (c - 64) + (j8 >> 2)) + 16 + (j8 & 3);
      v[i] = LDNT((const u32x4*)(wo + (size_t)row * D_ + ng * 64 + (lane & 7) * 8));
    }
#pragma unroll
    for (int i = 0; i < 4; ++i) *(u32x4*)(t + (i * 8 + j8) * 144 + (lane & 7) * 16) = v[i];
    const char* pp = t + (8 * (lane >> 4) + ((lane & 15) >> 2)) * 144 + (lane & 3) * 8;
#pragma unroll
    for (int f = 0; f < 4; ++f) {
      const bf16x4 lo = lds_tr(pp + f * 32), hi = lds_tr(pp + 4 * 144 + f * 32);
      K0ST(ws + OFF_WO + ((size_t)(ng * 4 + f) * 20 + kt) * 1024 + lane * 16, __builtin_bit_cast(u32x4, (bf16x8)__builtin_shufflevector(lo, hi, 0, 1, 2, 3, 4, 5, 6, 7)));
    }
    return;
  }
  b -= K0_WO;
  {
    u32x4* p = (u32x4*)(ws + (b < 8 ? OFF_XCH + b * 16384 : OFF_CNT + (b - 8) * 16384)) + tid;
    const unsigned f = b < 8 ? ~0u : 0u;
#pragma unroll
    for (int i = 0; i < 4; ++i) p[i * 256] = (u32x4){f, f, f, f};
  }
}

constexpr float QSCALE = 0.22360679774997896f * 1.4426950408889634f;
constexpr int K1_W = 3, K1_WB = 5, K1_WB0 = 1;

template <bool ISV, class FIN>
DI void k1_mma(const char* __restrict__ bsrc, const char* smem, int lane, FIN&& fin) {
  f32x4 acc[8][2];
  constexpr int PD = 4;
  bf16x8 bfr[K1_WB + 1][4], af[PD + 1][2];
  const char* lds[2];
#pragma unroll
  for (int k = 0; k < 2; ++k) lds[k] = smem + k * 8192 + (lane ^ (4 * k + (lane >> 4))) * 16;
#pragma unroll
  for (int i = 0; i < 4 * K1_WB0; ++i) bfr[i >> 2][i & 3] = *(const bf16x8*)(bsrc + i * 1024);
#pragma unroll
  for (int m = 0; m < 8; ++m) { acc[m][0] = (f32x4){}; acc[m][1] = (f32x4){}; }
  BAR();
#pragma unroll
  for (int i = 4 * K1_WB0; i < 4 * K1_WB; ++i) bfr[i >> 2][i & 3] = *(const bf16x8*)(bsrc + i * 1024);
  BAR();
  __builtin_amdgcn_s_setprio(1);
#define K1_RD(t, k) (af[(t) % (PD + 1)][k] = *(const bf16x8*)(lds[k] + (((t) >> 3) % 3) * 16384 + ((t) & 7) * 1024))
#define K1_MM(c, b, a) (c = ISV ? MFMA16(a, b, c) : MFMA16(b, a, c))
#pragma unroll
  for (int t = 0; t < PD; ++t) { K1_RD(t, 0); K1_RD(t, 1); }
  SCHED();
  sfor<80>([&](auto T) __attribute__((always_inline)) {
    constexpr int t = T, s = t >> 3, m = t & 7, nx = t + PD;
    if constexpr (nx < 80 && (nx & 7) == 0) asm volatile("s_barrier" ::: "memory");
    if constexpr (nx < 80) K1_RD(nx, 0);
    K1_MM(acc[m][0], bfr[s % (K1_WB + 1)][0], af[t % (PD + 1)][0]); K1_MM(acc[m][0], bfr[s % (K1_WB + 1)][2], af[t % (PD + 1)][1]); PIN(acc[m][0]);
    SCHED();
    if constexpr (nx < 80) K1_RD(nx, 1);
    if constexpr (s + K1_WB < 10 && (m & 1)) bfr[(s + K1_WB) % (K1_WB + 1)][m >> 1] = *(const bf16x8*)(bsrc + ((s + K1_WB) * 4 + (m >> 1)) * 1024);
    K1_MM(acc[m][1], bfr[s % (K1_WB + 1)][1], af[t % (PD + 1)][0]); K1_MM(acc[m][1], bfr[s % (K1_WB + 1)][3], af[t % (PD + 1)][1]); PIN(acc[m][1]);
    SCHED();
    if constexpr (t >= 74) { fin(t - 74, acc[t - 74]); SCHED(); }
  });
  fin(6, acc[6]); fin(7, acc[7]);
  __builtin_amdgcn_s_setprio(0);
}


DI void phase_qkv(char* __restrict__ ws, rsrc_t rs, char* smem, int wid) {
  const int tid = fresh_tid(), lane = tid & 63;
  const int xcd = blockIdx.x & 7, idx = blockIdx.x >> 3;
  int ctr;
  if ((xcd & 1) && idx >= 28) {
    ctr = C_MOD;
    if (tid < 160) {
      const int col = 1280 + ((xcd >> 1) * 4 + idx - 28) * 160 + tid;
      const float* pp = (const float*)(ws + OFF_MODP) + col;
      float v = rbf((pp[0] + pp[3840]) + (pp[7680] + pp[11520]));
      if (col / 640 == 4) v = rbf(1.f + v);
      stc4(rs, col * 4, OFF_MOD, __float_as_uint(v));
    }
  } else {
    const int mt = (xcd >> 1) * 4 + (idx & 3), nt = (xcd & 1) * 8 + (idx >> 2);
    ctr = C_QKV + nt;
    if (wid < 4) {
      float* cst = (float*)(smem + 49152);
      const int c = lane & 7, rr = lane >> 3;
      const char* src = ws + OFF_XHAT + (size_t)(mt * 128 + wid * 32 + rr) * 1280 + c * 16;
      char* wdst = smem + (c >> 2) * 8192 + wid * 2048 + (c & 3) * 256 + (rr ^ c) * 16;
      u32x4 a[10][4];
      f32x4 c0[4], c1[4];
#pragma unroll
      for (int i = 0; i < 4; ++i) { c0[i] = *(const f32x4*)(ws + OFF_MODP + i * 15360 + tid * 16); if (wid == 0) c1[i] = *(const f32x4*)(ws + OFF_MODP + i * 15360 + 4096 + tid * 16); }
#pragma unroll
      for (int s = 0; s < K1_W; ++s)
#pragma unroll
        for (int m = 0; m < 4; ++m) a[s][m] = *(const u32x4*)(src + m * 10240 + s * 128);
      {
        f32x4 v = (c0[0] + c0[1]) + (c0[2] + c0[3]); const float one = tid >= 160 ? 1.f : 0.f;
        *(f32x4*)(cst + tid * 4) = (f32x4){rbf(one + rbf(v[0])), rbf(one + rbf(v[1])), rbf(one + rbf(v[2])), rbf(one + rbf(v[3]))};
        if (wid == 0) { v = (c1[0] + c1[1]) + (c1[2] + c1[3]); *(f32x4*)(cst + 1024 + tid * 4) = (f32x4){rbf(1.f + rbf(v[0])), rbf(1.f + rbf(v[1])), rbf(1.f + rbf(v[2])), rbf(1.f + rbf(v[3]))}; }
      }
      BAR();
      const float* cp = cst + c * 8;
      f32x4 kc[10][4];
#define K1_KC(s) do { kc[s][0] = *(const f32x4*)(cp + (s) * 64); kc[s][1] = *(const f32x4*)(cp + (s) * 64 + 4); kc[s][2] = *(const f32x4*)(cp + (s) * 64 + 640); kc[s][3] = *(const f32x4*)(cp + (s) * 64 + 644); } while (0)
#pragma unroll
      for (int s = 0; s < 6; ++s) K1_KC(s);
      SCHED();
      u32x4 o[2][4];
#define K1_XF(s, m) (o[(s) & 1][m] = modulate8(a[s][m], kc[s][2], kc[s][3], kc[s][0], kc[s][1]))
#define K1_WR(s, m) (*(u32x4*)(wdst + ((s) % 3) * 16384 + ((m) >> 1) * 1024 + ((m) & 1) * 128) = o[(s) & 1][m])
#pragma unroll
      for (int m = 0; m < 4; ++m) { K1_XF(0, m); K1_WR(0, m); }
      BAR();
#pragma unroll
      for (int m = 0; m < 4; ++m) a[K1_W][m] = *(const u32x4*)(src + m * 10240 + K1_W * 128);
#pragma unroll
      for (int m = 0; m < 4; ++m) K1_XF(1, m);
      SCHED();
#pragma unroll
      for (int s = 1; s < 10; ++s) {
#pragma unroll
        for (int m = 0; m < 4; ++m) {
          if (m == 0) K1_WR(s, 0);
          if (m < 3) K1_WR(s, m + 1);
          if (s + K1_W < 10) a[s + K1_W][m] = *(const u32x4*)(src + m * 10240 + (s + K1_W) * 128);
          if (s < 5) kc[s + 5][m] = *(const f32x4*)(cp + (s + 5) * 64 + (m & 1) * 4 + (m >> 1) * 640);
          SCHED();
          if (s < 9) K1_XF(s + 1, m);
          SCHED();
        }
        asm volatile("" :: "v"(o[s & 1][0]), "v"(o[s & 1][1]), "v"(o[s & 1][2]), "v"(o[s & 1][3]));
        BAR();
      }
    } else {
      const int cw = wid - 4;
      const char* bsrc = ws + OFF_WQKV + (size_t)(nt * 4 + cw) * 40 * 1024 + lane * 16;
      const int r = lane & 15, g = lane >> 4;
      if (nt >= 10) {
        const unsigned so = OFF_KV + (mt >> 1) * 20480 + 10240 + (mt & 1) * 5120;
        unsigned vo[2];
#pragma unroll
        for (int x = 0; x < 2; ++x)
          vo[x] = (nt < 14 ? ((nt - 10) * 8 + cw * 2 + x) * 163840u + ((g & 1) * 16 + r) * 16 : (cw * 8 + x * 4 + (r >> 2)) * 163840u + 512 + ((g & 1) * 4 + (r & 3)) * 16) + (g >> 1) * 8;
        k1_mma<true>(bsrc, smem, lane, [&](int m, const f32x4 (&a)[2]) __attribute__((always_inline)) {
#pragma unroll
          for (int x = 0; x < 2; ++x) stc8(rs, vo[x] + (m & 3) * 640, so + (m >> 2) * 2560, (u32x2){pack2(a[x][0], a[x][1]), pack2(a[x][2], a[x][3])});
        });
      } else {
        const bool isQ = nt < 5; const float sc = isQ ? QSCALE : 1.f;
        const unsigned so = isQ ? OFF_Q + mt * 5120 : OFF_KV + (mt >> 1) * 20480 + (mt & 1) * 5120, hs = isQ ? 81920u : 163840u;
        const int n5 = isQ ? nt : nt - 5;
        const bool wide = n5 < 4;
        const unsigned vo = wide ? (2 * (n5 * 4 + cw) + (g >> 1)) * hs + (g & 1) * 512 + r * 16 : (8 * cw + g) * hs + 1024 + r * 8;
        const f32x4 sc4 = (f32x4){sc, sc, sc, sc};
        k1_mma<false>(bsrc, smem, lane, [&](int m, const f32x4 (&a)[2]) __attribute__((always_inline)) {
          const f32x4 v0 = mul4s(a[0], sc4), v1 = mul4s(a[1], sc4);
          const u32x4 w = (u32x4){pack2(v0[0], v0[1]), pack2(v0[2], v0[3]), pack2(v1[0], v1[1]), pack2(v1[2], v1[3])};
          if (wide) stc16(rs, vo + (m & 1) * 256, so + (m >> 1) * 1280, w);
          else { stc8(rs, vo + (m & 1) * 128, so + (m >> 1) * 1280, (u32x2){w[0], w[1]}); stc8(rs, vo + (m & 1) * 128, so + 4 * hs + (m >> 1) * 1280, (u32x2){w[2], w[3]}); }
        });
      }
    }
  }
  waitv<0>();
  BAR();
  if (wid == 7 && lane < 8) __hip_atomic_fetch_add((GAS unsigned*)(ws + OFF_CNT + (lane * 16 + ctr) * 256), 1u, __ATOMIC_RELAXED, __HIP_MEMORY_SCOPE_AGENT);
  if (ctr == C_MOD && wid == 7) { __builtin_amdgcn_s_sleep(127); __builtin_amdgcn_s_sleep(60); }
}

constexpr int proj_pos(int f) { return 1024 + f * 1024; }
template <bool FINAL> DI u32x4 proj_ld(rsrc_t rs, int i, int wid, int lane) {
  constexpr int NF = FINAL ? 135 : 100;
  return ldc16(rs, lane * 16, (FINAL ? OFF_WOUT : OFF_WO) + (blk_cb() * NF + (i * 8 + 7 < NF ? i * 8 + wid : min(i * 8 + wid, NF - 1))) * 1024);
}
template <bool FINAL> DI void proj_small(rsrc_t rs, const u16* __restrict__ x, int wid, int lane, u32x2 (&res)[3], f32x4 (&gate)[3], f32x4 (&cbv)[3]) {
  const int mt = blk_panel() * 4 + (wid & 3), g = lane >> 4;
#pragma unroll
  for (int i = 0; i < 3; ++i) {
    const int n = (blk_cb() * 5 + min((wid >> 2) * 3 + i, 4)) * 16;
    if (!FINAL) { res[i] = *(const u32x2*)(x + (size_t)(mt * 16 + (lane & 15)) * D_ + n + g * 4); cbv[i] = ldc16f(rs, g * 16, OFF_MOD + (4 * 640 + n) * 4); }
    gate[i] = ldc16f(rs, g * 16, OFF_MOD + ((FINAL ? 5 : 2) * 640 + n) * 4);
  }
}
DI void proj_park(char* smem, int i, int tid, u32x4 v) { *(u32x4*)(smem + proj_pos(i * 8) + tid * 16) = v; }

DI void split3(float m, unsigned& w_h0, unsigned& w_h1) {
  const float n = -m; const float a = rbf(n), b = rbf(n - a), c = rbf(n - a - b);
  w_h0 = pack2(a, b); w_h1 = pack2(c, 0.f);
}

DI void phase_attn(char* __restrict__ ws, rsrc_t rs, const char* __restrict__ wsp, const u16* __restrict__ win, const u16* __restrict__ wout, const u16* __restrict__ x,
                   char* smem, int wid, u32x2 (&res)[3], f32x4 (&gate)[3], f32x4 (&cbv)[3]) {
  const int tid = fresh_tid(), lane = tid & 63;
  const int xcd = blockIdx.x & 7, idx = blockIdx.x >> 3, head = xcd * 4 + (idx >> 3), qb = idx & 7;
  const int r = lane & 31, h = lane >> 5, qg = wid & 3, kh = wid >> 2;
  unsigned kvo = OFF_KV + head * 163840, qo = OFF_Q + head * 81920 + (qb * 8 + qg * 2) * 1280;
  const unsigned kconst = h == 0 ? 0x3f803f80u : 0x00003f80u;
  const unsigned so16 = (tid & 255) * 16, l16 = lane * 16, l2 = 1024 + r * 8 + h * 4;
  u32x4 wq[13];
  for (int attempt = 0; attempt < 2; ++attempt) {
    asm volatile("" : "+s"(kvo), "+s"(qo));
    const char* kvs = ws + kvo + kh * 81920 + so16;
    const unsigned ldst = lds_addr(smem) + kh * 81920 + qg * 1024;
    u32x4 qA1w, qB1w, k1w; unsigned qA2w, qB2w, k2, m0sv;
    const char* d0 = kvs + (wid == 0 ? 4096 : 0); const char* d1 = kvs + 4096; const char* d2 = kvs + 8192; const char* d3 = kvs + 12288; const char* d4 = kvs + 16384;
    PIN(d0); PIN(d1); PIN(d2); PIN(d3); PIN(d4);
    if (attempt == 0) {
      if (wid == 7) {
        const int l7 = lane & 7;
        const int c = l7 == 0 ? head >> 3 : l7 == 1 ? 4 : l7 == 2 ? 5 + (head >> 3) : l7 == 3 ? 9 : l7 == 4 ? 10 + (head >> 3) : l7 == 5 ? 14 : C_MOD;
        poll_ge(ws, xcd * 16 + c, 16);
      }
      BAR();
    }
    asm volatile(
        "s_nop 4\n\tbuffer_load_dwordx4 %0, %7, %9, %19 offen sc1\n\tbuffer_load_dwordx4 %1, %7, %9, %10 offen sc1\n\tbuffer_load_dwordx4 %2, %7, %9, %11 offen sc1\n\t"
        "buffer_load_dword %3, %8, %9, %19 offen sc1\n\tbuffer_load_dword %4, %8, %9, %10 offen sc1\n\tbuffer_load_dword %5, %8, %9, %11 offen sc1\n\t"
        "s_mov_b32 %6, m0\n\ts_mov_b32 m0, %17\n\ts_nop 0\n\tglobal_load_lds_dwordx4 %12, off sc1\n\t"
        "s_add_u32 m0, %18, 0x1000\n\ts_nop 0\n\tglobal_load_lds_dwordx4 %13, off sc1\n\t"
        "s_add_u32 m0, %18, 0x2000\n\ts_nop 0\n\tglobal_load_lds_dwordx4 %14, off sc1\n\t"
        "s_add_u32 m0, %18, 0x3000\n\ts_nop 0\n\tglobal_load_lds_dwordx4 %15, off sc1\n\t"
        "s_add_u32 m0, %18, 0x4000\n\ts_nop 0\n\tglobal_load_lds_dwordx4 %16, off sc1\n\t"
        "s_mov_b32 m0, %6\n\ts_waitcnt vmcnt(5)"
        : "=&v"(qA1w), "=&v"(qB1w), "=&v"(k1w), "=&v"(qA2w), "=&v"(qB2w), "=&v"(k2), "=&s"(m0sv)
        : "v"(l16), "v"(l2), "s"(rs), "s"(qo + kh * 1280), "s"(kvo), "v"(d0), "v"(d1), "v"(d2), "v"(d3), "v"(d4),
          "s"(ldst + (wid == 0 ? 4096 : 0)), "s"(ldst), "s"(qo + (kh ^ 1) * 1280)
        : "memory", "scc");
    if (wid == 0) { const unsigned f = (fresh_tid() & 1) ? 0u : 0x3f803f80u; *(u32x4*)(smem + l16) = (u32x4){f, f, f, f}; }
    const bf16x8 qA1 = BF8(qA1w), qB1 = BF8(qB1w);
    bf16x8 k1 = BF8(k1w);
    float mA = -INFINITY, mB = -INFINITY;
    {
      const bf16x4 qA2 = BF4(((u32x2){qA2w, 0u})), qB2 = BF4(((u32x2){qB2w, 0u}));
      const int nt = attempt ? 64 : 1;
      bf16x8 t1 = k1; unsigned t2 = k2;
      for (int t = 0; t < nt; ++t) {
        if (t) { const unsigned to = kvo + (t >> 3) * 20480 + (t & 7) * 1280; t1 = BF8(ldc16(rs, l16, to)); t2 = ldc4(rs, l2, to); }
        const bf16x4 t2v = BF4(((u32x2){t2, kconst}));
        f32x16 a = MFMA32(t1, qA1, (f32x16){}); a = MFMA32K8(t2v, qA2, a);
        f32x16 b = MFMA32(t1, qB1, (f32x16){}); b = MFMA32K8(t2v, qB2, b);
#pragma unroll
        for (int i = 0; i < 16; ++i) { mA = fmaxf(mA, a[i]); mB = fmaxf(mB, b[i]); }
      }
      mA = fmaxf(mA, xhalf(mA)); mB = fmaxf(mB, xhalf(mB));
    }
    unsigned a0, a1, b0, b1; split3(mA, a0, a1); split3(mB, b0, b1);
    const bf16x4 qA2 = BF4(((u32x2){qA2w, h ? a1 : a0})), qB2 = BF4(((u32x2){qB2w, h ? b1 : b0}));
    char* lh = smem + kh * 81920;
    waitv<0>();
    BAR();
    f32x16 oA = (f32x16){}, oB = (f32x16){};
    const char* kp1 = lh + l16;
    const char* kp2 = lh + l2;
    const bool vreal = r < 20;
    const char* vp = vreal ? lh + 10240 + (r < 16 ? h * 16 + r : 16 + h * 4 + r) * 16 : smem + (r == 20 ? 0 : 16);
    const int vinc = vreal ? 1280 : 0, vjump = vreal ? 10240 : 0;
    if (kh) { k1 = *(const bf16x8*)kp1; k2 = *(const unsigned*)kp2; }
    bf16x8 kf1[2], vf[2][2]; u32x2 kf2[2];
    kf1[0] = k1; kf2[0] = (u32x2){k2, kconst}; kf2[1] = (u32x2){0u, kconst};
    vf[1][0] = (bf16x8){}; vf[1][1] = (bf16x8){};
    u32x4 pA0, pA1, pB0 = (u32x4){}, pB1 = (u32x4){};
    f32x4 e0 = (f32x4){}, e1;
    f32x16 sA = MFMA32(kf1[0], qA1, (f32x16){}), sB;
    sA = MFMA32K8(BF4(kf2[0]), qA2, sA);
#define EX2(x) __builtin_amdgcn_exp2f(x)
#define GRP(MF, S, i, EN, EP, P, w) do { MF; SCHED(); EN[0] = EX2(S[i]); P[w] = pack2(EP[0], EP[1]); EN[1] = EX2(S[i + 1]); P[w + 1] = pack2(EP[2], EP[3]); \
      EN[2] = EX2(S[i + 2]); EN[3] = EX2(S[i + 3]); PIN(P); PIN(EN); SCHED(); } while (0)
    const unsigned relsc = lds_addr(smem) + 768;
    auto group = [&](auto FIRST, int jump, int j) __attribute__((always_inline)) {
#pragma unroll
      for (int t = 0; t < 8; ++t) {
        const int c = t & 1, n = c ^ 1;
        if (FIRST) { if (t == 0) __builtin_amdgcn_s_setprio(3); if (t == 3) __builtin_amdgcn_s_setprio(2); if (t == 5) __builtin_amdgcn_s_setprio(1); if (t == 6) __builtin_amdgcn_s_setprio(0); if (t == 7) __builtin_amdgcn_s_setprio(3); }
        else {
          if (t == 4 && j == 2) __builtin_amdgcn_s_setprio(2);
          if (t == 3 && j == 3) {
            __builtin_amdgcn_s_setprio(1);
            const int wf = fresh_s(wid);
#pragma unroll
            for (int i = 0; i < 13; ++i) wq[i] = proj_ld<false>(rs, i, wf, lane);
            proj_small<false>(rs, x, wf, fresh_tid() & 63, res, gate, cbv);
          }
          if (t == 6 && j == 3) { __builtin_amdgcn_s_setprio(0); if (wid == 7) glds4c(ws + OFF_CNT + (C_REL + (lane & 7)) * 256, relsc); }
        }
        if (FIRST && t < 5) {
#pragma unroll
          for (int i = 5 + 3 * t; i < 8 + 3 * t; ++i) glds16c(kvs + i * 4096, fresh_s(ldst) + i * 4096);
        }
        if (t == 7) {
          if (FIRST) { waitv<0>(); BAR(); }
          kp1 += jump; kp2 += jump;
        }
        SCHED();
        kf1[n] = *(const bf16x8*)(kp1 + ((t + 1) & 7) * 1280); kf2[n][0] = *(const unsigned*)(kp2 + ((t + 1) & 7) * 1280);
        GRP(sB = MFMA32(kf1[c], qB1, (f32x16){}), sA, 0, e1, e0, pB1, 2);
        vf[c][0] = *(const bf16x8*)vp; vf[c][1] = *(const bf16x8*)(vp + 640); vp += vinc;
        GRP(sB = MFMA32K8(BF4(kf2[c]), qB2, sB), sA, 4, e0, e1, pA0, 0);
        GRP(oB = MFMA32(vf[n][0], BF8(pB0), oB), sA, 8, e1, e0, pA0, 2);
        GRP(oB = MFMA32(vf[n][1], BF8(pB1), oB), sA, 12, e0, e1, pA1, 0);
        GRP(sA = MFMA32(kf1[n], qA1, (f32x16){}), sB, 0, e1, e0, pA1, 2);
        GRP(sA = MFMA32K8(BF4(kf2[n]), qA2, sA), sB, 4, e0, e1, pB0, 0);
        GRP(oA = MFMA32(vf[c][0], BF8(pA0), oA), sB, 8, e1, e0, pB0, 2);
        GRP(oA = MFMA32(vf[c][1], BF8(pA1), oA), sB, 12, e0, e1, pB1, 0);
      }
      vp += vjump;
    };
    group(std::true_type{}, 20480, 0);
    const int blk = fresh_s(blockIdx.x);
    const int j2 = (blk - 135) * 4 + qg;
    const bool side = kh == 0 && attempt == 0 && j2 < 270;
    char* stile = smem + 1024 + qg * 4096;
    const char* svec = smem + 81920 + qg * 256;
    const int kt = blk < 135 ? 4 * (blk % 5) + qg : j2 / 10, ng = blk < 135 ? blk / 5 : j2 % 10;
    if (side) {
      const u16* W = blk < 135 ? win : wout; const int N = blk < 135 ? F_ : D_;
      const int lf = fresh_tid() & 63, j8 = lf >> 3;
#pragma unroll
      for (int i = 0; i < 4; ++i) {
        const int row = kt * 32 + i * 8 + j8;
        glds16(W + (size_t)row * N + ng * 64 + ((lf & 7) ^ j8) * 8, lds_addr(stile) + i * 1024);
      }
      glds4c((const float*)(ws + OFF_MOD) + (4 - h) * 640 + (kt < 20 ? kt : 0) * 32 + r, lds_addr(svec));
    }
#pragma nounroll
    for (int j = 1; j < 4; ++j) {
      if (j == 2) {
        if (side) {
          waitv<0>();
          const int lf = fresh_tid() & 63, g4 = lf >> 4, r4 = lf & 15, row = 8 * g4 + (r4 >> 2);
          const f32x4 sv0 = *(const f32x4*)(svec + (r4 & 1) * 128 + g4 * 32), sv1 = *(const f32x4*)(svec + (r4 & 1) * 128 + g4 * 32 + 16);
          const bf16x8 vec = BF8(((u32x4){pack2(sv0[0], sv0[1]), pack2(sv0[2], sv0[3]), pack2(sv1[0], sv1[1]), pack2(sv1[2], sv1[3])}));
          const unsigned nks = blk < 135 ? 20 : 27, dso = (blk < 135 ? OFF_WIN : OFF_WOUT) + (ng * 4 * nks + kt) * 1024;
          bf16x8 fr[4]; f32x4 pr[4];
#pragma unroll
          for (int f = 0; f < 4; ++f) {
            const int ch = 2 * f + ((r4 & 3) >> 1);
            const bf16x4 lo = lds_tr(stile + row * 128 + (ch ^ (row & 7)) * 16 + (r4 & 1) * 8), hi = lds_tr(stile + (row + 4) * 128 + (ch ^ ((row + 4) & 7)) * 16 + (r4 & 1) * 8);
            fr[f] = __builtin_shufflevector(lo, hi, 0, 1, 2, 3, 4, 5, 6, 7);
          }
#pragma unroll
          for (int f = 0; f < 4; ++f) { stc16(rs, l16, dso + f * nks * 1024, __builtin_bit_cast(u32x4, fr[f])); pr[f] = MFMA16(fr[f], vec, (f32x4){}); }
          if (r4 < 2) {
#pragma unroll
            for (int f = 0; f < 4; ++f) *(f32x4*)(stile + 3072 + r4 * 256 + f * 64 + g4 * 16) = pr[f];
          }
        }
      }
      if (kh && j == 2) l2touch(wsp + (size_t)(blk & 7) * 1048576 + (((blk >> 3) * 4 + qg) * 64 + lane) * 128, lds_addr(smem) + 256);
      if (j == 3 && kh == 0 && attempt == 0) { waitv<0>(); arrive(ws, C_REL + xcd, lane); }
      group(std::false_type{}, j < 3 ? 20480 : 0, j);
    }
    pB1[2] = pack2(e0[0], e0[1]); pB1[3] = pack2(e0[2], e0[3]);
    oB = MFMA32(vf[1][0], BF8(pB0), oB); oB = MFMA32(vf[1][1], BF8(pB1), oB);
    {
      f32x4* ex = (f32x4*)(lh + 1024 + qg * 4096) + lane;
#pragma unroll
      for (int g = 0; g < 3; ++g) ex[g * 64] = (f32x4){oA[4 * g], oA[4 * g + 1], oA[4 * g + 2], oA[4 * g + 3]};
    }
    int* flag = (int*)(smem + 528);
    BAR();
    float o[12];
    {
      const f32x4* ex = (const f32x4*)(smem + (kh ^ 1) * 81920 + 1024 + qg * 4096) + lane;
#pragma unroll
      for (int g = 0; g < 3; ++g) { const f32x4 t = ex[g * 64];
#pragma unroll
        for (int e = 0; e < 4; ++e) o[4 * g + e] = oB[4 * g + e] + t[e]; }
    }
    const float lo = xhalf(o[8]); const float l = h ? o[8] : lo;
    const float rl = __builtin_amdgcn_rcpf(l);
    const int tok = qb * 256 + qg * 64 + kh * 32 + r;
    const unsigned vo = (tok >> 4) * 20480 + (tok & 15) * 16;
    stc16(rs, vo + h * 256, OFF_O + (head >> 1) * 1024 + (head & 1) * 512,
          (u32x4){pack2(o[0] * rl, o[1] * rl), pack2(o[2] * rl, o[3] * rl), pack2(o[4] * rl, o[5] * rl), pack2(o[6] * rl, o[7] * rl)});
    if (h == 0) stc8(rs, vo - (tok & 15) * 8, OFF_O + 16384 + (head >> 3) * 1024 + (head & 1) * 512 + ((head >> 1) & 3) * 128, (u32x2){pack2(o[8] * rl, o[9] * rl), pack2(o[10] * rl, o[11] * rl)});
    if (wid == 4 && blk < 135 && attempt == 0 && lane < 32) {
      const char* pp = smem + 1024 + 3072 + lane * 16;
      const f32x4 v = (*(const f32x4*)pp + *(const f32x4*)(pp + 4096)) + (*(const f32x4*)(pp + 8192) + *(const f32x4*)(pp + 12288));
      stc16(rs, (lane >> 4) * 8192 + (lane & 15) * 16, OFF_CBP + (blk % 5) * 16384 + (blk / 5) * 256, __builtin_bit_cast(u32x4, v));
    }
    bool ok = (l > 1e-30f) && (l < 1e30f);
#pragma unroll
    for (int i = 0; i < 12; ++i) ok = ok && (fabsf(o[i]) < 3e38f);
    if (!ok && attempt == 0) *flag = 1;
    drain();
    BAR();
    const int redo = __builtin_amdgcn_readfirstlane(*(volatile LAS int*)flag);
    if (!redo) break;
    BAR();
  }
  if (wid == 7) arrive(ws, C_ATT + qb, lane);
#pragma unroll
  for (int i = 0; i < 13; ++i) proj_park(smem, i, tid, wq[i]);
  if (!__all(*(volatile LAS unsigned*)(smem + 768 + (lane & 7) * 4) >= 128u)) poll_ge(ws, C_REL + (lane & 7), 128);
}

template <bool FINAL, int KH, class HOOK>
DI void proj_body(char* __restrict__ ws, rsrc_t rs, const u16* __restrict__ x, u16* __restrict__ out, char* smem, int wid, u32x2 (&res)[3], const f32x4 (&gate)[3], const f32x4 (&cbv)[3], HOOK&& after_mma) {
  const int lane = fresh_tid() & 63;
  const int panel = blk_panel(), cb = blk_cb();
  constexpr int NKS = FINAL ? 27 : 20;
  constexpr int K0 = KH ? (NKS + 1) / 2 : 0, NK = KH ? NKS / 2 : (NKS + 1) / 2, NKEEP = KH ? 2 : 3, UK = KH ? 3 : 0, UG = KH ? 0 : 3;
  const int m = wid & 3, mt = panel * 4 + m, g = lane >> 4, r = lane & 15;
  unsigned l16 = lane * 16; PIN(l16);
  const unsigned aso = fresh_s((FINAL ? OFF_U : OFF_O) + (mt * NKS + K0) * 1024);
  if (wid == 7) poll_ge(ws, FINAL ? C_U + panel : C_ATT + (panel >> 2), FINAL ? 8 : 32);
  BAR();
  bf16x8 a[NK];
#pragma unroll
  for (int k = 0; k < NK; ++k) {
    if (!FINAL && K0 + k >= 16) {
      const u32x2 lo = ldc8(rs, (l16 >> 1) + (k & 3) * 1024, aso + (k >> 2) * 4096), hi = ldc8(rs, (l16 >> 1) + (k & 3) * 1024 + 512, aso + (k >> 2) * 4096);
      a[k] = BF8(((u32x4){lo[0], lo[1], hi[0], hi[1]}));
    } else a[k] = BF8(ldc16(rs, l16 + (k & 3) * 1024, aso + (k >> 2) * 4096));
  }
  SCHED();
  constexpr int PD = FINAL ? 5 : 6;
  const char* bl = smem + lane * 16;
  bf16x8 bf[PD + 1][5]; f32x4 acc[5];
#pragma unroll
  for (int u = 0; u < 5; ++u) acc[u] = (f32x4){};
#pragma unroll
  for (int k = 0; k < PD; ++k)
#pragma unroll
    for (int u = 0; u < 5; ++u) bf[k][u] = *(const bf16x8*)(bl + proj_pos(u * NKS + K0 + k));
  SCHED();
#pragma unroll
  for (int k = 0; k < NK; ++k) {
#pragma unroll
    for (int u = 0; u < 5; ++u) {
      if (k + PD < NK) bf[(k + PD) % (PD + 1)][u] = *(const bf16x8*)(bl + proj_pos(u * NKS + K0 + k + PD));
      acc[u] = MFMA16(bf[k % (PD + 1)][u], a[k], acc[u]);
      SCHED();
    }
  }
  f32x4* ex = (f32x4*)(smem + proj_pos(FINAL ? 136 : 104)) + m * 5 * 64 + lane;
#pragma unroll
  for (int i = 0; i < 5 - NKEEP; ++i) ex[(UG + i) * 64] = acc[UG + i];
  f32x4 part[NKEEP];
#pragma unroll
  for (int i = 0; i < NKEEP; ++i) {
    part[i] = fma4s(gate[i], acc[UK + i], (f32x4){bflo(res[i][0]), bfhi(res[i][0]), bflo(res[i][1]), bfhi(res[i][1])});
    PIN(part[i]);
  }
  SCHED();
  after_mma();
  BAR();
  f32x4 oth[NKEEP];
#pragma unroll
  for (int i = 0; i < NKEEP; ++i) oth[i] = ex[(UK + i) * 64];
  f32x4 sv = (f32x4){}, qv = (f32x4){};
#pragma unroll
  for (int i = 0; i < NKEEP; ++i) {
    const f32x4 v = part[i] + gate[i] * oth[i];
    const u32x2 o = (u32x2){pack2(v[0], v[1]), pack2(v[2], v[3])};
    if (FINAL) *(u32x2*)(out + (size_t)(mt * 16 + r) * D_ + (cb * 5 + UK + i) * 16 + g * 4) = o;
    else {
      res[i] = o; PIN(res[i]);
      const f32x4 w = v * cbv[i]; const int un = cb * 5 + UK + i;
      stc8(rs, ((g >> 1) * 16 + r) * 16 + (g & 1) * 8, OFF_X1S + mt * 20480 + un * 512, (u32x2){pack2(w[0], w[1]), pack2(w[2], w[3])});
      sv += v; qv += v * v;
    }
  }
  if (!FINAL) {
    const float s = sum_g((sv[0] + sv[1]) + (sv[2] + sv[3])), q = sum_g((qv[0] + qv[1]) + (qv[2] + qv[3]));
    if (g == 0) stc8(rs, r * 8, OFF_ST1 + ((panel * 16 + cb * 2 + KH) * 64 + m * 16) * 8, (u32x2){__float_as_uint(s), __float_as_uint(q)});
    drain();
    BAR();
    if (wid == 7) arrive(ws, C_X1 + panel, lane);
  }
}

constexpr float GC1 = -2.8853900817779268f * 0.7978845608028654f, GC2 = GC1 * 0.044715f;
DI void gelu8(f32x4& a0, const f32x4& l0, f32x4& a1, const f32x4& l1) {
  float t[8];
#define G8(expr0, expr1) do { _Pragma("unroll") for (int i = 0; i < 4; ++i) { t[i] = opq(expr0); } _Pragma("unroll") for (int i = 0; i < 4; ++i) { t[4 + i] = opq(expr1); } SCHED(); } while (0)
  G8(a0[i] * a0[i], a1[i] * a1[i]);
  G8(__builtin_fmaf(t[i], l0[3], l0[2]), __builtin_fmaf(t[4 + i], l1[3], l1[2]));
  G8(a0[i] * t[i], a1[i] * t[4 + i]);
  G8(__builtin_amdgcn_exp2f(t[i]), __builtin_amdgcn_exp2f(t[4 + i]));
  G8(__builtin_fmaf(t[i], l0[0], l0[0]), __builtin_fmaf(t[4 + i], l1[0], l1[0]));
  G8(__builtin_amdgcn_rcpf(t[i]), __builtin_amdgcn_rcpf(t[4 + i]));
  G8(a0[i] * t[i], a1[i] * t[4 + i]);
  a0 = (f32x4){t[0], t[1], t[2], t[3]}; a1 = (f32x4){t[4], t[5], t[6], t[7]};
}
constexpr int L4_Z1 = 114688, L4_TR = L4_Z1 + 16384, L4_LN = L4_TR + 16384, L4_ST2 = L4_LN + 1024, L4_FIN = L4_ST2 + 3584, L4_CB = L4_FIN + 512, L4_CNT = L4_CB + 2048;
constexpr int K5_R = 7;
constexpr unsigned DEAD = 0x80000000u;
DI int sp_stage(int s) { const int cb = blk_cb(); return (s + cb * 2) & 15; }
DI unsigned sp_voff(int wid, int lane, int i) { return ((wid & 3) * 16 + i * 8 + (lane >> 3)) * 4096 + (wid >> 2) * 128 + ((lane & 7) ^ (4 * i + (lane >> 4))) * 16; }
DI const char* sp_base(const u16* wsp, int panel, int s) { return (const char*)wsp + (size_t)panel * 262144 + sp_stage(s) * 256; }
DI void sp_prefetch(u32x4 (&wr)[16 - K5_R][2], const u16* __restrict__ wsp, int panel, int wid, int lane) {
  const unsigned v0 = sp_voff(wid, lane, 0), v1 = sp_voff(wid, lane, 1);
#pragma unroll
  for (int s = K5_R; s < 16; ++s) { wr[s - K5_R][0] = *(const u32x4*)(sp_base(wsp, panel, s) + v0); wr[s - K5_R][1] = *(const u32x4*)(sp_base(wsp, panel, s) + v1); }
  asm volatile("" ::: "memory");
}
template <int S0, int S1>
DI void sp_ring(const u16* __restrict__ wsp, char* smem, int panel, int wid, int lane) {
  const unsigned v0 = sp_voff(wid, lane, 0), v1 = sp_voff(wid, lane, 1);
#pragma unroll
  for (int s = S0; s < S1; ++s) { glds16(sp_base(wsp, panel, s) + v0, lds_addr(smem) + wid * 2048 + s * 16384); glds16(sp_base(wsp, panel, s) + v1, lds_addr(smem) + wid * 2048 + s * 16384 + 1024); }
}
DI unsigned win_boff(int wid, int lane) { const int cb = blk_cb(); return lane * 16 + (wid == 6 && cb >= 6 ? DEAD : 0u); }
DI unsigned win_soff(int wid, bool z2) { return OFF_WIN + ((z2 ? 54 : 0) + unit0_of(blk_cb()) + wid) * 20480; }
#define WIN_LD(so, k) BF8(ldc16(rs, bo + ((k) & 3) * 1024, (so) + ((k) >> 2) * 4096))
DI void win_prefetch(rsrc_t rs, int wid, bf16x8 (&b2)[20]) {
  const unsigned bo = win_boff(wid, fresh_tid() & 63), so = win_soff(wid, true);
#pragma unroll
  for (int k = 0; k < 20; ++k) b2[k] = WIN_LD(so, k);
}
template <class HOOK>
DI void win_mma(const char* al, const bf16x8 (&b)[20], f32x4 (&acc)[4], HOOK&& hook) {
  constexpr int PD = 4;
  bf16x8 af[PD + 1][2];
#define WIN_RD(i) do { af[(i) % (PD + 1)][0] = *(const bf16x8*)(al + ((i) & 3) * 20480 + ((i) >> 2) * 2048); af[(i) % (PD + 1)][1] = *(const bf16x8*)(al + ((i) & 3) * 20480 + ((i) >> 2) * 2048 + 1024); } while (0)
#pragma unroll
  for (int i = 0; i < PD; ++i) WIN_RD(i);
  SCHED();
  sfor<40>([&](auto I) __attribute__((always_inline)) {
    constexpr int i = I, m = i & 3, k = (i >> 2) * 2;
    if constexpr (i + PD < 40) WIN_RD(i + PD);
    acc[m] = MFMA16(b[k], af[i % (PD + 1)][0], acc[m]);
    acc[m] = MFMA16(b[k + 1], af[i % (PD + 1)][1], acc[m]); PIN(acc[m]);
    hook(I);
    SCHED();
  });
}
DI void stage_x1s(char* __restrict__ ws, char* smem, int panel, int wid, int lane) {
  const char* src = ws + OFF_X1S + (size_t)panel * 81920 + lane * 16;
#pragma unroll
  for (int i = 0; i < 12; ++i) { const int f = i < 11 ? i * 7 + wid : min(77 + wid, 79); glds16c(src + f * 1024, lds_addr(smem) + f * 1024); }
}
DI void load_bb(const u16* __restrict__ bsp, int panel, int wid, int lane, u16 (&bb)[2]) { const u16* p = bsp + panel * 64 + (wid >> 2) * 32 + (lane & 15); bb[0] = p[0]; bb[1] = p[16]; asm volatile("" ::: "memory"); }
DI void win_tail(char* __restrict__ ws, const u16* __restrict__ wsp, char* smem, int panel, int cb, int wid, int lane) {
  BAR();
  if (wid == 7 && lane < 4) __hip_atomic_fetch_add((GAS unsigned*)(ws + OFF_CNT + (C_Z2 + lane * 8 + cb) * 256), 1u, __ATOMIC_RELAXED, __HIP_MEMORY_SCOPE_AGENT);
}

template <bool EARLY>
DI void win_body(char* __restrict__ ws, rsrc_t rs, const u16* __restrict__ wsp, char* smem, int wid, const bf16x8 (&b2)[20], u32x4 (&wr)[16 - K5_R][2], const u16* __restrict__ bsp, u16 (&bb)[2]) {
  const int tid = fresh_tid(), lane = tid & 63, g = lane >> 4, r = lane & 15;
  const int panel = blk_panel(), cb = blk_cb(), u0 = unit0_of(cb);
  float* lnp = (float*)(smem + L4_LN); float* st2 = (float*)(smem + L4_ST2); float* fin = (float*)(smem + L4_FIN); float* cbl = (float*)(smem + L4_CB);
  int* flg = (int*)(smem + L4_CNT);
  __builtin_amdgcn_s_setprio(1);
  const unsigned bo = win_boff(wid, lane), so1 = win_soff(wid, false);
  bf16x8 b1[20];
  BAR();
  stage_x1s(ws, smem, panel, wid, lane);
  BAR();
  f32x4 ln[4], z2[4], z1[4];
#pragma unroll
  for (int m = 0; m < 4; ++m) ln[m] = *(const f32x4*)(lnp + (m * 16 + r) * 4);
  {
    const f32x4 cs2 = *(const f32x4*)(cbl + 112 + wid * 16 + g * 4), bi2 = *(const f32x4*)(cbl + 256 + 112 + wid * 16 + g * 4);
    const f32x4 cs1 = *(const f32x4*)(cbl + wid * 16 + g * 4), bi1 = *(const f32x4*)(cbl + 256 + wid * 16 + g * 4);
#pragma unroll
    for (int m = 0; m < 4; ++m) {
#pragma unroll
      for (int e = 0; e < 4; ++e) { z2[m][e] = opq(__builtin_fmaf(ln[m][1], cs2[e], opq(ln[m][0] * bi2[e]))); z1[m][e] = opq(__builtin_fmaf(ln[m][1], cs1[e], opq(ln[m][0] * bi1[e]))); }
      PIN(z1[m]); PIN(z2[m]);
    }
  }
#pragma unroll
  for (int k = 0; k < 4; ++k) b1[k] = WIN_LD(so1, k);
  waitv<4>();
  BAR();
  load_bb(bsp, panel, wid, lane, bb);
  win_mma(smem + lane * 16, b2, z2, [&](auto I) __attribute__((always_inline)) { constexpr int i = I; if constexpr (i < 32 && (i & 1)) b1[4 + i / 2] = WIN_LD(so1, 4 + i / 2); });
  gelu8(z2[0], ln[0], z2[1], ln[1]); gelu8(z2[2], ln[2], z2[3], ln[3]);
  {
    float s[4], q[4];
#pragma unroll
    for (int m = 0; m < 4; ++m) { s[m] = opq(opq(z2[m][0] + z2[m][1]) + opq(z2[m][2] + z2[m][3])); q[m] = opq(__builtin_fmaf(z2[m][0], z2[m][0], opq(z2[m][1] * z2[m][1])) + opq(__builtin_fmaf(z2[m][2], z2[m][2], opq(z2[m][3] * z2[m][3])))); }
    auto pa = __builtin_amdgcn_permlane32_swap(__float_as_uint(s[0]), __float_as_uint(s[2]), false, false);
    auto pb = __builtin_amdgcn_permlane32_swap(__float_as_uint(s[1]), __float_as_uint(s[3]), false, false);
    auto pc = __builtin_amdgcn_permlane32_swap(__float_as_uint(q[0]), __float_as_uint(q[2]), false, false);
    auto pd = __builtin_amdgcn_permlane32_swap(__float_as_uint(q[1]), __float_as_uint(q[3]), false, false);
    const float sa = opq(__uint_as_float(pa[0]) + __uint_as_float(pa[1])), sb = opq(__uint_as_float(pb[0]) + __uint_as_float(pb[1]));
    const float qa = opq(__uint_as_float(pc[0]) + __uint_as_float(pc[1])), qb = opq(__uint_as_float(pd[0]) + __uint_as_float(pd[1]));
    auto ps = __builtin_amdgcn_permlane16_swap(__float_as_uint(sa), __float_as_uint(sb), false, false);
    auto pq = __builtin_amdgcn_permlane16_swap(__float_as_uint(qa), __float_as_uint(qb), false, false);
    *(f32x2*)(st2 + (wid * 64 + lane) * 2) = (f32x2){opq(__uint_as_float(ps[0]) + __uint_as_float(ps[1])), opq(__uint_as_float(pq[0]) + __uint_as_float(pq[1]))};
  }
  asm volatile("" ::: "memory");
  if (lane == 0) __hip_atomic_fetch_add(flg, 1, __ATOMIC_RELAXED, __HIP_MEMORY_SCOPE_WORKGROUP);
  asm volatile("" ::: "memory");
  __builtin_amdgcn_s_setprio(0);
  const unsigned wvo[2] = {sp_voff(wid, lane, 0), sp_voff(wid, lane, 1)};
  win_mma(smem + lane * 16, b1, z1, [&](auto I) __attribute__((always_inline)) {
    constexpr int i = I, j = i < 8 ? i : (i & 1) ? -1 : 4 + i / 2;
    if constexpr (EARLY && j >= 0 && j < 18) wr[j >> 1][j & 1] = *(const u32x4*)(sp_base(wsp, panel, K5_R + (j >> 1)) + wvo[j & 1]);
  });
  gelu8(z1[0], ln[0], z1[1], ln[1]); gelu8(z1[2], ln[2], z1[3], ln[3]);
#pragma unroll
  for (int mm = 0; mm < 2; ++mm)
    *(u32x4*)(smem + L4_Z1 + wid * 2048 + mm * 1024 + lane * 16) = (u32x4){pack2(z1[2 * mm][0], z1[2 * mm][1]), pack2(z1[2 * mm][2], z1[2 * mm][3]), pack2(z1[2 * mm + 1][0], z1[2 * mm + 1][1]), pack2(z1[2 * mm + 1][2], z1[2 * mm + 1][3])};
  BAR();
  char* tr = smem + L4_TR + wid * 2048;
#pragma unroll
  for (int m = 0; m < 4; ++m) {
    const f32x2 f = *(const f32x2*)(fin + (m * 16 + r) * 2);
    const f32x4 v = z2[m] * f[0] - f[1];
    *(u32x2*)(tr + (m * 16 + r) * 32 + g * 8) = (u32x2){pack2(v[0], v[1]), pack2(v[2], v[3])};
  }
  {
    const char* p = tr + (8 * g + (r >> 2)) * 32 + (r & 3) * 8;
    const bf16x4 lo0 = lds_tr(p), hi0 = lds_tr(p + 128), lo1 = lds_tr(p + 1024), hi1 = lds_tr(p + 1152);
    stc16(rs, bo, OFF_Z2 + (panel * 2 * 54 + u0 + wid) * 1024, __builtin_bit_cast(u32x4, (bf16x8)__builtin_shufflevector(lo0, hi0, 0, 1, 2, 3, 4, 5, 6, 7)));
    stc16(rs, bo, OFF_Z2 + ((panel * 2 + 1) * 54 + u0 + wid) * 1024, __builtin_bit_cast(u32x4, (bf16x8)__builtin_shufflevector(lo1, hi1, 0, 1, 2, 3, 4, 5, 6, 7)));
  }
  SCHED();
  waitv<0>();
  win_tail(ws, wsp, smem, panel, cb, wid, lane);
  sp_ring<0, 5>(wsp, smem, panel, wid, lane);
  if (!EARLY) sp_prefetch(wr, wsp, panel, wid, lane);
}

DI void win_agent(char* __restrict__ ws, rsrc_t rs, const u16* __restrict__ wsp, char* smem, u32x4 (&wr)[16 - K5_R][2], const u16* __restrict__ bsp, u16 (&bb)[2]) {
  const int tid = fresh_tid(), lane = tid & 63, wid = 7;
  const int panel = blk_panel(), cb = blk_cb(), u0 = unit0_of(cb);
  float* lnp = (float*)(smem + L4_LN); float* st2 = (float*)(smem + L4_ST2); float* fin = (float*)(smem + L4_FIN); float* cbl = (float*)(smem + L4_CB);
  int* cnt = (int*)(smem + L4_CNT);
  __builtin_amdgcn_s_setprio(2);
  if (lane == 0) *cnt = 0;
  poll_ge(ws, lane < 8 ? C_ATT + lane : C_X1 + panel, lane < 8 ? 32 : 8);
  BAR();
  {
    u32x2 t[16]; f32x4 cp[5][2];
#pragma unroll
    for (int i = 0; i < 16; ++i) t[i] = ldc8(rs, lane * 8 + (i & 7) * 512, OFF_ST1 + (panel * 16 + (i & 8)) * 512);
    const unsigned co = lane < 28 ? (u0 * 16 + lane * 4) * 4 : min((54 + u0) * 16 + min(lane - 28, 27) * 4, F_ - 4) * 4;
#pragma unroll
    for (int i = 0; i < 5; ++i) { cp[i][0] = ldc16f(rs, co, OFF_CBP + i * 16384); cp[i][1] = ldc16f(rs, co, OFF_CBP + i * 16384 + 8192); }
    float s = 0.f, q = 0.f;
#pragma unroll
    for (int i = 0; i < 16; ++i) { s += __uint_as_float(t[i][0]); q += __uint_as_float(t[i][1]); }
    const float mean = s * (1.f / 640.f), var = fmaxf(q * (1.f / 640.f) - mean * mean, 0.f);
    const float den = ln_den(var), ri = __builtin_amdgcn_rcpf(den);
    *(f32x4*)(lnp + lane * 4) = (f32x4){den, -rbf(mean), GC1 * ri, GC2 * ri * ri * ri};
#pragma unroll
    for (int v = 0; v < 2; ++v) *(f32x4*)(cbl + v * 256 + lane * 4) = ((cp[0][v] + cp[1][v]) + (cp[2][v] + cp[3][v])) + cp[4][v];
  }
  BAR();
  BAR();
  __builtin_amdgcn_s_setprio(0);
  __builtin_amdgcn_s_sleep(8);
  load_bb(bsp, panel, wid, lane, bb);
  sp_prefetch(wr, wsp, panel, wid, lane);
#pragma nounroll
  for (int w = 0; w < 8; ++w) sp_ring<5, 7>(wsp, smem, panel, w, lane);
  __builtin_amdgcn_s_setprio(2);
#pragma nounroll
  for (unsigned spin = 0; spin < (1u << 22); ++spin) {
    if (__hip_atomic_load(cnt, __ATOMIC_RELAXED, __HIP_MEMORY_SCOPE_WORKGROUP) >= 7) break;
    __builtin_amdgcn_s_sleep(1);
  }
  asm volatile("" ::: "memory");
  float s, q;
  {
    f32x2 t[7];
#pragma unroll
    for (int i = 0; i < 7; ++i) t[i] = *(const f32x2*)(st2 + (i * 64 + lane) * 2);
    s = t[0][0]; q = t[0][1];
#pragma unroll
    for (int i = 1; i < 6; ++i) { s = opq(s + t[i][0]); q = opq(q + t[i][1]); }
    if (cb < 6) { s = opq(s + t[6][0]); q = opq(q + t[6][1]); }
  }
  GAS u64* xch = (GAS u64*)(ws + OFF_XCH) + panel * 512;
  unsigned qb = __float_as_uint(q); if (qb == ~0u) qb = 0x7fc00000u;
  __hip_atomic_store(xch + cb * 64 + lane, ((u64)qb << 32) | __float_as_uint(s), __ATOMIC_RELAXED, __HIP_MEMORY_SCOPE_AGENT);
  u64 v[8];
#pragma nounroll
  for (unsigned spin = 0; spin < (1u << 19); ++spin) {
    bool ok = true;
#pragma unroll
    for (int i = 0; i < 8; ++i) { v[i] = __hip_atomic_load(xch + i * 64 + lane, __ATOMIC_RELAXED, __HIP_MEMORY_SCOPE_AGENT); ok = ok && ((unsigned)(v[i] >> 32) != ~0u); }
    if (__all(ok)) break;
    __builtin_amdgcn_s_sleep(1);
  }
  s = 0.f; q = 0.f;
#pragma unroll
  for (int i = 0; i < 8; ++i) { s = opq(s + __uint_as_float((unsigned)v[i])); q = opq(q + __uint_as_float((unsigned)(v[i] >> 32))); }
  const float mean = opq(s * (1.f / 864.f)), var = fmaxf(opq(q * (1.f / 864.f)) - mean * mean, 0.f);
  const float ri = __builtin_amdgcn_rcpf(ln_den(var));
  *(f32x2*)(fin + lane * 2) = (f32x2){ri, rbf(mean) * ri};
  __builtin_amdgcn_s_setprio(0);
  BAR();
  __builtin_amdgcn_s_sleep(6);
  sp_ring<0, 5>(wsp, smem, panel, wid, lane);
  win_tail(ws, wsp, smem, panel, cb, wid, lane);
}

constexpr int sp_q(int t) { const int w = 4 * 2 + t / 3; return t + (w < 4 * 4 ? w : 4 * 4); }
DI void phase_sp(char* __restrict__ ws, rsrc_t rs, const u16 (&bb)[2], char* smem, int wid, const u32x4 (&wr)[16 - K5_R][2], u32x2 (&res)[3], f32x4 (&gate)[3]) {
  const int tid = fresh_tid(), lane = tid & 63;
  const int panel = blk_panel(), cb = blk_cb();
  const int kh = wid >> 2, up = wid & 3, u0 = unit0_of(cb) + 2 * up, nu = cb < 6 ? 7 : 6, g = lane >> 4, r = lane & 15;
  const unsigned dead[2] = {2 * up < nu ? 0u : DEAD, 2 * up + 1 < nu ? 0u : DEAD};
  unsigned bvo[2] = {lane * 16 + dead[0], lane * 16 + 1024 + dead[1]};
  const char* al[2][2];
#pragma unroll
  for (int k = 0; k < 2; ++k)
#pragma unroll
    for (int j = 0; j < 2; ++j) al[k][j] = smem + kh * 8192 + (j ^ kh) * 4096 + r * 128 + ((4 * k + g) ^ (r >> 1)) * 16;
  constexpr int LB = 4, PD = 3;
  bf16x8 bq[LB + 1][4], af[PD + 1][2]; f32x4 acc[2][4]; u32x4 wq[17];
#pragma unroll
  for (int i = 0; i < 2; ++i) {
    const float b0 = bf2f(bb[0]), b1 = bf2f(bb[1]);
    acc[i][0] = (f32x4){b0, b0, b0, b0}; acc[i][1] = (f32x4){b1, b1, b1, b1}; acc[i][2] = (f32x4){}; acc[i][3] = (f32x4){};
  }
  const unsigned bso = fresh_s(OFF_Z2 + (kh * 2 * 54 + u0) * 1024);
  PIN(bvo[0]); PIN(bvo[1]);
  if (wid == 7) poll_ge(ws, C_Z2 + (panel >> 3) * 8 + cb, 32);
  else if (kh && up < 3) waitv<2 * (16 - K5_R) + 6>(); else waitv<6>();
  BAR();
#define SP_LD(s, j) (bq[(s) % (LB + 1)][j] = BF8(ldc16(rs, bvo[(j) & 1], bso + (sp_stage(s) * 4 + ((j) >> 1)) * 55296)))
#define SP_RD(t, k) (af[(t) % (PD + 1)][k] = *(const bf16x8*)(al[k][((t) >> 1) & 1] + (((t) >> 2) % K5_R) * 16384 + ((t) & 1) * 2048))
#pragma unroll
  for (int q = 0; q < sp_q(0); ++q) SP_LD(q >> 2, q & 3);
#pragma unroll
  for (int t = 0; t < PD; ++t) { SP_RD(t, 0); SP_RD(t, 1); }
  SCHED();
  sfor<64>([&](auto T) __attribute__((always_inline)) {
    constexpr int t = T, nx = t + PD, s = t >> 2, m = t & 3, tl = sp_q(t), t2 = sp_q(t + 1) - 1;
    if constexpr (nx == 8) asm volatile("s_barrier" ::: "memory");
    if constexpr (nx == 16 || nx == 28 || nx == 44 || nx == 56) BAR();
    constexpr int c = nx >= 16 && nx < 24 ? nx - 16 : nx >= 28 && nx < 34 ? nx - 20 : nx >= 44 && nx < 48 ? nx - 30 : -1;
    if constexpr (nx < 64) SP_RD(nx, 0);
    acc[0][m] = MFMA16(bq[s % (LB + 1)][0], af[t % (PD + 1)][0], acc[0][m]);
    acc[0][m] = MFMA16(bq[s % (LB + 1)][2], af[t % (PD + 1)][1], acc[0][m]); PIN(acc[0][m]);
    if constexpr (tl < 64) SP_LD(tl >> 2, tl & 3); else if constexpr (tl - 64 < 17) wq[tl - 64] = proj_ld<true>(rs, tl - 64, wid, lane);
    SCHED();
    if constexpr (nx < 64) SP_RD(nx, 1);
    acc[1][m] = MFMA16(bq[s % (LB + 1)][1], af[t % (PD + 1)][0], acc[1][m]);
    acc[1][m] = MFMA16(bq[s % (LB + 1)][3], af[t % (PD + 1)][1], acc[1][m]); PIN(acc[1][m]);
    if constexpr (t2 > tl && t2 < 64) SP_LD(t2 >> 2, t2 & 3);
    if constexpr (c >= 0) *(u32x4*)(smem + ((c >> 1) % K5_R) * 16384 + wid * 2048 + (c & 1) * 1024 + lane * 16) = wr[c >> 1][c & 1];
    if constexpr (t == 63 && 4 * LB < 17) { _Pragma("unroll") for (int i = 4 * LB; i < 17; ++i) wq[i] = proj_ld<true>(rs, i, wid, lane); }
    if constexpr (t == 62) proj_small<true>(rs, nullptr, wid, lane, res, gate, gate);
    SCHED();
  });
  f32x4* exw = (f32x4*)(smem + 32768 + wid * 4096) + lane;
  const f32x4* exr = (const f32x4*)(smem + 32768 + (wid ^ 4) * 4096) + lane;
  u32x4 z[2];
#pragma unroll
  for (int i = 0; i < 2; ++i) z[i] = *(const u32x4*)(smem + L4_Z1 + (2 * up + i) * 2048 + kh * 1024 + lane * 16);
#pragma unroll
  for (int i = 0; i < 2; ++i)
#pragma unroll
    for (int j = 0; j < 2; ++j) exw[(i * 2 + j) * 64] = acc[i][2 + j];
  f32x4 zf[2][2];
#pragma unroll
  for (int i = 0; i < 2; ++i)
#pragma unroll
    for (int j = 0; j < 2; ++j) {
      zf[i][j] = (f32x4){bflo(z[i][2 * j]), bfhi(z[i][2 * j]), bflo(z[i][2 * j + 1]), bfhi(z[i][2 * j + 1])};
      acc[i][j] = mul4s(acc[i][j], zf[i][j]); PIN(zf[i][j]); PIN(acc[i][j]);
    }
  BAR();
  f32x4 oth[2][2];
#pragma unroll
  for (int i = 0; i < 2; ++i)
#pragma unroll
    for (int j = 0; j < 2; ++j) oth[i][j] = exr[(i * 2 + j) * 64];
#pragma unroll
  for (int i = 0; i < 2; ++i)
#pragma unroll
    for (int j = 0; j < 2; ++j) {
      const f32x4 v = acc[i][j] + zf[i][j] * oth[i][j];
      stc8(rs, ((g >> 1) * 16 + r) * 16 + (g & 1) * 8 + dead[i], OFF_U + (panel * 4 + 2 * kh + j) * 27648 + (u0 + i) * 512, (u32x2){pack2(v[0], v[1]), pack2(v[2], v[3])});
    }
  drain();
  BAR();
  if (wid == 7) arrive(ws, C_U + panel, lane);
#pragma unroll
  for (int i = 0; i < 17; ++i) proj_park(smem, i, tid, wq[i]);
}

extern "C" __global__ __launch_bounds__(512, 2) OCC2 void k_mega(char* __restrict__ ws, const u16* __restrict__ x, const u16* __restrict__ win,
                                                     const u16* __restrict__ wsp, const u16* __restrict__ bsp, const u16* __restrict__ wout, u16* __restrict__ out) {
  extern __shared__ __attribute__((aligned(16))) char smem[];
  const int wid = __builtin_amdgcn_readfirstlane(threadIdx.x >> 6);
  const rsrc_t rs = __builtin_amdgcn_make_buffer_rsrc(ws, 0, WS_USED, 0x00020000);
  u32x2 res[3]; f32x4 gate[3], cbv[3];
  phase_qkv(ws, rs, smem, wid);
  phase_attn(ws, rs, (const char*)wsp, win, wout, x, smem, wid, res, gate, cbv);
  u32x4 wr[16 - K5_R][2];
  auto nohook = []() __attribute__((always_inline)) {};
  u16 bb[2];
  if (wid >= 4) {
    proj_body<false, 1>(ws, rs, x, out, smem, wid, res, gate, cbv, nohook);
    if (wid == 7) win_agent(ws, rs, wsp, smem, wr, bsp, bb);
    else { bf16x8 b2[20]; win_prefetch(rs, wid, b2); win_body<false>(ws, rs, wsp, smem, wid, b2, wr, bsp, bb); }
#pragma unroll
    for (int i = 0; i < 3; ++i) { gate[i] = (f32x4){}; cbv[i] = (f32x4){}; }
  }
  const int w2 = fresh_s(wid);
  if (w2 < 4) {
    bf16x8 b2[20];
    proj_body<false, 0>(ws, rs, x, out, smem, w2, res, gate, cbv, [&]() __attribute__((always_inline)) { win_prefetch(rs, w2, b2); });
    win_body<true>(ws, rs, wsp, smem, w2, b2, wr, bsp, bb);
  }
  phase_sp(ws, rs, bb, smem, wid, wr, res, gate);
  if (wid < 4) proj_body<true, 0>(ws, rs, x, out, smem, wid, res, gate, cbv, nohook);
  else proj_body<true, 1>(ws, rs, x, out, smem, wid, res, gate, cbv, nohook);
  asm volatile("k_mega_code_end:");
}

extern "C" void kernel_launch(void* const* d_in, const int* in_sizes, int n_in, void* d_out, int out_size, void* d_ws, size_t ws_size,
                              hipStream_t stream) {
  const u16* x = (const u16*)d_in[0];
  const u16* wqkv = (const u16*)d_in[1];
  const u16* wo = (const u16*)d_in[2];
  const u16* win = (const u16*)d_in[4];
  const u16* wsp = (const u16*)d_in[5];
  const u16* bsp = (const u16*)d_in[6];
  const u16* wout = (const u16*)d_in[7];
  const u16* cvec = (const u16*)d_in[8];
  const u16* wmod = (const u16*)d_in[9];
  char* ws = (char*)d_ws;
  (void)hipFuncSetAttribute((const void*)k_mega, hipFuncAttributeMaxDynamicSharedMemorySize, 163840);
  k0_prep<<<K0_GRID, 256, 0, stream>>>(x, wqkv, wo, cvec, wmod, ws);
  k_mega<<<256, 512, 163840, stream>>>(ws, x, win, wsp, bsp, wout, (u16*)d_out);
}
```

```cpp
#include <hip/hip_runtime.h>
#include <hip/hip_bf16.h>
#include <stdint.h>
#include <utility>

typedef __attribute__((ext_vector_type(8))) short bf16x8;
typedef __attribute__((ext_vector_type(4))) short bf16x4;
typedef __attribute__((ext_vector_type(2))) float f32x2;
typedef __attribute__((ext_vector_type(4))) float f32x4;
typedef __attribute__((ext_vector_type(16))) float f32x16;
typedef __attribute__((ext_vector_type(2))) unsigned u32x2;
typedef __attribute__((ext_vector_type(4))) unsigned u32x4;
typedef __attribute__((ext_vector_type(2))) __bf16 bf2_t;
typedef __attribute__((ext_vector_type(2))) _Float16 h2_t;
typedef unsigned short u16;
typedef unsigned long long u64;
#define LAS __attribute__((address_space(3)))
#define GAS __attribute__((address_space(1)))
#define DI __device__ __forceinline__

constexpr int D_ = 640, NQKV = 1920, F_ = 1728;

constexpr unsigned OFF_CNT = 0;
constexpr unsigned OFF_MOD = 65536;
constexpr unsigned OFF_MODP = OFF_MOD + 16384;
constexpr unsigned OFF_CBP = OFF_MODP + 61440;
constexpr unsigned OFF_XCH = OFF_CBP + 5 * 16384;
constexpr unsigned OFF_ST1 = OFF_XCH + 131072;
constexpr unsigned OFF_XHAT = OFF_ST1 + 262144;
constexpr unsigned OFF_WQKV = OFF_XHAT + 2621440;
constexpr unsigned OFF_WO = OFF_WQKV + 2457600;
constexpr unsigned OFF_WIN = OFF_WO + 819200;
constexpr unsigned OFF_WOUT = OFF_WIN + 2211840;
constexpr unsigned OFF_Q = OFF_WOUT + 1105920;
constexpr unsigned OFF_KV = OFF_Q + 32 * 81920;
constexpr unsigned OFF_O = OFF_KV + 32 * 163840;
constexpr unsigned OFF_X1S = OFF_O + 2621440;
constexpr unsigned OFF_Z2 = OFF_X1S + 2621440;
constexpr unsigned OFF_U = OFF_Z2 + 3538944;
constexpr unsigned WS_USED = OFF_U + 3538944;
constexpr int C_QKV = 0, C_MOD = 15  , C_ATT = 128, C_REL = 136, C_X1 = 144, C_U = 176, C_Z2 = 208  ;

DI unsigned pack2(float lo, float hi) { return __builtin_bit_cast(unsigned, __builtin_convertvector((f32x2){lo, hi}, bf2_t)); }
DI float opq(float x) { asm("" : "+v"(x)); return x; }
DI f32x4 fma4s(f32x4 a, f32x4 b, f32x4 c) { return (f32x4){opq(__builtin_fmaf(a[0], b[0], c[0])), opq(__builtin_fmaf(a[1], b[1], c[1])), opq(__builtin_fmaf(a[2], b[2], c[2])), opq(__builtin_fmaf(a[3], b[3], c[3]))}; }
DI f32x4 mul4s(f32x4 a, f32x4 b) { return (f32x4){opq(a[0] * b[0]), opq(a[1] * b[1]), opq(a[2] * b[2]), opq(a[3] * b[3])}; }
DI unsigned pack2h(float lo, float hi) { return __builtin_bit_cast(unsigned, __builtin_convertvector((f32x2){lo, hi}, h2_t)); }
DI float bflo(unsigned u) { return __uint_as_float(u << 16); }
DI float bfhi(unsigned u) { return __uint_as_float(u & 0xffff0000u); }
DI float rbf(float x) { return bflo(pack2(x, x)); }
DI float bf2f(u16 b) { return __uint_as_float((unsigned)b << 16); }
#define MFMA32(a, b, c) __builtin_amdgcn_mfma_f32_32x32x16_bf16((a), (b), (c), 0, 0, 0)
#define MFMA32K8(a, b, c) __builtin_amdgcn_mfma_f32_32x32x8bf16_1k((a), (b), (c), 0, 0, 0)
#define MFMA16(a, b, c) __builtin_amdgcn_mfma_f32_16x16x32_bf16((a), (b), (c), 0, 0, 0)
#define BAR() asm volatile("s_waitcnt lgkmcnt(0)\n\ts_barrier" ::: "memory")
#define SCHED() __builtin_amdgcn_sched_barrier(0)
#define PIN(x) asm volatile("" : "+v"(x))
#define OCC2 __attribute__((amdgpu_waves_per_eu(2, 2)))
#define BF8(x) __builtin_bit_cast(bf16x8, x)
#define BF4(x) __builtin_bit_cast(bf16x4, x)
template <int N> DI void waitv() { asm volatile("s_waitcnt vmcnt(%0)" ::"i"(N) : "memory"); }
DI void drain() { waitv<0>(); __builtin_amdgcn_s_waitcnt(0x0F70); }
template <class F, int... I> DI void sfor_impl(F&& f, std::integer_sequence<int, I...>) { (f(std::integral_constant<int, I>{}), ...); }
template <int N, class F> DI void sfor(F&& f) { sfor_impl(f, std::make_integer_sequence<int, N>{}); }
typedef short v4i16_t __attribute__((ext_vector_type(4)));
DI bf16x4 lds_tr(const char* p) { return __builtin_bit_cast(bf16x4, __builtin_amdgcn_ds_read_tr16_b64_v4i16((LAS v4i16_t*)p)); }
DI void glds16(const void* g, unsigned lds_base) {
  unsigned sv;
  asm volatile("s_mov_b32 %0, m0\n\ts_mov_b32 m0, %2\n\ts_nop 0\n\tglobal_load_lds_dwordx4 %1, off\n\ts_mov_b32 m0, %0" : "=&s"(sv) : "v"(g), "s"(lds_base) : "memory");
}
DI void glds16c(const void* g, unsigned lds_base) {
  unsigned sv;
  asm volatile("s_mov_b32 %0, m0\n\ts_mov_b32 m0, %2\n\ts_nop 0\n\tglobal_load_lds_dwordx4 %1, off sc1\n\ts_mov_b32 m0, %0" : "=&s"(sv) : "v"(g), "s"(lds_base) : "memory");
}
DI void glds4c(const void* g, unsigned lds_base) {
  unsigned sv;
  asm volatile("s_mov_b32 %0, m0\n\ts_mov_b32 m0, %2\n\ts_nop 0\n\tglobal_load_lds_dword %1, off sc1\n\ts_mov_b32 m0, %0" : "=&s"(sv) : "v"(g), "s"(lds_base) : "memory");
}
typedef __amdgpu_buffer_rsrc_t rsrc_t;
DI u32x4 ldc16(rsrc_t rs, unsigned voff, unsigned soff) { return __builtin_amdgcn_raw_buffer_load_b128(rs, voff, soff, 16); }
DI u32x2 ldc8(rsrc_t rs, unsigned voff, unsigned soff) { return __builtin_amdgcn_raw_buffer_load_b64(rs, voff, soff, 16); }
DI unsigned ldc4(rsrc_t rs, unsigned voff, unsigned soff) { return __builtin_amdgcn_raw_buffer_load_b32(rs, voff, soff, 16); }
DI void stc16(rsrc_t rs, unsigned voff, unsigned soff, u32x4 v) { __builtin_amdgcn_raw_buffer_store_b128(v, rs, voff, soff, 16); asm volatile("s_nop 1" ::"v"(v) : "memory"); }
DI void stc8(rsrc_t rs, unsigned voff, unsigned soff, u32x2 v) { __builtin_amdgcn_raw_buffer_store_b64(v, rs, voff, soff, 16); }
DI void stc4(rsrc_t rs, unsigned voff, unsigned soff, unsigned v) { __builtin_amdgcn_raw_buffer_store_b32(v, rs, voff, soff, 16); }
DI f32x4 ldc16f(rsrc_t rs, unsigned voff, unsigned soff) { return __builtin_bit_cast(f32x4, ldc16(rs, voff, soff)); }
DI void arrive(char* ws, int c, int lane) {
  unsigned o = c * 256; asm("" : "+v"(o));
  if (lane == 0) __hip_atomic_fetch_add((GAS unsigned*)(ws + OFF_CNT + o), 1u, __ATOMIC_RELAXED, __HIP_MEMORY_SCOPE_AGENT);
}
DI void poll_ge(char* ws, int c, unsigned thr) {
  const GAS unsigned* p = (const GAS unsigned*)(ws + OFF_CNT + c * 256);
#pragma nounroll
  for (unsigned spin = 0; spin < (1u << 19); ++spin) {
    if (__all(__hip_atomic_load(p, __ATOMIC_RELAXED, __HIP_MEMORY_SCOPE_AGENT) >= thr)) break;
    __builtin_amdgcn_s_sleep(1);
  }
  asm volatile("" ::: "memory");
}
DI int fresh_s(int v) { asm volatile("" : "+s"(v)); return v; }
DI int fresh_tid() { int t = threadIdx.x; asm volatile("" : "+v"(t)); return t; }
DI void l2touch(const void* g, unsigned lds_scratch) {
  unsigned sv;
  asm volatile("s_mov_b32 %0, m0\n\ts_mov_b32 m0, %2\n\ts_nop 0\n\tglobal_load_lds_dword %1, off\n\ts_mov_b32 m0, %0" : "=&s"(sv) : "v"(g), "s"(lds_scratch) : "memory");
}
#define LDNT(p) __builtin_nontemporal_load(p)
DI float silu_bf(u16 c) { const float f = __uint_as_float((unsigned)c << 16); const float v = f / (1.f + __expf(-f)); return __uint_as_float(__builtin_bit_cast(unsigned, __builtin_convertvector((f32x2){v, v}, bf2_t)) << 16); }
DI unsigned lds_addr(const void* p) { return (unsigned)(uintptr_t)(const LAS void*)p; }
DI float xhalf(float v) {
  auto rr = __builtin_amdgcn_permlane32_swap(__float_as_uint(v), __float_as_uint(v), false, false);
  return __uint_as_float((threadIdx.x & 32) ? rr[0] : rr[1]);
}
DI float sum_g(float v) {
  auto a = __builtin_amdgcn_permlane16_swap(__float_as_uint(v), __float_as_uint(v), false, false);
  v = __uint_as_float(a[0]) + __uint_as_float(a[1]);
  auto b = __builtin_amdgcn_permlane32_swap(__float_as_uint(v), __float_as_uint(v), false, false);
  return __uint_as_float(b[0]) + __uint_as_float(b[1]);
}
template <int CTRL> DI float dpp_add(float v) { return v + __uint_as_float(__builtin_amdgcn_update_dpp(0u, __float_as_uint(v), CTRL, 0xf, 0xf, true)); }
DI float sum16(float v) {
  v = dpp_add<0x128>(v); v = dpp_add<0x124>(v); v = dpp_add<0x122>(v); return dpp_add<0x121>(v);
}
DI u32x4 modulate8(u32x4 v, f32x4 sc0, f32x4 sc1, f32x4 sh0, f32x4 sh1) {
  unsigned o0, o1, o2, o3; float t0, t1, t2, t3;
  asm volatile(
      "v_fma_mix_f32 %0, %8, %12, %20 op_sel_hi:[1,0,0]\n\tv_fma_mix_f32 %4, %8, %13, %21 op_sel:[1,0,0] op_sel_hi:[1,0,0]\n\t"
      "v_fma_mix_f32 %1, %9, %14, %22 op_sel_hi:[1,0,0]\n\tv_fma_mix_f32 %5, %9, %15, %23 op_sel:[1,0,0] op_sel_hi:[1,0,0]\n\t"
      "v_fma_mix_f32 %2, %10, %16, %24 op_sel_hi:[1,0,0]\n\tv_fma_mix_f32 %6, %10, %17, %25 op_sel:[1,0,0] op_sel_hi:[1,0,0]\n\t"
      "v_fma_mix_f32 %3, %11, %18, %26 op_sel_hi:[1,0,0]\n\tv_fma_mix_f32 %7, %11, %19, %27 op_sel:[1,0,0] op_sel_hi:[1,0,0]\n\t"
      "v_cvt_pk_bf16_f32 %0, %0, %4\n\tv_cvt_pk_bf16_f32 %1, %1, %5\n\tv_cvt_pk_bf16_f32 %2, %2, %6\n\tv_cvt_pk_bf16_f32 %3, %3, %7"
      : "=&v"(o0), "=&v"(o1), "=&v"(o2), "=&v"(o3), "=&v"(t0), "=&v"(t1), "=&v"(t2), "=&v"(t3)
      : "v"(v[0]), "v"(v[1]), "v"(v[2]), "v"(v[3]), "v"(sc0[0]), "v"(sc0[1]), "v"(sc0[2]), "v"(sc0[3]), "v"(sc1[0]), "v"(sc1[1]), "v"(sc1[2]), "v"(sc1[3]),
        "v"(sh0[0]), "v"(sh0[1]), "v"(sh0[2]), "v"(sh0[3]), "v"(sh1[0]), "v"(sh1[1]), "v"(sh1[2]), "v"(sh1[3]));
  return (u32x4){o0, o1, o2, o3};
}
DI float ln_den(float var) { return rbf(__builtin_amdgcn_sqrtf(rbf(rbf(var) + 1e-5f))); }
DI int blk_panel() { return ((blockIdx.x & 7) >> 1) * 8 + (blockIdx.x >> 5); }
DI int blk_cb() { return (blockIdx.x & 1) * 4 + ((blockIdx.x >> 3) & 3); }
DI int unit0_of(int j) { return j < 6 ? 7 * j : 42 + 6 * (j - 6); }

DI void K0ST(char* p, u32x4 v) { *(u32x4*)p = v; }
constexpr int K0_MOD = 240, K0_LN = 128, K0_RL = 240, K0_WO = 50, K0_INIT = 12, K0_WARM = 24;
constexpr int K0_GRID = K0_WARM + K0_MOD + K0_LN + K0_RL + K0_WO + K0_INIT;

__global__ __launch_bounds__(256) void k0_prep(const u16* __restrict__ x, const u16* __restrict__ wqkv, const u16* __restrict__ wo, const u16* __restrict__ cvec,
                                               const u16* __restrict__ wmod, char* __restrict__ ws) {
  __shared__ __attribute__((aligned(16))) char smem[4 * 4608];
  const int tid = threadIdx.x, lane = tid & 63, wid = __builtin_amdgcn_readfirstlane(tid >> 6);
  int b = blockIdx.x;
  if (b < K0_WARM) {
    u64 pc; unsigned len;
    asm volatile("s_getpc_b64 s[30:31]\n\ts_add_u32 s30, s30, k_mega@rel32@lo+4\n\ts_addc_u32 s31, s31, k_mega@rel32@hi+12\n\ts_mov_b64 %0, s[30:31]\n\t"
                 "s_mov_b32 %1, k_mega_code_end-k_mega" : "=s"(pc), "=s"(len) :: "scc", "s30", "s31");
    l2touch((const char*)pc + min(((b >> 3) * 256 + tid) * 128u, len - 4u), lds_addr(smem) + wid * 256);
    return;
  }
  b -= K0_WARM;
  if (b < K0_MOD) {
    const int cg = b >> 2, ks = b & 3, c = tid & 7, row0 = ks * 160 + (tid >> 3), col0 = cg * 64;
    u32x4 w[5]; u16 cv[5];
#pragma unroll
    for (int i = 0; i < 5; ++i) { w[i] = LDNT((const u32x4*)(wmod + (size_t)(row0 + 32 * i) * 3840 + col0 + c * 8)); cv[i] = cvec[row0 + 32 * i]; }
    float acc[8] = {0, 0, 0, 0, 0, 0, 0, 0};
#pragma unroll
    for (int i = 0; i < 5; ++i) {
      const float s = silu_bf(cv[i]);
#pragma unroll
      for (int e = 0; e < 4; ++e) { acc[2 * e] += s * bflo(w[i][e]); acc[2 * e + 1] += s * bfhi(w[i][e]); }
    }
#pragma unroll
    for (int e = 0; e < 8; ++e) acc[e] = sum_g(dpp_add<0x128>(acc[e]));
    float* red = (float*)smem;
    if (lane < 8) { *(f32x4*)(red + wid * 64 + lane * 8) = (f32x4){acc[0], acc[1], acc[2], acc[3]}; *(f32x4*)(red + wid * 64 + lane * 8 + 4) = (f32x4){acc[4], acc[5], acc[6], acc[7]}; }
    __syncthreads();
    if (tid < 64) ((float*)(ws + OFF_MODP))[ks * 3840 + cg * 64 + tid] = (red[tid] + red[64 + tid]) + (red[128 + tid] + red[192 + tid]);
    return;
  }
  b -= K0_MOD;
  if (b < K0_LN) {
    b = (b & 7) * 16 + (b >> 3);
    const int row = b * 16 + wid * 4 + (lane >> 4), c = lane & 15;
    const u16* xr = x + (size_t)row * D_ + c * 8;
    u32x4 v[5];
#pragma unroll
    for (int i = 0; i < 5; ++i) v[i] = *(const u32x4*)(xr + i * 128);
    float s = 0.f, q = 0.f;
#pragma unroll
    for (int i = 0; i < 5; ++i)
#pragma unroll
      for (int e = 0; e < 4; ++e) s += bflo(v[i][e]) + bfhi(v[i][e]);
    const float mean = sum16(s) * (1.f / 640.f);
#pragma unroll
    for (int i = 0; i < 5; ++i)
#pragma unroll
      for (int e = 0; e < 4; ++e) { const float a = bflo(v[i][e]) - mean, d = bfhi(v[i][e]) - mean; q += a * a + d * d; }
    const float var = sum16(q) * (1.f / 640.f);
    const float rinv = __builtin_amdgcn_rcpf(ln_den(var)), mb = rbf(mean);
    char* dst = ws + OFF_XHAT + (size_t)row * 1280 + c * 16;
#pragma unroll
    for (int i = 0; i < 5; ++i) {
      u32x4 o;
#pragma unroll
      for (int e = 0; e < 4; ++e) o[e] = pack2h((bflo(v[i][e]) - mb) * rinv, (bfhi(v[i][e]) - mb) * rinv);
      K0ST(dst + i * 256, o);
    }
    return;
  }
  b -= K0_LN;
  if (b < K0_RL) {
    const int kt = b / 12, G = b % 12, sec = G >> 2, gq = G & 3;
    u32x4 v[3];
#pragma unroll
    for (int i = 0; i < 3; ++i) { const int ci = min(i * 256 + tid, 639); v[i] = LDNT((const u32x4*)(wqkv + (size_t)(kt * 32 + ci / 20) * NQKV + G * 160 + (ci % 20) * 8)); }
#pragma unroll
    for (int i = 0; i < 3; ++i) { const int ci = min(i * 256 + tid, 639); *(u32x4*)(smem + (ci / 20) * 336 + (ci % 20) * 16) = v[i]; }
    __syncthreads();
    const int g = lane >> 4, q = (lane & 15) >> 2, p = lane & 3;
    for (int f = wid; f < 10; f += 4) {
      const int t = f >> 1, x2 = f & 1;
      const int col = t == 4 ? 20 * (4 * x2 + p) + 16 : sec == 2 ? 20 * f + 4 * p : 20 * (2 * t + (p >> 1)) + 8 * (p & 1) + 4 * x2;
      const int T = sec * 20 + (t < 4 ? 4 * gq + t : 16 + gq);
      const char* pp = smem + (8 * g + q) * 336 + col * 2;
      const bf16x4 lo = lds_tr(pp), hi = lds_tr(pp + 4 * 336);
      K0ST(ws + OFF_WQKV + (((size_t)T * 20 + kt) * 2 + x2) * 1024 + lane * 16, __builtin_bit_cast(u32x4, (bf16x8)__builtin_shufflevector(lo, hi, 0, 1, 2, 3, 4, 5, 6, 7)));
    }
    return;
  }
  b -= K0_RL;
  if (b < K0_WO) {
    const int job = b * 4 + wid, kt = job / 10, ng = job % 10, j8 = lane >> 3;
    char* t = smem + wid * 4608;
    u32x4 v[4];
#pragma unroll
    for (int i = 0; i < 4; ++i) {
      const int c = 4 * kt + i, row = c < 64 ? 20 * (c >> 1) + 4 * (c & 1) + (j8 & 3) + 8 * (j8 >> 2) : 20 * (2 * (c - 64) + (j8 >> 2)) + 16 + (j8 & 3);
      v[i] = LDNT((const u32x4*)(wo + (size_t)row * D_ + ng * 64 + (lane & 7) * 8));
    }
#pragma unroll
    for (int i = 0; i < 4; ++i) *(u32x4*)(t + (i * 8 + j8) * 144 + (lane & 7) * 16) = v[i];
    const char* pp = t + (8 * (lane >> 4) + ((lane & 15) >> 2)) * 144 + (lane & 3) * 8;
#pragma unroll
    for (int f = 0; f < 4; ++f) {
      const bf16x4 lo = lds_tr(pp + f * 32), hi = lds_tr(pp + 4 * 144 + f * 32);
      K0ST(ws + OFF_WO + ((size_t)(ng * 4 + f) * 20 + kt) * 1024 + lane * 16, __builtin_bit_cast(u32x4, (bf16x8)__builtin_shufflevector(lo, hi, 0, 1, 2, 3, 4, 5, 6, 7)));
    }
    return;
  }
  b -= K0_WO;
  {
    u32x4* p = (u32x4*)(ws + (b < 8 ? OFF_XCH + b * 16384 : OFF_CNT + (b - 8) * 16384)) + tid;
    const unsigned f = b < 8 ? ~0u : 0u;
#pragma unroll
    for (int i = 0; i < 4; ++i) p[i * 256] = (u32x4){f, f, f, f};
  }
}

constexpr float QSCALE = 0.22360679774997896f * 1.4426950408889634f;
constexpr int K1_W = 3, K1_WB = 5, K1_WB0 = 1;

template <bool ISV, class FIN>
DI void k1_mma(const char* __restrict__ bsrc, const char* smem, int lane, FIN&& fin) {
  f32x4 acc[8][2];
  constexpr int PD = 4;
  bf16x8 bfr[K1_WB + 1][4], af[PD + 1][2];
  const char* lds[2];
#pragma unroll
  for (int k = 0; k < 2; ++k) lds[k] = smem + k * 8192 + (lane ^ (4 * k + (lane >> 4))) * 16;
#pragma unroll
  for (int i = 0; i < 4 * K1_WB0; ++i) bfr[i >> 2][i & 3] = *(const bf16x8*)(bsrc + i * 1024);
#pragma unroll
  for (int m = 0; m < 8; ++m) { acc[m][0] = (f32x4){}; acc[m][1] = (f32x4){}; }
  BAR();
#pragma unroll
  for (int i = 4 * K1_WB0; i < 4 * K1_WB; ++i) bfr[i >> 2][i & 3] = *(const bf16x8*)(bsrc + i * 1024);
  BAR();
  __builtin_amdgcn_s_setprio(1);
#define K1_RD(t, k) (af[(t) % (PD + 1)][k] = *(const bf16x8*)(lds[k] + (((t) >> 3) % 3) * 16384 + ((t) & 7) * 1024))
#define K1_MM(c, b, a) (c = ISV ? MFMA16(a, b, c) : MFMA16(b, a, c))
#pragma unroll
  for (int t = 0; t < PD; ++t) { K1_RD(t, 0); K1_RD(t, 1); }
  SCHED();
  sfor<80>([&](auto T) __attribute__((always_inline)) {
    constexpr int t = T, s = t >> 3, m = t & 7, nx = t + PD;
    if constexpr (nx < 80 && (nx & 7) == 0) asm volatile("s_barrier" ::: "memory");
    if constexpr (nx < 80) K1_RD(nx, 0);
    K1_MM(acc[m][0], bfr[s % (K1_WB + 1)][2], af[t % (PD + 1)][1]); K1_MM(acc[m][0], bfr[s % (K1_WB + 1)][0], af[t % (PD + 1)][0]); PIN(acc[m][0]);
    SCHED();
    if constexpr (nx < 80) K1_RD(nx, 1);
    if constexpr (s + K1_WB < 10 && (m & 1)) bfr[(s + K1_WB) % (K1_WB + 1)][m >> 1] = *(const bf16x8*)(bsrc + ((s + K1_WB) * 4 + (m >> 1)) * 1024);
    K1_MM(acc[m][1], bfr[s % (K1_WB + 1)][1], af[t % (PD + 1)][0]); K1_MM(acc[m][1], bfr[s % (K1_WB + 1)][3], af[t % (PD + 1)][1]); PIN(acc[m][1]);
    SCHED();
    if constexpr (t >= 74) { fin(t - 74, acc[t - 74]); SCHED(); }
  });
  fin(6, acc[6]); fin(7, acc[7]);
  __builtin_amdgcn_s_setprio(0);
}


DI void phase_qkv(char* __restrict__ ws, rsrc_t rs, char* smem, int wid) {
  const int tid = fresh_tid(), lane = tid & 63;
  const int xcd = blockIdx.x & 7, idx = blockIdx.x >> 3;
  int ctr;
  if ((xcd & 1) && idx >= 28) {
    ctr = C_MOD;
    if (tid < 160) {
      const int col = 1280 + ((xcd >> 1) * 4 + idx - 28) * 160 + tid;
      const float* pp = (const float*)(ws + OFF_MODP) + col;
      float v = rbf((pp[0] + pp[3840]) + (pp[7680] + pp[11520]));
      if (col / 640 == 4) v = rbf(1.f + v);
      stc4(rs, col * 4, OFF_MOD, __float_as_uint(v));
    }
  } else {
    const int mt = (xcd >> 1) * 4 + (idx & 3), nt = (xcd & 1) * 8 + (idx >> 2);
    ctr = C_QKV + nt;
    if (wid < 4) {
      float* cst = (float*)(smem + 49152);
      const int c = lane & 7, rr = lane >> 3;
      const char* src = ws + OFF_XHAT + (size_t)(mt * 128 + wid * 32 + rr) * 1280 + c * 16;
      char* wdst = smem + (c >> 2) * 8192 + wid * 2048 + (c & 3) * 256 + (rr ^ c) * 16;
      u32x4 a[10][4];
      f32x4 c0[4], c1[4];
#pragma unroll
      for (int i = 0; i < 4; ++i) { c0[i] = *(const f32x4*)(ws + OFF_MODP + i * 15360 + tid * 16); if (wid == 0) c1[i] = *(const f32x4*)(ws + OFF_MODP + i * 15360 + 4096 + tid * 16); }
#pragma unroll
      for (int s = 0; s < K1_W; ++s)
#pragma unroll
        for (int m = 0; m < 4; ++m) a[s][m] = *(const u32x4*)(src + m * 10240 + s * 128);
      {
        f32x4 v = (c0[0] + c0[1]) + (c0[2] + c0[3]); const float one = tid >= 160 ? 1.f : 0.f;
        *(f32x4*)(cst + tid * 4) = (f32x4){rbf(one + rbf(v[0])), rbf(one + rbf(v[1])), rbf(one + rbf(v[2])), rbf(one + rbf(v[3]))};
        if (wid == 0) { v = (c1[0] + c1[1]) + (c1[2] + c1[3]); *(f32x4*)(cst + 1024 + tid * 4) = (f32x4){rbf(1.f + rbf(v[0])), rbf(1.f + rbf(v[1])), rbf(1.f + rbf(v[2])), rbf(1.f + rbf(v[3]))}; }
      }
      BAR();
      const float* cp = cst + c * 8;
      f32x4 kc[10][4];
#define K1_KC(s) do { kc[s][0] = *(const f32x4*)(cp + (s) * 64); kc[s][1] = *(const f32x4*)(cp + (s) * 64 + 4); kc[s][2] = *(const f32x4*)(cp + (s) * 64 + 640); kc[s][3] = *(const f32x4*)(cp + (s) * 64 + 644); } while (0)
#pragma unroll
      for (int s = 0; s < 6; ++s) K1_KC(s);
      SCHED();
      u32x4 o[2][4];
#define K1_XF(s, m) (o[(s) & 1][m] = modulate8(a[s][m], kc[s][2], kc[s][3], kc[s][0], kc[s][1]))
#define K1_WR(s, m) (*(u32x4*)(wdst + ((s) % 3) * 16384 + ((m) >> 1) * 1024 + ((m) & 1) * 128) = o[(s) & 1][m])
#pragma unroll
      for (int m = 0; m < 4; ++m) { K1_XF(0, m); K1_WR(0, m); }
      BAR();
#pragma unroll
      for (int m = 0; m < 4; ++m) a[K1_W][m] = *(const u32x4*)(src + m * 10240 + K1_W * 128);
#pragma unroll
      for (int m = 0; m < 4; ++m) K1_XF(1, m);
      SCHED();
#pragma unroll
      for (int s = 1; s < 10; ++s) {
#pragma unroll
        for (int m = 0; m < 4; ++m) {
          if (m == 0) K1_WR(s, 0);
          if (m < 3) K1_WR(s, m + 1);
          if (s + K1_W < 10) a[s + K1_W][m] = *(const u32x4*)(src + m * 10240 + (s + K1_W) * 128);
          if (s < 5) kc[s + 5][m] = *(const f32x4*)(cp + (s + 5) * 64 + (m & 1) * 4 + (m >> 1) * 640);
          SCHED();
          if (s < 9) K1_XF(s + 1, m);
          SCHED();
        }
        asm volatile("" :: "v"(o[s & 1][0]), "v"(o[s & 1][1]), "v"(o[s & 1][2]), "v"(o[s & 1][3]));
        BAR();
      }
    } else {
      const int cw = wid - 4;
      const char* bsrc = ws + OFF_WQKV + (size_t)(nt * 4 + cw) * 40 * 1024 + lane * 16;
      const int r = lane & 15, g = lane >> 4;
      if (nt >= 10) {
        const unsigned so = OFF_KV + (mt >> 1) * 20480 + 10240 + (mt & 1) * 5120;
        unsigned vo[2];
#pragma unroll
        for (int x = 0; x < 2; ++x)
          vo[x] = (nt < 14 ? ((nt - 10) * 8 + cw * 2 + x) * 163840u + ((g & 1) * 16 + r) * 16 : (cw * 8 + x * 4 + (r >> 2)) * 163840u + 512 + ((g & 1) * 4 + (r & 3)) * 16) + (g >> 1) * 8;
        k1_mma<true>(bsrc, smem, lane, [&](int m, const f32x4 (&a)[2]) __attribute__((always_inline)) {
#pragma unroll
          for (int x = 0; x < 2; ++x) stc8(rs, vo[x] + (m & 3) * 640, so + (m >> 2) * 2560, (u32x2){pack2(a[x][0], a[x][1]), pack2(a[x][2], a[x][3])});
        });
      } else {
        const bool isQ = nt < 5; const float sc = isQ ? QSCALE : 1.f;
        const unsigned so = isQ ? OFF_Q + mt * 5120 : OFF_KV + (mt >> 1) * 20480 + (mt & 1) * 5120, hs = isQ ? 81920u : 163840u;
        const int n5 = isQ ? nt : nt - 5;
        const bool wide = n5 < 4;
        const unsigned vo = wide ? (2 * (n5 * 4 + cw) + (g >> 1)) * hs + (g & 1) * 512 + r * 16 : (8 * cw + g) * hs + 1024 + r * 8;
        const f32x4 sc4 = (f32x4){sc, sc, sc, sc};
        k1_mma<false>(bsrc, smem, lane, [&](int m, const f32x4 (&a)[2]) __attribute__((always_inline)) {
          const f32x4 v0 = mul4s(a[0], sc4), v1 = mul4s(a[1], sc4);
          const u32x4 w = (u32x4){pack2(v0[0], v0[1]), pack2(v0[2], v0[3]), pack2(v1[0], v1[1]), pack2(v1[2], v1[3])};
          if (wide) stc16(rs, vo + (m & 1) * 256, so + (m >> 1) * 1280, w);
          else { stc8(rs, vo + (m & 1) * 128, so + (m >> 1) * 1280, (u32x2){w[0], w[1]}); stc8(rs, vo + (m & 1) * 128, so + 4 * hs + (m >> 1) * 1280, (u32x2){w[2], w[3]}); }
        });
      }
    }
  }
  waitv<0>();
  BAR();
  if (wid == 7 && lane < 8) __hip_atomic_fetch_add((GAS unsigned*)(ws + OFF_CNT + (lane * 16 + ctr) * 256), 1u, __ATOMIC_RELAXED, __HIP_MEMORY_SCOPE_AGENT);
  if (ctr == C_MOD && wid == 7) { __builtin_amdgcn_s_sleep(127); __builtin_amdgcn_s_sleep(60); }
}

constexpr int proj_pos(int f) { return 1024 + f * 1024; }
template <bool FINAL> DI u32x4 proj_ld(rsrc_t rs, int i, int wid, int lane) {
  constexpr int NF = FINAL ? 135 : 100;
  return ldc16(rs, lane * 16, (FINAL ? OFF_WOUT : OFF_WO) + (blk_cb() * NF + (i * 8 + 7 < NF ? i * 8 + wid : min(i * 8 + wid, NF - 1))) * 1024);
}
template <bool FINAL> DI void proj_small(rsrc_t rs, const u16* __restrict__ x, int wid, int lane, u32x2 (&res)[3], f32x4 (&gate)[3], f32x4 (&cbv)[3]) {
  const int mt = blk_panel() * 4 + (wid & 3), g = lane >> 4;
#pragma unroll
  for (int i = 0; i < 3; ++i) {
    const int n = (blk_cb() * 5 + min((wid >> 2) * 3 + i, 4)) * 16;
    if (!FINAL) { res[i] = *(const u32x2*)(x + (size_t)(mt * 16 + (lane & 15)) * D_ + n + g * 4); cbv[i] = ldc16f(rs, g * 16, OFF_MOD + (4 * 640 + n) * 4); }
    gate[i] = ldc16f(rs, g * 16, OFF_MOD + ((FINAL ? 5 : 2) * 640 + n) * 4);
  }
}
DI void proj_park(char* smem, int i, int tid, u32x4 v) { *(u32x4*)(smem + proj_pos(i * 8) + tid * 16) = v; }

DI void split3(float m, unsigned& w_h0, unsigned& w_h1) {
  const float n = -m; const float a = rbf(n), b = rbf(n - a), c = rbf(n - a - b);
  w_h0 = pack2(a, b); w_h1 = pack2(c, 0.f);
}

DI void phase_attn(char* __restrict__ ws, rsrc_t rs, const char* __restrict__ wsp, const u16* __restrict__ win, const u16* __restrict__ wout, const u16* __restrict__ x,
                   char* smem, int wid, u32x2 (&res)[3], f32x4 (&gate)[3], f32x4 (&cbv)[3]) {
  const int tid = fresh_tid(), lane = tid & 63;
  const int xcd = blockIdx.x & 7, idx = blockIdx.x >> 3, head = xcd * 4 + (idx >> 3), qb = idx & 7;
  const int r = lane & 31, h = lane >> 5, qg = wid & 3, kh = wid >> 2;
  unsigned kvo = OFF_KV + head * 163840, qo = OFF_Q + head * 81920 + (qb * 8 + qg * 2) * 1280;
  const unsigned kconst = h == 0 ? 0x3f803f80u : 0x00003f80u;
  const unsigned so16 = (tid & 255) * 16, l16 = lane * 16, l2 = 1024 + r * 8 + h * 4;
  u32x4 wq[13];
  for (int attempt = 0; attempt < 2; ++attempt) {
    asm volatile("" : "+s"(kvo), "+s"(qo));
    const char* kvs = ws + kvo + kh * 81920 + so16;
    const unsigned ldst = lds_addr(smem) + kh * 81920 + qg * 1024;
    u32x4 qA1w, qB1w, k1w; unsigned qA2w, qB2w, k2, m0sv;
    const char* d0 = kvs + (wid == 0 ? 4096 : 0); const char* d1 = kvs + 4096; const char* d2 = kvs + 8192; const char* d3 = kvs + 12288; const char* d4 = kvs + 16384;
    PIN(d0); PIN(d1); PIN(d2); PIN(d3); PIN(d4);
    if (attempt == 0) {
      if (wid == 7) {
        const int l7 = lane & 7;
        const int c = l7 == 0 ? head >> 3 : l7 == 1 ? 4 : l7 == 2 ? 5 + (head >> 3) : l7 == 3 ? 9 : l7 == 4 ? 10 + (head >> 3) : l7 == 5 ? 14 : C_MOD;
        poll_ge(ws, xcd * 16 + c, 16);
      }
      BAR();
    }
    asm volatile(
        "s_nop 4\n\tbuffer_load_dwordx4 %0, %7, %9, %19 offen sc1\n\tbuffer_load_dwordx4 %1, %7, %9, %10 offen sc1\n\tbuffer_load_dwordx4 %2, %7, %9, %11 offen sc1\n\t"
        "buffer_load_dword %3, %8, %9, %19 offen sc1\n\tbuffer_load_dword %4, %8, %9, %10 offen sc1\n\tbuffer_load_dword %5, %8, %9, %11 offen sc1\n\t"
        "s_mov_b32 %6, m0\n\ts_mov_b32 m0, %17\n\ts_nop 0\n\tglobal_load_lds_dwordx4 %12, off sc1\n\t"
        "s_add_u32 m0, %18, 0x1000\n\ts_nop 0\n\tglobal_load_lds_dwordx4 %13, off sc1\n\t"
        "s_add_u32 m0, %18, 0x2000\n\ts_nop 0\n\tglobal_load_lds_dwordx4 %14, off sc1\n\t"
        "s_add_u32 m0, %18, 0x3000\n\ts_nop 0\n\tglobal_load_lds_dwordx4 %15, off sc1\n\t"
        "s_add_u32 m0, %18, 0x4000\n\ts_nop 0\n\tglobal_load_lds_dwordx4 %16, off sc1\n\t"
        "s_mov_b32 m0, %6\n\ts_waitcnt vmcnt(5)"
        : "=&v"(qA1w), "=&v"(qB1w), "=&v"(k1w), "=&v"(qA2w), "=&v"(qB2w), "=&v"(k2), "=&s"(m0sv)
        : "v"(l16), "v"(l2), "s"(rs), "s"(qo + kh * 1280), "s"(kvo), "v"(d0), "v"(d1), "v"(d2), "v"(d3), "v"(d4),
          "s"(ldst + (wid == 0 ? 4096 : 0)), "s"(ldst), "s"(qo + (kh ^ 1) * 1280)
        : "memory", "scc");
    if (wid == 0) { const unsigned f = (fresh_tid() & 1) ? 0u : 0x3f803f80u; *(u32x4*)(smem + l16) = (u32x4){f, f, f, f}; }
    const bf16x8 qA1 = BF8(qA1w), qB1 = BF8(qB1w);
    bf16x8 k1 = BF8(k1w);
    float mA = -INFINITY, mB = -INFINITY;
    {
      const bf16x4 qA2 = BF4(((u32x2){qA2w, 0u})), qB2 = BF4(((u32x2){qB2w, 0u}));
      const int nt = attempt ? 64 : 1;
      bf16x8 t1 = k1; unsigned t2 = k2;
      for (int t = 0; t < nt; ++t) {
        if (t) { const unsigned to = kvo + (t >> 3) * 20480 + (t & 7) * 1280; t1 = BF8(ldc16(rs, l16, to)); t2 = ldc4(rs, l2, to); }
        const bf16x4 t2v = BF4(((u32x2){t2, kconst}));
        f32x16 a = MFMA32(t1, qA1, (f32x16){}); a = MFMA32K8(t2v, qA2, a);
        f32x16 b = MFMA32(t1, qB1, (f32x16){}); b = MFMA32K8(t2v, qB2, b);
#pragma unroll
        for (int i = 0; i < 16; ++i) { mA = fmaxf(mA, a[i]); mB = fmaxf(mB, b[i]); }
      }
      mA = fmaxf(mA, xhalf(mA)); mB = fmaxf(mB, xhalf(mB));
    }
    unsigned a0, a1, b0, b1; split3(mA, a0, a1); split3(mB, b0, b1);
    const bf16x4 qA2 = BF4(((u32x2){qA2w, h ? a1 : a0})), qB2 = BF4(((u32x2){qB2w, h ? b1 : b0}));
    char* lh = smem + kh * 81920;
    waitv<0>();
    BAR();
    f32x16 oA = (f32x16){}, oB = (f32x16){};
    const char* kp1 = lh + l16;
    const char* kp2 = lh + l2;
    const bool vreal = r < 20;
    const char* vp = vreal ? lh + 10240 + (r < 16 ? h * 16 + r : 16 + h * 4 + r) * 16 : smem + (r == 20 ? 0 : 16);
    const int vinc = vreal ? 1280 : 0, vjump = vreal ? 10240 : 0;
    if (kh) { k1 = *(const bf16x8*)kp1; k2 = *(const unsigned*)kp2; }
    bf16x8 kf1[2], vf[2][2]; u32x2 kf2[2];
    kf1[0] = k1; kf2[0] = (u32x2){k2, kconst}; kf2[1] = (u32x2){0u, kconst};
    vf[1][0] = (bf16x8){}; vf[1][1] = (bf16x8){};
    u32x4 pA0, pA1, pB0 = (u32x4){}, pB1 = (u32x4){};
    f32x4 e0 = (f32x4){}, e1;
    f32x16 sA = MFMA32(kf1[0], qA1, (f32x16){}), sB;
    sA = MFMA32K8(BF4(kf2[0]), qA2, sA);
#define EX2(x) __builtin_amdgcn_exp2f(x)
#define GRP(MF, S, i, EN, EP, P, w) do { MF; SCHED(); EN[0] = EX2(S[i]); P[w] = pack2(EP[0], EP[1]); EN[1] = EX2(S[i + 1]); P[w + 1] = pack2(EP[2], EP[3]); \
      EN[2] = EX2(S[i + 2]); EN[3] = EX2(S[i + 3]); PIN(P); PIN(EN); SCHED(); } while (0)
    const unsigned relsc = lds_addr(smem) + 768;
    auto group = [&](auto FIRST, int jump, int j) __attribute__((always_inline)) {
#pragma unroll
      for (int t = 0; t < 8; ++t) {
        const int c = t & 1, n = c ^ 1;
        if (FIRST) { if (t == 0) __builtin_amdgcn_s_setprio(3); if (t == 3) __builtin_amdgcn_s_setprio(2); if (t == 5) __builtin_amdgcn_s_setprio(1); if (t == 6) __builtin_amdgcn_s_setprio(0); if (t == 7) __builtin_amdgcn_s_setprio(3); }
        else {
          if (t == 4 && j == 2) __builtin_amdgcn_s_setprio(2);
          if (t == 3 && j == 3) {
            __builtin_amdgcn_s_setprio(1);
            const int wf = fresh_s(wid);
#pragma unroll
            for (int i = 0; i < 13; ++i) wq[i] = proj_ld<false>(rs, i, wf, lane);
            proj_small<false>(rs, x, wf, fresh_tid() & 63, res, gate, cbv);
          }
          if (t == 6 && j == 3) { __builtin_amdgcn_s_setprio(0); if (wid == 7) glds4c(ws + OFF_CNT + (C_REL + (lane & 7)) * 256, relsc); }
        }
        if (FIRST && t < 5) {
#pragma unroll
          for (int i = 5 + 3 * t; i < 8 + 3 * t; ++i) glds16c(kvs + i * 4096, fresh_s(ldst) + i * 4096);
        }
        if (t == 7) {
          if (FIRST) { waitv<0>(); BAR(); }
          kp1 += jump; kp2 += jump;
        }
        SCHED();
        kf1[n] = *(const bf16x8*)(kp1 + ((t + 1) & 7) * 1280); kf2[n][0] = *(const unsigned*)(kp2 + ((t + 1) & 7) * 1280);
        GRP(sB = MFMA32(kf1[c], qB1, (f32x16){}), sA, 0, e1, e0, pB1, 2);
        vf[c][0] = *(const bf16x8*)vp; vf[c][1] = *(const bf16x8*)(vp + 640); vp += vinc;
        GRP(sB = MFMA32K8(BF4(kf2[c]), qB2, sB), sA, 4, e0, e1, pA0, 0);
        GRP(oB = MFMA32(vf[n][0], BF8(pB0), oB), sA, 8, e1, e0, pA0, 2);
        GRP(oB = MFMA32(vf[n][1], BF8(pB1), oB), sA, 12, e0, e1, pA1, 0);
        GRP(sA = MFMA32(kf1[n], qA1, (f32x16){}), sB, 0, e1, e0, pA1, 2);
        GRP(sA = MFMA32K8(BF4(kf2[n]), qA2, sA), sB, 4, e0, e1, pB0, 0);
        GRP(oA = MFMA32(vf[c][0], BF8(pA0), oA), sB, 8, e1, e0, pB0, 2);
        GRP(oA = MFMA32(vf[c][1], BF8(pA1), oA), sB, 12, e0, e1, pB1, 0);
      }
      vp += vjump;
    };
    group(std::true_type{}, 20480, 0);
    const int blk = fresh_s(blockIdx.x);
    const int j2 = (blk - 135) * 4 + qg;
    const bool side = kh == 0 && attempt == 0 && j2 < 270;
    char* stile = smem + 1024 + qg * 4096;
    const char* svec = smem + 81920 + qg * 256;
    const int kt = blk < 135 ? 4 * (blk % 5) + qg : j2 / 10, ng = blk < 135 ? blk / 5 : j2 % 10;
    if (side) {
      const u16* W = blk < 135 ? win : wout; const int N = blk < 135 ? F_ : D_;
      const int lf = fresh_tid() & 63, j8 = lf >> 3;
#pragma unroll
      for (int i = 0; i < 4; ++i) {
        const int row = kt * 32 + i * 8 + j8;
        glds16(W + (size_t)row * N + ng * 64 + ((lf & 7) ^ j8) * 8, lds_addr(stile) + i * 1024);
      }
      glds4c((const float*)(ws + OFF_MOD) + (4 - h) * 640 + (kt < 20 ? kt : 0) * 32 + r, lds_addr(svec));
    }
#pragma nounroll
    for (int j = 1; j < 4; ++j) {
      if (j == 2) {
        if (side) {
          waitv<0>();
          const int lf = fresh_tid() & 63, g4 = lf >> 4, r4 = lf & 15, row = 8 * g4 + (r4 >> 2);
          const f32x4 sv0 = *(const f32x4*)(svec + (r4 & 1) * 128 + g4 * 32), sv1 = *(const f32x4*)(svec + (r4 & 1) * 128 + g4 * 32 + 16);
          const bf16x8 vec = BF8(((u32x4){pack2(sv0[0], sv0[1]), pack2(sv0[2], sv0[3]), pack2(sv1[0], sv1[1]), pack2(sv1[2], sv1[3])}));
          const unsigned nks = blk < 135 ? 20 : 27, dso = (blk < 135 ? OFF_WIN : OFF_WOUT) + (ng * 4 * nks + kt) * 1024;
          bf16x8 fr[4]; f32x4 pr[4];
#pragma unroll
          for (int f = 0; f < 4; ++f) {
            const int ch = 2 * f + ((r4 & 3) >> 1);
            const bf16x4 lo = lds_tr(stile + row * 128 + (ch ^ (row & 7)) * 16 + (r4 & 1) * 8), hi = lds_tr(stile + (row + 4) * 128 + (ch ^ ((row + 4) & 7)) * 16 + (r4 & 1) * 8);
            fr[f] = __builtin_shufflevector(lo, hi, 0, 1, 2, 3, 4, 5, 6, 7);
          }
#pragma unroll
          for (int f = 0; f < 4; ++f) { stc16(rs, l16, dso + f * nks * 1024, __builtin_bit_cast(u32x4, fr[f])); pr[f] = MFMA16(fr[f], vec, (f32x4){}); }
          if (r4 < 2) {
#pragma unroll
            for (int f = 0; f < 4; ++f) *(f32x4*)(stile + 3072 + r4 * 256 + f * 64 + g4 * 16) = pr[f];
          }
        }
      }
      if (kh && j == 2) l2touch(wsp + (size_t)(blk & 7) * 1048576 + (((blk >> 3) * 4 + qg) * 64 + lane) * 128, lds_addr(smem) + 256);
      if (j == 3 && kh == 0 && attempt == 0) { waitv<0>(); arrive(ws, C_REL + xcd, lane); }
      group(std::false_type{}, j < 3 ? 20480 : 0, j);
    }
    pB1[2] = pack2(e0[0], e0[1]); pB1[3] = pack2(e0[2], e0[3]);
    oB = MFMA32(vf[1][0], BF8(pB0), oB); oB = MFMA32(vf[1][1], BF8(pB1), oB); PIN(oB);
    {
      f32x4* ex = (f32x4*)(lh + 1024 + qg * 4096) + lane;
#pragma unroll
      for (int g = 0; g < 3; ++g) ex[g * 64] = (f32x4){oA[4 * g], oA[4 * g + 1], oA[4 * g + 2], oA[4 * g + 3]};
    }
    int* flag = (int*)(smem + 528);
    BAR();
    float o[12];
    {
      const f32x4* ex = (const f32x4*)(smem + (kh ^ 1) * 81920 + 1024 + qg * 4096) + lane;
#pragma unroll
      for (int g = 0; g < 3; ++g) { const f32x4 t = ex[g * 64];
#pragma unroll
        for (int e = 0; e < 4; ++e) o[4 * g + e] = oB[4 * g + e] + t[e]; }
    }
    const float lo = xhalf(o[8]); const float l = h ? o[8] : lo;
    const float rl = __builtin_amdgcn_rcpf(l);
    const int tok = qb * 256 + qg * 64 + kh * 32 + r;
    const unsigned vo = (tok >> 4) * 20480 + (tok & 15) * 16;
    stc16(rs, vo + h * 256, OFF_O + (head >> 1) * 1024 + (head & 1) * 512,
          (u32x4){pack2(o[0] * rl, o[1] * rl), pack2(o[2] * rl, o[3] * rl), pack2(o[4] * rl, o[5] * rl), pack2(o[6] * rl, o[7] * rl)});
    if (h == 0) stc8(rs, vo - (tok & 15) * 8, OFF_O + 16384 + (head >> 3) * 1024 + (head & 1) * 512 + ((head >> 1) & 3) * 128, (u32x2){pack2(o[8] * rl, o[9] * rl), pack2(o[10] * rl, o[11] * rl)});
    if (wid == 4 && blk < 135 && attempt == 0 && lane < 32) {
      const char* pp = smem + 1024 + 3072 + lane * 16;
      const f32x4 v = (*(const f32x4*)pp + *(const f32x4*)(pp + 4096)) + (*(const f32x4*)(pp + 8192) + *(const f32x4*)(pp + 12288));
      stc16(rs, (lane >> 4) * 8192 + (lane & 15) * 16, OFF_CBP + (blk % 5) * 16384 + (blk / 5) * 256, __builtin_bit_cast(u32x4, v));
    }
    bool ok = (l > 1e-30f) && (l < 1e30f);
#pragma unroll
    for (int i = 0; i < 12; ++i) ok = ok && (fabsf(o[i]) < 3e38f);
    if (!ok && attempt == 0) *flag = 1;
    drain();
    BAR();
    const int redo = __builtin_amdgcn_readfirstlane(*(volatile LAS int*)flag);
    if (!redo) break;
    BAR();
  }
  if (wid == 7) arrive(ws, C_ATT + qb, lane);
#pragma unroll
  for (int i = 0; i < 13; ++i) proj_park(smem, i, tid, wq[i]);
  if (!__all(*(volatile LAS unsigned*)(smem + 768 + (lane & 7) * 4) >= 128u)) poll_ge(ws, C_REL + (lane & 7), 128);
}

template <bool FINAL, int KH, class HOOK>
DI void proj_body(char* __restrict__ ws, rsrc_t rs, const u16* __restrict__ x, u16* __restrict__ out, char* smem, int wid, u32x2 (&res)[3], const f32x4 (&gate)[3], const f32x4 (&cbv)[3], HOOK&& after_mma) {
  const int lane = fresh_tid() & 63;
  const int panel = blk_panel(), cb = blk_cb();
  constexpr int NKS = FINAL ? 27 : 20;
  constexpr int K0 = KH ? (NKS + 1) / 2 : 0, NK = KH ? NKS / 2 : (NKS + 1) / 2, NKEEP = KH ? 2 : 3, UK = KH ? 3 : 0, UG = KH ? 0 : 3;
  const int m = wid & 3, mt = panel * 4 + m, g = lane >> 4, r = lane & 15;
  unsigned l16 = lane * 16; PIN(l16);
  const unsigned aso = fresh_s((FINAL ? OFF_U : OFF_O) + (mt * NKS + K0) * 1024);
  if (wid == 7) poll_ge(ws, FINAL ? C_U + panel : C_ATT + (panel >> 2), FINAL ? 8 : 32);
  BAR();
  bf16x8 a[NK];
#pragma unroll
  for (int k = 0; k < NK; ++k) {
    if (!FINAL && K0 + k >= 16) {
      const u32x2 lo = ldc8(rs, (l16 >> 1) + (k & 3) * 1024, aso + (k >> 2) * 4096), hi = ldc8(rs, (l16 >> 1) + (k & 3) * 1024 + 512, aso + (k >> 2) * 4096);
      a[k] = BF8(((u32x4){lo[0], lo[1], hi[0], hi[1]}));
    } else a[k] = BF8(ldc16(rs, l16 + (k & 3) * 1024, aso + (k >> 2) * 4096));
  }
  SCHED();
  constexpr int PD = FINAL ? 4 : 5, RG = PD + 2;
  const char* bl = smem + lane * 16;
  bf16x8 bf[RG][5]; f32x4 acc[5];
#pragma unroll
  for (int u = 0; u < 5; ++u) acc[u] = (f32x4){};
#define PJ_RD(k, u) (bf[(k) % RG][u] = *(const bf16x8*)(bl + proj_pos((u) * NKS + K0 + (k))))
#pragma unroll
  for (int k = 0; k < PD; ++k)
#pragma unroll
    for (int u = 0; u < 5; ++u) PJ_RD(k, u);
  SCHED();
#pragma unroll
  for (int k = 0; k < NK; k += 2) {
#pragma unroll
    for (int u = 0; u < 5; ++u) {
      if (k + PD < NK) PJ_RD(k + PD, u);
      if (k + PD + 1 < NK) PJ_RD(k + PD + 1, u);
      if (k + 1 < NK) acc[u] = MFMA16(bf[(k + 1) % RG][u], a[k + 1], acc[u]);
      acc[u] = MFMA16(bf[k % RG][u], a[k], acc[u]); PIN(acc[u]);
      SCHED();
    }
  }
  f32x4* ex = (f32x4*)(smem + proj_pos(FINAL ? 136 : 104)) + m * 5 * 64 + lane;
#pragma unroll
  for (int i = 0; i < 5 - NKEEP; ++i) ex[(UG + i) * 64] = acc[UG + i];
  f32x4 part[NKEEP];
#pragma unroll
  for (int i = 0; i < NKEEP; ++i) {
    part[i] = fma4s(gate[i], acc[UK + i], (f32x4){bflo(res[i][0]), bfhi(res[i][0]), bflo(res[i][1]), bfhi(res[i][1])});
    PIN(part[i]);
  }
  SCHED();
  after_mma();
  BAR();
  f32x4 oth[NKEEP];
#pragma unroll
  for (int i = 0; i < NKEEP; ++i) oth[i] = ex[(UK + i) * 64];
  f32x4 sv = (f32x4){}, qv = (f32x4){};
#pragma unroll
  for (int i = 0; i < NKEEP; ++i) {
    const f32x4 v = part[i] + gate[i] * oth[i];
    const u32x2 o = (u32x2){pack2(v[0], v[1]), pack2(v[2], v[3])};
    if (FINAL) *(u32x2*)(out + (size_t)(mt * 16 + r) * D_ + (cb * 5 + UK + i) * 16 + g * 4) = o;
    else {
      res[i] = o; PIN(res[i]);
      const f32x4 w = v * cbv[i]; const int un = cb * 5 + UK + i;
      stc8(rs, ((g >> 1) * 16 + r) * 16 + (g & 1) * 8, OFF_X1S + mt * 20480 + un * 512, (u32x2){pack2(w[0], w[1]), pack2(w[2], w[3])});
      sv += v; qv += v * v;
    }
  }
  if (!FINAL) {
    const float s = sum_g((sv[0] + sv[1]) + (sv[2] + sv[3])), q = sum_g((qv[0] + qv[1]) + (qv[2] + qv[3]));
    if (g == 0) stc8(rs, r * 8, OFF_ST1 + ((panel * 16 + cb * 2 + KH) * 64 + m * 16) * 8, (u32x2){__float_as_uint(s), __float_as_uint(q)});
    drain();
    BAR();
    if (wid == 7) arrive(ws, C_X1 + panel, lane);
  }
}

constexpr float GC1 = -2.8853900817779268f * 0.7978845608028654f, GC2 = GC1 * 0.044715f;
DI void gelu8(f32x4& a0, const f32x4& l0, f32x4& a1, const f32x4& l1) {
  float t[8];
#define G8(expr0, expr1) do { _Pragma("unroll") for (int i = 0; i < 4; ++i) { t[i] = opq(expr0); } _Pragma("unroll") for (int i = 0; i < 4; ++i) { t[4 + i] = opq(expr1); } SCHED(); } while (0)
  G8(a0[i] * a0[i], a1[i] * a1[i]);
  G8(__builtin_fmaf(t[i], l0[3], l0[2]), __builtin_fmaf(t[4 + i], l1[3], l1[2]));
  G8(a0[i] * t[i], a1[i] * t[4 + i]);
  G8(__builtin_amdgcn_exp2f(t[i]), __builtin_amdgcn_exp2f(t[4 + i]));
  G8(__builtin_fmaf(t[i], l0[0], l0[0]), __builtin_fmaf(t[4 + i], l1[0], l1[0]));
  G8(__builtin_amdgcn_rcpf(t[i]), __builtin_amdgcn_rcpf(t[4 + i]));
  G8(a0[i] * t[i], a1[i] * t[4 + i]);
  a0 = (f32x4){t[0], t[1], t[2], t[3]}; a1 = (f32x4){t[4], t[5], t[6], t[7]};
}
constexpr int L4_Z1 = 114688, L4_TR = L4_Z1 + 16384, L4_LN = L4_TR + 16384, L4_ST2 = L4_LN + 1024, L4_FIN = L4_ST2 + 3584, L4_CB = L4_FIN + 512, L4_CNT = L4_CB + 2048;
constexpr int K5_R = 7;
constexpr unsigned DEAD = 0x80000000u;
DI int sp_stage(int s) { const int cb = blk_cb(); return (s + cb * 2) & 15; }
DI unsigned sp_voff(int wid, int lane, int i) { return ((wid & 3) * 16 + i * 8 + (lane >> 3)) * 4096 + (wid >> 2) * 128 + ((lane & 7) ^ (4 * i + (lane >> 4))) * 16; }
DI const char* sp_base(const u16* wsp, int panel, int s) { return (const char*)wsp + (size_t)panel * 262144 + sp_stage(s) * 256; }
DI void sp_prefetch(u32x4 (&wr)[16 - K5_R][2], const u16* __restrict__ wsp, int panel, int wid, int lane) {
  const unsigned v0 = sp_voff(wid, lane, 0), v1 = sp_voff(wid, lane, 1);
#pragma unroll
  for (int s = K5_R; s < 16; ++s) { wr[s - K5_R][0] = *(const u32x4*)(sp_base(wsp, panel, s) + v0); wr[s - K5_R][1] = *(const u32x4*)(sp_base(wsp, panel, s) + v1); }
  asm volatile("" ::: "memory");
}
template <int S0, int S1>
DI void sp_ring(const u16* __restrict__ wsp, char* smem, int panel, int wid, int lane) {
  const unsigned v0 = sp_voff(wid, lane, 0), v1 = sp_voff(wid, lane, 1);
#pragma unroll
  for (int s = S0; s < S1; ++s) { glds16(sp_base(wsp, panel, s) + v0, lds_addr(smem) + wid * 2048 + s * 16384); glds16(sp_base(wsp, panel, s) + v1, lds_addr(smem) + wid * 2048 + s * 16384 + 1024); }
}
DI unsigned win_boff(int wid, int lane) { const int cb = blk_cb(); return lane * 16 + (wid == 6 && cb >= 6 ? DEAD : 0u); }
DI unsigned win_soff(int wid, bool z2) { return OFF_WIN + ((z2 ? 54 : 0) + unit0_of(blk_cb()) + wid) * 20480; }
#define WIN_LD(so, k) BF8(ldc16(rs, bo + ((k) & 3) * 1024, (so) + ((k) >> 2) * 4096))
DI void win_prefetch(rsrc_t rs, int wid, bf16x8 (&b2)[20]) {
  const unsigned bo = win_boff(wid, fresh_tid() & 63), so = win_soff(wid, true);
#pragma unroll
  for (int k = 0; k < 20; ++k) b2[k] = WIN_LD(so, k);
}
template <class HOOK>
DI void win_mma(const char* al, const bf16x8 (&b)[20], f32x4 (&acc)[4], HOOK&& hook) {
  constexpr int PD = 4;
  bf16x8 af[PD + 1][2];
#define WIN_RD(i) do { af[(i) % (PD + 1)][0] = *(const bf16x8*)(al + ((i) & 3) * 20480 + ((i) >> 2) * 2048); af[(i) % (PD + 1)][1] = *(const bf16x8*)(al + ((i) & 3) * 20480 + ((i) >> 2) * 2048 + 1024); } while (0)
#pragma unroll
  for (int i = 0; i < PD; ++i) WIN_RD(i);
  SCHED();
  sfor<40>([&](auto I) __attribute__((always_inline)) {
    constexpr int i = I, m = i & 3, k = (i >> 2) * 2;
    if constexpr (i + PD < 40) WIN_RD(i + PD);
    acc[m] = MFMA16(b[k + 1], af[i % (PD + 1)][1], acc[m]);
    acc[m] = MFMA16(b[k], af[i % (PD + 1)][0], acc[m]); PIN(acc[m]);
    hook(I);
    SCHED();
  });
}
DI void stage_x1s(char* __restrict__ ws, char* smem, int panel, int wid, int lane) {
  const char* src = ws + OFF_X1S + (size_t)panel * 81920 + lane * 16;
#pragma unroll
  for (int i = 0; i < 12; ++i) { const int f = i < 11 ? i * 7 + wid : min(77 + wid, 79); glds16c(src + f * 1024, lds_addr(smem) + f * 1024); }
}
DI void load_bb(const u16* __restrict__ bsp, int panel, int wid, int lane, u16 (&bb)[2]) { const u16* p = bsp + panel * 64 + (wid >> 2) * 32 + (lane & 15); bb[0] = p[0]; bb[1] = p[16]; asm volatile("" ::: "memory"); }
DI void win_tail(char* __restrict__ ws, const u16* __restrict__ wsp, char* smem, int panel, int cb, int wid, int lane) {
  BAR();
  if (wid == 7 && lane < 4) __hip_atomic_fetch_add((GAS unsigned*)(ws + OFF_CNT + (C_Z2 + lane * 8 + cb) * 256), 1u, __ATOMIC_RELAXED, __HIP_MEMORY_SCOPE_AGENT);
}

template <bool EARLY>
DI void win_body(char* __restrict__ ws, rsrc_t rs, const u16* __restrict__ wsp, char* smem, int wid, const bf16x8 (&b2)[20], u32x4 (&wr)[16 - K5_R][2], const u16* __restrict__ bsp, u16 (&bb)[2]) {
  const int tid = fresh_tid(), lane = tid & 63, g = lane >> 4, r = lane & 15;
  const int panel = blk_panel(), cb = blk_cb(), u0 = unit0_of(cb);
  float* lnp = (float*)(smem + L4_LN); float* st2 = (float*)(smem + L4_ST2); float* fin = (float*)(smem + L4_FIN); float* cbl = (float*)(smem + L4_CB);
  int* flg = (int*)(smem + L4_CNT);
  __builtin_amdgcn_s_setprio(1);
  const unsigned bo = win_boff(wid, lane), so1 = win_soff(wid, false);
  bf16x8 b1[20];
  BAR();
  stage_x1s(ws, smem, panel, wid, lane);
  BAR();
  f32x4 ln[4], z2[4], z1[4];
#pragma unroll
  for (int m = 0; m < 4; ++m) ln[m] = *(const f32x4*)(lnp + (m * 16 + r) * 4);
  {
    const f32x4 cs2 = *(const f32x4*)(cbl + 112 + wid * 16 + g * 4), bi2 = *(const f32x4*)(cbl + 256 + 112 + wid * 16 + g * 4);
    const f32x4 cs1 = *(const f32x4*)(cbl + wid * 16 + g * 4), bi1 = *(const f32x4*)(cbl + 256 + wid * 16 + g * 4);
#pragma unroll
    for (int m = 0; m < 4; ++m) {
#pragma unroll
      for (int e = 0; e < 4; ++e) { z2[m][e] = opq(__builtin_fmaf(ln[m][1], cs2[e], opq(ln[m][0] * bi2[e]))); z1[m][e] = opq(__builtin_fmaf(ln[m][1], cs1[e], opq(ln[m][0] * bi1[e]))); }
      PIN(z1[m]); PIN(z2[m]);
    }
  }
#pragma unroll
  for (int k = 0; k < 4; ++k) b1[k] = WIN_LD(so1, k);
  waitv<4>();
  BAR();
  load_bb(bsp, panel, wid, lane, bb);
  win_mma(smem + lane * 16, b2, z2, [&](auto I) __attribute__((always_inline)) { constexpr int i = I; if constexpr (i < 32 && (i & 1)) b1[4 + i / 2] = WIN_LD(so1, 4 + i / 2); });
  gelu8(z2[0], ln[0], z2[1], ln[1]); gelu8(z2[2], ln[2], z2[3], ln[3]);
  {
    float s[4], q[4];
#pragma unroll
    for (int m = 0; m < 4; ++m) { s[m] = opq(opq(z2[m][0] + z2[m][1]) + opq(z2[m][2] + z2[m][3])); q[m] = opq(__builtin_fmaf(z2[m][0], z2[m][0], opq(z2[m][1] * z2[m][1])) + opq(__builtin_fmaf(z2[m][2], z2[m][2], opq(z2[m][3] * z2[m][3])))); }
    auto pa = __builtin_amdgcn_permlane32_swap(__float_as_uint(s[0]), __float_as_uint(s[2]), false, false);
    auto pb = __builtin_amdgcn_permlane32_swap(__float_as_uint(s[1]), __float_as_uint(s[3]), false, false);
    auto pc = __builtin_amdgcn_permlane32_swap(__float_as_uint(q[0]), __float_as_uint(q[2]), false, false);
    auto pd = __builtin_amdgcn_permlane32_swap(__float_as_uint(q[1]), __float_as_uint(q[3]), false, false);
    const float sa = opq(__uint_as_float(pa[0]) + __uint_as_float(pa[1])), sb = opq(__uint_as_float(pb[0]) + __uint_as_float(pb[1]));
    const float qa = opq(__uint_as_float(pc[0]) + __uint_as_float(pc[1])), qb = opq(__uint_as_float(pd[0]) + __uint_as_float(pd[1]));
    auto ps = __builtin_amdgcn_permlane16_swap(__float_as_uint(sa), __float_as_uint(sb), false, false);
    auto pq = __builtin_amdgcn_permlane16_swap(__float_as_uint(qa), __float_as_uint(qb), false, false);
    *(f32x2*)(st2 + (wid * 64 + lane) * 2) = (f32x2){opq(__uint_as_float(ps[0]) + __uint_as_float(ps[1])), opq(__uint_as_float(pq[0]) + __uint_as_float(pq[1]))};
  }
  asm volatile("" ::: "memory");
  { unsigned o = lds_addr(flg); asm("" : "+v"(o)); if (lane == 0) __hip_atomic_fetch_add((LAS int*)(uintptr_t)o, 1, __ATOMIC_RELAXED, __HIP_MEMORY_SCOPE_WORKGROUP); }
  asm volatile("" ::: "memory");
  __builtin_amdgcn_s_setprio(0);
  const unsigned wvo[2] = {sp_voff(wid, lane, 0), sp_voff(wid, lane, 1)};
  win_mma(smem + lane * 16, b1, z1, [&](auto I) __attribute__((always_inline)) {
    constexpr int i = I, j = i < 8 ? i : (i & 1) ? -1 : 4 + i / 2;
    if constexpr (EARLY && j >= 0 && j < 18) wr[j >> 1][j & 1] = *(const u32x4*)(sp_base(wsp, panel, K5_R + (j >> 1)) + wvo[j & 1]);
  });
  gelu8(z1[0], ln[0], z1[1], ln[1]); gelu8(z1[2], ln[2], z1[3], ln[3]);
#pragma unroll
  for (int mm = 0; mm < 2; ++mm)
    *(u32x4*)(smem + L4_Z1 + wid * 2048 + mm * 1024 + lane * 16) = (u32x4){pack2(z1[2 * mm][0], z1[2 * mm][1]), pack2(z1[2 * mm][2], z1[2 * mm][3]), pack2(z1[2 * mm + 1][0], z1[2 * mm + 1][1]), pack2(z1[2 * mm + 1][2], z1[2 * mm + 1][3])};
  BAR();
  char* tr = smem + L4_TR + wid * 2048;
#pragma unroll
  for (int m = 0; m < 4; ++m) {
    const f32x2 f = *(const f32x2*)(fin + (m * 16 + r) * 2);
    const f32x4 v = z2[m] * f[0] - f[1];
    *(u32x2*)(tr + (m * 16 + r) * 32 + g * 8) = (u32x2){pack2(v[0], v[1]), pack2(v[2], v[3])};
  }
  {
    const char* p = tr + (8 * g + (r >> 2)) * 32 + (r & 3) * 8;
    const bf16x4 lo0 = lds_tr(p), hi0 = lds_tr(p + 128), lo1 = lds_tr(p + 1024), hi1 = lds_tr(p + 1152);
    stc16(rs, bo, OFF_Z2 + (panel * 2 * 54 + u0 + wid) * 1024, __builtin_bit_cast(u32x4, (bf16x8)__builtin_shufflevector(lo0, hi0, 0, 1, 2, 3, 4, 5, 6, 7)));
    stc16(rs, bo, OFF_Z2 + ((panel * 2 + 1) * 54 + u0 + wid) * 1024, __builtin_bit_cast(u32x4, (bf16x8)__builtin_shufflevector(lo1, hi1, 0, 1, 2, 3, 4, 5, 6, 7)));
  }
  SCHED();
  waitv<0>();
  win_tail(ws, wsp, smem, panel, cb, wid, lane);
  sp_ring<0, 5>(wsp, smem, panel, wid, lane);
  if (!EARLY) sp_prefetch(wr, wsp, panel, wid, lane);
}

DI void win_agent(char* __restrict__ ws, rsrc_t rs, const u16* __restrict__ wsp, char* smem, u32x4 (&wr)[16 - K5_R][2], const u16* __restrict__ bsp, u16 (&bb)[2]) {
  const int tid = fresh_tid(), lane = tid & 63, wid = 7;
  const int panel = blk_panel(), cb = blk_cb(), u0 = unit0_of(cb);
  float* lnp = (float*)(smem + L4_LN); float* st2 = (float*)(smem + L4_ST2); float* fin = (float*)(smem + L4_FIN); float* cbl = (float*)(smem + L4_CB);
  int* cnt = (int*)(smem + L4_CNT);
  __builtin_amdgcn_s_setprio(2);
  if (lane == 0) *cnt = 0;
  poll_ge(ws, lane < 8 ? C_ATT + lane : C_X1 + panel, lane < 8 ? 32 : 8);
  BAR();
  {
    u32x2 t[16]; f32x4 cp[5][2];
#pragma unroll
    for (int i = 0; i < 16; ++i) t[i] = ldc8(rs, lane * 8 + (i & 7) * 512, OFF_ST1 + (panel * 16 + (i & 8)) * 512);
    const unsigned co = lane < 28 ? (u0 * 16 + lane * 4) * 4 : min((54 + u0) * 16 + min(lane - 28, 27) * 4, F_ - 4) * 4;
#pragma unroll
    for (int i = 0; i < 5; ++i) { cp[i][0] = ldc16f(rs, co, OFF_CBP + i * 16384); cp[i][1] = ldc16f(rs, co, OFF_CBP + i * 16384 + 8192); }
    float s = 0.f, q = 0.f;
#pragma unroll
    for (int i = 0; i < 16; ++i) { s += __uint_as_float(t[i][0]); q += __uint_as_float(t[i][1]); }
    const float mean = s * (1.f / 640.f), var = fmaxf(q * (1.f / 640.f) - mean * mean, 0.f);
    const float den = ln_den(var), ri = __builtin_amdgcn_rcpf(den);
    *(f32x4*)(lnp + lane * 4) = (f32x4){den, -rbf(mean), GC1 * ri, GC2 * ri * ri * ri};
#pragma unroll
    for (int v = 0; v < 2; ++v) *(f32x4*)(cbl + v * 256 + lane * 4) = ((cp[0][v] + cp[1][v]) + (cp[2][v] + cp[3][v])) + cp[4][v];
  }
  BAR();
  BAR();
  __builtin_amdgcn_s_setprio(0);
  __builtin_amdgcn_s_sleep(8);
  load_bb(bsp, panel, wid, lane, bb);
  sp_prefetch(wr, wsp, panel, wid, lane);
#pragma nounroll
  for (int w = 0; w < 8; ++w) sp_ring<5, 7>(wsp, smem, panel, w, lane);
  __builtin_amdgcn_s_setprio(2);
#pragma nounroll
  for (unsigned spin = 0; spin < (1u << 22); ++spin) {
    if (__hip_atomic_load(cnt, __ATOMIC_RELAXED, __HIP_MEMORY_SCOPE_WORKGROUP) >= 7) break;
    __builtin_amdgcn_s_sleep(1);
  }
  asm volatile("" ::: "memory");
  float s, q;
  {
    f32x2 t[7];
#pragma unroll
    for (int i = 0; i < 7; ++i) t[i] = *(const f32x2*)(st2 + (i * 64 + lane) * 2);
    s = t[0][0]; q = t[0][1];
#pragma unroll
    for (int i = 1; i < 6; ++i) { s = opq(s + t[i][0]); q = opq(q + t[i][1]); }
    if (cb < 6) { s = opq(s + t[6][0]); q = opq(q + t[6][1]); }
  }
  GAS u64* xch = (GAS u64*)(ws + OFF_XCH) + panel * 512;
  unsigned qb = __float_as_uint(q); if (qb == ~0u) qb = 0x7fc00000u;
  __hip_atomic_store(xch + cb * 64 + lane, ((u64)qb << 32) | __float_as_uint(s), __ATOMIC_RELAXED, __HIP_MEMORY_SCOPE_AGENT);
  u64 v[8];
#pragma nounroll
  for (unsigned spin = 0; spin < (1u << 19); ++spin) {
    bool ok = true;
#pragma unroll
    for (int i = 0; i < 8; ++i) { v[i] = __hip_atomic_load(xch + i * 64 + lane, __ATOMIC_RELAXED, __HIP_MEMORY_SCOPE_AGENT); ok = ok && ((unsigned)(v[i] >> 32) != ~0u); }
    if (__all(ok)) break;
    __builtin_amdgcn_s_sleep(1);
  }
  s = 0.f; q = 0.f;
#pragma unroll
  for (int i = 0; i < 8; ++i) { s = opq(s + __uint_as_float((unsigned)v[i])); q = opq(q + __uint_as_float((unsigned)(v[i] >> 32))); }
  const float mean = opq(s * (1.f / 864.f)), var = fmaxf(opq(q * (1.f / 864.f)) - mean * mean, 0.f);
  const float ri = __builtin_amdgcn_rcpf(ln_den(var));
  *(f32x2*)(fin + lane * 2) = (f32x2){ri, rbf(mean) * ri};
  __builtin_amdgcn_s_setprio(0);
  BAR();
  __builtin_amdgcn_s_sleep(6);
  sp_ring<0, 5>(wsp, smem, panel, wid, lane);
  win_tail(ws, wsp, smem, panel, cb, wid, lane);
}

constexpr int sp_q(int t) { const int w = 4 * 2 + t / 3; return t + (w < 4 * 4 ? w : 4 * 4); }
DI void phase_sp(char* __restrict__ ws, rsrc_t rs, const u16 (&bb)[2], char* smem, int wid, const u32x4 (&wr)[16 - K5_R][2], u32x2 (&res)[3], f32x4 (&gate)[3]) {
  const int tid = fresh_tid(), lane = tid & 63;
  const int panel = blk_panel(), cb = blk_cb();
  const int kh = wid >> 2, up = wid & 3, u0 = unit0_of(cb) + 2 * up, nu = cb < 6 ? 7 : 6, g = lane >> 4, r = lane & 15;
  const unsigned dead[2] = {2 * up < nu ? 0u : DEAD, 2 * up + 1 < nu ? 0u : DEAD};
  unsigned bvo[2] = {lane * 16 + dead[0], lane * 16 + 1024 + dead[1]};
  const char* al[2][2];
#pragma unroll
  for (int k = 0; k < 2; ++k)
#pragma unroll
    for (int j = 0; j < 2; ++j) al[k][j] = smem + kh * 8192 + (j ^ kh) * 4096 + r * 128 + ((4 * k + g) ^ (r >> 1)) * 16;
  constexpr int LB = 4, PD = 3;
  bf16x8 bq[LB + 1][4], af[PD + 1][2]; f32x4 acc[2][4]; u32x4 wq[17];
#pragma unroll
  for (int i = 0; i < 2; ++i) {
    const float b0 = bf2f(bb[0]), b1 = bf2f(bb[1]);
    acc[i][0] = (f32x4){b0, b0, b0, b0}; acc[i][1] = (f32x4){b1, b1, b1, b1}; acc[i][2] = (f32x4){}; acc[i][3] = (f32x4){};
  }
  const unsigned bso = fresh_s(OFF_Z2 + (kh * 2 * 54 + u0) * 1024);
  PIN(bvo[0]); PIN(bvo[1]);
  if (wid == 7) poll_ge(ws, C_Z2 + (panel >> 3) * 8 + cb, 32);
  else if (kh && up < 3) waitv<2 * (16 - K5_R) + 6>(); else waitv<6>();
  BAR();
#define SP_LD(s, j) (bq[(s) % (LB + 1)][j] = BF8(ldc16(rs, bvo[(j) & 1], bso + (sp_stage(s) * 4 + ((j) >> 1)) * 55296)))
#define SP_RD(t, k) (af[(t) % (PD + 1)][k] = *(const bf16x8*)(al[k][((t) >> 1) & 1] + (((t) >> 2) % K5_R) * 16384 + ((t) & 1) * 2048))
#pragma unroll
  for (int q = 0; q < sp_q(0); ++q) SP_LD(q >> 2, q & 3);
#pragma unroll
  for (int t = 0; t < PD; ++t) { SP_RD(t, 0); SP_RD(t, 1); }
  SCHED();
  sfor<64>([&](auto T) __attribute__((always_inline)) {
    constexpr int t = T, nx = t + PD, s = t >> 2, m = t & 3, tl = sp_q(t), t2 = sp_q(t + 1) - 1;
    if constexpr (nx == 8) asm volatile("s_barrier" ::: "memory");
    if constexpr (nx == 16 || nx == 28 || nx == 44 || nx == 56) BAR();
    constexpr int c = nx >= 16 && nx < 24 ? nx - 16 : nx >= 28 && nx < 34 ? nx - 20 : nx >= 44 && nx < 48 ? nx - 30 : -1;
    if constexpr (nx < 64) SP_RD(nx, 0);
    acc[0][m] = MFMA16(bq[s % (LB + 1)][2], af[t % (PD + 1)][1], acc[0][m]);
    acc[0][m] = MFMA16(bq[s % (LB + 1)][0], af[t % (PD + 1)][0], acc[0][m]); PIN(acc[0][m]);
    if constexpr (tl < 64) SP_LD(tl >> 2, tl & 3); else if constexpr (tl - 64 < 17) wq[tl - 64] = proj_ld<true>(rs, tl - 64, wid, lane);
    SCHED();
    if constexpr (nx < 64) SP_RD(nx, 1);
    acc[1][m] = MFMA16(bq[s % (LB + 1)][1], af[t % (PD + 1)][0], acc[1][m]);
    acc[1][m] = MFMA16(bq[s % (LB + 1)][3], af[t % (PD + 1)][1], acc[1][m]); PIN(acc[1][m]);
    if constexpr (t2 > tl && t2 < 64) SP_LD(t2 >> 2, t2 & 3);
    if constexpr (c >= 0) *(u32x4*)(smem + ((c >> 1) % K5_R) * 16384 + wid * 2048 + (c & 1) * 1024 + lane * 16) = wr[c >> 1][c & 1];
    if constexpr (t == 63 && 4 * LB < 17) { _Pragma("unroll") for (int i = 4 * LB; i < 17; ++i) wq[i] = proj_ld<true>(rs, i, wid, lane); }
    if constexpr (t == 62) proj_small<true>(rs, nullptr, wid, lane, res, gate, gate);
    SCHED();
  });
  f32x4* exw = (f32x4*)(smem + 32768 + wid * 4096) + lane;
  const f32x4* exr = (const f32x4*)(smem + 32768 + (wid ^ 4) * 4096) + lane;
  u32x4 z[2];
#pragma unroll
  for (int i = 0; i < 2; ++i) z[i] = *(const u32x4*)(smem + L4_Z1 + (2 * up + i) * 2048 + kh * 1024 + lane * 16);
#pragma unroll
  for (int i = 0; i < 2; ++i)
#pragma unroll
    for (int j = 0; j < 2; ++j) exw[(i * 2 + j) * 64] = acc[i][2 + j];
  f32x4 zf[2][2];
#pragma unroll
  for (int i = 0; i < 2; ++i)
#pragma unroll
    for (int j = 0; j < 2; ++j) {
      zf[i][j] = (f32x4){bflo(z[i][2 * j]), bfhi(z[i][2 * j]), bflo(z[i][2 * j + 1]), bfhi(z[i][2 * j + 1])};
      acc[i][j] = mul4s(acc[i][j], zf[i][j]); PIN(zf[i][j]); PIN(acc[i][j]);
    }
  BAR();
  f32x4 oth[2][2];
#pragma unroll
  for (int i = 0; i < 2; ++i)
#pragma unroll
    for (int j = 0; j < 2; ++j) oth[i][j] = exr[(i * 2 + j) * 64];
#pragma unroll
  for (int i = 0; i < 2; ++i)
#pragma unroll
    for (int j = 0; j < 2; ++j) {
      const f32x4 v = acc[i][j] + zf[i][j] * oth[i][j];
      stc8(rs, ((g >> 1) * 16 + r) * 16 + (g & 1) * 8 + dead[i], OFF_U + (panel * 4 + 2 * kh + j) * 27648 + (u0 + i) * 512, (u32x2){pack2(v[0], v[1]), pack2(v[2], v[3])});
    }
  drain();
  BAR();
  if (wid == 7) arrive(ws, C_U + panel, lane);
#pragma unroll
  for (int i = 0; i < 17; ++i) proj_park(smem, i, tid, wq[i]);
}

extern "C" __global__ __launch_bounds__(512, 2) OCC2 void k_mega(char* __restrict__ ws, const u16* __restrict__ x, const u16* __restrict__ win,
                                                     const u16* __restrict__ wsp, const u16* __restrict__ bsp, const u16* __restrict__ wout, u16* __restrict__ out) {
  extern __shared__ __attribute__((aligned(16))) char smem[];
  const int wid = __builtin_amdgcn_readfirstlane(threadIdx.x >> 6);
  const rsrc_t rs = __builtin_amdgcn_make_buffer_rsrc(ws, 0, WS_USED, 0x00020000);
  u32x2 res[3]; f32x4 gate[3], cbv[3];
  phase_qkv(ws, rs, smem, wid);
  phase_attn(ws, rs, (const char*)wsp, win, wout, x, smem, wid, res, gate, cbv);
  u32x4 wr[16 - K5_R][2];
  auto nohook = []() __attribute__((always_inline)) {};
  u16 bb[2];
  if (wid >= 4) {
    proj_body<false, 1>(ws, rs, x, out, smem, wid, res, gate, cbv, nohook);
    if (wid == 7) win_agent(ws, rs, wsp, smem, wr, bsp, bb);
    else { bf16x8 b2[20]; win_prefetch(rs, wid, b2); win_body<false>(ws, rs, wsp, smem, wid, b2, wr, bsp, bb); }
#pragma unroll
    for (int i = 0; i < 3; ++i) { gate[i] = (f32x4){}; cbv[i] = (f32x4){}; }
  }
  const int w2 = fresh_s(wid);
  if (w2 < 4) {
    bf16x8 b2[20];
    proj_body<false, 0>(ws, rs, x, out, smem, w2, res, gate, cbv, [&]() __attribute__((always_inline)) { win_prefetch(rs, w2, b2); });
    win_body<true>(ws, rs, wsp, smem, w2, b2, wr, bsp, bb);
  }
  phase_sp(ws, rs, bb, smem, wid, wr, res, gate);
  if (wid < 4) proj_body<true, 0>(ws, rs, x, out, smem, wid, res, gate, cbv, nohook);
  else proj_body<true, 1>(ws, rs, x, out, smem, wid, res, gate, cbv, nohook);
  asm volatile("k_mega_code_end:");
}

extern "C" void kernel_launch(void* const* d_in, const int* in_sizes, int n_in, void* d_out, int out_size, void* d_ws, size_t ws_size,
                              hipStream_t stream) {
  const u16* x = (const u16*)d_in[0];
  const u16* wqkv = (const u16*)d_in[1];
  const u16* wo = (const u16*)d_in[2];
  const u16* win = (const u16*)d_in[4];
  const u16* wsp = (const u16*)d_in[5];
  const u16* bsp = (const u16*)d_in[6];
  const u16* wout = (const u16*)d_in[7];
  const u16* cvec = (const u16*)d_in[8];
  const u16* wmod = (const u16*)d_in[9];
  char* ws = (char*)d_ws;
  (void)hipFuncSetAttribute((const void*)k_mega, hipFuncAttributeMaxDynamicSharedMemorySize, 163840);
  k0_prep<<<K0_GRID, 256, 0, stream>>>(x, wqkv, wo, cvec, wmod, ws);
  k_mega<<<256, 512, 163840, stream>>>(ws, x, win, wsp, bsp, wout, (u16*)d_out);
}
```
